```python
import torch

def _softmax(x, axis=-1):
    return torch.softmax(x, dim=axis)
def _gelu(x):
    return torch.nn.functional.gelu(x, approximate="tanh")
def _silu(x):
    return torch.nn.functional.silu(x)
def _sigmoid(x):
    return torch.sigmoid(x)
def _layernorm(x, axis=-1, eps=1e-5):
    m = x.mean(dim=axis, keepdim=True)
    v = x.var(dim=axis, keepdim=True, unbiased=False)
    return (x - m) / torch.sqrt(v + eps)
def _scatter_add(tgt, idx, src, axis=0):
    return tgt.clone().index_add_(axis, idx, src)
def _segment_sum(src, seg, n):
    out = torch.zeros((n,) + src.shape[1:], dtype=src.dtype)
    return out.index_add_(0, seg, src)
def _pad(x, pw):
    flat = tuple(v for pair in reversed(pw) for v in pair)
    return torch.nn.functional.pad(x, flat)

kernel_name = 'gated_mix__linear_attn_block__mixer_channel_mlp__prompt'

def setup_inputs():

    _w = (lambda a: (lambda t: t.to(torch.bfloat16) if t.is_floating_point() else t)(torch.as_tensor(a)))
    def _A_setup():
        B, S, D = 1, 2048, 576
        return {
            "x": _w((lambda *s: torch.randn(*s, dtype=torch.bfloat16) * 0.05)(B, S, D)),
            "wqkv": _w((lambda *s: torch.randn(*s, dtype=torch.bfloat16) * 0.8)(D, 3 * D)),
            "wo": _w((lambda *s: torch.randn(*s, dtype=torch.bfloat16) * 0.2)(D, D)),
        }
    _A_in = _A_setup()

    def _B_setup():
        B, S, D, F = 1, 2048, 576, 4608
        return {
            "x": _w((lambda *s: torch.randn(*s, dtype=torch.bfloat16) * 0.05)(B, S, D)),
            "w1": _w((lambda *s: torch.randn(*s, dtype=torch.bfloat16) * 1.6)(D, F) * (D ** -0.5)),
            "b1": _w((lambda *s: torch.randn(*s, dtype=torch.bfloat16) * 0.4)(F)),
            "w2": _w((lambda *s: torch.randn(*s, dtype=torch.bfloat16) * 0.05)(F, D) * (F ** -0.5)),
            "b2": _w((lambda *s: torch.randn(*s, dtype=torch.bfloat16) * 0.05)(D)),
        }
    _B_in = _B_setup()

    return {
        "A_x": _A_in["x"],
        "A_wqkv": _A_in["wqkv"],
        "A_wo": _A_in["wo"],
        "B_x": _B_in["x"],
        "B_w1": _B_in["w1"],
        "B_b1": _B_in["b1"],
        "B_w2": _B_in["w2"],
        "B_b2": _B_in["b2"],
        "gate_w": _w((lambda *s: torch.randn(*s, dtype=torch.bfloat16) * 0.05)(576) * 16.0),
    }

def reference(A_x, A_wqkv, A_wo, B_x, B_w1, B_b1, B_w2, B_b2, gate_w):
    def _A_ref(x, wqkv, wo):
        B, S, D = x.shape
        H = 32; Dh = D // H
        qkv = (lambda x, *d: x.permute(*d))((x @ wqkv).reshape(B, S, 3, H, Dh), 2, 0, 3, 1, 4)
        q, k, v = qkv[0], qkv[1], qkv[2]
        phi = lambda t: torch.where(t > 0, t + 1, torch.exp(t)) + 1e-6
        pq, pk = phi(q), phi(k)
        kv = torch.einsum("bhsd,bhse->bhde", pk, v)
        z = torch.einsum("bhsd->bhd", pk)
        num = torch.einsum("bhqd,bhde->bhqe", pq, kv)
        den = torch.einsum("bhqd,bhd->bhq", pq, z)[..., None] + 1e-6
        o = num / den
        return (lambda x, *d: x.permute(*d))(o, 0, 2, 1, 3).reshape(B, S, D) @ wo

    def _B_ref(x, w1, b1, w2, b2):
        y = _layernorm(x, axis=-1)
        h = _gelu(torch.einsum("bsd,df->bsf", y, w1) + b1)
        return torch.einsum("bsf,fd->bsd", h, w2) + b2

    x = A_x
    g = _sigmoid((x * gate_w).sum(-1, keepdims=True))
    a = _A_ref(x, A_wqkv, A_wo)
    b = _B_ref(x, B_w1, B_b1, B_w2, B_b2)
    return g * a + (1 - g) * b
```

```cpp
#include <hip/hip_runtime.h>
#include <hip/hip_bf16.h>
typedef unsigned short u16;
__device__ __forceinline__ float bf2f(u16 v){ return __uint_as_float(((unsigned)v)<<16); }
__device__ __forceinline__ u16 f2bf(float f){ __bf16 b=(__bf16)f; return __builtin_bit_cast(u16,b); }
constexpr int S=2048,D=576,F=4608,H=32,DH=18;

__global__ void k_prep(const u16* x, const u16* gw, float* xf, float* yf, float* g){
  int r=blockIdx.x; __shared__ float red[3][64];
  int t=threadIdx.x; float s=0,s2=0,gd=0;
  for(int k=t;k<D;k+=64){ float v=bf2f(x[r*D+k]); s+=v; gd+=v*bf2f(gw[k]); }
  for(int o=32;o;o>>=1){ s+=__shfl_xor(s,o); gd+=__shfl_xor(gd,o);}
  float m=s/D;
  for(int k=t;k<D;k+=64){ float v=bf2f(x[r*D+k])-m; s2+=v*v; }
  for(int o=32;o;o>>=1){ s2+=__shfl_xor(s2,o);}
  float rs=1.0f/sqrtf(s2/D+1e-5f);
  for(int k=t;k<D;k+=64){ float v=bf2f(x[r*D+k]); xf[r*D+k]=v; yf[r*D+k]=bf2f(f2bf((v-m)*rs)); }
  if(t==0) g[r]=1.f/(1.f+expf(-gd));
}
template<int MODE>
__global__ void k_gemm(const float* A, const u16* B, const u16* bias, float* C, int K, int N){
  __shared__ float As[16][64];
  int n=blockIdx.x*256+threadIdx.x; int m0=blockIdx.y*16;
  float acc[16]; for(int i=0;i<16;i++)acc[i]=0;
  for(int k0=0;k0<K;k0+=64){
    __syncthreads();
    for(int i=threadIdx.x;i<16*64;i+=256) As[i/64][i%64]=A[(size_t)(m0+i/64)*K+k0+i%64];
    __syncthreads();
    if(n<N) for(int k=0;k<64;k++){ float b=bf2f(B[(size_t)(k0+k)*N+n]);
      #pragma unroll
      for(int i=0;i<16;i++)acc[i]+=As[i][k]*b; }
  }
  if(n<N) for(int i=0;i<16;i++){ float v=acc[i];
    if(MODE==1){ v+=bf2f(bias[n]); float u=v; v=0.5f*u*(1.f+tanhf(0.7978845608f*(u+0.044715f*u*u*u))); v=bf2f(f2bf(v)); }
    if(MODE==2){ v+=bf2f(bias[n]); }
    if(MODE==3){ if(n<2*D){ v=(v>0? v+1.f: expf(v))+1e-6f; } v=bf2f(f2bf(v)); }
    C[(size_t)(m0+i)*N+n]=v; }
}
__global__ void k_kv(const float* qkv, float* kv){
  int h=blockIdx.x, t=threadIdx.x; if(t>=DH*19) return; int d=t/19,e=t%19; float s=0;
  for(int r=0;r<S;r++){ float pk=qkv[(size_t)r*3*D+D+h*DH+d]; float v=(e<DH)? qkv[(size_t)r*3*D+2*D+h*DH+e]:1.f; s+=pk*v; }
  kv[(h*DH+d)*19+e]=s;
}
__global__ void k_o(const float* qkv, const float* kv, float* o){
  int r=blockIdx.x, c=threadIdx.x; int h=c/DH,e=c%DH; float num=0,den=0;
  for(int d=0;d<DH;d++){ float pq=qkv[(size_t)r*3*D+h*DH+d]; num+=pq*kv[(h*DH+d)*19+e]; den+=pq*kv[(h*DH+d)*19+18]; }
  o[r*D+c]=bf2f(f2bf(num/(den+1e-6f)));
}
__global__ void k_fin(const float* a,const float* b,const float* g,u16* out){
  int r=blockIdx.x,c=threadIdx.x; float gg=g[r]; out[r*D+c]=f2bf(gg*a[r*D+c]+(1.f-gg)*b[r*D+c]);
}
extern "C" void kernel_launch(void* const* d_in, const int* in_sizes, int n_in, void* d_out, int out_size, void* d_ws, size_t ws_size, hipStream_t stream) {
  const u16* x=(const u16*)d_in[0]; const u16* wqkv=(const u16*)d_in[1]; const u16* wo=(const u16*)d_in[2];
  const u16* w1=(const u16*)d_in[4]; const u16* b1=(const u16*)d_in[5]; const u16* w2=(const u16*)d_in[6]; const u16* b2=(const u16*)d_in[7]; const u16* gw=(const u16*)d_in[8];
  float* p=(float*)d_ws; float* xf=p; p+=S*D; float* yf=p; p+=S*D; float* g=p; p+=S; float* qkv=p; p+=S*3*D; float* kv=p; p+=16384; float* o=p; p+=S*D; float* a=p; p+=S*D; float* h=p; p+=(size_t)S*F; float* b=p; p+=S*D;
  k_prep<<<S,64,0,stream>>>(x,gw,xf,yf,g);
  k_gemm<3><<<dim3((3*D+255)/256,S/16),256,0,stream>>>(xf,wqkv,nullptr,qkv,D,3*D);
  k_kv<<<H,384,0,stream>>>(qkv,kv);
  k_o<<<S,D,0,stream>>>(qkv,kv,o);
  k_gemm<0><<<dim3((D+255)/256,S/16),256,0,stream>>>(o,wo,nullptr,a,D,D);
  k_gemm<1><<<dim3((F+255)/256,S/16),256,0,stream>>>(yf,w1,b1,h,D,F);
  k_gemm<2><<<dim3((D+255)/256,S/16),256,0,stream>>>(h,w2,b2,b,F,D);
  k_fin<<<S,D,0,stream>>>(a,b,g,(u16*)d_out);
}
```

```cpp
#include <hip/hip_runtime.h>
typedef unsigned short u16;

__global__ __launch_bounds__(256,1) void fused(const u16* x,const u16* wqkv,const u16* wo,const u16* w1,const u16* b1,const u16* w2,const u16* b2,const u16* gw,u16* out,char* ws){
  asm volatile(
    ".LL_START_%=:\n"
    "s_load_dwordx2 s[56:57], %11, 0\n"
    "v_lshrrev_b32 v2, 6, %0\n"
    "v_and_b32 v1, 63, %0\n"
    "s_bfe_u32 s91, %1, 0x30003\n"
    "v_readfirstlane_b32 s80, v2\n"
    "s_and_b32 s58, %1, 1\n"
    "s_lshl_b32 s79, s58, 3\n"
    "s_or_b32 s79, s79, s91\n"
    "s_lshr_b32 s96, %1, 6\n"
    "s_mul_i32 s61, s79, 147456\n"
    "v_lshrrev_b32 v2, 3, v1\n"
    "v_lshrrev_b32 v3, 4, v1\n"
    "v_and_b32 v4, 7, v1\n"
    "s_mov_b64 s[40:41], %2\n"
    "s_add_u32 s40, s40, s61\n"
    "s_addc_u32 s41, s41, 0\n"
    "s_mov_b32 s42, 147456\n"
    "s_mov_b32 s43, 0x20000\n"
    "s_lshl_b32 s61, s80, 5\n"
    "v_xor_b32 v5, v3, v4\n"
    "v_add_u32 v7, s61, v2\n"
    "s_mul_i32 s61, s96, 2\n"
    "v_mul_u32_u24 v7, 1152, v7\n"
    "s_mul_i32 s94, s91, 1\n"
    "s_add_u32 s94, s94, s61\n"
    "v_lshl_add_u32 v6, v5, 4, v7\n"
    "s_cmp_ge_u32 s94, 9\n"
    "s_cselect_b32 s61, 9, 0\n"
    "s_sub_u32 s88, s94, s61\n"
    "s_lshl_b32 s94, s88, 7\n"
    "s_lshl_b32 s90, s80, 12\n"
    "s_add_u32 s90, s90, 81920\n"
    "s_mov_b32 m0, s90\n"
    "v_xor_b32 v7, 64, v6\n"
    "buffer_load_dwordx4 v6, s[40:43], s94 offen lds\n"
    "v_add_u32 v7, 8192, v7\n"
    "v_add_u32 v8, 16384, v6\n"
    "v_add_u32 v9, 16384, v7\n"
    "buffer_load_dwordx4 v7, s[40:43], s94 offen offset:1024 lds\n"
    "buffer_load_dwordx4 v8, s[40:43], s94 offen offset:2048 lds\n"
    "buffer_load_dwordx4 v9, s[40:43], s94 offen offset:3072 lds\n"
    "s_add_u32 s94, s94, 128\n"
    "s_cmp_eq_u32 s94, 1152\n"
    "s_cselect_b32 s94, 0, s94\n"
    "s_bfe_u32 s60, %1, 0x20001\n"
    "s_cmp_eq_u32 s96, 3\n"
    "s_cbranch_scc1 .LL_ATT_%=\n"
    "s_mul_i32 s83, s60, 3\n"
    "s_add_u32 s83, s83, s96\n"
    "s_mul_i32 s61, s83, 768\n"
    "s_sub_u32 s60, 5308416, s61\n"
    "s_lshl_b32 s89, s80, 4\n"
    "v_add_u32 v11, s89, v2\n"
    "v_and_b32 v5, 1, v3\n"
    "s_mov_b64 s[36:37], %5\n"
    "s_add_u32 s36, s36, s61\n"
    "s_addc_u32 s37, s37, 0\n"
    "s_mov_b32 s38, s60\n"
    "s_mov_b32 s39, 0x20000\n"
    "s_mul_i32 s60, s91, 11\n"
    "s_lshr_b32 s60, s60, 5\n"
    "s_sub_u32 s60, s58, s60\n"
    "s_mul_i32 s60, s60, 3\n"
    "s_add_u32 s84, s91, s60\n"
    "v_lshlrev_b32 v5, 2, v5\n"
    "v_xor_b32 v5, v5, v4\n"
    "v_mul_u32_u24 v10, 9216, v11\n"
    "s_lshl_b32 s86, s84, 7\n"
    "s_mul_i32 s64, s88, 589824\n"
    "s_lshl_b32 s89, s80, 11\n"
    "v_lshl_add_u32 v10, v5, 4, v10\n"
    "v_mul_u32_u24 v19, 1152, v11\n"
    "s_add_u32 s85, s64, s86\n"
    "s_add_u32 m0, s89, 0\n"
    "v_add_u32 v11, 72704, v10\n"
    "buffer_load_dwordx4 v10, s[36:39], s85 offen lds\n"
    "buffer_load_dwordx4 v11, s[36:39], s85 offen offset:1024 lds\n"
    "s_add_u32 m0, s90, 16384\n"
    "s_nop 0\n"
    "buffer_load_dwordx4 v6, s[40:43], s94 offen lds\n"
    "s_getpc_b64 s[28:29]\n"
    ".LL_PCREF0_%=:\n"
    "s_sub_u32 s28, s28, .LL_PCREF0_%=-.LL_START_%=\n"
    "s_subb_u32 s29, s29, 0\n"
    "s_lshr_b32 s30, %1, 3\n"
    "s_lshl_b32 s30, s30, 2\n"
    "s_add_u32 s30, s30, s80\n"
    "s_mul_i32 s30, s30, 5\n"
    "s_mov_b32 s31, .LL_CODEEND_%=-.LL_START_%=-4\n"
    "v_min_u32 v31, 4, v1\n"
    "v_add_lshl_u32 v31, v31, s30, 7\n"
    "v_min_u32 v31, s31, v31\n"
    "global_load_dword v31, v31, s[28:29]\n"
    "s_mov_b64 s[48:49], %6\n"
    "s_add_u32 s48, s48, s61\n"
    "s_addc_u32 s49, s49, 0\n"
    "s_mov_b32 s50, 768\n"
    "s_mov_b32 s51, 0x20000\n"
    "s_mov_b64 s[44:45], %11\n"
    "s_mov_b32 s46, 50331648\n"
    "s_mov_b32 s47, 0x20000\n"
    "s_add_u32 s65, s64, 589824\n"
    "s_cmp_eq_u32 s65, 5308416\n"
    "s_cselect_b32 s65, 0, s65\n"
    "s_add_u32 s66, s65, 589824\n"
    "s_cmp_eq_u32 s66, 5308416\n"
    "s_cselect_b32 s66, 0, s66\n"
    "s_add_u32 s67, s66, 589824\n"
    "s_cmp_eq_u32 s67, 5308416\n"
    "s_cselect_b32 s67, 0, s67\n"
    "s_add_u32 s68, s67, 589824\n"
    "s_cmp_eq_u32 s68, 5308416\n"
    "s_cselect_b32 s68, 0, s68\n"
    "s_add_u32 s69, s68, 589824\n"
    "s_cmp_eq_u32 s69, 5308416\n"
    "s_cselect_b32 s69, 0, s69\n"
    "s_add_u32 s70, s69, 589824\n"
    "s_cmp_eq_u32 s70, 5308416\n"
    "s_cselect_b32 s70, 0, s70\n"
    "s_add_u32 s71, s70, 589824\n"
    "s_cmp_eq_u32 s71, 5308416\n"
    "s_cselect_b32 s71, 0, s71\n"
    "s_add_u32 s72, s71, 589824\n"
    "s_cmp_eq_u32 s72, 5308416\n"
    "s_cselect_b32 s72, 0, s72\n"
    "s_lshr_b32 s81, s80, 1\n"
    "s_and_b32 s82, s80, 1\n"
    "v_lshlrev_b32 v5, 1, v3\n"
    "v_xor_b32 v5, v5, v4\n"
    "v_lshl_add_u32 v19, v5, 4, v19\n"
    "v_add_u32 v20, 8192, v19\n"
    "v_bfe_u32 v2, v1, 2, 2\n"
    "v_and_b32 v4, 3, v1\n"
    "v_lshlrev_b32 v4, 3, v4\n"
    "s_lshl_b32 s60, s82, 6\n"
    "v_or_b32 v4, s60, v4\n"
    "v_and_b32 v5, 1, v3\n"
    "v_lshl_or_b32 v12, v5, 5, v4\n"
    "v_lshrrev_b32 v13, 1, v2\n"
    "v_lshlrev_b32 v13, 6, v13\n"
    "v_xor_b32 v12, v12, v13\n"
    "v_lshl_add_u32 v12, v2, 7, v12\n"
    "v_lshrrev_b32 v13, 1, v3\n"
    "v_lshl_add_u32 v12, v13, 10, v12\n"
    "v_lshl_add_u32 v5, v5, 2, v2\n"
    "v_lshrrev_b32 v22, 1, v5\n"
    "v_lshlrev_b32 v22, 5, v22\n"
    "v_xor_b32 v21, v22, v4\n"
    "v_lshl_add_u32 v21, v5, 7, v21\n"
    "v_lshl_add_u32 v21, v13, 11, v21\n"
    "v_xor_b32 v22, 32, v21\n"
    "v_add_u32 v13, 0x10000, v12\n"
    "v_and_b32 v2, 31, v1\n"
    "v_lshrrev_b32 v3, 5, v1\n"
    "v_bfe_u32 v4, v1, 1, 3\n"
    "v_xor_b32 v4, v4, v3\n"
    "v_lshlrev_b32 v4, 4, v4\n"
    "v_lshl_add_u32 v4, v2, 7, v4\n"
    "v_add_u32 v24, s90, v4\n"
    "v_xor_b32 v25, 32, v24\n"
    "v_xor_b32 v26, 64, v24\n"
    "v_xor_b32 v27, 0x60, v24\n"
    "v_xor_b32 v64, 0x1000, v24\n"
    "v_xor_b32 v65, 0x1000, v25\n"
    "v_xor_b32 v66, 0x1000, v26\n"
    "v_xor_b32 v67, 0x1000, v27\n"
    "v_mov_b32 v28, 0x3f803f80\n"
    "v_mov_b32 v29, 0x3f803f80\n"
    "s_lshl_b32 s60, s82, 5\n"
    "v_add_u32 v5, s60, v2\n"
    "v_lshlrev_b32 v17, 1, v5\n"
    "v_lshlrev_b32 v16, 3, v5\n"
    "s_waitcnt lgkmcnt(0)\n"
    "s_add_u32 s56, s56, 1\n"
    "s_addc_u32 s57, s57, 0\n"
    "buffer_load_dwordx4 v7, s[40:43], s94 offen offset:1024 lds\n"
    "buffer_load_dwordx4 v8, s[40:43], s94 offen offset:2048 lds\n"
    "buffer_load_dwordx4 v9, s[40:43], s94 offen offset:3072 lds\n"
    "s_add_u32 s94, s94, 128\n"
    "s_cmp_eq_u32 s94, 1152\n"
    "s_cselect_b32 s94, 0, s94\n"
    "s_add_u32 s85, s65, s86\n"
    "s_add_u32 m0, s89, 8192\n"
    "buffer_load_ushort v125, v17, s[48:51], s86 offen\n"
    "buffer_load_dwordx4 v10, s[36:39], s85 offen lds\n"
    "buffer_load_dwordx4 v11, s[36:39], s85 offen offset:1024 lds\n"
    "v_add_u32 v68, 0x10000, v24\n"
    "v_add_u32 v72, 0x10000, v64\n"
    "v_add_u32 v69, 0x10000, v25\n"
    "v_add_u32 v73, 0x10000, v65\n"
    "v_add_u32 v70, 0x10000, v26\n"
    "v_add_u32 v74, 0x10000, v66\n"
    "v_add_u32 v71, 0x10000, v27\n"
    "v_add_u32 v75, 0x10000, v67\n"
    "v_mov_b32 v164, s56\n"
    "v_mov_b32 v165, s57\n"
    "v_mov_b32 v166, s56\n"
    "v_mov_b32 v167, s57\n"
    "v_and_b32 v4, 15, v1\n"
    "v_lshl_add_u32 v4, v3, 4, v4\n"
    "v_lshlrev_b32 v4, 4, v4\n"
    "v_lshrrev_b32 v14, 4, v2\n"
    "v_lshl_add_u32 v14, v14, 10, v4\n"
    "s_mul_i32 s60, s82, 10240\n"
    "s_lshl_b32 s61, s81, 12\n"
    "s_add_u32 s60, s60, s61\n"
    "s_add_u32 s60, s60, 81920\n"
    "v_add_u32 v14, s60, v14\n"
    "v_xor_b32 v15, 0x800, v14\n"
    "v_lshlrev_b32 v23, 4, v1\n"
    "v_add_u32 v23, s61, v23\n"
    "s_mul_i32 s92, s83, 3072\n"
    "s_add_u32 s92, s92, 8192\n"
    "s_mov_b32 s87, 0xc09d30fc\n"
    "v_mov_b32 v126, 0\n"
    "v_mov_b32 v127, 0\n"
    "v_mov_b32 v130, 0\n"
    "v_mov_b32 v131, 0\n"
    "v_mov_b32 v134, 0\n"
    "v_mov_b32 v135, 0\n"
    "s_mul_i32 s60, s80, 22\n"
    "v_cmp_gt_u32 vcc, 22, v1\n"
    "v_add_lshl_u32 v18, v1, s60, 7\n"
    "v_mov_b32 v4, 1879048192\n"
    "s_mul_i32 s93, s91, 55296\n"
    "v_cndmask_b32 v18, v4, v18, vcc\n"
    "s_mul_i32 s61, s83, 442368\n"
    "s_lshl_b32 s60, %1, 7\n"
    "s_add_u32 s31, s60, 65536\n"
    "s_mov_b64 s[52:53], %7\n"
    "s_add_u32 s52, s52, s61\n"
    "s_addc_u32 s53, s53, 0\n"
    "s_mov_b32 s54, 442368\n"
    "s_mov_b32 s55, 0x20000\n"
    "s_mul_i32 s73, s84, 73728\n"
    "s_add_u32 s74, s73, 73728\n"
    "s_cmp_eq_u32 s74, 442368\n"
    "s_cselect_b32 s74, 0, s74\n"
    "s_add_u32 s75, s74, 73728\n"
    "s_cmp_eq_u32 s75, 442368\n"
    "s_cselect_b32 s75, 0, s75\n"
    "s_add_u32 s76, s75, 73728\n"
    "s_cmp_eq_u32 s76, 442368\n"
    "s_cselect_b32 s76, 0, s76\n"
    "s_add_u32 s77, s76, 73728\n"
    "s_cmp_eq_u32 s77, 442368\n"
    "s_cselect_b32 s77, 0, s77\n"
    "s_add_u32 s78, s77, 73728\n"
    "s_cmp_eq_u32 s78, 442368\n"
    "s_cselect_b32 s78, 0, s78\n"
    "v_min_u32 v210, 11, v1\n"
    "v_lshlrev_b32 v210, 7, v210\n"
    "v_and_b32 v2, 3, v1\n"
    "v_lshlrev_b32 v211, 7, v2\n"
    "v_cmp_gt_u32 vcc, 8, %0\n"
    "v_lshlrev_b32 v4, 4, v1\n"
    "v_mov_b32 v168, 1879048192\n"
    "s_lshl_b32 s60, s80, 8\n"
    "v_cndmask_b32 v168, v168, v4, vcc\n"
    "v_bfe_u32 v3, v1, 2, 2\n"
    "v_lshlrev_b32 v3, 4, v3\n"
    "v_lshl_add_u32 v3, v2, 6, v3\n"
    "v_lshrrev_b32 v4, 4, v1\n"
    "v_lshl_add_u32 v3, v4, 10, v3\n"
    "v_add_u32 v212, s60, v3\n"
    "v_add_u32 v213, 0x1000, v212\n"
    "v_add_u32 v214, 0x2000, v212\n"
    "v_add_u32 v215, 0x3000, v212\n"
    "v_lshlrev_b32 v3, 4, v1\n"
    "s_lshl_b32 s60, s80, 13\n"
    "s_add_u32 s60, s60, 131072\n"
    "v_add_u32 v170, s60, v3\n"
    "s_lshl_b32 s60, s79, 4\n"
    "s_add_u32 s60, s60, s83\n"
    "s_lshl_b32 s98, s79, 11\n"
    "s_add_u32 s98, s98, 196608\n"
    "s_lshl_b32 s97, s83, 7\n"
    "s_add_u32 s97, s97, s98\n"
    "s_mul_i32 s99, s60, 147456\n"
    "s_add_u32 s99, s99, 4194304\n"
    "v_and_b32 v3, 15, v1\n"
    "v_and_b32 v4, 1, v3\n"
    "v_lshlrev_b32 v4, 4, v4\n"
    "v_mov_b32 v5, 0x3f80\n"
    "v_lshlrev_b32 v4, v4, v5\n"
    "v_lshrrev_b32 v5, 1, v3\n"
    "v_cmp_eq_u32 vcc, 0, v5\n"
    "v_cmp_eq_u32 s[62:63], 1, v5\n"
    "s_nop 0\n"
    "v_cndmask_b32 v172, 0, v4, vcc\n"
    "v_cndmask_b32 v173, 0, v4, s[62:63]\n"
    "v_cmp_eq_u32 vcc, 2, v5\n"
    "v_cmp_eq_u32 s[62:63], 3, v5\n"
    "s_nop 0\n"
    "v_cndmask_b32 v174, 0, v4, vcc\n"
    "v_cndmask_b32 v175, 0, v4, s[62:63]\n"
    "v_cmp_eq_u32 vcc, 4, v5\n"
    "v_cmp_eq_u32 s[62:63], 5, v5\n"
    "s_nop 0\n"
    "v_cndmask_b32 v176, 0, v4, vcc\n"
    "v_cndmask_b32 v177, 0, v4, s[62:63]\n"
    "v_cmp_eq_u32 vcc, 6, v5\n"
    "v_cmp_eq_u32 s[62:63], 7, v5\n"
    "s_nop 0\n"
    "v_cndmask_b32 v178, 0, v4, vcc\n"
    "v_cndmask_b32 v179, 0, v4, s[62:63]\n"
    "s_lshr_b32 s60, s83, 1\n"
    "s_lshl_b32 s60, s60, 2\n"
    "s_add_u32 s60, s60, s80\n"
    "s_lshl_b32 s60, s60, 2\n"
    "v_lshrrev_b32 v3, 2, v3\n"
    "v_add_u32 v3, s60, v3\n"
    "v_mul_u32_u24 v171, 1152, v3\n"
    "v_lshrrev_b32 v2, 3, %0\n"
    "v_and_b32 v3, 7, %0\n"
    "v_cmp_gt_u32 vcc, 72, %0\n"
    "v_lshlrev_b32 v2, 15, v2\n"
    "v_lshl_add_u32 v2, v3, 4, v2\n"
    "v_mov_b32 v192, 1879048192\n"
    "v_cndmask_b32 v192, v192, v2, vcc\n"
    "v_not_b32 v188, v164\n"
    "v_not_b32 v189, v164\n"
    "v_not_b32 v190, v164\n"
    "v_not_b32 v191, v164\n"
    "s_lshl_b32 s60, s79, 4\n"
    "s_lshl_b32 s61, s81, 3\n"
    "s_add_u32 s60, s60, s61\n"
    "s_add_u32 s60, s60, s82\n"
    "s_mul_i32 s95, s60, 147456\n"
    "s_lshl_b32 s61, s83, 10\n"
    "s_add_u32 s95, s95, s61\n"
    "s_add_u32 s95, s95, 4194304\n"
    "s_waitcnt vmcnt(8)\n"
    "s_barrier\n"
    "ds_read_b128 a[0:3], v24 offset:0\n"
    "ds_read_b128 a[8:11], v25 offset:0\n"
    "ds_read_b128 a[16:19], v26 offset:0\n"
    "ds_read_b128 a[24:27], v27 offset:0\n"
    "ds_read_b64_tr_b16 v[96:97], v12 offset:0\n"
    "ds_read_b64_tr_b16 v[98:99], v12 offset:512\n"
    "ds_read_b64_tr_b16 v[100:101], v12 offset:2048\n"
    "ds_read_b64_tr_b16 v[102:103], v12 offset:2560\n"
    "ds_read_b64_tr_b16 v[104:105], v12 offset:4096\n"
    "ds_read_b64_tr_b16 v[106:107], v12 offset:4608\n"
    "ds_read_b128 a[4:7], v64 offset:0\n"
    "ds_read_b128 a[12:15], v65 offset:0\n"
    "ds_read_b128 a[20:23], v66 offset:0\n"
    "ds_read_b128 a[28:31], v67 offset:0\n"
    "s_waitcnt lgkmcnt(10)\n"
    "v_mfma_f32_4x4x4_16b_bf16 v[136:139], v[28:29], a[0:1], 0\n"
    "v_mfma_f32_4x4x4_16b_bf16 v[144:147], a[0:1], a[0:1], 0\n"
    "v_mfma_f32_4x4x4_16b_bf16 v[140:143], v[28:29], a[2:3], 0\n"
    "v_mfma_f32_4x4x4_16b_bf16 v[148:151], a[2:3], a[2:3], 0\n"
    "v_mfma_f32_4x4x4_16b_bf16 v[136:139], v[28:29], a[8:9], v[136:139]\n"
    "v_mfma_f32_4x4x4_16b_bf16 v[144:147], a[8:9], a[8:9], v[144:147]\n"
    "v_mfma_f32_4x4x4_16b_bf16 v[140:143], v[28:29], a[10:11], v[140:143]\n"
    "v_mfma_f32_4x4x4_16b_bf16 v[148:151], a[10:11], a[10:11], v[148:151]\n"
    "v_mfma_f32_4x4x4_16b_bf16 v[136:139], v[28:29], a[16:17], v[136:139]\n"
    "v_mfma_f32_4x4x4_16b_bf16 v[144:147], a[16:17], a[16:17], v[144:147]\n"
    "v_mfma_f32_4x4x4_16b_bf16 v[140:143], v[28:29], a[18:19], v[140:143]\n"
    "v_mfma_f32_4x4x4_16b_bf16 v[148:151], a[18:19], a[18:19], v[148:151]\n"
    "v_mfma_f32_4x4x4_16b_bf16 v[136:139], v[28:29], a[24:25], v[136:139]\n"
    "v_mfma_f32_4x4x4_16b_bf16 v[144:147], a[24:25], a[24:25], v[144:147]\n"
    "v_mfma_f32_4x4x4_16b_bf16 v[140:143], v[28:29], a[26:27], v[140:143]\n"
    "v_mfma_f32_4x4x4_16b_bf16 v[148:151], a[26:27], a[26:27], v[148:151]\n"
    "s_add_u32 m0, s90, 32768\n"
    "s_add_u32 s85, s66, s86\n"
    "buffer_load_dwordx4 v6, s[40:43], s94 offen lds\n"
    "buffer_load_dwordx4 v7, s[40:43], s94 offen offset:1024 lds\n"
    "buffer_load_dwordx4 v8, s[40:43], s94 offen offset:2048 lds\n"
    "buffer_load_dwordx4 v9, s[40:43], s94 offen offset:3072 lds\n"
    "s_add_u32 m0, s89, 16384\n"
    "s_add_u32 s94, s94, 128\n"
    "s_cmp_eq_u32 s94, 1152\n"
    "s_cselect_b32 s94, 0, s94\n"
    "buffer_load_dwordx4 v10, s[36:39], s85 offen lds\n"
    "buffer_load_dwordx4 v11, s[36:39], s85 offen offset:1024 lds\n"
    "s_waitcnt lgkmcnt(0)\n"
    "v_mfma_f32_32x32x16_bf16 v[32:47], v[96:99], a[0:3], 0\n"
    "ds_read_b64_tr_b16 v[108:109], v12 offset:6144\n"
    "ds_read_b64_tr_b16 v[110:111], v12 offset:6656\n"
    "v_mfma_f32_4x4x4_16b_bf16 v[152:155], v[28:29], v[96:97], 0\n"
    "v_mfma_f32_32x32x16_bf16 v[48:63], v[96:99], a[4:7], 0\n"
    "s_waitcnt vmcnt(6) lgkmcnt(8)\n"
    "s_barrier\n"
    "ds_read_b128 a[32:35], v24 offset:16384\n"
    "v_mfma_f32_4x4x4_16b_bf16 v[152:155], v[28:29], v[98:99], v[152:155]\n"
    "v_mfma_f32_32x32x16_bf16 v[32:47], v[100:103], a[8:11], v[32:47]\n"
    "ds_read_b64_tr_b16 v[96:97], v12 offset:8192\n"
    "ds_read_b64_tr_b16 v[98:99], v12 offset:8704\n"
    "ds_read_b128 a[36:39], v64 offset:16384\n"
    "v_mfma_f32_4x4x4_16b_bf16 v[152:155], v[28:29], v[100:101], v[152:155]\n"
    "v_mfma_f32_32x32x16_bf16 v[48:63], v[100:103], a[12:15], v[48:63]\n"
    "s_waitcnt lgkmcnt(10)\n"
    "ds_read_b128 a[40:43], v25 offset:16384\n"
    "v_mfma_f32_4x4x4_16b_bf16 v[152:155], v[28:29], v[102:103], v[152:155]\n"
    "v_mfma_f32_32x32x16_bf16 v[32:47], v[104:107], a[16:19], v[32:47]\n"
    "s_waitcnt lgkmcnt(4)\n"
    "ds_read_b64_tr_b16 v[100:101], v12 offset:10240\n"
    "ds_read_b64_tr_b16 v[102:103], v12 offset:10752\n"
    "ds_read_b128 a[44:47], v65 offset:16384\n"
    "v_mfma_f32_4x4x4_16b_bf16 v[152:155], v[28:29], v[104:105], v[152:155]\n"
    "v_mfma_f32_4x4x4_16b_bf16 v[136:139], v[28:29], a[32:33], v[136:139]\n"
    "v_mfma_f32_4x4x4_16b_bf16 v[144:147], a[32:33], a[32:33], v[144:147]\n"
    "v_mfma_f32_32x32x16_bf16 v[48:63], v[104:107], a[20:23], v[48:63]\n"
    "s_waitcnt lgkmcnt(8)\n"
    "ds_read_b128 a[48:51], v26 offset:16384\n"
    "v_mfma_f32_4x4x4_16b_bf16 v[152:155], v[28:29], v[106:107], v[152:155]\n"
    "v_mfma_f32_4x4x4_16b_bf16 v[140:143], v[28:29], a[34:35], v[140:143]\n"
    "v_mfma_f32_4x4x4_16b_bf16 v[148:151], a[34:35], a[34:35], v[148:151]\n"
    "v_mfma_f32_32x32x16_bf16 v[32:47], v[108:111], a[24:27], v[32:47]\n"
    "s_waitcnt lgkmcnt(4)\n"
    "ds_read_b64_tr_b16 v[104:105], v12 offset:12288\n"
    "ds_read_b64_tr_b16 v[106:107], v12 offset:12800\n"
    "ds_read_b128 a[52:55], v66 offset:16384\n"
    "v_mfma_f32_4x4x4_16b_bf16 v[152:155], v[28:29], v[108:109], v[152:155]\n"
    "v_mfma_f32_4x4x4_16b_bf16 v[136:139], v[28:29], a[40:41], v[136:139]\n"
    "v_mfma_f32_4x4x4_16b_bf16 v[144:147], a[40:41], a[40:41], v[144:147]\n"
    "v_mfma_f32_32x32x16_bf16 v[48:63], v[108:111], a[28:31], v[48:63]\n"
    "s_waitcnt lgkmcnt(8)\n"
    "ds_read_b128 a[56:59], v27 offset:16384\n"
    "v_mfma_f32_4x4x4_16b_bf16 v[152:155], v[28:29], v[110:111], v[152:155]\n"
    "v_mfma_f32_4x4x4_16b_bf16 v[140:143], v[28:29], a[42:43], v[140:143]\n"
    "v_mfma_f32_4x4x4_16b_bf16 v[148:151], a[42:43], a[42:43], v[148:151]\n"
    "s_add_u32 m0, s90, 49152\n"
    "s_nop 0\n"
    "buffer_load_dwordx4 v6, s[40:43], s94 offen lds\n"
    "buffer_load_dwordx4 v7, s[40:43], s94 offen offset:1024 lds\n"
    "buffer_load_dwordx4 v8, s[40:43], s94 offen offset:2048 lds\n"
    "buffer_load_dwordx4 v9, s[40:43], s94 offen offset:3072 lds\n"
    "s_add_u32 s85, s67, s86\n"
    "s_add_u32 m0, s89, 24576\n"
    "s_nop 0\n"
    "buffer_load_dwordx4 v10, s[36:39], s85 offen lds\n"
    "buffer_load_dwordx4 v11, s[36:39], s85 offen offset:1024 lds\n"
    "v_mfma_f32_32x32x16_bf16 v[32:47], v[96:99], a[32:35], v[32:47]\n"
    "s_waitcnt lgkmcnt(4)\n"
    "s_add_u32 s94, s94, 128\n"
    "s_cmp_eq_u32 s94, 1152\n"
    "s_cselect_b32 s94, 0, s94\n"
    "ds_read_b64_tr_b16 v[108:109], v12 offset:14336\n"
    "ds_read_b64_tr_b16 v[110:111], v12 offset:14848\n"
    "ds_read_b128 a[60:63], v67 offset:16384\n"
    "v_mfma_f32_4x4x4_16b_bf16 v[152:155], v[28:29], v[96:97], v[152:155]\n"
    "v_mfma_f32_4x4x4_16b_bf16 v[136:139], v[28:29], a[48:49], v[136:139]\n"
    "v_mfma_f32_4x4x4_16b_bf16 v[144:147], a[48:49], a[48:49], v[144:147]\n"
    "v_mfma_f32_32x32x16_bf16 v[48:63], v[96:99], a[36:39], v[48:63]\n"
    "s_waitcnt vmcnt(6) lgkmcnt(8)\n"
    "s_barrier\n"
    "ds_read_b128 a[64:67], v24 offset:32768\n"
    "v_mfma_f32_4x4x4_16b_bf16 v[152:155], v[28:29], v[98:99], v[152:155]\n"
    "v_mfma_f32_4x4x4_16b_bf16 v[140:143], v[28:29], a[50:51], v[140:143]\n"
    "v_mfma_f32_4x4x4_16b_bf16 v[148:151], a[50:51], a[50:51], v[148:151]\n"
    "v_mfma_f32_32x32x16_bf16 v[32:47], v[100:103], a[40:43], v[32:47]\n"
    "s_waitcnt lgkmcnt(4)\n"
    "s_add_u32 m0, s90, 65536\n"
    "ds_read_b64_tr_b16 v[96:97], v12 offset:16384\n"
    "ds_read_b64_tr_b16 v[98:99], v12 offset:16896\n"
    "ds_read_b128 a[68:71], v64 offset:32768\n"
    "buffer_load_dwordx4 v6, s[40:43], s94 offen lds\n"
    "v_mfma_f32_4x4x4_16b_bf16 v[152:155], v[28:29], v[100:101], v[152:155]\n"
    "v_mfma_f32_4x4x4_16b_bf16 v[136:139], v[28:29], a[56:57], v[136:139]\n"
    "v_mfma_f32_4x4x4_16b_bf16 v[144:147], a[56:57], a[56:57], v[144:147]\n"
    "v_mfma_f32_32x32x16_bf16 v[48:63], v[100:103], a[44:47], v[48:63]\n"
    "s_waitcnt lgkmcnt(8)\n"
    "ds_read_b128 a[72:75], v25 offset:32768\n"
    "buffer_load_dwordx4 v7, s[40:43], s94 offen offset:1024 lds\n"
    "v_mfma_f32_4x4x4_16b_bf16 v[152:155], v[28:29], v[102:103], v[152:155]\n"
    "v_mfma_f32_4x4x4_16b_bf16 v[140:143], v[28:29], a[58:59], v[140:143]\n"
    "v_mfma_f32_4x4x4_16b_bf16 v[148:151], a[58:59], a[58:59], v[148:151]\n"
    "v_mfma_f32_32x32x16_bf16 v[32:47], v[104:107], a[48:51], v[32:47]\n"
    "s_waitcnt lgkmcnt(4)\n"
    "ds_read_b64_tr_b16 v[100:101], v12 offset:18432\n"
    "ds_read_b64_tr_b16 v[102:103], v12 offset:18944\n"
    "ds_read_b128 a[76:79], v65 offset:32768\n"
    "buffer_load_dwordx4 v8, s[40:43], s94 offen offset:2048 lds\n"
    "v_mfma_f32_4x4x4_16b_bf16 v[152:155], v[28:29], v[104:105], v[152:155]\n"
    "v_mfma_f32_4x4x4_16b_bf16 v[136:139], v[28:29], a[64:65], v[136:139]\n"
    "v_mfma_f32_4x4x4_16b_bf16 v[144:147], a[64:65], a[64:65], v[144:147]\n"
    "v_mfma_f32_32x32x16_bf16 v[48:63], v[104:107], a[52:55], v[48:63]\n"
    "s_waitcnt lgkmcnt(8)\n"
    "ds_read_b128 a[80:83], v26 offset:32768\n"
    "buffer_load_dwordx4 v9, s[40:43], s94 offen offset:3072 lds\n"
    "v_mfma_f32_4x4x4_16b_bf16 v[152:155], v[28:29], v[106:107], v[152:155]\n"
    "v_mfma_f32_4x4x4_16b_bf16 v[140:143], v[28:29], a[66:67], v[140:143]\n"
    "v_mfma_f32_4x4x4_16b_bf16 v[148:151], a[66:67], a[66:67], v[148:151]\n"
    "v_mfma_f32_32x32x16_bf16 v[32:47], v[108:111], a[56:59], v[32:47]\n"
    "s_waitcnt lgkmcnt(4)\n"
    "s_add_u32 s85, s68, s86\n"
    "s_add_u32 m0, s89, 32768\n"
    "ds_read_b64_tr_b16 v[104:105], v12 offset:20480\n"
    "ds_read_b64_tr_b16 v[106:107], v12 offset:20992\n"
    "ds_read_b128 a[84:87], v66 offset:32768\n"
    "buffer_load_dwordx4 v10, s[36:39], s85 offen lds\n"
    "v_mfma_f32_4x4x4_16b_bf16 v[152:155], v[28:29], v[108:109], v[152:155]\n"
    "v_mfma_f32_4x4x4_16b_bf16 v[136:139], v[28:29], a[72:73], v[136:139]\n"
    "v_mfma_f32_4x4x4_16b_bf16 v[144:147], a[72:73], a[72:73], v[144:147]\n"
    "v_mfma_f32_32x32x16_bf16 v[48:63], v[108:111], a[60:63], v[48:63]\n"
    "s_waitcnt lgkmcnt(8)\n"
    "ds_read_b128 a[88:91], v27 offset:32768\n"
    "buffer_load_dwordx4 v11, s[36:39], s85 offen offset:1024 lds\n"
    "v_mfma_f32_4x4x4_16b_bf16 v[152:155], v[28:29], v[110:111], v[152:155]\n"
    "v_mfma_f32_4x4x4_16b_bf16 v[140:143], v[28:29], a[74:75], v[140:143]\n"
    "v_mfma_f32_4x4x4_16b_bf16 v[148:151], a[74:75], a[74:75], v[148:151]\n"
    "v_mfma_f32_32x32x16_bf16 v[32:47], v[96:99], a[64:67], v[32:47]\n"
    "s_waitcnt lgkmcnt(4)\n"
    "s_add_u32 s94, s94, 128\n"
    "s_cmp_eq_u32 s94, 1152\n"
    "s_cselect_b32 s94, 0, s94\n"
    "ds_read_b64_tr_b16 v[108:109], v12 offset:22528\n"
    "ds_read_b64_tr_b16 v[110:111], v12 offset:23040\n"
    "ds_read_b128 a[92:95], v67 offset:32768\n"
    "v_mfma_f32_4x4x4_16b_bf16 v[152:155], v[28:29], v[96:97], v[152:155]\n"
    "v_mfma_f32_4x4x4_16b_bf16 v[136:139], v[28:29], a[80:81], v[136:139]\n"
    "v_mfma_f32_4x4x4_16b_bf16 v[144:147], a[80:81], a[80:81], v[144:147]\n"
    "v_mfma_f32_32x32x16_bf16 v[48:63], v[96:99], a[68:71], v[48:63]\n"
    "s_waitcnt vmcnt(6) lgkmcnt(8)\n"
    "s_barrier\n"
    "ds_read_b128 a[96:99], v24 offset:49152\n"
    "v_mfma_f32_4x4x4_16b_bf16 v[152:155], v[28:29], v[98:99], v[152:155]\n"
    "v_mfma_f32_4x4x4_16b_bf16 v[140:143], v[28:29], a[82:83], v[140:143]\n"
    "v_mfma_f32_4x4x4_16b_bf16 v[148:151], a[82:83], a[82:83], v[148:151]\n"
    "v_mfma_f32_32x32x16_bf16 v[32:47], v[100:103], a[72:75], v[32:47]\n"
    "s_waitcnt lgkmcnt(4)\n"
    "s_add_u32 m0, s90, 0\n"
    "ds_read_b64_tr_b16 v[96:97], v12 offset:24576\n"
    "ds_read_b64_tr_b16 v[98:99], v12 offset:25088\n"
    "ds_read_b128 a[100:103], v64 offset:49152\n"
    "buffer_load_dwordx4 v6, s[40:43], s94 offen lds\n"
    "v_mfma_f32_4x4x4_16b_bf16 v[152:155], v[28:29], v[100:101], v[152:155]\n"
    "v_mfma_f32_4x4x4_16b_bf16 v[136:139], v[28:29], a[88:89], v[136:139]\n"
    "v_mfma_f32_4x4x4_16b_bf16 v[144:147], a[88:89], a[88:89], v[144:147]\n"
    "v_mfma_f32_32x32x16_bf16 v[48:63], v[100:103], a[76:79], v[48:63]\n"
    "s_waitcnt lgkmcnt(8)\n"
    "ds_read_b128 a[104:107], v25 offset:49152\n"
    "buffer_load_dwordx4 v7, s[40:43], s94 offen offset:1024 lds\n"
    "v_mfma_f32_4x4x4_16b_bf16 v[152:155], v[28:29], v[102:103], v[152:155]\n"
    "v_mfma_f32_4x4x4_16b_bf16 v[140:143], v[28:29], a[90:91], v[140:143]\n"
    "v_mfma_f32_4x4x4_16b_bf16 v[148:151], a[90:91], a[90:91], v[148:151]\n"
    "v_mfma_f32_32x32x16_bf16 v[32:47], v[104:107], a[80:83], v[32:47]\n"
    "s_waitcnt lgkmcnt(4)\n"
    "ds_read_b64_tr_b16 v[100:101], v12 offset:26624\n"
    "ds_read_b64_tr_b16 v[102:103], v12 offset:27136\n"
    "ds_read_b128 a[108:111], v65 offset:49152\n"
    "buffer_load_dwordx4 v8, s[40:43], s94 offen offset:2048 lds\n"
    "v_mfma_f32_4x4x4_16b_bf16 v[152:155], v[28:29], v[104:105], v[152:155]\n"
    "v_mfma_f32_4x4x4_16b_bf16 v[136:139], v[28:29], a[96:97], v[136:139]\n"
    "v_mfma_f32_4x4x4_16b_bf16 v[144:147], a[96:97], a[96:97], v[144:147]\n"
    "v_mfma_f32_32x32x16_bf16 v[48:63], v[104:107], a[84:87], v[48:63]\n"
    "s_waitcnt lgkmcnt(8)\n"
    "ds_read_b128 a[112:115], v26 offset:49152\n"
    "buffer_load_dwordx4 v9, s[40:43], s94 offen offset:3072 lds\n"
    "v_mfma_f32_4x4x4_16b_bf16 v[152:155], v[28:29], v[106:107], v[152:155]\n"
    "v_mfma_f32_4x4x4_16b_bf16 v[140:143], v[28:29], a[98:99], v[140:143]\n"
    "v_mfma_f32_4x4x4_16b_bf16 v[148:151], a[98:99], a[98:99], v[148:151]\n"
    "v_mfma_f32_32x32x16_bf16 v[32:47], v[108:111], a[88:91], v[32:47]\n"
    "s_waitcnt lgkmcnt(4)\n"
    "s_add_u32 s85, s69, s86\n"
    "s_add_u32 m0, s89, 40960\n"
    "ds_read_b64_tr_b16 v[104:105], v12 offset:28672\n"
    "ds_read_b64_tr_b16 v[106:107], v12 offset:29184\n"
    "ds_read_b128 a[116:119], v66 offset:49152\n"
    "buffer_load_dwordx4 v10, s[36:39], s85 offen lds\n"
    "v_mfma_f32_4x4x4_16b_bf16 v[152:155], v[28:29], v[108:109], v[152:155]\n"
    "v_mfma_f32_4x4x4_16b_bf16 v[136:139], v[28:29], a[104:105], v[136:139]\n"
    "v_mfma_f32_4x4x4_16b_bf16 v[144:147], a[104:105], a[104:105], v[144:147]\n"
    "v_mfma_f32_32x32x16_bf16 v[48:63], v[108:111], a[92:95], v[48:63]\n"
    "s_waitcnt lgkmcnt(8)\n"
    "ds_read_b128 a[120:123], v27 offset:49152\n"
    "buffer_load_dwordx4 v11, s[36:39], s85 offen offset:1024 lds\n"
    "v_mfma_f32_4x4x4_16b_bf16 v[152:155], v[28:29], v[110:111], v[152:155]\n"
    "v_mfma_f32_4x4x4_16b_bf16 v[140:143], v[28:29], a[106:107], v[140:143]\n"
    "v_mfma_f32_4x4x4_16b_bf16 v[148:151], a[106:107], a[106:107], v[148:151]\n"
    "v_mfma_f32_32x32x16_bf16 v[32:47], v[96:99], a[96:99], v[32:47]\n"
    "s_waitcnt lgkmcnt(4)\n"
    "s_add_u32 s94, s94, 128\n"
    "s_cmp_eq_u32 s94, 1152\n"
    "s_cselect_b32 s94, 0, s94\n"
    "ds_read_b64_tr_b16 v[108:109], v12 offset:30720\n"
    "ds_read_b64_tr_b16 v[110:111], v12 offset:31232\n"
    "ds_read_b128 a[124:127], v67 offset:49152\n"
    "v_mfma_f32_4x4x4_16b_bf16 v[152:155], v[28:29], v[96:97], v[152:155]\n"
    "v_mfma_f32_4x4x4_16b_bf16 v[136:139], v[28:29], a[112:113], v[136:139]\n"
    "v_mfma_f32_4x4x4_16b_bf16 v[144:147], a[112:113], a[112:113], v[144:147]\n"
    "v_mfma_f32_32x32x16_bf16 v[48:63], v[96:99], a[100:103], v[48:63]\n"
    "s_waitcnt vmcnt(6) lgkmcnt(8)\n"
    "s_barrier\n"
    "ds_read_b128 a[128:131], v68 offset:0\n"
    "v_mfma_f32_4x4x4_16b_bf16 v[152:155], v[28:29], v[98:99], v[152:155]\n"
    "v_mfma_f32_4x4x4_16b_bf16 v[140:143], v[28:29], a[114:115], v[140:143]\n"
    "v_mfma_f32_4x4x4_16b_bf16 v[148:151], a[114:115], a[114:115], v[148:151]\n"
    "v_mfma_f32_32x32x16_bf16 v[32:47], v[100:103], a[104:107], v[32:47]\n"
    "s_waitcnt lgkmcnt(4)\n"
    "s_add_u32 m0, s90, 16384\n"
    "ds_read_b64_tr_b16 v[96:97], v12 offset:32768\n"
    "ds_read_b64_tr_b16 v[98:99], v12 offset:33280\n"
    "ds_read_b128 a[132:135], v72 offset:0\n"
    "buffer_load_dwordx4 v6, s[40:43], s94 offen lds\n"
    "v_mfma_f32_4x4x4_16b_bf16 v[152:155], v[28:29], v[100:101], v[152:155]\n"
    "v_mfma_f32_4x4x4_16b_bf16 v[136:139], v[28:29], a[120:121], v[136:139]\n"
    "v_mfma_f32_4x4x4_16b_bf16 v[144:147], a[120:121], a[120:121], v[144:147]\n"
    "v_mfma_f32_32x32x16_bf16 v[48:63], v[100:103], a[108:111], v[48:63]\n"
    "s_waitcnt lgkmcnt(8)\n"
    "ds_read_b128 a[136:139], v69 offset:0\n"
    "buffer_load_dwordx4 v7, s[40:43], s94 offen offset:1024 lds\n"
    "v_mfma_f32_4x4x4_16b_bf16 v[152:155], v[28:29], v[102:103], v[152:155]\n"
    "v_mfma_f32_4x4x4_16b_bf16 v[140:143], v[28:29], a[122:123], v[140:143]\n"
    "v_mfma_f32_4x4x4_16b_bf16 v[148:151], a[122:123], a[122:123], v[148:151]\n"
    "v_mfma_f32_32x32x16_bf16 v[32:47], v[104:107], a[112:115], v[32:47]\n"
    "s_waitcnt lgkmcnt(4)\n"
    "ds_read_b64_tr_b16 v[100:101], v12 offset:34816\n"
    "ds_read_b64_tr_b16 v[102:103], v12 offset:35328\n"
    "ds_read_b128 a[140:143], v73 offset:0\n"
    "buffer_load_dwordx4 v8, s[40:43], s94 offen offset:2048 lds\n"
    "v_mfma_f32_4x4x4_16b_bf16 v[152:155], v[28:29], v[104:105], v[152:155]\n"
    "v_mfma_f32_4x4x4_16b_bf16 v[136:139], v[28:29], a[128:129], v[136:139]\n"
    "v_mfma_f32_4x4x4_16b_bf16 v[144:147], a[128:129], a[128:129], v[144:147]\n"
    "v_mfma_f32_32x32x16_bf16 v[48:63], v[104:107], a[116:119], v[48:63]\n"
    "s_waitcnt lgkmcnt(8)\n"
    "ds_read_b128 a[144:147], v70 offset:0\n"
    "buffer_load_dwordx4 v9, s[40:43], s94 offen offset:3072 lds\n"
    "v_mfma_f32_4x4x4_16b_bf16 v[152:155], v[28:29], v[106:107], v[152:155]\n"
    "v_mfma_f32_4x4x4_16b_bf16 v[140:143], v[28:29], a[130:131], v[140:143]\n"
    "v_mfma_f32_4x4x4_16b_bf16 v[148:151], a[130:131], a[130:131], v[148:151]\n"
    "v_mfma_f32_32x32x16_bf16 v[32:47], v[108:111], a[120:123], v[32:47]\n"
    "s_waitcnt lgkmcnt(4)\n"
    "s_add_u32 s85, s70, s86\n"
    "s_add_u32 m0, s89, 49152\n"
    "ds_read_b64_tr_b16 v[104:105], v12 offset:36864\n"
    "ds_read_b64_tr_b16 v[106:107], v12 offset:37376\n"
    "ds_read_b128 a[148:151], v74 offset:0\n"
    "buffer_load_dwordx4 v10, s[36:39], s85 offen lds\n"
    "v_mfma_f32_4x4x4_16b_bf16 v[152:155], v[28:29], v[108:109], v[152:155]\n"
    "v_mfma_f32_4x4x4_16b_bf16 v[136:139], v[28:29], a[136:137], v[136:139]\n"
    "v_mfma_f32_4x4x4_16b_bf16 v[144:147], a[136:137], a[136:137], v[144:147]\n"
    "v_mfma_f32_32x32x16_bf16 v[48:63], v[108:111], a[124:127], v[48:63]\n"
    "s_waitcnt lgkmcnt(8)\n"
    "ds_read_b128 a[152:155], v71 offset:0\n"
    "buffer_load_dwordx4 v11, s[36:39], s85 offen offset:1024 lds\n"
    "v_mfma_f32_4x4x4_16b_bf16 v[152:155], v[28:29], v[110:111], v[152:155]\n"
    "v_mfma_f32_4x4x4_16b_bf16 v[140:143], v[28:29], a[138:139], v[140:143]\n"
    "v_mfma_f32_4x4x4_16b_bf16 v[148:151], a[138:139], a[138:139], v[148:151]\n"
    "v_mfma_f32_32x32x16_bf16 v[32:47], v[96:99], a[128:131], v[32:47]\n"
    "s_waitcnt lgkmcnt(4)\n"
    "s_add_u32 s94, s94, 128\n"
    "s_cmp_eq_u32 s94, 1152\n"
    "s_cselect_b32 s94, 0, s94\n"
    "ds_read_b64_tr_b16 v[108:109], v12 offset:38912\n"
    "ds_read_b64_tr_b16 v[110:111], v12 offset:39424\n"
    "ds_read_b128 a[156:159], v75 offset:0\n"
    "v_mfma_f32_4x4x4_16b_bf16 v[152:155], v[28:29], v[96:97], v[152:155]\n"
    "v_mfma_f32_4x4x4_16b_bf16 v[136:139], v[28:29], a[144:145], v[136:139]\n"
    "v_mfma_f32_4x4x4_16b_bf16 v[144:147], a[144:145], a[144:145], v[144:147]\n"
    "v_mfma_f32_32x32x16_bf16 v[48:63], v[96:99], a[132:135], v[48:63]\n"
    "s_waitcnt vmcnt(6) lgkmcnt(8)\n"
    "s_barrier\n"
    "ds_read_b128 a[160:163], v24 offset:0\n"
    "v_mfma_f32_4x4x4_16b_bf16 v[152:155], v[28:29], v[98:99], v[152:155]\n"
    "v_mfma_f32_4x4x4_16b_bf16 v[140:143], v[28:29], a[146:147], v[140:143]\n"
    "v_mfma_f32_4x4x4_16b_bf16 v[148:151], a[146:147], a[146:147], v[148:151]\n"
    "v_mfma_f32_32x32x16_bf16 v[32:47], v[100:103], a[136:139], v[32:47]\n"
    "s_waitcnt lgkmcnt(4)\n"
    "s_add_u32 m0, s90, 32768\n"
    "ds_read_b64_tr_b16 v[96:97], v12 offset:40960\n"
    "ds_read_b64_tr_b16 v[98:99], v12 offset:41472\n"
    "ds_read_b128 a[164:167], v64 offset:0\n"
    "buffer_load_dwordx4 v6, s[40:43], s94 offen lds\n"
    "v_mfma_f32_4x4x4_16b_bf16 v[152:155], v[28:29], v[100:101], v[152:155]\n"
    "v_mfma_f32_4x4x4_16b_bf16 v[136:139], v[28:29], a[152:153], v[136:139]\n"
    "v_mfma_f32_4x4x4_16b_bf16 v[144:147], a[152:153], a[152:153], v[144:147]\n"
    "v_mfma_f32_32x32x16_bf16 v[48:63], v[100:103], a[140:143], v[48:63]\n"
    "s_waitcnt lgkmcnt(8)\n"
    "ds_read_b128 a[168:171], v25 offset:0\n"
    "buffer_load_dwordx4 v7, s[40:43], s94 offen offset:1024 lds\n"
    "v_mfma_f32_4x4x4_16b_bf16 v[152:155], v[28:29], v[102:103], v[152:155]\n"
    "v_mfma_f32_4x4x4_16b_bf16 v[140:143], v[28:29], a[154:155], v[140:143]\n"
    "v_mfma_f32_4x4x4_16b_bf16 v[148:151], a[154:155], a[154:155], v[148:151]\n"
    "v_mfma_f32_32x32x16_bf16 v[32:47], v[104:107], a[144:147], v[32:47]\n"
    "s_waitcnt lgkmcnt(4)\n"
    "ds_read_b64_tr_b16 v[100:101], v12 offset:43008\n"
    "ds_read_b64_tr_b16 v[102:103], v12 offset:43520\n"
    "ds_read_b128 a[172:175], v65 offset:0\n"
    "buffer_load_dwordx4 v8, s[40:43], s94 offen offset:2048 lds\n"
    "v_mfma_f32_4x4x4_16b_bf16 v[152:155], v[28:29], v[104:105], v[152:155]\n"
    "v_mfma_f32_4x4x4_16b_bf16 v[136:139], v[28:29], a[160:161], v[136:139]\n"
    "v_mfma_f32_4x4x4_16b_bf16 v[144:147], a[160:161], a[160:161], v[144:147]\n"
    "v_mfma_f32_32x32x16_bf16 v[48:63], v[104:107], a[148:151], v[48:63]\n"
    "s_waitcnt lgkmcnt(8)\n"
    "ds_read_b128 a[176:179], v26 offset:0\n"
    "buffer_load_dwordx4 v9, s[40:43], s94 offen offset:3072 lds\n"
    "v_mfma_f32_4x4x4_16b_bf16 v[152:155], v[28:29], v[106:107], v[152:155]\n"
    "v_mfma_f32_4x4x4_16b_bf16 v[140:143], v[28:29], a[162:163], v[140:143]\n"
    "v_mfma_f32_4x4x4_16b_bf16 v[148:151], a[162:163], a[162:163], v[148:151]\n"
    "v_mfma_f32_32x32x16_bf16 v[32:47], v[108:111], a[152:155], v[32:47]\n"
    "s_waitcnt lgkmcnt(4)\n"
    "s_add_u32 s85, s71, s86\n"
    "s_add_u32 m0, s89, 57344\n"
    "ds_read_b64_tr_b16 v[104:105], v12 offset:45056\n"
    "ds_read_b64_tr_b16 v[106:107], v12 offset:45568\n"
    "ds_read_b128 a[180:183], v66 offset:0\n"
    "buffer_load_dwordx4 v10, s[36:39], s85 offen lds\n"
    "v_mfma_f32_4x4x4_16b_bf16 v[152:155], v[28:29], v[108:109], v[152:155]\n"
    "v_mfma_f32_4x4x4_16b_bf16 v[136:139], v[28:29], a[168:169], v[136:139]\n"
    "v_mfma_f32_4x4x4_16b_bf16 v[144:147], a[168:169], a[168:169], v[144:147]\n"
    "v_mfma_f32_32x32x16_bf16 v[48:63], v[108:111], a[156:159], v[48:63]\n"
    "s_waitcnt lgkmcnt(8)\n"
    "ds_read_b128 a[184:187], v27 offset:0\n"
    "buffer_load_dwordx4 v11, s[36:39], s85 offen offset:1024 lds\n"
    "v_mfma_f32_4x4x4_16b_bf16 v[152:155], v[28:29], v[110:111], v[152:155]\n"
    "v_mfma_f32_4x4x4_16b_bf16 v[140:143], v[28:29], a[170:171], v[140:143]\n"
    "v_mfma_f32_4x4x4_16b_bf16 v[148:151], a[170:171], a[170:171], v[148:151]\n"
    "v_mfma_f32_32x32x16_bf16 v[32:47], v[96:99], a[160:163], v[32:47]\n"
    "s_waitcnt lgkmcnt(4)\n"
    "s_add_u32 s94, s94, 128\n"
    "s_cmp_eq_u32 s94, 1152\n"
    "s_cselect_b32 s94, 0, s94\n"
    "ds_read_b64_tr_b16 v[108:109], v12 offset:47104\n"
    "ds_read_b64_tr_b16 v[110:111], v12 offset:47616\n"
    "ds_read_b128 a[188:191], v67 offset:0\n"
    "v_mfma_f32_4x4x4_16b_bf16 v[152:155], v[28:29], v[96:97], v[152:155]\n"
    "v_mfma_f32_4x4x4_16b_bf16 v[136:139], v[28:29], a[176:177], v[136:139]\n"
    "v_mfma_f32_4x4x4_16b_bf16 v[144:147], a[176:177], a[176:177], v[144:147]\n"
    "v_mfma_f32_32x32x16_bf16 v[48:63], v[96:99], a[164:167], v[48:63]\n"
    "s_waitcnt vmcnt(6) lgkmcnt(8)\n"
    "s_barrier\n"
    "ds_read_b128 a[192:195], v24 offset:16384\n"
    "v_mfma_f32_4x4x4_16b_bf16 v[152:155], v[28:29], v[98:99], v[152:155]\n"
    "v_mfma_f32_4x4x4_16b_bf16 v[140:143], v[28:29], a[178:179], v[140:143]\n"
    "v_mfma_f32_4x4x4_16b_bf16 v[148:151], a[178:179], a[178:179], v[148:151]\n"
    "v_mfma_f32_32x32x16_bf16 v[32:47], v[100:103], a[168:171], v[32:47]\n"
    "s_waitcnt lgkmcnt(4)\n"
    "s_add_u32 m0, s90, 49152\n"
    "ds_read_b64_tr_b16 v[96:97], v12 offset:49152\n"
    "ds_read_b64_tr_b16 v[98:99], v12 offset:49664\n"
    "ds_read_b128 a[196:199], v64 offset:16384\n"
    "buffer_load_dwordx4 v6, s[40:43], s94 offen lds\n"
    "v_mfma_f32_4x4x4_16b_bf16 v[152:155], v[28:29], v[100:101], v[152:155]\n"
    "v_mfma_f32_4x4x4_16b_bf16 v[136:139], v[28:29], a[184:185], v[136:139]\n"
    "v_mfma_f32_4x4x4_16b_bf16 v[144:147], a[184:185], a[184:185], v[144:147]\n"
    "v_mfma_f32_32x32x16_bf16 v[48:63], v[100:103], a[172:175], v[48:63]\n"
    "s_waitcnt lgkmcnt(8)\n"
    "ds_read_b128 a[200:203], v25 offset:16384\n"
    "buffer_load_dwordx4 v7, s[40:43], s94 offen offset:1024 lds\n"
    "v_mfma_f32_4x4x4_16b_bf16 v[152:155], v[28:29], v[102:103], v[152:155]\n"
    "v_mfma_f32_4x4x4_16b_bf16 v[140:143], v[28:29], a[186:187], v[140:143]\n"
    "v_mfma_f32_4x4x4_16b_bf16 v[148:151], a[186:187], a[186:187], v[148:151]\n"
    "v_mfma_f32_32x32x16_bf16 v[32:47], v[104:107], a[176:179], v[32:47]\n"
    "s_waitcnt lgkmcnt(4)\n"
    "ds_read_b64_tr_b16 v[100:101], v12 offset:51200\n"
    "ds_read_b64_tr_b16 v[102:103], v12 offset:51712\n"
    "ds_read_b128 a[204:207], v65 offset:16384\n"
    "buffer_load_dwordx4 v8, s[40:43], s94 offen offset:2048 lds\n"
    "v_mfma_f32_4x4x4_16b_bf16 v[152:155], v[28:29], v[104:105], v[152:155]\n"
    "v_mfma_f32_4x4x4_16b_bf16 v[136:139], v[28:29], a[192:193], v[136:139]\n"
    "v_mfma_f32_4x4x4_16b_bf16 v[144:147], a[192:193], a[192:193], v[144:147]\n"
    "v_mfma_f32_32x32x16_bf16 v[48:63], v[104:107], a[180:183], v[48:63]\n"
    "s_waitcnt lgkmcnt(8)\n"
    "ds_read_b128 a[208:211], v26 offset:16384\n"
    "buffer_load_dwordx4 v9, s[40:43], s94 offen offset:3072 lds\n"
    "v_mfma_f32_4x4x4_16b_bf16 v[152:155], v[28:29], v[106:107], v[152:155]\n"
    "v_mfma_f32_4x4x4_16b_bf16 v[140:143], v[28:29], a[194:195], v[140:143]\n"
    "v_mfma_f32_4x4x4_16b_bf16 v[148:151], a[194:195], a[194:195], v[148:151]\n"
    "v_mfma_f32_32x32x16_bf16 v[32:47], v[108:111], a[184:187], v[32:47]\n"
    "s_waitcnt lgkmcnt(4)\n"
    "s_add_u32 s85, s72, s86\n"
    "s_add_u32 m0, s89, 65536\n"
    "ds_read_b64_tr_b16 v[104:105], v12 offset:53248\n"
    "ds_read_b64_tr_b16 v[106:107], v12 offset:53760\n"
    "ds_read_b128 a[212:215], v66 offset:16384\n"
    "buffer_load_dwordx4 v10, s[36:39], s85 offen lds\n"
    "v_mfma_f32_4x4x4_16b_bf16 v[152:155], v[28:29], v[108:109], v[152:155]\n"
    "v_mfma_f32_4x4x4_16b_bf16 v[136:139], v[28:29], a[200:201], v[136:139]\n"
    "v_mfma_f32_4x4x4_16b_bf16 v[144:147], a[200:201], a[200:201], v[144:147]\n"
    "v_mfma_f32_32x32x16_bf16 v[48:63], v[108:111], a[188:191], v[48:63]\n"
    "s_waitcnt lgkmcnt(8)\n"
    "ds_read_b128 a[216:219], v27 offset:16384\n"
    "buffer_load_dwordx4 v11, s[36:39], s85 offen offset:1024 lds\n"
    "v_mfma_f32_4x4x4_16b_bf16 v[152:155], v[28:29], v[110:111], v[152:155]\n"
    "v_mfma_f32_4x4x4_16b_bf16 v[140:143], v[28:29], a[202:203], v[140:143]\n"
    "v_mfma_f32_4x4x4_16b_bf16 v[148:151], a[202:203], a[202:203], v[148:151]\n"
    "v_mfma_f32_32x32x16_bf16 v[32:47], v[96:99], a[192:195], v[32:47]\n"
    "s_waitcnt lgkmcnt(4)\n"
    "ds_read_b64_tr_b16 v[108:109], v12 offset:55296\n"
    "ds_read_b64_tr_b16 v[110:111], v12 offset:55808\n"
    "ds_read_b128 a[220:223], v67 offset:16384\n"
    "v_mfma_f32_4x4x4_16b_bf16 v[152:155], v[28:29], v[96:97], v[152:155]\n"
    "v_mfma_f32_4x4x4_16b_bf16 v[136:139], v[28:29], a[208:209], v[136:139]\n"
    "v_mfma_f32_4x4x4_16b_bf16 v[144:147], a[208:209], a[208:209], v[144:147]\n"
    "v_mfma_f32_32x32x16_bf16 v[48:63], v[96:99], a[196:199], v[48:63]\n"
    "s_waitcnt vmcnt(6) lgkmcnt(8)\n"
    "s_barrier\n"
    "ds_read_b128 a[224:227], v24 offset:32768\n"
    "v_mfma_f32_4x4x4_16b_bf16 v[152:155], v[28:29], v[98:99], v[152:155]\n"
    "v_mfma_f32_4x4x4_16b_bf16 v[140:143], v[28:29], a[210:211], v[140:143]\n"
    "v_mfma_f32_4x4x4_16b_bf16 v[148:151], a[210:211], a[210:211], v[148:151]\n"
    "v_mfma_f32_32x32x16_bf16 v[32:47], v[100:103], a[200:203], v[32:47]\n"
    "s_waitcnt lgkmcnt(4)\n"
    "s_add_u32 s86, s86, 128\n"
    "s_cmp_eq_u32 s86, 768\n"
    "s_cselect_b32 s86, 0, s86\n"
    "s_add_u32 s85, s64, s86\n"
    "s_add_u32 m0, s89, 0\n"
    "ds_read_b64_tr_b16 v[96:97], v12 offset:57344\n"
    "ds_read_b64_tr_b16 v[98:99], v12 offset:57856\n"
    "ds_read_b128 a[228:231], v64 offset:32768\n"
    "buffer_load_dwordx4 v10, s[36:39], s85 offen lds\n"
    "v_mfma_f32_4x4x4_16b_bf16 v[152:155], v[28:29], v[100:101], v[152:155]\n"
    "v_mfma_f32_4x4x4_16b_bf16 v[136:139], v[28:29], a[216:217], v[136:139]\n"
    "v_mfma_f32_4x4x4_16b_bf16 v[144:147], a[216:217], a[216:217], v[144:147]\n"
    "v_mfma_f32_32x32x16_bf16 v[48:63], v[100:103], a[204:207], v[48:63]\n"
    "s_waitcnt lgkmcnt(8)\n"
    "ds_read_b128 a[232:235], v25 offset:32768\n"
    "buffer_load_dwordx4 v11, s[36:39], s85 offen offset:1024 lds\n"
    "v_mfma_f32_4x4x4_16b_bf16 v[152:155], v[28:29], v[102:103], v[152:155]\n"
    "v_mfma_f32_4x4x4_16b_bf16 v[140:143], v[28:29], a[218:219], v[140:143]\n"
    "v_mfma_f32_4x4x4_16b_bf16 v[148:151], a[218:219], a[218:219], v[148:151]\n"
    "v_mfma_f32_32x32x16_bf16 v[32:47], v[104:107], a[208:211], v[32:47]\n"
    "s_waitcnt lgkmcnt(4)\n"
    "s_add_u32 s85, s65, s86\n"
    "s_add_u32 m0, s89, 8192\n"
    "ds_read_b64_tr_b16 v[100:101], v12 offset:59392\n"
    "ds_read_b64_tr_b16 v[102:103], v12 offset:59904\n"
    "ds_read_b128 a[236:239], v65 offset:32768\n"
    "buffer_load_dwordx4 v10, s[36:39], s85 offen lds\n"
    "v_mfma_f32_4x4x4_16b_bf16 v[152:155], v[28:29], v[104:105], v[152:155]\n"
    "v_mfma_f32_4x4x4_16b_bf16 v[136:139], v[28:29], a[224:225], v[136:139]\n"
    "v_mfma_f32_4x4x4_16b_bf16 v[144:147], a[224:225], a[224:225], v[144:147]\n"
    "v_mfma_f32_32x32x16_bf16 v[48:63], v[104:107], a[212:215], v[48:63]\n"
    "s_waitcnt lgkmcnt(8)\n"
    "ds_read_b128 a[240:243], v26 offset:32768\n"
    "buffer_load_dwordx4 v11, s[36:39], s85 offen offset:1024 lds\n"
    "v_mfma_f32_4x4x4_16b_bf16 v[152:155], v[28:29], v[106:107], v[152:155]\n"
    "v_mfma_f32_4x4x4_16b_bf16 v[140:143], v[28:29], a[226:227], v[140:143]\n"
    "v_mfma_f32_4x4x4_16b_bf16 v[148:151], a[226:227], a[226:227], v[148:151]\n"
    "v_mfma_f32_32x32x16_bf16 v[32:47], v[108:111], a[216:219], v[32:47]\n"
    "s_waitcnt lgkmcnt(4)\n"
    "s_add_u32 s85, s66, s86\n"
    "s_add_u32 m0, s89, 16384\n"
    "ds_read_b64_tr_b16 v[104:105], v12 offset:61440\n"
    "ds_read_b64_tr_b16 v[106:107], v12 offset:61952\n"
    "ds_read_b128 a[244:247], v66 offset:32768\n"
    "buffer_load_dwordx4 v10, s[36:39], s85 offen lds\n"
    "v_mfma_f32_4x4x4_16b_bf16 v[152:155], v[28:29], v[108:109], v[152:155]\n"
    "v_mfma_f32_4x4x4_16b_bf16 v[136:139], v[28:29], a[232:233], v[136:139]\n"
    "v_mfma_f32_4x4x4_16b_bf16 v[144:147], a[232:233], a[232:233], v[144:147]\n"
    "v_mfma_f32_32x32x16_bf16 v[48:63], v[108:111], a[220:223], v[48:63]\n"
    "s_waitcnt lgkmcnt(8)\n"
    "ds_read_b128 a[248:251], v27 offset:32768\n"
    "buffer_load_dwordx4 v11, s[36:39], s85 offen offset:1024 lds\n"
    "v_mfma_f32_4x4x4_16b_bf16 v[152:155], v[28:29], v[110:111], v[152:155]\n"
    "v_mfma_f32_4x4x4_16b_bf16 v[140:143], v[28:29], a[234:235], v[140:143]\n"
    "v_mfma_f32_4x4x4_16b_bf16 v[148:151], a[234:235], a[234:235], v[148:151]\n"
    "v_mfma_f32_32x32x16_bf16 v[32:47], v[96:99], a[224:227], v[32:47]\n"
    "s_waitcnt lgkmcnt(4)\n"
    "ds_read_b64_tr_b16 v[108:109], v12 offset:63488\n"
    "ds_read_b64_tr_b16 v[110:111], v12 offset:64000\n"
    "ds_read_b128 a[252:255], v67 offset:32768\n"
    "v_mfma_f32_4x4x4_16b_bf16 v[152:155], v[28:29], v[96:97], v[152:155]\n"
    "v_mfma_f32_4x4x4_16b_bf16 v[136:139], v[28:29], a[240:241], v[136:139]\n"
    "v_mfma_f32_4x4x4_16b_bf16 v[144:147], a[240:241], a[240:241], v[144:147]\n"
    "v_mfma_f32_32x32x16_bf16 v[48:63], v[96:99], a[228:231], v[48:63]\n"
    "s_waitcnt vmcnt(6) lgkmcnt(8)\n"
    "s_barrier\n"
    "ds_read_b128 v[224:227], v24 offset:49152\n"
    "ds_read_b128 v[232:235], v25 offset:49152\n"
    "v_mfma_f32_4x4x4_16b_bf16 v[152:155], v[28:29], v[98:99], v[152:155]\n"
    "v_mfma_f32_4x4x4_16b_bf16 v[140:143], v[28:29], a[242:243], v[140:143]\n"
    "v_mfma_f32_4x4x4_16b_bf16 v[148:151], a[242:243], a[242:243], v[148:151]\n"
    "v_mfma_f32_32x32x16_bf16 v[32:47], v[100:103], a[232:235], v[32:47]\n"
    "s_waitcnt lgkmcnt(5)\n"
    "s_add_u32 s85, s67, s86\n"
    "s_add_u32 m0, s89, 24576\n"
    "ds_read_b64_tr_b16 v[96:97], v13 offset:0\n"
    "ds_read_b64_tr_b16 v[98:99], v13 offset:512\n"
    "ds_read_b128 v[240:243], v26 offset:49152\n"
    "buffer_load_dwordx4 v10, s[36:39], s85 offen lds\n"
    "v_mfma_f32_4x4x4_16b_bf16 v[152:155], v[28:29], v[100:101], v[152:155]\n"
    "v_mfma_f32_4x4x4_16b_bf16 v[136:139], v[28:29], a[248:249], v[136:139]\n"
    "v_mfma_f32_4x4x4_16b_bf16 v[144:147], a[248:249], a[248:249], v[144:147]\n"
    "v_mfma_f32_32x32x16_bf16 v[48:63], v[100:103], a[236:239], v[48:63]\n"
    "s_waitcnt lgkmcnt(9)\n"
    "ds_read_b128 v[248:251], v27 offset:49152\n"
    "ds_read_b128 v[228:231], v64 offset:49152\n"
    "buffer_load_dwordx4 v11, s[36:39], s85 offen offset:1024 lds\n"
    "v_mfma_f32_4x4x4_16b_bf16 v[152:155], v[28:29], v[102:103], v[152:155]\n"
    "v_mfma_f32_4x4x4_16b_bf16 v[140:143], v[28:29], a[250:251], v[140:143]\n"
    "v_mfma_f32_4x4x4_16b_bf16 v[148:151], a[250:251], a[250:251], v[148:151]\n"
    "v_mfma_f32_32x32x16_bf16 v[32:47], v[104:107], a[240:243], v[32:47]\n"
    "s_waitcnt lgkmcnt(6)\n"
    "s_add_u32 s85, s68, s86\n"
    "s_add_u32 m0, s89, 32768\n"
    "ds_read_b64_tr_b16 v[100:101], v13 offset:2048\n"
    "ds_read_b64_tr_b16 v[102:103], v13 offset:2560\n"
    "buffer_load_dwordx4 v10, s[36:39], s85 offen lds\n"
    "v_mfma_f32_4x4x4_16b_bf16 v[152:155], v[28:29], v[104:105], v[152:155]\n"
    "v_mfma_f32_4x4x4_16b_bf16 v[136:139], v[28:29], v[224:225], v[136:139]\n"
    "v_mfma_f32_4x4x4_16b_bf16 v[144:147], v[224:225], v[224:225], v[144:147]\n"
    "v_mfma_f32_4x4x4_16b_bf16 v[140:143], v[28:29], v[226:227], v[140:143]\n"
    "v_mfma_f32_4x4x4_16b_bf16 v[148:151], v[226:227], v[226:227], v[148:151]\n"
    "v_mfma_f32_32x32x16_bf16 v[48:63], v[104:107], a[244:247], v[48:63]\n"
    "s_waitcnt lgkmcnt(7)\n"
    "ds_read_b128 v[236:239], v65 offset:49152\n"
    "ds_read_b128 v[244:247], v66 offset:49152\n"
    "buffer_load_dwordx4 v11, s[36:39], s85 offen offset:1024 lds\n"
    "v_mfma_f32_4x4x4_16b_bf16 v[152:155], v[28:29], v[106:107], v[152:155]\n"
    "v_mfma_f32_4x4x4_16b_bf16 v[136:139], v[28:29], v[232:233], v[136:139]\n"
    "v_mfma_f32_4x4x4_16b_bf16 v[144:147], v[232:233], v[232:233], v[144:147]\n"
    "v_mfma_f32_4x4x4_16b_bf16 v[140:143], v[28:29], v[234:235], v[140:143]\n"
    "v_mfma_f32_4x4x4_16b_bf16 v[148:151], v[234:235], v[234:235], v[148:151]\n"
    "v_mfma_f32_32x32x16_bf16 v[32:47], v[108:111], a[248:251], v[32:47]\n"
    "s_waitcnt lgkmcnt(6)\n"
    "ds_read_b64_tr_b16 v[104:105], v13 offset:4096\n"
    "ds_read_b64_tr_b16 v[106:107], v13 offset:4608\n"
    "buffer_store_dwordx4 v[164:167], v168, s[44:47], s31 offen sc1\n"
    "v_mfma_f32_4x4x4_16b_bf16 v[152:155], v[28:29], v[108:109], v[152:155]\n"
    "v_mfma_f32_4x4x4_16b_bf16 v[136:139], v[28:29], v[240:241], v[136:139]\n"
    "v_mfma_f32_4x4x4_16b_bf16 v[144:147], v[240:241], v[240:241], v[144:147]\n"
    "v_mfma_f32_4x4x4_16b_bf16 v[140:143], v[28:29], v[242:243], v[140:143]\n"
    "v_mfma_f32_4x4x4_16b_bf16 v[148:151], v[242:243], v[242:243], v[148:151]\n"
    "v_mfma_f32_32x32x16_bf16 v[48:63], v[108:111], a[252:255], v[48:63]\n"
    "s_waitcnt lgkmcnt(6)\n"
    "ds_read_b128 v[252:255], v67 offset:49152\n"
    "buffer_store_dwordx4 v[188:191], v192, s[44:47], s97 offen sc1\n"
    "v_mfma_f32_4x4x4_16b_bf16 v[152:155], v[28:29], v[110:111], v[152:155]\n"
    "v_mfma_f32_4x4x4_16b_bf16 v[136:139], v[28:29], v[248:249], v[136:139]\n"
    "v_mfma_f32_4x4x4_16b_bf16 v[144:147], v[248:249], v[248:249], v[144:147]\n"
    "v_mfma_f32_4x4x4_16b_bf16 v[140:143], v[28:29], v[250:251], v[140:143]\n"
    "v_mfma_f32_4x4x4_16b_bf16 v[148:151], v[250:251], v[250:251], v[148:151]\n"
    "v_mfma_f32_32x32x16_bf16 v[32:47], v[96:99], v[224:227], v[32:47]\n"
    "ds_read_b64_tr_b16 v[108:109], v13 offset:6144\n"
    "ds_read_b64_tr_b16 v[110:111], v13 offset:6656\n"
    "s_mov_b32 s60, 0x3ae38e39\n"
    "v_and_b32 v2, 3, v1\n"
    "v_add_f32 v136, v136, v140\n"
    "v_cmp_eq_u32 vcc, 1, v2\n"
    "v_cmp_eq_u32 s[62:63], 2, v2\n"
    "v_mov_b32 v137, v136\n"
    "v_mfma_f32_4x4x4_16b_bf16 v[152:155], v[28:29], v[96:97], v[152:155]\n"
    "v_mfma_f32_32x32x16_bf16 v[48:63], v[96:99], v[228:231], v[48:63]\n"
    "s_waitcnt vmcnt(8) lgkmcnt(6)\n"
    "s_barrier\n"
    "v_cndmask_b32 v144, v144, v145, vcc\n"
    "v_cndmask_b32 v148, v148, v149, vcc\n"
    "v_cmp_eq_u32 vcc, 3, v2\n"
    "v_cndmask_b32 v144, v144, v146, s[62:63]\n"
    "v_cndmask_b32 v148, v148, v150, s[62:63]\n"
    "v_permlane32_swap_b32 v136, v137\n"
    "v_mfma_f32_4x4x4_16b_bf16 v[152:155], v[28:29], v[98:99], v[152:155]\n"
    "v_mfma_f32_32x32x16_bf16 v[32:47], v[100:103], v[232:235], v[32:47]\n"
    "s_add_u32 s85, s69, s86\n"
    "s_add_u32 m0, s89, 40960\n"
    "ds_read_b64_tr_b16 v[96:97], v12 offset:0\n"
    "ds_read_b64_tr_b16 v[98:99], v12 offset:512\n"
    "buffer_load_dwordx4 v10, s[36:39], s85 offen lds\n"
    "v_cndmask_b32 v144, v144, v147, vcc\n"
    "v_cndmask_b32 v148, v148, v151, vcc\n"
    "v_add_f32 v136, v136, v137\n"
    "v_add_f32 v144, v144, v148\n"
    "v_mul_f32 v136, 0xbae38e39, v136\n"
    "v_mov_b32 v145, v144\n"
    "v_mfma_f32_4x4x4_16b_bf16 v[152:155], v[28:29], v[100:101], v[152:155]\n"
    "v_mfma_f32_32x32x16_bf16 v[48:63], v[100:103], v[236:239], v[48:63]\n"
    "s_waitcnt lgkmcnt(5)\n"
    "buffer_load_dwordx4 v11, s[36:39], s85 offen offset:1024 lds\n"
    "v_cmp_gt_u32 vcc, 32, v1\n"
    "v_cvt_pk_bf16_f32 v137, v136, v136\n"
    "v_permlane32_swap_b32 v144, v145\n"
    "v_mul_f32 v146, v136, v136\n"
    "v_add_f32 v144, v144, v145\n"
    "v_lshlrev_b32 v138, 16, v137\n"
    "v_mfma_f32_4x4x4_16b_bf16 v[152:155], v[28:29], v[102:103], v[152:155]\n"
    "v_mfma_f32_32x32x16_bf16 v[32:47], v[104:107], v[240:243], v[32:47]\n"
    "s_add_u32 s85, s70, s86\n"
    "s_add_u32 m0, s89, 49152\n"
    "ds_read_b64_tr_b16 v[100:101], v12 offset:2048\n"
    "ds_read_b64_tr_b16 v[102:103], v12 offset:2560\n"
    "buffer_load_dwordx4 v10, s[36:39], s85 offen lds\n"
    "v_fma_f32 v144, v144, s60, -v146\n"
    "v_sub_f32 v138, v136, v138\n"
    "v_max_f32 v144, 0, v144\n"
    "v_cvt_pk_bf16_f32 v138, v138, v138\n"
    "v_add_f32 v144, 0x3727c5ac, v144\n"
    "v_cndmask_b32 v156, v138, v137, vcc\n"
    "v_mfma_f32_4x4x4_16b_bf16 v[152:155], v[28:29], v[104:105], v[152:155]\n"
    "v_mfma_f32_32x32x16_bf16 v[48:63], v[104:107], v[244:247], v[48:63]\n"
    "s_waitcnt lgkmcnt(4)\n"
    "buffer_load_dwordx4 v11, s[36:39], s85 offen offset:1024 lds\n"
    "v_rsq_f32 v145, v144\n"
    "v_lshlrev_b32 v2, 4, %0\n"
    "v_mul_f32 v144, v144, v145\n"
    "v_mul_f32 v158, 0x3eeff558, v145\n"
    "v_cvt_pk_bf16_f32 v146, v144, 0\n"
    "v_add_u32 v2, 73728, v2\n"
    "v_mfma_f32_4x4x4_16b_bf16 v[152:155], v[28:29], v[106:107], v[152:155]\n"
    "v_mfma_f32_32x32x16_bf16 v[32:47], v[108:111], v[248:251], v[32:47]\n"
    "ds_read_b64_tr_b16 v[104:105], v12 offset:4096\n"
    "ds_read_b64_tr_b16 v[106:107], v12 offset:4608\n"
    "v_lshlrev_b32 v147, 16, v146\n"
    "v_xor_b32 v3, 0x400, v2\n"
    "v_sub_f32 v147, v144, v147\n"
    "v_cvt_pk_bf16_f32 v147, v147, 0\n"
    "v_cndmask_b32 v157, v147, v146, vcc\n"
    "v_mfma_f32_4x4x4_16b_bf16 v[152:155], v[28:29], v[108:109], v[152:155]\n"
    "v_mfma_f32_32x32x16_bf16 v[48:63], v[108:111], v[252:255], v[48:63]\n"
    "s_waitcnt lgkmcnt(4)\n"
    "ds_write_b96 v2, v[156:158]\n"
    "v_mfma_f32_4x4x4_16b_bf16 v[152:155], v[28:29], v[110:111], v[152:155]\n"
    "s_waitcnt lgkmcnt(0)\n"
    "s_barrier\n"
    "ds_read_b96 v[160:162], v3\n"
    "s_cmp_lt_u32 s91, 3\n"
    "s_cselect_b32 s60, 0, 2\n"
    "v_mov_b32 v153, v152\n"
    "s_or_b32 s60, s60, s81\n"
    "s_lshl_b32 s60, s60, 5\n"
    "v_permlane32_swap_b32 v152, v153\n"
    "v_or_b32 v2, s60, v1\n"
    "v_add_f32 v152, v152, v153\n"
    "v_cmp_gt_u32 vcc, 32, v2\n"
    "v_cvt_pk_bf16_f32 v153, v152, v152\n"
    "v_mov_b32 v3, 1879048192\n"
    "v_lshlrev_b32 v153, 16, v153\n"
    "v_cndmask_b32 v3, v3, v16, vcc\n"
    "v_sub_f32 v153, v152, v153\n"
    "v_mov_b32 v128, v156\n"
    "v_cvt_pk_bf16_f32 v124, v152, v153\n"
    "v_mov_b32 v129, v157\n"
    "v_mov_b32 v122, v158\n"
    "v_mov_b32 v120, v124\n"
    "v_mov_b32 v121, s56\n"
    "s_lshl_b32 s60, s84, 9\n"
    "s_add_u32 s60, s60, s92\n"
    "v_mfma_f32_32x32x16_bf16 v[64:79], v[124:127], v[128:131], v[32:47]\n"
    "s_waitcnt lgkmcnt(0)\n"
    "buffer_store_dwordx2 v[120:121], v3, s[44:47], s60 offen sc1\n"
    "v_mov_b32 v132, v160\n"
    "v_mov_b32 v133, v161\n"
    "v_mov_b32 v123, v162\n"
    "s_mov_b32 s88, 5\n"
    "v_mfma_f32_32x32x16_bf16 v[80:95], v[124:127], v[132:135], v[48:63]\n"
    "s_and_b32 s60, s80, 3\n"
    "s_cmp_eq_u32 s60, 1\n"
    "s_cbranch_scc1 .LL_STEP1_%=\n"
    "s_and_b32 s60, s80, 3\n"
    "s_cmp_eq_u32 s60, 2\n"
    "s_cbranch_scc1 .LL_STEP2_%=\n"
    "s_and_b32 s60, s80, 3\n"
    "s_cmp_eq_u32 s60, 3\n"
    "s_cbranch_scc1 .LL_STEP3_%=\n"
    ".LL_STEP0_%=:\n"
    "v_mfma_f32_32x32x16_bf16 v[32:47], v[96:99], a[0:3], 0\n"
    "s_add_u32 s85, s71, s86\n"
    "ds_read_b64_tr_b16 v[108:109], v12 offset:6144\n"
    "ds_read_b64_tr_b16 v[110:111], v12 offset:6656\n"
    "v_mfma_f32_32x32x16_bf16 v[48:63], v[96:99], a[4:7], 0\n"
    "s_waitcnt lgkmcnt(6)\n"
    "v_mul_f32 v64, v64, v122\n"
    "v_fma_f32 v112, -v64, v64, s87\n"
    "v_mul_f32 v112, v64, v112\n"
    "v_mul_f32 v65, v65, v122\n"
    "v_exp_f32 v112, v112\n"
    "v_mfma_f32_32x32x16_bf16 v[32:47], v[100:103], a[8:11], v[32:47]\n"
    "s_add_u32 m0, s89, 57344\n"
    "ds_read_b64_tr_b16 v[96:97], v12 offset:8192\n"
    "ds_read_b64_tr_b16 v[98:99], v12 offset:8704\n"
    "v_fma_f32 v113, -v65, v65, s87\n"
    "v_add_f32 v112, 1.0, v112\n"
    "v_mul_f32 v113, v65, v113\n"
    "v_rcp_f32 v112, v112\n"
    "v_mfma_f32_32x32x16_bf16 v[48:63], v[100:103], a[12:15], v[48:63]\n"
    "s_waitcnt lgkmcnt(5)\n"
    "buffer_load_dwordx4 v10, s[36:39], s85 offen lds\n"
    "buffer_load_dwordx4 v11, s[36:39], s85 offen offset:1024 lds\n"
    "v_mul_f32 v66, v66, v122\n"
    "v_exp_f32 v113, v113\n"
    "v_mfma_f32_32x32x16_bf16 v[32:47], v[104:107], a[16:19], v[32:47]\n"
    "s_lshl_b32 s60, s86, 2\n"
    "ds_read_b64_tr_b16 v[100:101], v12 offset:10240\n"
    "ds_read_b64_tr_b16 v[102:103], v12 offset:10752\n"
    "v_fma_f32 v114, -v66, v66, s87\n"
    "v_mul_f32 v64, v64, v112\n"
    "v_add_f32 v113, 1.0, v113\n"
    "v_mul_f32 v114, v66, v114\n"
    "v_rcp_f32 v113, v113\n"
    "v_mfma_f32_32x32x16_bf16 v[48:63], v[104:107], a[20:23], v[48:63]\n"
    "s_waitcnt lgkmcnt(4)\n"
    "v_mul_f32 v67, v67, v122\n"
    "v_exp_f32 v114, v114\n"
    "v_fma_f32 v112, -v67, v67, s87\n"
    "v_mul_f32 v65, v65, v113\n"
    "v_add_f32 v114, 1.0, v114\n"
    "v_mul_f32 v112, v67, v112\n"
    "v_mfma_f32_32x32x16_bf16 v[32:47], v[108:111], a[24:27], v[32:47]\n"
    "s_add_u32 s60, s60, s92\n"
    "ds_read_b64_tr_b16 v[104:105], v12 offset:12288\n"
    "ds_read_b64_tr_b16 v[106:107], v12 offset:12800\n"
    "v_cvt_pk_bf16_f32 v116, v64, v65\n"
    "v_rcp_f32 v114, v114\n"
    "v_mul_f32 v68, v68, v122\n"
    "v_exp_f32 v112, v112\n"
    "v_mfma_f32_32x32x16_bf16 v[48:63], v[108:111], a[28:31], v[48:63]\n"
    "s_waitcnt lgkmcnt(4)\n"
    "v_fma_f32 v113, -v68, v68, s87\n"
    "v_mul_f32 v66, v66, v114\n"
    "v_add_f32 v112, 1.0, v112\n"
    "v_mul_f32 v113, v68, v113\n"
    "v_rcp_f32 v112, v112\n"
    "v_mul_f32 v69, v69, v122\n"
    "v_mfma_f32_32x32x16_bf16 v[32:47], v[96:99], a[32:35], v[32:47]\n"
    "s_add_u32 s85, s72, s86\n"
    "ds_read_b64_tr_b16 v[108:109], v12 offset:14336\n"
    "ds_read_b64_tr_b16 v[110:111], v12 offset:14848\n"
    "v_exp_f32 v113, v113\n"
    "v_fma_f32 v114, -v69, v69, s87\n"
    "v_mul_f32 v67, v67, v112\n"
    "v_add_f32 v113, 1.0, v113\n"
    "v_mfma_f32_32x32x16_bf16 v[48:63], v[96:99], a[36:39], v[48:63]\n"
    "s_waitcnt vmcnt(9) lgkmcnt(4)\n"
    "s_barrier\n"
    "v_mul_f32 v114, v69, v114\n"
    "v_cvt_pk_bf16_f32 v117, v66, v67\n"
    "v_rcp_f32 v113, v113\n"
    "v_mfma_f32_32x32x16_bf16 v[32:47], v[100:103], a[40:43], v[32:47]\n"
    "s_add_u32 m0, s89, 65536\n"
    "ds_read_b64_tr_b16 v[96:97], v12 offset:16384\n"
    "ds_read_b64_tr_b16 v[98:99], v12 offset:16896\n"
    "v_mul_f32 v70, v70, v122\n"
    "v_exp_f32 v114, v114\n"
    "v_fma_f32 v112, -v70, v70, s87\n"
    "v_mul_f32 v68, v68, v113\n"
    "v_mfma_f32_32x32x16_bf16 v[48:63], v[100:103], a[44:47], v[48:63]\n"
    "s_waitcnt lgkmcnt(4)\n"
    "buffer_load_dwordx4 v10, s[36:39], s85 offen lds\n"
    "buffer_load_dwordx4 v11, s[36:39], s85 offen offset:1024 lds\n"
    "v_add_f32 v114, 1.0, v114\n"
    "v_mul_f32 v112, v70, v112\n"
    "v_rcp_f32 v114, v114\n"
    "v_mfma_f32_32x32x16_bf16 v[32:47], v[104:107], a[48:51], v[32:47]\n"
    "s_mov_b32 s61, s86\n"
    "s_add_u32 s86, s86, 128\n"
    "ds_read_b64_tr_b16 v[100:101], v12 offset:18432\n"
    "ds_read_b64_tr_b16 v[102:103], v12 offset:18944\n"
    "v_mul_f32 v71, v71, v122\n"
    "v_exp_f32 v112, v112\n"
    "v_fma_f32 v113, -v71, v71, s87\n"
    "v_mfma_f32_32x32x16_bf16 v[48:63], v[104:107], a[52:55], v[48:63]\n"
    "s_waitcnt lgkmcnt(4)\n"
    "ds_write_b64 v14, v[116:117] offset:0\n"
    "v_mul_f32 v69, v69, v114\n"
    "v_add_f32 v112, 1.0, v112\n"
    "v_mul_f32 v113, v71, v113\n"
    "v_mfma_f32_32x32x16_bf16 v[32:47], v[108:111], a[56:59], v[32:47]\n"
    "s_cmp_eq_u32 s86, 768\n"
    "s_cselect_b32 s86, 0, s86\n"
    "s_cmp_eq_u32 s88, 1\n"
    "ds_read_b64_tr_b16 v[104:105], v12 offset:20480\n"
    "ds_read_b64_tr_b16 v[106:107], v12 offset:20992\n"
    "v_cvt_pk_bf16_f32 v118, v68, v69\n"
    "v_rcp_f32 v112, v112\n"
    "v_mfma_f32_32x32x16_bf16 v[48:63], v[108:111], a[60:63], v[48:63]\n"
    "s_waitcnt lgkmcnt(5)\n"
    "v_mul_f32 v72, v72, v122\n"
    "v_exp_f32 v113, v113\n"
    "v_fma_f32 v114, -v72, v72, s87\n"
    "v_mul_f32 v70, v70, v112\n"
    "v_add_f32 v113, 1.0, v113\n"
    "v_mfma_f32_32x32x16_bf16 v[32:47], v[96:99], a[64:67], v[32:47]\n"
    "s_cbranch_scc0 .LL_SKSW0_%=\n"
    "s_mov_b64 s[36:37], s[52:53]\n"
    "s_mov_b32 s38, s54\n"
    "s_mov_b32 s86, 0\n"
    "s_mov_b32 s64, s73\n"
    "s_mov_b32 s65, s74\n"
    "s_mov_b32 s66, s75\n"
    "s_mov_b32 s67, s76\n"
    "s_mov_b32 s68, s77\n"
    "s_mov_b32 s69, s78\n"
    "s_mov_b32 s70, 1879048192\n"
    "v_mov_b32 v10, v19\n"
    "v_mov_b32 v11, v20\n"
    ".LL_SKSW0_%=:\n"
    "s_add_u32 s85, s64, s86\n"
    "ds_read_b64_tr_b16 v[108:109], v12 offset:22528\n"
    "ds_read_b64_tr_b16 v[110:111], v12 offset:23040\n"
    "v_mul_f32 v114, v72, v114\n"
    "v_rcp_f32 v113, v113\n"
    "v_mfma_f32_32x32x16_bf16 v[48:63], v[96:99], a[68:71], v[48:63]\n"
    "s_waitcnt lgkmcnt(5)\n"
    "v_mul_f32 v73, v73, v122\n"
    "v_exp_f32 v114, v114\n"
    "v_fma_f32 v112, -v73, v73, s87\n"
    "v_mul_f32 v71, v71, v113\n"
    "v_add_f32 v114, 1.0, v114\n"
    "v_mfma_f32_32x32x16_bf16 v[32:47], v[100:103], a[72:75], v[32:47]\n"
    "s_add_u32 m0, s89, 0\n"
    "ds_read_b64_tr_b16 v[96:97], v12 offset:24576\n"
    "ds_read_b64_tr_b16 v[98:99], v12 offset:25088\n"
    "v_mul_f32 v112, v73, v112\n"
    "v_cvt_pk_bf16_f32 v119, v70, v71\n"
    "v_rcp_f32 v114, v114\n"
    "v_mul_f32 v74, v74, v122\n"
    "v_mfma_f32_32x32x16_bf16 v[48:63], v[100:103], a[76:79], v[48:63]\n"
    "s_waitcnt lgkmcnt(4)\n"
    "buffer_load_dwordx4 v10, s[36:39], s85 offen lds\n"
    "buffer_load_dwordx4 v11, s[36:39], s85 offen offset:1024 lds\n"
    "v_exp_f32 v112, v112\n"
    "v_mfma_f32_32x32x16_bf16 v[32:47], v[104:107], a[80:83], v[32:47]\n"
    "ds_read_b64_tr_b16 v[100:101], v12 offset:26624\n"
    "ds_read_b64_tr_b16 v[102:103], v12 offset:27136\n"
    "v_fma_f32 v113, -v74, v74, s87\n"
    "v_mul_f32 v72, v72, v114\n"
    "v_add_f32 v112, 1.0, v112\n"
    "v_mul_f32 v113, v74, v113\n"
    "v_mfma_f32_32x32x16_bf16 v[48:63], v[104:107], a[84:87], v[48:63]\n"
    "s_waitcnt lgkmcnt(4)\n"
    "ds_write_b64 v14, v[118:119] offset:8\n"
    "v_rcp_f32 v112, v112\n"
    "v_mul_f32 v75, v75, v122\n"
    "v_exp_f32 v113, v113\n"
    "v_mfma_f32_32x32x16_bf16 v[32:47], v[108:111], a[88:91], v[32:47]\n"
    "ds_read_b64_tr_b16 v[104:105], v12 offset:28672\n"
    "ds_read_b64_tr_b16 v[106:107], v12 offset:29184\n"
    "v_fma_f32 v114, -v75, v75, s87\n"
    "v_mul_f32 v73, v73, v112\n"
    "v_add_f32 v113, 1.0, v113\n"
    "v_mul_f32 v114, v75, v114\n"
    "v_mfma_f32_32x32x16_bf16 v[48:63], v[108:111], a[92:95], v[48:63]\n"
    "s_waitcnt lgkmcnt(5)\n"
    "buffer_load_dword v31, v18, s[52:55], s93 offen\n"
    "v_cvt_pk_bf16_f32 v116, v72, v73\n"
    "v_rcp_f32 v113, v113\n"
    "v_mul_f32 v76, v76, v122\n"
    "v_mfma_f32_32x32x16_bf16 v[32:47], v[96:99], a[96:99], v[32:47]\n"
    "s_add_u32 s85, s65, s86\n"
    "ds_read_b64_tr_b16 v[108:109], v12 offset:30720\n"
    "ds_read_b64_tr_b16 v[110:111], v12 offset:31232\n"
    "v_exp_f32 v114, v114\n"
    "v_fma_f32 v112, -v76, v76, s87\n"
    "v_mul_f32 v74, v74, v113\n"
    "v_mfma_f32_32x32x16_bf16 v[48:63], v[96:99], a[100:103], v[48:63]\n"
    "s_waitcnt vmcnt(9) lgkmcnt(5)\n"
    "s_barrier\n"
    "v_add_f32 v114, 1.0, v114\n"
    "v_mul_f32 v112, v76, v112\n"
    "v_rcp_f32 v114, v114\n"
    "v_mfma_f32_32x32x16_bf16 v[32:47], v[100:103], a[104:107], v[32:47]\n"
    "s_add_u32 m0, s89, 8192\n"
    "ds_read_b64_tr_b16 v[96:97], v12 offset:32768\n"
    "ds_read_b64_tr_b16 v[98:99], v12 offset:33280\n"
    "v_mul_f32 v77, v77, v122\n"
    "v_exp_f32 v112, v112\n"
    "v_fma_f32 v113, -v77, v77, s87\n"
    "v_mfma_f32_32x32x16_bf16 v[48:63], v[100:103], a[108:111], v[48:63]\n"
    "s_waitcnt lgkmcnt(4)\n"
    "buffer_load_dwordx4 v10, s[36:39], s85 offen lds\n"
    "buffer_load_dwordx4 v11, s[36:39], s85 offen offset:1024 lds\n"
    "v_mul_f32 v75, v75, v114\n"
    "v_add_f32 v112, 1.0, v112\n"
    "v_mfma_f32_32x32x16_bf16 v[32:47], v[104:107], a[112:115], v[32:47]\n"
    "s_add_u32 s93, s93, 11264\n"
    "ds_read_b64_tr_b16 v[100:101], v12 offset:34816\n"
    "ds_read_b64_tr_b16 v[102:103], v12 offset:35328\n"
    "v_mul_f32 v113, v77, v113\n"
    "v_cvt_pk_bf16_f32 v117, v74, v75\n"
    "v_rcp_f32 v112, v112\n"
    "v_mfma_f32_32x32x16_bf16 v[48:63], v[104:107], a[116:119], v[48:63]\n"
    "s_waitcnt lgkmcnt(4)\n"
    "ds_write_b64 v14, v[116:117] offset:512\n"
    "v_mul_f32 v78, v78, v122\n"
    "v_exp_f32 v113, v113\n"
    "v_fma_f32 v114, -v78, v78, s87\n"
    "v_mfma_f32_32x32x16_bf16 v[32:47], v[108:111], a[120:123], v[32:47]\n"
    "ds_read_b64_tr_b16 v[104:105], v12 offset:36864\n"
    "ds_read_b64_tr_b16 v[106:107], v12 offset:37376\n"
    "v_mul_f32 v76, v76, v112\n"
    "v_add_f32 v113, 1.0, v113\n"
    "v_mul_f32 v114, v78, v114\n"
    "v_rcp_f32 v113, v113\n"
    "v_mfma_f32_32x32x16_bf16 v[48:63], v[108:111], a[124:127], v[48:63]\n"
    "s_waitcnt lgkmcnt(5)\n"
    "v_mul_f32 v79, v79, v122\n"
    "v_exp_f32 v114, v114\n"
    "v_fma_f32 v112, -v79, v79, s87\n"
    "v_mul_f32 v77, v77, v113\n"
    "v_add_f32 v114, 1.0, v114\n"
    "v_mfma_f32_32x32x16_bf16 v[32:47], v[96:99], a[128:131], v[32:47]\n"
    "s_add_u32 s85, s66, s86\n"
    "ds_read_b64_tr_b16 v[108:109], v12 offset:38912\n"
    "ds_read_b64_tr_b16 v[110:111], v12 offset:39424\n"
    "v_mul_f32 v112, v79, v112\n"
    "v_cvt_pk_bf16_f32 v118, v76, v77\n"
    "v_rcp_f32 v114, v114\n"
    "v_mfma_f32_32x32x16_bf16 v[48:63], v[96:99], a[132:135], v[48:63]\n"
    "s_waitcnt lgkmcnt(5)\n"
    "v_mul_f32 v80, v80, v123\n"
    "v_exp_f32 v112, v112\n"
    "v_fma_f32 v113, -v80, v80, s87\n"
    "v_mul_f32 v78, v78, v114\n"
    "v_add_f32 v112, 1.0, v112\n"
    "v_mfma_f32_32x32x16_bf16 v[32:47], v[100:103], a[136:139], v[32:47]\n"
    "s_add_u32 m0, s89, 16384\n"
    "ds_read_b64_tr_b16 v[96:97], v12 offset:40960\n"
    "ds_read_b64_tr_b16 v[98:99], v12 offset:41472\n"
    "v_mul_f32 v113, v80, v113\n"
    "v_rcp_f32 v112, v112\n"
    "v_mul_f32 v81, v81, v123\n"
    "v_mfma_f32_32x32x16_bf16 v[48:63], v[100:103], a[140:143], v[48:63]\n"
    "s_waitcnt lgkmcnt(4)\n"
    "buffer_load_dwordx4 v10, s[36:39], s85 offen lds\n"
    "buffer_load_dwordx4 v11, s[36:39], s85 offen offset:1024 lds\n"
    "v_exp_f32 v113, v113\n"
    "v_fma_f32 v114, -v81, v81, s87\n"
    "v_mfma_f32_32x32x16_bf16 v[32:47], v[104:107], a[144:147], v[32:47]\n"
    "ds_read_b64_tr_b16 v[100:101], v12 offset:43008\n"
    "ds_read_b64_tr_b16 v[102:103], v12 offset:43520\n"
    "v_mul_f32 v79, v79, v112\n"
    "v_add_f32 v113, 1.0, v113\n"
    "v_mul_f32 v114, v81, v114\n"
    "v_cvt_pk_bf16_f32 v119, v78, v79\n"
    "v_mfma_f32_32x32x16_bf16 v[48:63], v[104:107], a[148:151], v[48:63]\n"
    "s_waitcnt lgkmcnt(4)\n"
    "ds_write_b64 v14, v[118:119] offset:520\n"
    "v_rcp_f32 v113, v113\n"
    "v_mul_f32 v82, v82, v123\n"
    "v_mfma_f32_32x32x16_bf16 v[32:47], v[108:111], a[152:155], v[32:47]\n"
    "ds_read_b64_tr_b16 v[104:105], v12 offset:45056\n"
    "ds_read_b64_tr_b16 v[106:107], v12 offset:45568\n"
    "v_exp_f32 v114, v114\n"
    "v_fma_f32 v112, -v82, v82, s87\n"
    "v_mul_f32 v80, v80, v113\n"
    "v_add_f32 v114, 1.0, v114\n"
    "v_mfma_f32_32x32x16_bf16 v[48:63], v[108:111], a[156:159], v[48:63]\n"
    "s_waitcnt lgkmcnt(5)\n"
    "buffer_load_ushort v125, v17, s[48:51], s61 offen\n"
    "v_mul_f32 v112, v82, v112\n"
    "v_rcp_f32 v114, v114\n"
    "v_mul_f32 v83, v83, v123\n"
    "v_mfma_f32_32x32x16_bf16 v[32:47], v[96:99], a[160:163], v[32:47]\n"
    "s_add_u32 s85, s67, s86\n"
    "ds_read_b64_tr_b16 v[108:109], v12 offset:47104\n"
    "ds_read_b64_tr_b16 v[110:111], v12 offset:47616\n"
    "v_exp_f32 v112, v112\n"
    "v_fma_f32 v113, -v83, v83, s87\n"
    "v_mul_f32 v81, v81, v114\n"
    "v_mfma_f32_32x32x16_bf16 v[48:63], v[96:99], a[164:167], v[48:63]\n"
    "s_waitcnt vmcnt(10) lgkmcnt(5)\n"
    "s_barrier\n"
    "v_add_f32 v112, 1.0, v112\n"
    "v_mul_f32 v113, v83, v113\n"
    "v_cvt_pk_bf16_f32 v116, v80, v81\n"
    "v_rcp_f32 v112, v112\n"
    "v_mfma_f32_32x32x16_bf16 v[32:47], v[100:103], a[168:171], v[32:47]\n"
    "s_add_u32 m0, s89, 24576\n"
    "ds_read_b64_tr_b16 v[96:97], v12 offset:49152\n"
    "ds_read_b64_tr_b16 v[98:99], v12 offset:49664\n"
    "v_mul_f32 v84, v84, v123\n"
    "v_exp_f32 v113, v113\n"
    "v_fma_f32 v114, -v84, v84, s87\n"
    "v_mfma_f32_32x32x16_bf16 v[48:63], v[100:103], a[172:175], v[48:63]\n"
    "s_waitcnt lgkmcnt(4)\n"
    "buffer_load_dwordx4 v10, s[36:39], s85 offen lds\n"
    "buffer_load_dwordx4 v11, s[36:39], s85 offen offset:1024 lds\n"
    "v_mul_f32 v82, v82, v112\n"
    "v_add_f32 v113, 1.0, v113\n"
    "v_mfma_f32_32x32x16_bf16 v[32:47], v[104:107], a[176:179], v[32:47]\n"
    "ds_read_b64_tr_b16 v[100:101], v12 offset:51200\n"
    "ds_read_b64_tr_b16 v[102:103], v12 offset:51712\n"
    "v_mul_f32 v114, v84, v114\n"
    "v_rcp_f32 v113, v113\n"
    "v_mul_f32 v85, v85, v123\n"
    "v_mfma_f32_32x32x16_bf16 v[48:63], v[104:107], a[180:183], v[48:63]\n"
    "s_waitcnt lgkmcnt(4)\n"
    "v_exp_f32 v114, v114\n"
    "v_fma_f32 v112, -v85, v85, s87\n"
    "v_mfma_f32_32x32x16_bf16 v[32:47], v[108:111], a[184:187], v[32:47]\n"
    "ds_read_b64_tr_b16 v[104:105], v12 offset:53248\n"
    "ds_read_b64_tr_b16 v[106:107], v12 offset:53760\n"
    "v_mul_f32 v83, v83, v113\n"
    "v_add_f32 v114, 1.0, v114\n"
    "v_mul_f32 v112, v85, v112\n"
    "v_cvt_pk_bf16_f32 v117, v82, v83\n"
    "v_mfma_f32_32x32x16_bf16 v[48:63], v[108:111], a[188:191], v[48:63]\n"
    "s_waitcnt lgkmcnt(4)\n"
    "buffer_load_dwordx2 v[120:121], v16, s[44:47], s60 offen sc1\n"
    "v_rcp_f32 v114, v114\n"
    "v_mul_f32 v86, v86, v123\n"
    "v_exp_f32 v112, v112\n"
    "v_mfma_f32_32x32x16_bf16 v[32:47], v[96:99], a[192:195], v[32:47]\n"
    "s_add_u32 s85, s68, s86\n"
    "ds_read_b64_tr_b16 v[108:109], v12 offset:55296\n"
    "ds_read_b64_tr_b16 v[110:111], v12 offset:55808\n"
    "v_fma_f32 v113, -v86, v86, s87\n"
    "v_mul_f32 v84, v84, v114\n"
    "v_add_f32 v112, 1.0, v112\n"
    "v_mul_f32 v113, v86, v113\n"
    "v_mfma_f32_32x32x16_bf16 v[48:63], v[96:99], a[196:199], v[48:63]\n"
    "s_waitcnt lgkmcnt(4)\n"
    "v_rcp_f32 v112, v112\n"
    "v_mul_f32 v87, v87, v123\n"
    "v_exp_f32 v113, v113\n"
    "v_fma_f32 v114, -v87, v87, s87\n"
    "v_mfma_f32_32x32x16_bf16 v[32:47], v[100:103], a[200:203], v[32:47]\n"
    "s_add_u32 m0, s89, 32768\n"
    "ds_read_b64_tr_b16 v[96:97], v12 offset:57344\n"
    "ds_read_b64_tr_b16 v[98:99], v12 offset:57856\n"
    "v_mul_f32 v85, v85, v112\n"
    "v_add_f32 v113, 1.0, v113\n"
    "v_mul_f32 v114, v87, v114\n"
    "v_cvt_pk_bf16_f32 v118, v84, v85\n"
    "v_rcp_f32 v113, v113\n"
    "v_mfma_f32_32x32x16_bf16 v[48:63], v[100:103], a[204:207], v[48:63]\n"
    "s_waitcnt lgkmcnt(4)\n"
    "buffer_load_dwordx4 v10, s[36:39], s85 offen lds\n"
    "buffer_load_dwordx4 v11, s[36:39], s85 offen offset:1024 lds\n"
    "v_mul_f32 v88, v88, v123\n"
    "v_exp_f32 v114, v114\n"
    "v_mfma_f32_32x32x16_bf16 v[32:47], v[104:107], a[208:211], v[32:47]\n"
    "ds_read_b64_tr_b16 v[100:101], v12 offset:59392\n"
    "ds_read_b64_tr_b16 v[102:103], v12 offset:59904\n"
    "v_fma_f32 v112, -v88, v88, s87\n"
    "v_mul_f32 v86, v86, v113\n"
    "v_add_f32 v114, 1.0, v114\n"
    "v_mul_f32 v112, v88, v112\n"
    "v_mfma_f32_32x32x16_bf16 v[48:63], v[104:107], a[212:215], v[48:63]\n"
    "s_waitcnt lgkmcnt(4)\n"
    "ds_write_b64 v15, v[116:117] offset:0\n"
    "v_rcp_f32 v114, v114\n"
    "v_mul_f32 v89, v89, v123\n"
    "v_mfma_f32_32x32x16_bf16 v[32:47], v[108:111], a[216:219], v[32:47]\n"
    "ds_read_b64_tr_b16 v[104:105], v12 offset:61440\n"
    "ds_read_b64_tr_b16 v[106:107], v12 offset:61952\n"
    "v_exp_f32 v112, v112\n"
    "v_fma_f32 v113, -v89, v89, s87\n"
    "v_mul_f32 v87, v87, v114\n"
    "v_mfma_f32_32x32x16_bf16 v[48:63], v[108:111], a[220:223], v[48:63]\n"
    "s_waitcnt lgkmcnt(5)\n"
    "v_add_f32 v112, 1.0, v112\n"
    "v_mul_f32 v113, v89, v113\n"
    "v_cvt_pk_bf16_f32 v119, v86, v87\n"
    "v_rcp_f32 v112, v112\n"
    "v_mul_f32 v90, v90, v123\n"
    "v_mfma_f32_32x32x16_bf16 v[32:47], v[96:99], a[224:227], v[32:47]\n"
    "s_add_u32 s85, s69, s86\n"
    "ds_read_b64_tr_b16 v[108:109], v12 offset:63488\n"
    "ds_read_b64_tr_b16 v[110:111], v12 offset:64000\n"
    "v_exp_f32 v113, v113\n"
    "v_fma_f32 v114, -v90, v90, s87\n"
    "v_mul_f32 v88, v88, v112\n"
    "v_mfma_f32_32x32x16_bf16 v[48:63], v[96:99], a[228:231], v[48:63]\n"
    "s_waitcnt vmcnt(13) lgkmcnt(5)\n"
    "s_barrier\n"
    "v_add_f32 v113, 1.0, v113\n"
    "v_mul_f32 v114, v90, v114\n"
    "v_rcp_f32 v113, v113\n"
    "v_mfma_f32_32x32x16_bf16 v[32:47], v[100:103], a[232:235], v[32:47]\n"
    "s_add_u32 m0, s89, 40960\n"
    "ds_read_b64_tr_b16 v[96:97], v13 offset:0\n"
    "ds_read_b64_tr_b16 v[98:99], v13 offset:512\n"
    "v_mul_f32 v91, v91, v123\n"
    "v_exp_f32 v114, v114\n"
    "v_fma_f32 v112, -v91, v91, s87\n"
    "v_mfma_f32_32x32x16_bf16 v[48:63], v[100:103], a[236:239], v[48:63]\n"
    "s_waitcnt lgkmcnt(4)\n"
    "buffer_load_dwordx4 v10, s[36:39], s85 offen lds\n"
    "buffer_load_dwordx4 v11, s[36:39], s85 offen offset:1024 lds\n"
    "v_mul_f32 v89, v89, v113\n"
    "v_add_f32 v114, 1.0, v114\n"
    "v_mfma_f32_32x32x16_bf16 v[32:47], v[104:107], a[240:243], v[32:47]\n"
    "ds_read_b64_tr_b16 v[100:101], v13 offset:2048\n"
    "ds_read_b64_tr_b16 v[102:103], v13 offset:2560\n"
    "v_mul_f32 v112, v91, v112\n"
    "v_cvt_pk_bf16_f32 v116, v88, v89\n"
    "v_rcp_f32 v114, v114\n"
    "v_mfma_f32_32x32x16_bf16 v[48:63], v[104:107], a[244:247], v[48:63]\n"
    "s_waitcnt lgkmcnt(4)\n"
    "ds_write_b64 v15, v[118:119] offset:8\n"
    "v_mul_f32 v92, v92, v123\n"
    "v_exp_f32 v112, v112\n"
    "v_mfma_f32_32x32x16_bf16 v[32:47], v[108:111], a[248:251], v[32:47]\n"
    "ds_read_b64_tr_b16 v[104:105], v13 offset:4096\n"
    "ds_read_b64_tr_b16 v[106:107], v13 offset:4608\n"
    "v_fma_f32 v113, -v92, v92, s87\n"
    "v_mul_f32 v90, v90, v114\n"
    "v_add_f32 v112, 1.0, v112\n"
    "v_mul_f32 v113, v92, v113\n"
    "v_rcp_f32 v112, v112\n"
    "v_mfma_f32_32x32x16_bf16 v[48:63], v[108:111], a[252:255], v[48:63]\n"
    "s_waitcnt lgkmcnt(5)\n"
    "v_mul_f32 v93, v93, v123\n"
    "v_exp_f32 v113, v113\n"
    "v_fma_f32 v114, -v93, v93, s87\n"
    "v_mul_f32 v91, v91, v112\n"
    "v_add_f32 v113, 1.0, v113\n"
    "v_mfma_f32_32x32x16_bf16 v[32:47], v[96:99], v[224:227], v[32:47]\n"
    "s_add_u32 s85, s70, s86\n"
    "ds_read_b64_tr_b16 v[108:109], v13 offset:6144\n"
    "ds_read_b64_tr_b16 v[110:111], v13 offset:6656\n"
    "v_mul_f32 v114, v93, v114\n"
    "v_cvt_pk_bf16_f32 v117, v90, v91\n"
    "v_rcp_f32 v113, v113\n"
    "v_mfma_f32_32x32x16_bf16 v[48:63], v[96:99], v[228:231], v[48:63]\n"
    "s_waitcnt vmcnt(10) lgkmcnt(5)\n"
    "s_barrier\n"
    "v_mul_f32 v94, v94, v123\n"
    "v_exp_f32 v114, v114\n"
    "v_mfma_f32_32x32x16_bf16 v[32:47], v[100:103], v[232:235], v[32:47]\n"
    "s_add_u32 m0, s89, 49152\n"
    "ds_read_b64_tr_b16 v[96:97], v12 offset:0\n"
    "ds_read_b64_tr_b16 v[98:99], v12 offset:512\n"
    "v_fma_f32 v112, -v94, v94, s87\n"
    "v_mul_f32 v92, v92, v113\n"
    "v_add_f32 v114, 1.0, v114\n"
    "v_mul_f32 v112, v94, v112\n"
    "v_mfma_f32_32x32x16_bf16 v[48:63], v[100:103], v[236:239], v[48:63]\n"
    "s_waitcnt lgkmcnt(4)\n"
    "buffer_load_dwordx4 v10, s[36:39], s85 offen lds\n"
    "buffer_load_dwordx4 v11, s[36:39], s85 offen offset:1024 lds\n"
    "v_rcp_f32 v114, v114\n"
    "v_mfma_f32_32x32x16_bf16 v[32:47], v[104:107], v[240:243], v[32:47]\n"
    "ds_read_b64_tr_b16 v[100:101], v12 offset:2048\n"
    "ds_read_b64_tr_b16 v[102:103], v12 offset:2560\n"
    "v_mul_f32 v95, v95, v123\n"
    "v_exp_f32 v112, v112\n"
    "v_fma_f32 v113, -v95, v95, s87\n"
    "v_mul_f32 v93, v93, v114\n"
    "v_mfma_f32_32x32x16_bf16 v[48:63], v[104:107], v[244:247], v[48:63]\n"
    "s_waitcnt lgkmcnt(4)\n"
    "ds_write_b64 v15, v[116:117] offset:512\n"
    "v_add_f32 v112, 1.0, v112\n"
    "v_mul_f32 v113, v95, v113\n"
    "v_cvt_pk_bf16_f32 v118, v92, v93\n"
    "v_mfma_f32_32x32x16_bf16 v[32:47], v[108:111], v[248:251], v[32:47]\n"
    "s_waitcnt vmcnt(6)\n"
    "ds_read_b64_tr_b16 v[104:105], v12 offset:4096\n"
    "ds_read_b64_tr_b16 v[106:107], v12 offset:4608\n"
    "v_cmp_ne_u32 vcc, s56, v121\n"
    "v_mov_b32 v124, v120\n"
    "s_cbranch_vccnz .LL_CSRETRY0_%=\n"
    ".LL_CSOK0_%=:\n"
    "v_rcp_f32 v112, v112\n"
    "v_mfma_f32_32x32x16_bf16 v[48:63], v[108:111], v[252:255], v[48:63]\n"
    "s_waitcnt lgkmcnt(5)\n"
    "v_exp_f32 v113, v113\n"
    "v_mul_f32 v94, v94, v112\n"
    "v_add_f32 v113, 1.0, v113\n"
    "v_rcp_f32 v113, v113\n"
    "v_mfma_f32_32x32x16_bf16 v[64:79], v[124:127], v[128:131], v[32:47]\n"
    "s_sub_u32 s88, s88, 1\n"
    "s_nop 0\n"
    "v_mul_f32 v95, v95, v113\n"
    "v_cvt_pk_bf16_f32 v119, v94, v95\n"
    "v_mfma_f32_32x32x16_bf16 v[80:95], v[124:127], v[132:135], v[48:63]\n"
    "s_cmp_lg_u32 s88, 0\n"
    "ds_write_b64 v15, v[118:119] offset:520\n"
    "v_add_u32 v14, 0x4000, v14\n"
    "v_add_u32 v15, 0x4000, v15\n"
    "s_cbranch_scc1 .LL_STEP0_%=\n"
    "v_add_u32 v12, 81920, v23\n"
    "v_add_u32 v13, 147456, v23\n"
    "v_mov_b32 v156, s87\n"
    "v_mov_b32 v158, v122\n"
    "v_mov_b32 v160, v123\n"
    "ds_read_b128 a[0:3], v12 offset:0\n"
    "ds_read_b128 a[4:7], v12 offset:1024\n"
    "ds_read_b128 a[8:11], v12 offset:2048\n"
    "ds_read_b128 a[12:15], v12 offset:3072\n"
    "v_add_u32 v16, 65536, v23\n"
    "v_subrev_u32 v14, 98304, v14\n"
    "v_subrev_u32 v15, 98304, v15\n"
    "s_mov_b32 s86, 128\n"
    "s_mov_b32 s92, 81920\n"
    "v_pk_mul_f32 v[64:65], v[64:65], v[158:159] op_sel_hi:[1,0]\n"
    "v_pk_mul_f32 v[66:67], v[66:67], v[158:159] op_sel_hi:[1,0]\n"
    "v_pk_mul_f32 v[68:69], v[68:69], v[158:159] op_sel_hi:[1,0]\n"
    "v_pk_mul_f32 v[70:71], v[70:71], v[158:159] op_sel_hi:[1,0]\n"
    "v_pk_fma_f32 v[32:33], v[64:65], v[64:65], v[156:157] op_sel_hi:[1,1,0] neg_lo:[1,0,0] neg_hi:[1,0,0]\n"
    "v_pk_fma_f32 v[34:35], v[66:67], v[66:67], v[156:157] op_sel_hi:[1,1,0] neg_lo:[1,0,0] neg_hi:[1,0,0]\n"
    "v_pk_fma_f32 v[36:37], v[68:69], v[68:69], v[156:157] op_sel_hi:[1,1,0] neg_lo:[1,0,0] neg_hi:[1,0,0]\n"
    "v_pk_fma_f32 v[38:39], v[70:71], v[70:71], v[156:157] op_sel_hi:[1,1,0] neg_lo:[1,0,0] neg_hi:[1,0,0]\n"
    "v_pk_mul_f32 v[32:33], v[64:65], v[32:33]\n"
    "v_pk_mul_f32 v[34:35], v[66:67], v[34:35]\n"
    "v_pk_mul_f32 v[36:37], v[68:69], v[36:37]\n"
    "v_pk_mul_f32 v[38:39], v[70:71], v[38:39]\n"
    "s_mov_b64 s[48:49], %9\n"
    "s_mov_b32 s50, 1152\n"
    "s_mov_b32 s51, 0x20000\n"
    "v_lshrrev_b32 v3, 4, v1\n"
    "v_and_b32 v2, 3, v1\n"
    "v_lshl_add_u32 v4, v3, 2, v2\n"
    "v_lshlrev_b32 v4, 4, v4\n"
    "v_add_u32 v5, v4, v171\n"
    "ds_read_b128 a[16:19], v12 offset:8192\n"
    "ds_read_b128 a[20:23], v12 offset:9216\n"
    "ds_read_b128 a[24:27], v12 offset:10240\n"
    "ds_read_b128 a[28:31], v12 offset:11264\n"
    "buffer_load_dwordx4 v[132:135], v4, s[48:51], 0 offen offset:0\n"
    "buffer_load_dwordx4 v[136:139], v4, s[48:51], 0 offen offset:256\n"
    "v_exp_f32 v32, v32\n"
    "v_exp_f32 v33, v33\n"
    "v_exp_f32 v34, v34\n"
    "v_exp_f32 v35, v35\n"
    "v_exp_f32 v36, v36\n"
    "v_exp_f32 v37, v37\n"
    "v_exp_f32 v38, v38\n"
    "v_exp_f32 v39, v39\n"
    "v_pk_add_f32 v[32:33], v[32:33], 1.0 op_sel_hi:[1,0]\n"
    "v_pk_add_f32 v[34:35], v[34:35], 1.0 op_sel_hi:[1,0]\n"
    "v_pk_add_f32 v[36:37], v[36:37], 1.0 op_sel_hi:[1,0]\n"
    "v_pk_add_f32 v[38:39], v[38:39], 1.0 op_sel_hi:[1,0]\n"
    "ds_read_b128 a[32:35], v12 offset:16384\n"
    "ds_read_b128 a[36:39], v12 offset:17408\n"
    "ds_read_b128 a[40:43], v12 offset:18432\n"
    "ds_read_b128 a[44:47], v12 offset:19456\n"
    "buffer_load_dwordx4 v[140:143], v4, s[48:51], 0 offen offset:512\n"
    "buffer_load_dwordx4 v[144:147], v4, s[48:51], 0 offen offset:768\n"
    "v_rcp_f32 v32, v32\n"
    "v_rcp_f32 v33, v33\n"
    "v_rcp_f32 v34, v34\n"
    "v_rcp_f32 v35, v35\n"
    "v_rcp_f32 v36, v36\n"
    "v_rcp_f32 v37, v37\n"
    "v_rcp_f32 v38, v38\n"
    "v_rcp_f32 v39, v39\n"
    "v_pk_mul_f32 v[64:65], v[64:65], v[32:33]\n"
    "v_pk_mul_f32 v[66:67], v[66:67], v[34:35]\n"
    "v_pk_mul_f32 v[68:69], v[68:69], v[36:37]\n"
    "v_pk_mul_f32 v[70:71], v[70:71], v[38:39]\n"
    "ds_read_b128 a[48:51], v12 offset:24576\n"
    "ds_read_b128 a[52:55], v12 offset:25600\n"
    "ds_read_b128 a[56:59], v12 offset:26624\n"
    "ds_read_b128 a[60:63], v12 offset:27648\n"
    "buffer_load_dwordx4 v[148:151], v4, s[48:51], 0 offen offset:1024\n"
    "buffer_load_dwordx4 v[112:115], v5, s[40:43], 0 offen offset:0\n"
    "v_cvt_pk_bf16_f32 v64, v64, v65\n"
    "v_cvt_pk_bf16_f32 v65, v66, v67\n"
    "v_cvt_pk_bf16_f32 v66, v68, v69\n"
    "v_cvt_pk_bf16_f32 v67, v70, v71\n"
    "v_pk_mul_f32 v[72:73], v[72:73], v[158:159] op_sel_hi:[1,0]\n"
    "v_pk_mul_f32 v[74:75], v[74:75], v[158:159] op_sel_hi:[1,0]\n"
    "v_pk_mul_f32 v[76:77], v[76:77], v[158:159] op_sel_hi:[1,0]\n"
    "v_pk_mul_f32 v[78:79], v[78:79], v[158:159] op_sel_hi:[1,0]\n"
    "v_pk_fma_f32 v[40:41], v[72:73], v[72:73], v[156:157] op_sel_hi:[1,1,0] neg_lo:[1,0,0] neg_hi:[1,0,0]\n"
    "v_pk_fma_f32 v[42:43], v[74:75], v[74:75], v[156:157] op_sel_hi:[1,1,0] neg_lo:[1,0,0] neg_hi:[1,0,0]\n"
    "v_pk_fma_f32 v[44:45], v[76:77], v[76:77], v[156:157] op_sel_hi:[1,1,0] neg_lo:[1,0,0] neg_hi:[1,0,0]\n"
    "v_pk_fma_f32 v[46:47], v[78:79], v[78:79], v[156:157] op_sel_hi:[1,1,0] neg_lo:[1,0,0] neg_hi:[1,0,0]\n"
    "v_pk_mul_f32 v[40:41], v[72:73], v[40:41]\n"
    "v_pk_mul_f32 v[42:43], v[74:75], v[42:43]\n"
    "v_pk_mul_f32 v[44:45], v[76:77], v[44:45]\n"
    "v_pk_mul_f32 v[46:47], v[78:79], v[46:47]\n"
    "s_waitcnt lgkmcnt(15)\n"
    "s_barrier\n"
    "ds_read_b128 a[64:67], v12 offset:32768\n"
    "ds_read_b128 a[68:71], v12 offset:33792\n"
    "ds_read_b128 a[72:75], v12 offset:34816\n"
    "ds_read_b128 a[76:79], v12 offset:35840\n"
    "buffer_load_dwordx4 v[116:119], v5, s[40:43], 0 offen offset:256\n"
    "buffer_load_dwordx4 v[120:123], v5, s[40:43], 0 offen offset:512\n"
    "v_exp_f32 v40, v40\n"
    "v_exp_f32 v41, v41\n"
    "v_exp_f32 v42, v42\n"
    "v_exp_f32 v43, v43\n"
    "v_exp_f32 v44, v44\n"
    "v_exp_f32 v45, v45\n"
    "v_exp_f32 v46, v46\n"
    "v_exp_f32 v47, v47\n"
    "v_pk_add_f32 v[40:41], v[40:41], 1.0 op_sel_hi:[1,0]\n"
    "v_pk_add_f32 v[42:43], v[42:43], 1.0 op_sel_hi:[1,0]\n"
    "v_pk_add_f32 v[44:45], v[44:45], 1.0 op_sel_hi:[1,0]\n"
    "v_pk_add_f32 v[46:47], v[46:47], 1.0 op_sel_hi:[1,0]\n"
    "ds_read_b128 a[80:83], v12 offset:40960\n"
    "ds_read_b128 a[84:87], v12 offset:41984\n"
    "ds_read_b128 a[88:91], v12 offset:43008\n"
    "ds_read_b128 a[92:95], v12 offset:44032\n"
    "buffer_load_dwordx4 v[124:127], v5, s[40:43], 0 offen offset:768\n"
    "v_rcp_f32 v40, v40\n"
    "v_rcp_f32 v41, v41\n"
    "v_rcp_f32 v42, v42\n"
    "v_rcp_f32 v43, v43\n"
    "v_rcp_f32 v44, v44\n"
    "v_rcp_f32 v45, v45\n"
    "v_rcp_f32 v46, v46\n"
    "v_rcp_f32 v47, v47\n"
    "v_pk_mul_f32 v[72:73], v[72:73], v[40:41]\n"
    "v_pk_mul_f32 v[74:75], v[74:75], v[42:43]\n"
    "v_pk_mul_f32 v[76:77], v[76:77], v[44:45]\n"
    "v_pk_mul_f32 v[78:79], v[78:79], v[46:47]\n"
    "v_cmp_gt_u32 vcc, 32, v1\n"
    "v_mov_b32 v4, 1879048192\n"
    "s_and_b32 s30, s83, 1\n"
    "v_cndmask_b32 v5, v4, v5, vcc\n"
    "s_mov_b64 s[48:49], %8\n"
    "ds_read_b128 a[96:99], v12 offset:49152\n"
    "ds_read_b128 a[100:103], v12 offset:50176\n"
    "ds_read_b128 a[104:107], v12 offset:51200\n"
    "ds_read_b128 a[108:111], v12 offset:52224\n"
    "buffer_load_dwordx4 v[128:131], v5, s[40:43], 0 offen offset:1024\n"
    "v_cvt_pk_bf16_f32 v68, v72, v73\n"
    "v_cvt_pk_bf16_f32 v69, v74, v75\n"
    "v_cvt_pk_bf16_f32 v70, v76, v77\n"
    "v_cvt_pk_bf16_f32 v71, v78, v79\n"
    "v_pk_mul_f32 v[80:81], v[80:81], v[160:161] op_sel_hi:[1,0]\n"
    "v_pk_mul_f32 v[82:83], v[82:83], v[160:161] op_sel_hi:[1,0]\n"
    "v_pk_mul_f32 v[84:85], v[84:85], v[160:161] op_sel_hi:[1,0]\n"
    "v_pk_mul_f32 v[86:87], v[86:87], v[160:161] op_sel_hi:[1,0]\n"
    "v_pk_fma_f32 v[32:33], v[80:81], v[80:81], v[156:157] op_sel_hi:[1,1,0] neg_lo:[1,0,0] neg_hi:[1,0,0]\n"
    "v_pk_fma_f32 v[34:35], v[82:83], v[82:83], v[156:157] op_sel_hi:[1,1,0] neg_lo:[1,0,0] neg_hi:[1,0,0]\n"
    "v_pk_fma_f32 v[36:37], v[84:85], v[84:85], v[156:157] op_sel_hi:[1,1,0] neg_lo:[1,0,0] neg_hi:[1,0,0]\n"
    "v_pk_fma_f32 v[38:39], v[86:87], v[86:87], v[156:157] op_sel_hi:[1,1,0] neg_lo:[1,0,0] neg_hi:[1,0,0]\n"
    "v_pk_mul_f32 v[32:33], v[80:81], v[32:33]\n"
    "v_pk_mul_f32 v[34:35], v[82:83], v[34:35]\n"
    "v_pk_mul_f32 v[36:37], v[84:85], v[36:37]\n"
    "v_pk_mul_f32 v[38:39], v[86:87], v[38:39]\n"
    "s_lshl_b32 s30, s30, 6\n"
    "v_lshlrev_b32 v2, 3, v2\n"
    "v_and_b32 v4, 1, v3\n"
    "v_lshl_add_u32 v2, v4, 5, v2\n"
    "v_lshrrev_b32 v4, 1, v3\n"
    "v_lshl_add_u32 v2, v4, 7, v2\n"
    "v_add_u32 v2, s30, v2\n"
    "v_add_u32 v171, v2, v171\n"
    "ds_read_b128 a[112:115], v12 offset:57344\n"
    "ds_read_b128 a[116:119], v12 offset:58368\n"
    "ds_read_b128 a[120:123], v12 offset:59392\n"
    "ds_read_b128 a[124:127], v12 offset:60416\n"
    "buffer_load_dwordx2 v[200:201], v2, s[48:51], 0 offen offset:0\n"
    "buffer_load_dwordx2 v[202:203], v2, s[48:51], 0 offen offset:256\n"
    "v_exp_f32 v32, v32\n"
    "v_exp_f32 v33, v33\n"
    "v_exp_f32 v34, v34\n"
    "v_exp_f32 v35, v35\n"
    "v_exp_f32 v36, v36\n"
    "v_exp_f32 v37, v37\n"
    "v_exp_f32 v38, v38\n"
    "v_exp_f32 v39, v39\n"
    "v_pk_add_f32 v[32:33], v[32:33], 1.0 op_sel_hi:[1,0]\n"
    "v_pk_add_f32 v[34:35], v[34:35], 1.0 op_sel_hi:[1,0]\n"
    "v_pk_add_f32 v[36:37], v[36:37], 1.0 op_sel_hi:[1,0]\n"
    "v_pk_add_f32 v[38:39], v[38:39], 1.0 op_sel_hi:[1,0]\n"
    "ds_read_b128 a[128:131], v13 offset:0\n"
    "ds_read_b128 a[132:135], v13 offset:1024\n"
    "ds_read_b128 a[136:139], v13 offset:2048\n"
    "ds_read_b128 a[140:143], v13 offset:3072\n"
    "buffer_load_dwordx2 v[204:205], v2, s[48:51], 0 offen offset:512\n"
    "buffer_load_dwordx2 v[206:207], v2, s[48:51], 0 offen offset:768\n"
    "v_rcp_f32 v32, v32\n"
    "v_rcp_f32 v33, v33\n"
    "v_rcp_f32 v34, v34\n"
    "v_rcp_f32 v35, v35\n"
    "v_rcp_f32 v36, v36\n"
    "v_rcp_f32 v37, v37\n"
    "v_rcp_f32 v38, v38\n"
    "v_rcp_f32 v39, v39\n"
    "v_pk_mul_f32 v[80:81], v[80:81], v[32:33]\n"
    "v_pk_mul_f32 v[82:83], v[82:83], v[34:35]\n"
    "v_pk_mul_f32 v[84:85], v[84:85], v[36:37]\n"
    "v_pk_mul_f32 v[86:87], v[86:87], v[38:39]\n"
    "ds_read_b128 a[144:147], v13 offset:8192\n"
    "ds_read_b128 a[148:151], v13 offset:9216\n"
    "ds_read_b128 a[152:155], v13 offset:10240\n"
    "ds_read_b128 a[156:159], v13 offset:11264\n"
    "buffer_load_dwordx2 v[208:209], v2, s[48:51], 0 offen offset:1024\n"
    "v_cvt_pk_bf16_f32 v72, v80, v81\n"
    "v_cvt_pk_bf16_f32 v73, v82, v83\n"
    "v_cvt_pk_bf16_f32 v74, v84, v85\n"
    "v_cvt_pk_bf16_f32 v75, v86, v87\n"
    "v_pk_mul_f32 v[88:89], v[88:89], v[160:161] op_sel_hi:[1,0]\n"
    "v_pk_mul_f32 v[90:91], v[90:91], v[160:161] op_sel_hi:[1,0]\n"
    "v_pk_mul_f32 v[92:93], v[92:93], v[160:161] op_sel_hi:[1,0]\n"
    "v_pk_mul_f32 v[94:95], v[94:95], v[160:161] op_sel_hi:[1,0]\n"
    "v_pk_fma_f32 v[40:41], v[88:89], v[88:89], v[156:157] op_sel_hi:[1,1,0] neg_lo:[1,0,0] neg_hi:[1,0,0]\n"
    "v_pk_fma_f32 v[42:43], v[90:91], v[90:91], v[156:157] op_sel_hi:[1,1,0] neg_lo:[1,0,0] neg_hi:[1,0,0]\n"
    "v_pk_fma_f32 v[44:45], v[92:93], v[92:93], v[156:157] op_sel_hi:[1,1,0] neg_lo:[1,0,0] neg_hi:[1,0,0]\n"
    "v_pk_fma_f32 v[46:47], v[94:95], v[94:95], v[156:157] op_sel_hi:[1,1,0] neg_lo:[1,0,0] neg_hi:[1,0,0]\n"
    "v_pk_mul_f32 v[40:41], v[88:89], v[40:41]\n"
    "v_pk_mul_f32 v[42:43], v[90:91], v[42:43]\n"
    "v_pk_mul_f32 v[44:45], v[92:93], v[44:45]\n"
    "v_pk_mul_f32 v[46:47], v[94:95], v[46:47]\n"
    "ds_read_b64_tr_b16 v[96:97], v21 offset:0\n"
    "ds_read_b64_tr_b16 v[98:99], v21 offset:1024\n"
    "ds_read_b64_tr_b16 v[100:101], v21 offset:4096\n"
    "ds_read_b64_tr_b16 v[102:103], v21 offset:5120\n"
    "v_exp_f32 v40, v40\n"
    "v_exp_f32 v41, v41\n"
    "v_exp_f32 v42, v42\n"
    "v_exp_f32 v43, v43\n"
    "v_exp_f32 v44, v44\n"
    "v_exp_f32 v45, v45\n"
    "v_exp_f32 v46, v46\n"
    "v_exp_f32 v47, v47\n"
    "v_pk_add_f32 v[40:41], v[40:41], 1.0 op_sel_hi:[1,0]\n"
    "v_pk_add_f32 v[42:43], v[42:43], 1.0 op_sel_hi:[1,0]\n"
    "v_pk_add_f32 v[44:45], v[44:45], 1.0 op_sel_hi:[1,0]\n"
    "v_pk_add_f32 v[46:47], v[46:47], 1.0 op_sel_hi:[1,0]\n"
    "v_rcp_f32 v40, v40\n"
    "v_rcp_f32 v41, v41\n"
    "v_rcp_f32 v42, v42\n"
    "v_rcp_f32 v43, v43\n"
    "v_rcp_f32 v44, v44\n"
    "v_rcp_f32 v45, v45\n"
    "v_rcp_f32 v46, v46\n"
    "v_rcp_f32 v47, v47\n"
    "v_pk_mul_f32 v[88:89], v[88:89], v[40:41]\n"
    "v_pk_mul_f32 v[90:91], v[90:91], v[42:43]\n"
    "v_pk_mul_f32 v[92:93], v[92:93], v[44:45]\n"
    "v_pk_mul_f32 v[94:95], v[94:95], v[46:47]\n"
    "v_cvt_pk_bf16_f32 v76, v88, v89\n"
    "v_cvt_pk_bf16_f32 v77, v90, v91\n"
    "v_cvt_pk_bf16_f32 v78, v92, v93\n"
    "v_cvt_pk_bf16_f32 v79, v94, v95\n"
    "s_waitcnt lgkmcnt(0)\n"
    "s_barrier\n"
    "s_add_u32 s85, s64, s86\n"
    "s_add_u32 m0, s89, 81920\n"
    "v_mfma_f32_16x16x32_bf16 v[32:35], v[96:99], a[0:3], 0\n"
    "buffer_load_dwordx4 v10, s[36:39], s85 offen lds\n"
    "v_mfma_f32_16x16x32_bf16 v[32:35], v[100:103], a[16:19], v[32:35]\n"
    "ds_read_b64_tr_b16 v[104:105], v22 offset:0\n"
    "v_and_b32 v184, 15, v1\n"
    "v_mfma_f32_16x16x32_bf16 v[36:39], v[96:99], a[4:7], 0\n"
    "ds_read_b64_tr_b16 v[106:107], v22 offset:1024\n"
    "v_lshrrev_b32 v185, 4, v1\n"
    "v_mfma_f32_16x16x32_bf16 v[36:39], v[100:103], a[20:23], v[36:39]\n"
    "ds_read_b64_tr_b16 v[108:109], v22 offset:4096\n"
    "v_lshrrev_b32 v6, 2, v184\n"
    "v_mfma_f32_16x16x32_bf16 v[40:43], v[96:99], a[8:11], 0\n"
    "buffer_load_dwordx4 v11, s[36:39], s85 offen offset:1024 lds\n"
    "v_mfma_f32_16x16x32_bf16 v[40:43], v[100:103], a[24:27], v[40:43]\n"
    "s_add_u32 s85, s65, s86\n"
    "ds_read_b64_tr_b16 v[110:111], v22 offset:5120\n"
    "v_lshlrev_b32 v6, 8, v6\n"
    "v_mfma_f32_16x16x32_bf16 v[44:47], v[96:99], a[12:15], 0\n"
    "s_add_u32 m0, s89, 90112\n"
    "ds_write_b64 v14, v[64:65] offset:0\n"
    "v_mfma_f32_16x16x32_bf16 v[44:47], v[100:103], a[28:31], v[44:47]\n"
    "s_waitcnt lgkmcnt(3)\n"
    "ds_write_b64 v14, v[66:67] offset:8\n"
    "v_mfma_f32_16x16x32_bf16 v[48:51], v[104:107], a[0:3], 0\n"
    "s_waitcnt lgkmcnt(2)\n"
    "buffer_load_dwordx4 v10, s[36:39], s85 offen lds\n"
    "v_mfma_f32_16x16x32_bf16 v[48:51], v[108:111], a[16:19], v[48:51]\n"
    "ds_read_b64_tr_b16 v[96:97], v21 offset:8192\n"
    "v_and_b32 v184, 3, v184\n"
    "v_mfma_f32_16x16x32_bf16 v[52:55], v[104:107], a[4:7], 0\n"
    "ds_read_b64_tr_b16 v[98:99], v21 offset:9216\n"
    "v_lshl_add_u32 v6, v184, 4, v6\n"
    "v_mfma_f32_16x16x32_bf16 v[52:55], v[108:111], a[20:23], v[52:55]\n"
    "ds_read_b64_tr_b16 v[100:101], v21 offset:12288\n"
    "v_lshl_add_u32 v6, v185, 6, v6\n"
    "v_mfma_f32_16x16x32_bf16 v[56:59], v[104:107], a[8:11], 0\n"
    "buffer_load_dwordx4 v11, s[36:39], s85 offen offset:1024 lds\n"
    "v_mfma_f32_16x16x32_bf16 v[56:59], v[108:111], a[24:27], v[56:59]\n"
    "s_add_u32 s85, s66, s86\n"
    "ds_read_b64_tr_b16 v[102:103], v21 offset:13312\n"
    "v_add_u32 v7, 294912, v6\n"
    "v_mfma_f32_16x16x32_bf16 v[60:63], v[104:107], a[12:15], 0\n"
    "s_add_u32 m0, s89, 98304\n"
    "ds_write_b64 v14, v[68:69] offset:512\n"
    "v_mfma_f32_16x16x32_bf16 v[60:63], v[108:111], a[28:31], v[60:63]\n"
    "s_waitcnt lgkmcnt(3)\n"
    "ds_write_b64 v14, v[70:71] offset:520\n"
    "v_mfma_f32_16x16x32_bf16 v[32:35], v[96:99], a[32:35], v[32:35]\n"
    "s_waitcnt lgkmcnt(2)\n"
    "buffer_load_dwordx4 v10, s[36:39], s85 offen lds\n"
    "v_mfma_f32_16x16x32_bf16 v[32:35], v[100:103], a[48:51], v[32:35]\n"
    "ds_read_b64_tr_b16 v[104:105], v22 offset:8192\n"
    "v_add_u32 v8, 589824, v6\n"
    "v_mfma_f32_16x16x32_bf16 v[36:39], v[96:99], a[36:39], v[36:39]\n"
    "ds_read_b64_tr_b16 v[106:107], v22 offset:9216\n"
    "v_add_u32 v9, 884736, v6\n"
    "v_mfma_f32_16x16x32_bf16 v[36:39], v[100:103], a[52:55], v[36:39]\n"
    "ds_read_b64_tr_b16 v[108:109], v22 offset:12288\n"
    "s_mov_b32 s88, 8\n"
    "v_mfma_f32_16x16x32_bf16 v[40:43], v[96:99], a[40:43], v[40:43]\n"
    "buffer_load_dwordx4 v11, s[36:39], s85 offen offset:1024 lds\n"
    "v_mfma_f32_16x16x32_bf16 v[40:43], v[100:103], a[56:59], v[40:43]\n"
    "s_add_u32 s85, s67, s86\n"
    "ds_read_b64_tr_b16 v[110:111], v22 offset:13312\n"
    "s_mov_b32 s58, 1879048192\n"
    "v_mfma_f32_16x16x32_bf16 v[44:47], v[96:99], a[44:47], v[44:47]\n"
    "s_add_u32 m0, s89, 106496\n"
    "ds_write_b64 v15, v[72:73] offset:0\n"
    "v_mfma_f32_16x16x32_bf16 v[44:47], v[100:103], a[60:63], v[44:47]\n"
    "s_waitcnt vmcnt(28) lgkmcnt(1)\n"
    "s_barrier\n"
    "v_mfma_f32_16x16x32_bf16 v[48:51], v[104:107], a[32:35], v[48:51]\n"
    "s_waitcnt lgkmcnt(1)\n"
    "buffer_load_dwordx4 v10, s[36:39], s85 offen lds\n"
    "v_mfma_f32_16x16x32_bf16 v[48:51], v[108:111], a[48:51], v[48:51]\n"
    "ds_read_b64_tr_b16 v[96:97], v21 offset:16384\n"
    "s_mov_b32 s59, 1879048192\n"
    "v_mfma_f32_16x16x32_bf16 v[52:55], v[104:107], a[36:39], v[52:55]\n"
    "ds_read_b64_tr_b16 v[98:99], v21 offset:17408\n"
    "s_mov_b32 s60, 1879048192\n"
    "v_mfma_f32_16x16x32_bf16 v[52:55], v[108:111], a[52:55], v[52:55]\n"
    "ds_read_b64_tr_b16 v[100:101], v21 offset:20480\n"
    "s_mov_b64 s[62:63], 0\n"
    "v_mfma_f32_16x16x32_bf16 v[56:59], v[104:107], a[40:43], v[56:59]\n"
    "buffer_load_dwordx4 v11, s[36:39], s85 offen offset:1024 lds\n"
    "v_mfma_f32_16x16x32_bf16 v[56:59], v[108:111], a[56:59], v[56:59]\n"
    "s_add_u32 s85, s68, s86\n"
    "ds_read_b64_tr_b16 v[102:103], v21 offset:21504\n"
    "v_mfma_f32_16x16x32_bf16 v[60:63], v[104:107], a[44:47], v[60:63]\n"
    "s_add_u32 m0, s89, 114688\n"
    "ds_write_b64 v15, v[74:75] offset:8\n"
    "v_mfma_f32_16x16x32_bf16 v[60:63], v[108:111], a[60:63], v[60:63]\n"
    "s_waitcnt lgkmcnt(3)\n"
    "ds_write_b64 v15, v[76:77] offset:512\n"
    "v_mfma_f32_16x16x32_bf16 v[32:35], v[96:99], a[64:67], v[32:35]\n"
    "s_waitcnt lgkmcnt(2)\n"
    "buffer_load_dwordx4 v10, s[36:39], s85 offen lds\n"
    "v_mfma_f32_16x16x32_bf16 v[32:35], v[100:103], a[80:83], v[32:35]\n"
    "ds_read_b64_tr_b16 v[104:105], v22 offset:16384\n"
    "v_mfma_f32_16x16x32_bf16 v[36:39], v[96:99], a[68:71], v[36:39]\n"
    "ds_read_b64_tr_b16 v[106:107], v22 offset:17408\n"
    "v_mfma_f32_16x16x32_bf16 v[36:39], v[100:103], a[84:87], v[36:39]\n"
    "ds_read_b64_tr_b16 v[108:109], v22 offset:20480\n"
    "v_mfma_f32_16x16x32_bf16 v[40:43], v[96:99], a[72:75], v[40:43]\n"
    "buffer_load_dwordx4 v11, s[36:39], s85 offen offset:1024 lds\n"
    "v_mfma_f32_16x16x32_bf16 v[40:43], v[100:103], a[88:91], v[40:43]\n"
    "s_add_u32 s85, s69, s86\n"
    "ds_read_b64_tr_b16 v[110:111], v22 offset:21504\n"
    "v_mfma_f32_16x16x32_bf16 v[44:47], v[96:99], a[76:79], v[44:47]\n"
    "s_add_u32 m0, s89, 122880\n"
    "ds_write_b64 v15, v[78:79] offset:520\n"
    "v_mfma_f32_16x16x32_bf16 v[44:47], v[100:103], a[92:95], v[44:47]\n"
    "s_waitcnt lgkmcnt(3)\n"
    "v_mfma_f32_16x16x32_bf16 v[48:51], v[104:107], a[64:67], v[48:51]\n"
    "s_waitcnt lgkmcnt(1)\n"
    "buffer_load_dwordx4 v10, s[36:39], s85 offen lds\n"
    "v_mfma_f32_16x16x32_bf16 v[48:51], v[108:111], a[80:83], v[48:51]\n"
    "ds_read_b64_tr_b16 v[96:97], v21 offset:24576\n"
    "v_mfma_f32_16x16x32_bf16 v[52:55], v[104:107], a[68:71], v[52:55]\n"
    "ds_read_b64_tr_b16 v[98:99], v21 offset:25600\n"
    "v_mfma_f32_16x16x32_bf16 v[52:55], v[108:111], a[84:87], v[52:55]\n"
    "ds_read_b64_tr_b16 v[100:101], v21 offset:28672\n"
    "v_mfma_f32_16x16x32_bf16 v[56:59], v[104:107], a[72:75], v[56:59]\n"
    "buffer_load_dwordx4 v11, s[36:39], s85 offen offset:1024 lds\n"
    "v_mfma_f32_16x16x32_bf16 v[56:59], v[108:111], a[88:91], v[56:59]\n"
    "s_mov_b32 s86, 256\n"
    "ds_read_b64_tr_b16 v[102:103], v21 offset:29696\n"
    "v_mfma_f32_16x16x32_bf16 v[60:63], v[104:107], a[76:79], v[60:63]\n"
    "s_add_u32 s85, s64, s86\n"
    "s_add_u32 m0, s89, 0\n"
    "v_mfma_f32_16x16x32_bf16 v[60:63], v[108:111], a[92:95], v[60:63]\n"
    "s_waitcnt lgkmcnt(2)\n"
    "v_mfma_f32_16x16x32_bf16 v[32:35], v[96:99], a[96:99], v[32:35]\n"
    "s_waitcnt lgkmcnt(0)\n"
    "buffer_load_dwordx4 v10, s[36:39], s85 offen lds\n"
    "v_mfma_f32_16x16x32_bf16 v[32:35], v[100:103], a[112:115], v[32:35]\n"
    "ds_read_b64_tr_b16 v[104:105], v22 offset:24576\n"
    "v_mfma_f32_16x16x32_bf16 v[36:39], v[96:99], a[100:103], v[36:39]\n"
    "ds_read_b64_tr_b16 v[106:107], v22 offset:25600\n"
    "v_mfma_f32_16x16x32_bf16 v[36:39], v[100:103], a[116:119], v[36:39]\n"
    "ds_read_b64_tr_b16 v[108:109], v22 offset:28672\n"
    "v_mfma_f32_16x16x32_bf16 v[40:43], v[96:99], a[104:107], v[40:43]\n"
    "buffer_load_dwordx4 v11, s[36:39], s85 offen offset:1024 lds\n"
    "v_mfma_f32_16x16x32_bf16 v[40:43], v[100:103], a[120:123], v[40:43]\n"
    "s_add_u32 s85, s65, s86\n"
    "ds_read_b64_tr_b16 v[110:111], v22 offset:29696\n"
    "v_mfma_f32_16x16x32_bf16 v[44:47], v[96:99], a[108:111], v[44:47]\n"
    "s_add_u32 m0, s89, 8192\n"
    "v_mfma_f32_16x16x32_bf16 v[44:47], v[100:103], a[124:127], v[44:47]\n"
    "s_waitcnt vmcnt(31) lgkmcnt(0)\n"
    "s_barrier\n"
    "v_mfma_f32_16x16x32_bf16 v[48:51], v[104:107], a[96:99], v[48:51]\n"
    "s_waitcnt lgkmcnt(0)\n"
    "buffer_load_dwordx4 v10, s[36:39], s85 offen lds\n"
    "v_mfma_f32_16x16x32_bf16 v[48:51], v[108:111], a[112:115], v[48:51]\n"
    "ds_read_b64_tr_b16 v[96:97], v21 offset:32768\n"
    "ds_read_b128 a[160:163], v16 offset:0\n"
    "v_mfma_f32_16x16x32_bf16 v[52:55], v[104:107], a[100:103], v[52:55]\n"
    "ds_read_b64_tr_b16 v[98:99], v21 offset:33792\n"
    "ds_read_b128 a[164:167], v16 offset:1024\n"
    "v_mfma_f32_16x16x32_bf16 v[52:55], v[108:111], a[116:119], v[52:55]\n"
    "ds_read_b64_tr_b16 v[100:101], v21 offset:36864\n"
    "ds_read_b128 a[168:171], v16 offset:2048\n"
    "v_mfma_f32_16x16x32_bf16 v[56:59], v[104:107], a[104:107], v[56:59]\n"
    "buffer_load_dwordx4 v11, s[36:39], s85 offen offset:1024 lds\n"
    "v_mfma_f32_16x16x32_bf16 v[56:59], v[108:111], a[120:123], v[56:59]\n"
    "s_add_u32 s85, s66, s86\n"
    "ds_read_b64_tr_b16 v[102:103], v21 offset:37888\n"
    "ds_read_b128 a[172:175], v16 offset:3072\n"
    "v_mfma_f32_16x16x32_bf16 v[60:63], v[104:107], a[108:111], v[60:63]\n"
    "s_add_u32 m0, s89, 16384\n"
    "ds_read_b128 a[176:179], v16 offset:8192\n"
    "v_mfma_f32_16x16x32_bf16 v[60:63], v[108:111], a[124:127], v[60:63]\n"
    "s_waitcnt lgkmcnt(6)\n"
    "ds_read_b128 a[180:183], v16 offset:9216\n"
    "v_mfma_f32_16x16x32_bf16 v[32:35], v[96:99], a[128:131], v[32:35]\n"
    "s_waitcnt lgkmcnt(3)\n"
    "buffer_load_dwordx4 v10, s[36:39], s85 offen lds\n"
    "v_mfma_f32_16x16x32_bf16 v[32:35], v[100:103], a[144:147], v[32:35]\n"
    "ds_read_b64_tr_b16 v[104:105], v22 offset:32768\n"
    "ds_read_b128 a[184:187], v16 offset:10240\n"
    "v_mfma_f32_16x16x32_bf16 v[36:39], v[96:99], a[132:135], v[36:39]\n"
    "ds_read_b64_tr_b16 v[106:107], v22 offset:33792\n"
    "ds_read_b128 a[188:191], v16 offset:11264\n"
    "v_mfma_f32_16x16x32_bf16 v[36:39], v[100:103], a[148:151], v[36:39]\n"
    "ds_read_b64_tr_b16 v[108:109], v22 offset:36864\n"
    "v_mfma_f32_16x16x32_bf16 v[40:43], v[96:99], a[136:139], v[40:43]\n"
    "buffer_load_dwordx4 v11, s[36:39], s85 offen offset:1024 lds\n"
    "v_mfma_f32_16x16x32_bf16 v[40:43], v[100:103], a[152:155], v[40:43]\n"
    "s_add_u32 s85, s67, s86\n"
    "ds_read_b64_tr_b16 v[110:111], v22 offset:37888\n"
    "v_mfma_f32_16x16x32_bf16 v[44:47], v[96:99], a[140:143], v[44:47]\n"
    "s_add_u32 m0, s89, 24576\n"
    "v_mfma_f32_16x16x32_bf16 v[44:47], v[100:103], a[156:159], v[44:47]\n"
    "s_waitcnt lgkmcnt(3)\n"
    "v_mfma_f32_16x16x32_bf16 v[48:51], v[104:107], a[128:131], v[48:51]\n"
    "s_waitcnt lgkmcnt(0)\n"
    "buffer_load_dwordx4 v10, s[36:39], s85 offen lds\n"
    "v_mfma_f32_16x16x32_bf16 v[48:51], v[108:111], a[144:147], v[48:51]\n"
    "ds_read_b64_tr_b16 v[96:97], v21 offset:40960\n"
    "v_mfma_f32_16x16x32_bf16 v[52:55], v[104:107], a[132:135], v[52:55]\n"
    "ds_read_b64_tr_b16 v[98:99], v21 offset:41984\n"
    "v_mfma_f32_16x16x32_bf16 v[52:55], v[108:111], a[148:151], v[52:55]\n"
    "ds_read_b64_tr_b16 v[100:101], v21 offset:45056\n"
    "v_mfma_f32_16x16x32_bf16 v[56:59], v[104:107], a[136:139], v[56:59]\n"
    "buffer_load_dwordx4 v11, s[36:39], s85 offen offset:1024 lds\n"
    "v_mfma_f32_16x16x32_bf16 v[56:59], v[108:111], a[152:155], v[56:59]\n"
    "s_add_u32 s85, s68, s86\n"
    "ds_read_b64_tr_b16 v[102:103], v21 offset:46080\n"
    "v_mfma_f32_16x16x32_bf16 v[60:63], v[104:107], a[140:143], v[60:63]\n"
    "s_add_u32 m0, s89, 32768\n"
    "v_mfma_f32_16x16x32_bf16 v[60:63], v[108:111], a[156:159], v[60:63]\n"
    "s_waitcnt lgkmcnt(2)\n"
    "v_mfma_f32_16x16x32_bf16 v[32:35], v[96:99], a[160:163], v[32:35]\n"
    "s_waitcnt lgkmcnt(0)\n"
    "ds_read_b64_tr_b16 v[104:105], v22 offset:40960\n"
    "v_add_u32 v21, s92, v21\n"
    "v_mfma_f32_16x16x32_bf16 v[32:35], v[100:103], a[176:179], v[32:35]\n"
    "ds_read_b64_tr_b16 v[106:107], v22 offset:41984\n"
    "v_mfma_f32_16x16x32_bf16 v[36:39], v[96:99], a[164:167], v[36:39]\n"
    "ds_read_b64_tr_b16 v[108:109], v22 offset:45056\n"
    "v_mfma_f32_16x16x32_bf16 v[36:39], v[100:103], a[180:183], v[36:39]\n"
    "ds_read_b64_tr_b16 v[110:111], v22 offset:46080\n"
    "v_mfma_f32_16x16x32_bf16 v[40:43], v[96:99], a[168:171], v[40:43]\n"
    "v_mfma_f32_16x16x32_bf16 v[40:43], v[100:103], a[184:187], v[40:43]\n"
    "v_mfma_f32_16x16x32_bf16 v[44:47], v[96:99], a[172:175], v[44:47]\n"
    "v_add_u32 v22, s92, v22\n"
    "s_sub_u32 s92, 0, s92\n"
    "v_mfma_f32_16x16x32_bf16 v[44:47], v[100:103], a[188:191], v[44:47]\n"
    "s_waitcnt vmcnt(16) lgkmcnt(0)\n"
    "s_barrier\n"
    "v_mfma_f32_16x16x32_bf16 v[48:51], v[104:107], a[160:163], v[48:51]\n"
    "s_waitcnt lgkmcnt(0)\n"
    "buffer_load_dwordx4 v10, s[36:39], s85 offen lds\n"
    "v_mfma_f32_16x16x32_bf16 v[48:51], v[108:111], a[176:179], v[48:51]\n"
    "ds_read_b64_tr_b16 v[96:97], v21 offset:0\n"
    "v_cvt_pk_bf16_f32 v80, v32, v33\n"
    "v_mfma_f32_16x16x32_bf16 v[52:55], v[104:107], a[164:167], v[52:55]\n"
    "ds_read_b64_tr_b16 v[98:99], v21 offset:1024\n"
    "v_cvt_pk_bf16_f32 v81, v34, v35\n"
    "v_mfma_f32_16x16x32_bf16 v[52:55], v[108:111], a[180:183], v[52:55]\n"
    "ds_read_b64_tr_b16 v[100:101], v21 offset:4096\n"
    "v_cvt_pk_bf16_f32 v84, v36, v37\n"
    "v_mfma_f32_16x16x32_bf16 v[56:59], v[104:107], a[168:171], v[56:59]\n"
    "ds_read_b64_tr_b16 v[102:103], v21 offset:5120\n"
    "v_cvt_pk_bf16_f32 v85, v38, v39\n"
    "v_mfma_f32_16x16x32_bf16 v[56:59], v[108:111], a[184:187], v[56:59]\n"
    "v_cvt_pk_bf16_f32 v88, v40, v41\n"
    "v_cvt_pk_bf16_f32 v89, v42, v43\n"
    "v_mfma_f32_16x16x32_bf16 v[60:63], v[104:107], a[172:175], v[60:63]\n"
    "v_cvt_pk_bf16_f32 v92, v44, v45\n"
    "v_cvt_pk_bf16_f32 v93, v46, v47\n"
    "v_mfma_f32_16x16x32_bf16 v[60:63], v[108:111], a[188:191], v[60:63]\n"
    "s_waitcnt lgkmcnt(2)\n"
    ".LL_CHUNK0_%=:\n"
    "v_mfma_f32_16x16x32_bf16 v[32:35], v[96:99], a[0:3], 0\n"
    "s_waitcnt lgkmcnt(0)\n"
    "buffer_load_dwordx4 v11, s[36:39], s85 offen offset:1024 lds\n"
    "v_mfma_f32_16x16x32_bf16 v[32:35], v[100:103], a[16:19], v[32:35]\n"
    "s_add_u32 s85, s69, s86\n"
    "ds_read_b64_tr_b16 v[104:105], v22 offset:0\n"
    "v_cvt_pk_bf16_f32 v82, v48, v49\n"
    "v_mfma_f32_16x16x32_bf16 v[36:39], v[96:99], a[4:7], 0\n"
    "s_add_u32 m0, s89, 40960\n"
    "ds_read_b64_tr_b16 v[106:107], v22 offset:1024\n"
    "v_cvt_pk_bf16_f32 v83, v50, v51\n"
    "v_mfma_f32_16x16x32_bf16 v[36:39], v[100:103], a[20:23], v[36:39]\n"
    "ds_read_b64_tr_b16 v[108:109], v22 offset:4096\n"
    "v_cvt_pk_bf16_f32 v86, v52, v53\n"
    "v_mfma_f32_16x16x32_bf16 v[40:43], v[96:99], a[8:11], 0\n"
    "ds_read_b64_tr_b16 v[110:111], v22 offset:5120\n"
    "v_cvt_pk_bf16_f32 v87, v54, v55\n"
    "v_mfma_f32_16x16x32_bf16 v[40:43], v[100:103], a[24:27], v[40:43]\n"
    "v_cvt_pk_bf16_f32 v90, v56, v57\n"
    "v_cvt_pk_bf16_f32 v91, v58, v59\n"
    "v_mfma_f32_16x16x32_bf16 v[44:47], v[96:99], a[12:15], 0\n"
    "v_cvt_pk_bf16_f32 v94, v60, v61\n"
    "v_cvt_pk_bf16_f32 v95, v62, v63\n"
    "v_mfma_f32_16x16x32_bf16 v[44:47], v[100:103], a[28:31], v[44:47]\n"
    "s_waitcnt lgkmcnt(2)\n"
    "v_mfma_f32_16x16x32_bf16 v[48:51], v[104:107], a[0:3], 0\n"
    "s_waitcnt lgkmcnt(0)\n"
    "buffer_load_dwordx4 v10, s[36:39], s85 offen lds\n"
    "v_mfma_f32_16x16x32_bf16 v[48:51], v[108:111], a[16:19], v[48:51]\n"
    "ds_read_b64_tr_b16 v[96:97], v21 offset:8192\n"
    "v_mfma_f32_16x16x32_bf16 v[52:55], v[104:107], a[4:7], 0\n"
    "ds_read_b64_tr_b16 v[98:99], v21 offset:9216\n"
    "v_mfma_f32_16x16x32_bf16 v[52:55], v[108:111], a[20:23], v[52:55]\n"
    "ds_read_b64_tr_b16 v[100:101], v21 offset:12288\n"
    "v_mfma_f32_16x16x32_bf16 v[56:59], v[104:107], a[8:11], 0\n"
    "buffer_store_dwordx4 v[80:83], v6, s[44:47], s95 offen sc1\n"
    "v_mfma_f32_16x16x32_bf16 v[56:59], v[108:111], a[24:27], v[56:59]\n"
    "ds_read_b64_tr_b16 v[102:103], v21 offset:13312\n"
    "v_mfma_f32_16x16x32_bf16 v[60:63], v[104:107], a[12:15], 0\n"
    "v_mfma_f32_16x16x32_bf16 v[60:63], v[108:111], a[28:31], v[60:63]\n"
    "s_waitcnt lgkmcnt(2)\n"
    "v_mfma_f32_16x16x32_bf16 v[32:35], v[96:99], a[32:35], v[32:35]\n"
    "s_waitcnt lgkmcnt(0)\n"
    "buffer_load_dwordx4 v11, s[36:39], s85 offen offset:1024 lds\n"
    "v_mfma_f32_16x16x32_bf16 v[32:35], v[100:103], a[48:51], v[32:35]\n"
    "s_sub_u32 s89, s89, s92\n"
    "ds_read_b64_tr_b16 v[104:105], v22 offset:8192\n"
    "v_mfma_f32_16x16x32_bf16 v[36:39], v[96:99], a[36:39], v[36:39]\n"
    "s_add_u32 s86, s86, 128\n"
    "ds_read_b64_tr_b16 v[106:107], v22 offset:9216\n"
    "v_mfma_f32_16x16x32_bf16 v[36:39], v[100:103], a[52:55], v[36:39]\n"
    "s_cmp_le_u32 s88, 2\n"
    "s_cselect_b32 s86, 1879048192, s86\n"
    "ds_read_b64_tr_b16 v[108:109], v22 offset:12288\n"
    "v_mfma_f32_16x16x32_bf16 v[40:43], v[96:99], a[40:43], v[40:43]\n"
    "s_add_u32 s85, s64, s86\n"
    "ds_read_b64_tr_b16 v[110:111], v22 offset:13312\n"
    "v_mfma_f32_16x16x32_bf16 v[40:43], v[100:103], a[56:59], v[40:43]\n"
    "s_add_u32 m0, s89, 0\n"
    "s_add_u32 s60, s60, 16384\n"
    "s_cmp_eq_u32 s88, 6\n"
    "s_cselect_b32 s60, s99, s60\n"
    "v_mfma_f32_16x16x32_bf16 v[44:47], v[96:99], a[44:47], v[44:47]\n"
    "v_mfma_f32_16x16x32_bf16 v[44:47], v[100:103], a[60:63], v[44:47]\n"
    "s_waitcnt vmcnt(13) lgkmcnt(0)\n"
    "s_barrier\n"
    "v_mfma_f32_16x16x32_bf16 v[48:51], v[104:107], a[32:35], v[48:51]\n"
    "s_waitcnt lgkmcnt(0)\n"
    "buffer_load_dwordx4 v10, s[36:39], s85 offen lds\n"
    "v_mfma_f32_16x16x32_bf16 v[48:51], v[108:111], a[48:51], v[48:51]\n"
    "ds_read_b64_tr_b16 v[96:97], v21 offset:16384\n"
    "v_mfma_f32_16x16x32_bf16 v[52:55], v[104:107], a[36:39], v[52:55]\n"
    "ds_read_b64_tr_b16 v[98:99], v21 offset:17408\n"
    "v_mfma_f32_16x16x32_bf16 v[52:55], v[108:111], a[52:55], v[52:55]\n"
    "ds_read_b64_tr_b16 v[100:101], v21 offset:20480\n"
    "v_mfma_f32_16x16x32_bf16 v[56:59], v[104:107], a[40:43], v[56:59]\n"
    "buffer_store_dwordx4 v[84:87], v7, s[44:47], s95 offen sc1\n"
    "v_mfma_f32_16x16x32_bf16 v[56:59], v[108:111], a[56:59], v[56:59]\n"
    "ds_read_b64_tr_b16 v[102:103], v21 offset:21504\n"
    "v_mfma_f32_16x16x32_bf16 v[60:63], v[104:107], a[44:47], v[60:63]\n"
    "buffer_store_dwordx4 v[164:167], v168, s[44:47], s58 offen sc1\n"
    "v_mfma_f32_16x16x32_bf16 v[60:63], v[108:111], a[60:63], v[60:63]\n"
    "s_waitcnt lgkmcnt(2)\n"
    "v_mfma_f32_16x16x32_bf16 v[32:35], v[96:99], a[64:67], v[32:35]\n"
    "s_waitcnt lgkmcnt(0)\n"
    "buffer_load_dwordx4 v11, s[36:39], s85 offen offset:1024 lds\n"
    "v_mfma_f32_16x16x32_bf16 v[32:35], v[100:103], a[80:83], v[32:35]\n"
    "s_add_u32 s85, s65, s86\n"
    "ds_read_b64_tr_b16 v[104:105], v22 offset:16384\n"
    "v_mfma_f32_16x16x32_bf16 v[36:39], v[96:99], a[68:71], v[36:39]\n"
    "s_add_u32 m0, s89, 8192\n"
    "ds_read_b64_tr_b16 v[106:107], v22 offset:17408\n"
    "v_mfma_f32_16x16x32_bf16 v[36:39], v[100:103], a[84:87], v[36:39]\n"
    "ds_read_b64_tr_b16 v[108:109], v22 offset:20480\n"
    "v_mfma_f32_16x16x32_bf16 v[40:43], v[96:99], a[72:75], v[40:43]\n"
    "buffer_store_dwordx4 v[88:91], v8, s[44:47], s95 offen sc1\n"
    "v_mfma_f32_16x16x32_bf16 v[40:43], v[100:103], a[88:91], v[40:43]\n"
    "ds_read_b64_tr_b16 v[110:111], v22 offset:21504\n"
    "v_mfma_f32_16x16x32_bf16 v[44:47], v[96:99], a[76:79], v[44:47]\n"
    "v_mfma_f32_16x16x32_bf16 v[44:47], v[100:103], a[92:95], v[44:47]\n"
    "s_waitcnt lgkmcnt(2)\n"
    "v_mfma_f32_16x16x32_bf16 v[48:51], v[104:107], a[64:67], v[48:51]\n"
    "s_waitcnt lgkmcnt(0)\n"
    "buffer_load_dwordx4 v10, s[36:39], s85 offen lds\n"
    "v_mfma_f32_16x16x32_bf16 v[48:51], v[108:111], a[80:83], v[48:51]\n"
    "ds_read_b64_tr_b16 v[96:97], v21 offset:24576\n"
    "v_mfma_f32_16x16x32_bf16 v[52:55], v[104:107], a[68:71], v[52:55]\n"
    "ds_read_b64_tr_b16 v[98:99], v21 offset:25600\n"
    "v_mfma_f32_16x16x32_bf16 v[52:55], v[108:111], a[84:87], v[52:55]\n"
    "ds_read_b64_tr_b16 v[100:101], v21 offset:28672\n"
    "v_mfma_f32_16x16x32_bf16 v[56:59], v[104:107], a[72:75], v[56:59]\n"
    "buffer_store_dwordx4 v[92:95], v9, s[44:47], s95 offen sc1\n"
    "v_mfma_f32_16x16x32_bf16 v[56:59], v[108:111], a[88:91], v[56:59]\n"
    "ds_read_b64_tr_b16 v[102:103], v21 offset:29696\n"
    "v_mfma_f32_16x16x32_bf16 v[60:63], v[104:107], a[76:79], v[60:63]\n"
    "s_waitcnt vmcnt(13)\n"
    "v_mfma_f32_16x16x32_bf16 v[184:187], v[172:175], v[188:191], 0\n"
    "v_mfma_f32_16x16x32_bf16 v[184:187], v[172:175], v[192:195], v[184:187]\n"
    "v_mfma_f32_16x16x32_bf16 v[184:187], v[172:175], v[196:199], v[184:187]\n"
    "v_mfma_f32_16x16x32_bf16 v[60:63], v[108:111], a[92:95], v[60:63]\n"
    "s_waitcnt lgkmcnt(2)\n"
    "v_mfma_f32_16x16x32_bf16 v[32:35], v[96:99], a[96:99], v[32:35]\n"
    "s_waitcnt lgkmcnt(0)\n"
    "buffer_load_dwordx4 v11, s[36:39], s85 offen offset:1024 lds\n"
    "v_mfma_f32_16x16x32_bf16 v[32:35], v[100:103], a[112:115], v[32:35]\n"
    "s_add_u32 s85, s66, s86\n"
    "ds_read_b64_tr_b16 v[104:105], v22 offset:24576\n"
    "v_mfma_f32_16x16x32_bf16 v[36:39], v[96:99], a[100:103], v[36:39]\n"
    "s_add_u32 m0, s89, 16384\n"
    "ds_read_b64_tr_b16 v[106:107], v22 offset:25600\n"
    "v_mfma_f32_16x16x32_bf16 v[36:39], v[100:103], a[116:119], v[36:39]\n"
    "ds_read_b64_tr_b16 v[108:109], v22 offset:28672\n"
    "v_mfma_f32_16x16x32_bf16 v[40:43], v[96:99], a[104:107], v[40:43]\n"
    "ds_read_b64_tr_b16 v[110:111], v22 offset:29696\n"
    "v_mfma_f32_16x16x32_bf16 v[40:43], v[100:103], a[120:123], v[40:43]\n"
    "v_mfma_f32_16x16x32_bf16 v[44:47], v[96:99], a[108:111], v[44:47]\n"
    "v_mfma_f32_16x16x32_bf16 v[44:47], v[100:103], a[124:127], v[44:47]\n"
    "s_waitcnt vmcnt(21) lgkmcnt(0)\n"
    "s_barrier\n"
    "v_mfma_f32_16x16x32_bf16 v[48:51], v[104:107], a[96:99], v[48:51]\n"
    "s_waitcnt lgkmcnt(0)\n"
    "buffer_load_dwordx4 v10, s[36:39], s85 offen lds\n"
    "v_mfma_f32_16x16x32_bf16 v[48:51], v[108:111], a[112:115], v[48:51]\n"
    "ds_read_b64_tr_b16 v[96:97], v21 offset:32768\n"
    "v_mfma_f32_16x16x32_bf16 v[52:55], v[104:107], a[100:103], v[52:55]\n"
    "ds_read_b64_tr_b16 v[98:99], v21 offset:33792\n"
    "v_mfma_f32_16x16x32_bf16 v[52:55], v[108:111], a[116:119], v[52:55]\n"
    "ds_read_b64_tr_b16 v[100:101], v21 offset:36864\n"
    "v_mfma_f32_16x16x32_bf16 v[56:59], v[104:107], a[104:107], v[56:59]\n"
    "buffer_load_dwordx4 v[188:191], v212, s[44:47], s60 offen sc1\n"
    "v_mfma_f32_16x16x32_bf16 v[56:59], v[108:111], a[120:123], v[56:59]\n"
    "ds_read_b64_tr_b16 v[102:103], v21 offset:37888\n"
    "v_mfma_f32_16x16x32_bf16 v[60:63], v[104:107], a[108:111], v[60:63]\n"
    "ds_write_b64 v170, v[184:185]\n"
    "v_mfma_f32_16x16x32_bf16 v[60:63], v[108:111], a[124:127], v[60:63]\n"
    "s_waitcnt lgkmcnt(3)\n"
    "v_mfma_f32_16x16x32_bf16 v[32:35], v[96:99], a[128:131], v[32:35]\n"
    "s_waitcnt lgkmcnt(1)\n"
    "buffer_load_dwordx4 v11, s[36:39], s85 offen offset:1024 lds\n"
    "v_mfma_f32_16x16x32_bf16 v[32:35], v[100:103], a[144:147], v[32:35]\n"
    "s_add_u32 s85, s67, s86\n"
    "ds_read_b64_tr_b16 v[104:105], v22 offset:32768\n"
    "v_mfma_f32_16x16x32_bf16 v[36:39], v[96:99], a[132:135], v[36:39]\n"
    "s_add_u32 m0, s89, 24576\n"
    "ds_read_b64_tr_b16 v[106:107], v22 offset:33792\n"
    "v_mfma_f32_16x16x32_bf16 v[36:39], v[100:103], a[148:151], v[36:39]\n"
    "ds_read_b64_tr_b16 v[108:109], v22 offset:36864\n"
    "v_mfma_f32_16x16x32_bf16 v[40:43], v[96:99], a[136:139], v[40:43]\n"
    "buffer_load_dwordx4 v[192:195], v213, s[44:47], s60 offen sc1\n"
    "v_mfma_f32_16x16x32_bf16 v[40:43], v[100:103], a[152:155], v[40:43]\n"
    "ds_read_b64_tr_b16 v[110:111], v22 offset:37888\n"
    "v_mfma_f32_16x16x32_bf16 v[44:47], v[96:99], a[140:143], v[44:47]\n"
    "ds_write_b64 v170, v[186:187] offset:8\n"
    "v_add_u32 v170, 0x200, v170\n"
    "v_mfma_f32_16x16x32_bf16 v[44:47], v[100:103], a[156:159], v[44:47]\n"
    "s_waitcnt lgkmcnt(3)\n"
    "s_add_u32 s95, s95, 16384\n"
    "s_mov_b32 s58, s97\n"
    "s_add_u32 s97, s97, 32768\n"
    "v_mfma_f32_16x16x32_bf16 v[48:51], v[104:107], a[128:131], v[48:51]\n"
    "s_waitcnt lgkmcnt(1)\n"
    "buffer_load_dwordx4 v10, s[36:39], s85 offen lds\n"
    "v_mfma_f32_16x16x32_bf16 v[48:51], v[108:111], a[144:147], v[48:51]\n"
    "ds_read_b64_tr_b16 v[96:97], v21 offset:40960\n"
    "v_mfma_f32_16x16x32_bf16 v[52:55], v[104:107], a[132:135], v[52:55]\n"
    "ds_read_b64_tr_b16 v[98:99], v21 offset:41984\n"
    "v_mfma_f32_16x16x32_bf16 v[52:55], v[108:111], a[148:151], v[52:55]\n"
    "ds_read_b64_tr_b16 v[100:101], v21 offset:45056\n"
    "v_mfma_f32_16x16x32_bf16 v[56:59], v[104:107], a[136:139], v[56:59]\n"
    "buffer_load_dwordx4 v[196:199], v214, s[44:47], s60 offen sc1\n"
    "v_mfma_f32_16x16x32_bf16 v[56:59], v[108:111], a[152:155], v[56:59]\n"
    "ds_read_b64_tr_b16 v[102:103], v21 offset:46080\n"
    "v_mfma_f32_16x16x32_bf16 v[60:63], v[104:107], a[140:143], v[60:63]\n"
    "s_sub_u32 s88, s88, 1\n"
    "v_mfma_f32_16x16x32_bf16 v[60:63], v[108:111], a[156:159], v[60:63]\n"
    "s_waitcnt lgkmcnt(2)\n"
    "v_mfma_f32_16x16x32_bf16 v[32:35], v[96:99], a[160:163], v[32:35]\n"
    "s_waitcnt lgkmcnt(0)\n"
    "buffer_load_dwordx4 v11, s[36:39], s85 offen offset:1024 lds\n"
    "v_mfma_f32_16x16x32_bf16 v[32:35], v[100:103], a[176:179], v[32:35]\n"
    "s_add_u32 s85, s68, s86\n"
    "ds_read_b64_tr_b16 v[104:105], v22 offset:40960\n"
    "v_add_u32 v21, s92, v21\n"
    "v_mfma_f32_16x16x32_bf16 v[36:39], v[96:99], a[164:167], v[36:39]\n"
    "s_add_u32 m0, s89, 32768\n"
    "ds_read_b64_tr_b16 v[106:107], v22 offset:41984\n"
    "v_mfma_f32_16x16x32_bf16 v[36:39], v[100:103], a[180:183], v[36:39]\n"
    "ds_read_b64_tr_b16 v[108:109], v22 offset:45056\n"
    "v_mfma_f32_16x16x32_bf16 v[40:43], v[96:99], a[168:171], v[40:43]\n"
    "ds_read_b64_tr_b16 v[110:111], v22 offset:46080\n"
    "v_mfma_f32_16x16x32_bf16 v[40:43], v[100:103], a[184:187], v[40:43]\n"
    "s_add_u32 s59, s59, 32768\n"
    "s_cmp_eq_u32 s88, 6\n"
    "s_cselect_b32 s59, s98, s59\n"
    "s_cselect_b64 s[62:63], -1, s[62:63]\n"
    "v_mfma_f32_16x16x32_bf16 v[44:47], v[96:99], a[172:175], v[44:47]\n"
    "v_add_u32 v22, s92, v22\n"
    "s_sub_u32 s92, 0, s92\n"
    "v_mfma_f32_16x16x32_bf16 v[44:47], v[100:103], a[188:191], v[44:47]\n"
    "s_waitcnt vmcnt(24) lgkmcnt(0)\n"
    "s_barrier\n"
    "v_mfma_f32_16x16x32_bf16 v[48:51], v[104:107], a[160:163], v[48:51]\n"
    "s_waitcnt lgkmcnt(0)\n"
    "buffer_load_dwordx4 v10, s[36:39], s85 offen lds\n"
    "v_mfma_f32_16x16x32_bf16 v[48:51], v[108:111], a[176:179], v[48:51]\n"
    "ds_read_b64_tr_b16 v[96:97], v21 offset:0\n"
    "v_cvt_pk_bf16_f32 v80, v32, v33\n"
    "v_mfma_f32_16x16x32_bf16 v[52:55], v[104:107], a[164:167], v[52:55]\n"
    "ds_read_b64_tr_b16 v[98:99], v21 offset:1024\n"
    "v_cvt_pk_bf16_f32 v81, v34, v35\n"
    "v_mfma_f32_16x16x32_bf16 v[52:55], v[108:111], a[180:183], v[52:55]\n"
    "ds_read_b64_tr_b16 v[100:101], v21 offset:4096\n"
    "v_cvt_pk_bf16_f32 v84, v36, v37\n"
    "v_mfma_f32_16x16x32_bf16 v[56:59], v[104:107], a[168:171], v[56:59]\n"
    "ds_read_b64_tr_b16 v[102:103], v21 offset:5120\n"
    "v_cvt_pk_bf16_f32 v85, v38, v39\n"
    "v_mfma_f32_16x16x32_bf16 v[56:59], v[108:111], a[184:187], v[56:59]\n"
    "v_cvt_pk_bf16_f32 v88, v40, v41\n"
    "v_cvt_pk_bf16_f32 v89, v42, v43\n"
    "v_mfma_f32_16x16x32_bf16 v[60:63], v[104:107], a[172:175], v[60:63]\n"
    "v_cvt_pk_bf16_f32 v92, v44, v45\n"
    "v_cvt_pk_bf16_f32 v93, v46, v47\n"
    "v_mfma_f32_16x16x32_bf16 v[60:63], v[108:111], a[188:191], v[60:63]\n"
    "s_waitcnt lgkmcnt(2)\n"
    "s_cmp_lg_u32 s88, 0\n"
    "s_cbranch_scc1 .LL_CHUNK0_%=\n"
    "s_branch .LL_TAIL_%=\n"
    ".LL_CSRETRY0_%=:\n"
    "s_mov_b32 s58, 0\n"
    ".LL_CSR0_%=:\n"
    "s_sleep 1\n"
    "buffer_load_dwordx2 v[120:121], v16, s[44:47], s60 offen sc1\n"
    "s_waitcnt vmcnt(0)\n"
    "v_cmp_ne_u32 vcc, s56, v121\n"
    "v_mov_b32 v124, v120\n"
    "s_add_u32 s58, s58, 1\n"
    "s_cmp_gt_u32 s58, 32768\n"
    "s_cbranch_scc1 .LL_CSOK0_%=\n"
    "s_cbranch_vccnz .LL_CSR0_%=\n"
    "s_branch .LL_CSOK0_%=\n"
    ".LL_STEP1_%=:\n"
    "v_mfma_f32_32x32x16_bf16 v[32:47], v[96:99], a[0:3], 0\n"
    "s_add_u32 s85, s71, s86\n"
    "ds_read_b64_tr_b16 v[108:109], v12 offset:6144\n"
    "ds_read_b64_tr_b16 v[110:111], v12 offset:6656\n"
    "v_mfma_f32_32x32x16_bf16 v[48:63], v[96:99], a[4:7], 0\n"
    "s_waitcnt lgkmcnt(6)\n"
    "v_mul_f32 v64, v64, v122\n"
    "v_fma_f32 v112, -v64, v64, s87\n"
    "v_mul_f32 v112, v64, v112\n"
    "v_mul_f32 v65, v65, v122\n"
    "v_exp_f32 v112, v112\n"
    "v_mfma_f32_32x32x16_bf16 v[32:47], v[100:103], a[8:11], v[32:47]\n"
    "s_add_u32 m0, s89, 57344\n"
    "ds_read_b64_tr_b16 v[96:97], v12 offset:8192\n"
    "ds_read_b64_tr_b16 v[98:99], v12 offset:8704\n"
    "v_fma_f32 v113, -v65, v65, s87\n"
    "v_add_f32 v112, 1.0, v112\n"
    "v_mul_f32 v113, v65, v113\n"
    "v_rcp_f32 v112, v112\n"
    "v_mfma_f32_32x32x16_bf16 v[48:63], v[100:103], a[12:15], v[48:63]\n"
    "s_waitcnt lgkmcnt(6)\n"
    "v_mul_f32 v66, v66, v122\n"
    "v_exp_f32 v113, v113\n"
    "v_fma_f32 v114, -v66, v66, s87\n"
    "v_mul_f32 v64, v64, v112\n"
    "v_add_f32 v113, 1.0, v113\n"
    "v_mul_f32 v114, v66, v114\n"
    "v_mfma_f32_32x32x16_bf16 v[32:47], v[104:107], a[16:19], v[32:47]\n"
    "s_lshl_b32 s60, s86, 2\n"
    "ds_read_b64_tr_b16 v[100:101], v12 offset:10240\n"
    "ds_read_b64_tr_b16 v[102:103], v12 offset:10752\n"
    "v_rcp_f32 v113, v113\n"
    "v_mul_f32 v67, v67, v122\n"
    "v_exp_f32 v114, v114\n"
    "v_mfma_f32_32x32x16_bf16 v[48:63], v[104:107], a[20:23], v[48:63]\n"
    "s_waitcnt lgkmcnt(4)\n"
    "buffer_load_dwordx4 v10, s[36:39], s85 offen lds\n"
    "buffer_load_dwordx4 v11, s[36:39], s85 offen offset:1024 lds\n"
    "v_fma_f32 v112, -v67, v67, s87\n"
    "v_mul_f32 v65, v65, v113\n"
    "v_add_f32 v114, 1.0, v114\n"
    "v_mfma_f32_32x32x16_bf16 v[32:47], v[108:111], a[24:27], v[32:47]\n"
    "s_add_u32 s60, s60, s92\n"
    "ds_read_b64_tr_b16 v[104:105], v12 offset:12288\n"
    "ds_read_b64_tr_b16 v[106:107], v12 offset:12800\n"
    "v_mul_f32 v112, v67, v112\n"
    "v_cvt_pk_bf16_f32 v116, v64, v65\n"
    "v_rcp_f32 v114, v114\n"
    "v_mul_f32 v68, v68, v122\n"
    "v_mfma_f32_32x32x16_bf16 v[48:63], v[108:111], a[28:31], v[48:63]\n"
    "s_waitcnt lgkmcnt(4)\n"
    "v_exp_f32 v112, v112\n"
    "v_fma_f32 v113, -v68, v68, s87\n"
    "v_mul_f32 v66, v66, v114\n"
    "v_add_f32 v112, 1.0, v112\n"
    "v_mul_f32 v113, v68, v113\n"
    "v_rcp_f32 v112, v112\n"
    "v_mfma_f32_32x32x16_bf16 v[32:47], v[96:99], a[32:35], v[32:47]\n"
    "s_add_u32 s85, s72, s86\n"
    "ds_read_b64_tr_b16 v[108:109], v12 offset:14336\n"
    "ds_read_b64_tr_b16 v[110:111], v12 offset:14848\n"
    "v_mul_f32 v69, v69, v122\n"
    "v_exp_f32 v113, v113\n"
    "v_fma_f32 v114, -v69, v69, s87\n"
    "v_mul_f32 v67, v67, v112\n"
    "v_mfma_f32_32x32x16_bf16 v[48:63], v[96:99], a[36:39], v[48:63]\n"
    "s_waitcnt vmcnt(8) lgkmcnt(4)\n"
    "s_barrier\n"
    "v_add_f32 v113, 1.0, v113\n"
    "v_mul_f32 v114, v69, v114\n"
    "v_cvt_pk_bf16_f32 v117, v66, v67\n"
    "v_rcp_f32 v113, v113\n"
    "v_mfma_f32_32x32x16_bf16 v[32:47], v[100:103], a[40:43], v[32:47]\n"
    "s_add_u32 m0, s89, 65536\n"
    "ds_read_b64_tr_b16 v[96:97], v12 offset:16384\n"
    "ds_read_b64_tr_b16 v[98:99], v12 offset:16896\n"
    "v_mul_f32 v70, v70, v122\n"
    "v_exp_f32 v114, v114\n"
    "v_fma_f32 v112, -v70, v70, s87\n"
    "v_mul_f32 v68, v68, v113\n"
    "v_mfma_f32_32x32x16_bf16 v[48:63], v[100:103], a[44:47], v[48:63]\n"
    "s_waitcnt lgkmcnt(4)\n"
    "v_add_f32 v114, 1.0, v114\n"
    "v_mul_f32 v112, v70, v112\n"
    "v_rcp_f32 v114, v114\n"
    "v_mul_f32 v71, v71, v122\n"
    "v_exp_f32 v112, v112\n"
    "v_mfma_f32_32x32x16_bf16 v[32:47], v[104:107], a[48:51], v[32:47]\n"
    "s_mov_b32 s61, s86\n"
    "s_add_u32 s86, s86, 128\n"
    "ds_read_b64_tr_b16 v[100:101], v12 offset:18432\n"
    "ds_read_b64_tr_b16 v[102:103], v12 offset:18944\n"
    "v_fma_f32 v113, -v71, v71, s87\n"
    "v_mul_f32 v69, v69, v114\n"
    "v_add_f32 v112, 1.0, v112\n"
    "v_mul_f32 v113, v71, v113\n"
    "v_mfma_f32_32x32x16_bf16 v[48:63], v[104:107], a[52:55], v[48:63]\n"
    "s_waitcnt lgkmcnt(4)\n"
    "buffer_load_dwordx4 v10, s[36:39], s85 offen lds\n"
    "buffer_load_dwordx4 v11, s[36:39], s85 offen offset:1024 lds\n"
    "v_cvt_pk_bf16_f32 v118, v68, v69\n"
    "v_rcp_f32 v112, v112\n"
    "v_mfma_f32_32x32x16_bf16 v[32:47], v[108:111], a[56:59], v[32:47]\n"
    "s_cmp_eq_u32 s86, 768\n"
    "s_cselect_b32 s86, 0, s86\n"
    "s_cmp_eq_u32 s88, 1\n"
    "ds_read_b64_tr_b16 v[104:105], v12 offset:20480\n"
    "ds_read_b64_tr_b16 v[106:107], v12 offset:20992\n"
    "v_mul_f32 v72, v72, v122\n"
    "v_exp_f32 v113, v113\n"
    "v_mfma_f32_32x32x16_bf16 v[48:63], v[108:111], a[60:63], v[48:63]\n"
    "s_waitcnt lgkmcnt(4)\n"
    "ds_write_b64 v14, v[116:117] offset:0\n"
    "v_fma_f32 v114, -v72, v72, s87\n"
    "v_mul_f32 v70, v70, v112\n"
    "v_add_f32 v113, 1.0, v113\n"
    "v_mfma_f32_32x32x16_bf16 v[32:47], v[96:99], a[64:67], v[32:47]\n"
    "s_cbranch_scc0 .LL_SKSW1_%=\n"
    "s_mov_b64 s[36:37], s[52:53]\n"
    "s_mov_b32 s38, s54\n"
    "s_mov_b32 s86, 0\n"
    "s_mov_b32 s64, s73\n"
    "s_mov_b32 s65, s74\n"
    "s_mov_b32 s66, s75\n"
    "s_mov_b32 s67, s76\n"
    "s_mov_b32 s68, s77\n"
    "s_mov_b32 s69, s78\n"
    "s_mov_b32 s70, 1879048192\n"
    "v_mov_b32 v10, v19\n"
    "v_mov_b32 v11, v20\n"
    ".LL_SKSW1_%=:\n"
    "s_add_u32 s85, s64, s86\n"
    "ds_read_b64_tr_b16 v[108:109], v12 offset:22528\n"
    "ds_read_b64_tr_b16 v[110:111], v12 offset:23040\n"
    "v_mul_f32 v114, v72, v114\n"
    "v_rcp_f32 v113, v113\n"
    "v_mfma_f32_32x32x16_bf16 v[48:63], v[96:99], a[68:71], v[48:63]\n"
    "s_waitcnt lgkmcnt(5)\n"
    "v_mul_f32 v73, v73, v122\n"
    "v_exp_f32 v114, v114\n"
    "v_fma_f32 v112, -v73, v73, s87\n"
    "v_mul_f32 v71, v71, v113\n"
    "v_add_f32 v114, 1.0, v114\n"
    "v_mfma_f32_32x32x16_bf16 v[32:47], v[100:103], a[72:75], v[32:47]\n"
    "s_add_u32 m0, s89, 0\n"
    "ds_read_b64_tr_b16 v[96:97], v12 offset:24576\n"
    "ds_read_b64_tr_b16 v[98:99], v12 offset:25088\n"
    "v_mul_f32 v112, v73, v112\n"
    "v_cvt_pk_bf16_f32 v119, v70, v71\n"
    "v_rcp_f32 v114, v114\n"
    "v_mul_f32 v74, v74, v122\n"
    "v_mfma_f32_32x32x16_bf16 v[48:63], v[100:103], a[76:79], v[48:63]\n"
    "s_waitcnt lgkmcnt(5)\n"
    "buffer_load_dword v31, v18, s[52:55], s93 offen\n"
    "v_exp_f32 v112, v112\n"
    "v_fma_f32 v113, -v74, v74, s87\n"
    "v_mul_f32 v72, v72, v114\n"
    "v_mfma_f32_32x32x16_bf16 v[32:47], v[104:107], a[80:83], v[32:47]\n"
    "ds_read_b64_tr_b16 v[100:101], v12 offset:26624\n"
    "ds_read_b64_tr_b16 v[102:103], v12 offset:27136\n"
    "v_add_f32 v112, 1.0, v112\n"
    "v_mul_f32 v113, v74, v113\n"
    "v_rcp_f32 v112, v112\n"
    "v_mfma_f32_32x32x16_bf16 v[48:63], v[104:107], a[84:87], v[48:63]\n"
    "s_waitcnt lgkmcnt(4)\n"
    "buffer_load_dwordx4 v10, s[36:39], s85 offen lds\n"
    "buffer_load_dwordx4 v11, s[36:39], s85 offen offset:1024 lds\n"
    "v_mul_f32 v75, v75, v122\n"
    "v_exp_f32 v113, v113\n"
    "v_mfma_f32_32x32x16_bf16 v[32:47], v[108:111], a[88:91], v[32:47]\n"
    "ds_read_b64_tr_b16 v[104:105], v12 offset:28672\n"
    "ds_read_b64_tr_b16 v[106:107], v12 offset:29184\n"
    "v_fma_f32 v114, -v75, v75, s87\n"
    "v_mul_f32 v73, v73, v112\n"
    "v_add_f32 v113, 1.0, v113\n"
    "v_mul_f32 v114, v75, v114\n"
    "v_mfma_f32_32x32x16_bf16 v[48:63], v[108:111], a[92:95], v[48:63]\n"
    "s_waitcnt lgkmcnt(4)\n"
    "ds_write_b64 v14, v[118:119] offset:8\n"
    "v_cvt_pk_bf16_f32 v116, v72, v73\n"
    "v_rcp_f32 v113, v113\n"
    "v_mfma_f32_32x32x16_bf16 v[32:47], v[96:99], a[96:99], v[32:47]\n"
    "s_add_u32 s85, s65, s86\n"
    "ds_read_b64_tr_b16 v[108:109], v12 offset:30720\n"
    "ds_read_b64_tr_b16 v[110:111], v12 offset:31232\n"
    "v_mul_f32 v76, v76, v122\n"
    "v_exp_f32 v114, v114\n"
    "v_fma_f32 v112, -v76, v76, s87\n"
    "v_mul_f32 v74, v74, v113\n"
    "v_mfma_f32_32x32x16_bf16 v[48:63], v[96:99], a[100:103], v[48:63]\n"
    "s_waitcnt vmcnt(9) lgkmcnt(5)\n"
    "s_barrier\n"
    "v_add_f32 v114, 1.0, v114\n"
    "v_mul_f32 v112, v76, v112\n"
    "v_rcp_f32 v114, v114\n"
    "v_mfma_f32_32x32x16_bf16 v[32:47], v[100:103], a[104:107], v[32:47]\n"
    "s_add_u32 m0, s89, 8192\n"
    "ds_read_b64_tr_b16 v[96:97], v12 offset:32768\n"
    "ds_read_b64_tr_b16 v[98:99], v12 offset:33280\n"
    "v_mul_f32 v77, v77, v122\n"
    "v_exp_f32 v112, v112\n"
    "v_fma_f32 v113, -v77, v77, s87\n"
    "v_mfma_f32_32x32x16_bf16 v[48:63], v[100:103], a[108:111], v[48:63]\n"
    "s_waitcnt lgkmcnt(5)\n"
    "v_mul_f32 v75, v75, v114\n"
    "v_add_f32 v112, 1.0, v112\n"
    "v_mul_f32 v113, v77, v113\n"
    "v_cvt_pk_bf16_f32 v117, v74, v75\n"
    "v_rcp_f32 v112, v112\n"
    "v_mfma_f32_32x32x16_bf16 v[32:47], v[104:107], a[112:115], v[32:47]\n"
    "s_add_u32 s93, s93, 11264\n"
    "ds_read_b64_tr_b16 v[100:101], v12 offset:34816\n"
    "ds_read_b64_tr_b16 v[102:103], v12 offset:35328\n"
    "v_mul_f32 v78, v78, v122\n"
    "v_exp_f32 v113, v113\n"
    "v_fma_f32 v114, -v78, v78, s87\n"
    "v_mfma_f32_32x32x16_bf16 v[48:63], v[104:107], a[116:119], v[48:63]\n"
    "s_waitcnt lgkmcnt(4)\n"
    "buffer_load_dwordx4 v10, s[36:39], s85 offen lds\n"
    "buffer_load_dwordx4 v11, s[36:39], s85 offen offset:1024 lds\n"
    "v_mul_f32 v76, v76, v112\n"
    "v_add_f32 v113, 1.0, v113\n"
    "v_mul_f32 v114, v78, v114\n"
    "v_mfma_f32_32x32x16_bf16 v[32:47], v[108:111], a[120:123], v[32:47]\n"
    "ds_read_b64_tr_b16 v[104:105], v12 offset:36864\n"
    "ds_read_b64_tr_b16 v[106:107], v12 offset:37376\n"
    "v_rcp_f32 v113, v113\n"
    "v_mul_f32 v79, v79, v122\n"
    "v_exp_f32 v114, v114\n"
    "v_mfma_f32_32x32x16_bf16 v[48:63], v[108:111], a[124:127], v[48:63]\n"
    "s_waitcnt lgkmcnt(4)\n"
    "ds_write_b64 v14, v[116:117] offset:512\n"
    "v_fma_f32 v112, -v79, v79, s87\n"
    "v_mul_f32 v77, v77, v113\n"
    "v_add_f32 v114, 1.0, v114\n"
    "v_mfma_f32_32x32x16_bf16 v[32:47], v[96:99], a[128:131], v[32:47]\n"
    "s_add_u32 s85, s66, s86\n"
    "ds_read_b64_tr_b16 v[108:109], v12 offset:38912\n"
    "ds_read_b64_tr_b16 v[110:111], v12 offset:39424\n"
    "v_mul_f32 v112, v79, v112\n"
    "v_cvt_pk_bf16_f32 v118, v76, v77\n"
    "v_rcp_f32 v114, v114\n"
    "v_mfma_f32_32x32x16_bf16 v[48:63], v[96:99], a[132:135], v[48:63]\n"
    "s_waitcnt lgkmcnt(5)\n"
    "v_mul_f32 v80, v80, v123\n"
    "v_exp_f32 v112, v112\n"
    "v_fma_f32 v113, -v80, v80, s87\n"
    "v_mul_f32 v78, v78, v114\n"
    "v_add_f32 v112, 1.0, v112\n"
    "v_mfma_f32_32x32x16_bf16 v[32:47], v[100:103], a[136:139], v[32:47]\n"
    "s_add_u32 m0, s89, 16384\n"
    "ds_read_b64_tr_b16 v[96:97], v12 offset:40960\n"
    "ds_read_b64_tr_b16 v[98:99], v12 offset:41472\n"
    "v_mul_f32 v113, v80, v113\n"
    "v_rcp_f32 v112, v112\n"
    "v_mul_f32 v81, v81, v123\n"
    "v_mfma_f32_32x32x16_bf16 v[48:63], v[100:103], a[140:143], v[48:63]\n"
    "s_waitcnt lgkmcnt(5)\n"
    "buffer_load_ushort v125, v17, s[48:51], s61 offen\n"
    "v_exp_f32 v113, v113\n"
    "v_fma_f32 v114, -v81, v81, s87\n"
    "v_mul_f32 v79, v79, v112\n"
    "v_add_f32 v113, 1.0, v113\n"
    "v_mfma_f32_32x32x16_bf16 v[32:47], v[104:107], a[144:147], v[32:47]\n"
    "ds_read_b64_tr_b16 v[100:101], v12 offset:43008\n"
    "ds_read_b64_tr_b16 v[102:103], v12 offset:43520\n"
    "v_mul_f32 v114, v81, v114\n"
    "v_cvt_pk_bf16_f32 v119, v78, v79\n"
    "v_rcp_f32 v113, v113\n"
    "v_mfma_f32_32x32x16_bf16 v[48:63], v[104:107], a[148:151], v[48:63]\n"
    "s_waitcnt lgkmcnt(4)\n"
    "buffer_load_dwordx4 v10, s[36:39], s85 offen lds\n"
    "buffer_load_dwordx4 v11, s[36:39], s85 offen offset:1024 lds\n"
    "v_mul_f32 v82, v82, v123\n"
    "v_exp_f32 v114, v114\n"
    "v_mfma_f32_32x32x16_bf16 v[32:47], v[108:111], a[152:155], v[32:47]\n"
    "ds_read_b64_tr_b16 v[104:105], v12 offset:45056\n"
    "ds_read_b64_tr_b16 v[106:107], v12 offset:45568\n"
    "v_fma_f32 v112, -v82, v82, s87\n"
    "v_mul_f32 v80, v80, v113\n"
    "v_add_f32 v114, 1.0, v114\n"
    "v_mul_f32 v112, v82, v112\n"
    "v_mfma_f32_32x32x16_bf16 v[48:63], v[108:111], a[156:159], v[48:63]\n"
    "s_waitcnt lgkmcnt(4)\n"
    "ds_write_b64 v14, v[118:119] offset:520\n"
    "v_rcp_f32 v114, v114\n"
    "v_mul_f32 v83, v83, v123\n"
    "v_mfma_f32_32x32x16_bf16 v[32:47], v[96:99], a[160:163], v[32:47]\n"
    "s_add_u32 s85, s67, s86\n"
    "ds_read_b64_tr_b16 v[108:109], v12 offset:47104\n"
    "ds_read_b64_tr_b16 v[110:111], v12 offset:47616\n"
    "v_exp_f32 v112, v112\n"
    "v_fma_f32 v113, -v83, v83, s87\n"
    "v_mul_f32 v81, v81, v114\n"
    "v_mfma_f32_32x32x16_bf16 v[48:63], v[96:99], a[164:167], v[48:63]\n"
    "s_waitcnt vmcnt(10) lgkmcnt(5)\n"
    "s_barrier\n"
    "v_add_f32 v112, 1.0, v112\n"
    "v_mul_f32 v113, v83, v113\n"
    "v_cvt_pk_bf16_f32 v116, v80, v81\n"
    "v_rcp_f32 v112, v112\n"
    "v_mfma_f32_32x32x16_bf16 v[32:47], v[100:103], a[168:171], v[32:47]\n"
    "s_add_u32 m0, s89, 24576\n"
    "ds_read_b64_tr_b16 v[96:97], v12 offset:49152\n"
    "ds_read_b64_tr_b16 v[98:99], v12 offset:49664\n"
    "v_mul_f32 v84, v84, v123\n"
    "v_exp_f32 v113, v113\n"
    "v_fma_f32 v114, -v84, v84, s87\n"
    "v_mfma_f32_32x32x16_bf16 v[48:63], v[100:103], a[172:175], v[48:63]\n"
    "s_waitcnt lgkmcnt(5)\n"
    "buffer_load_dwordx2 v[120:121], v16, s[44:47], s60 offen sc1\n"
    "v_mul_f32 v82, v82, v112\n"
    "v_add_f32 v113, 1.0, v113\n"
    "v_mul_f32 v114, v84, v114\n"
    "v_rcp_f32 v113, v113\n"
    "v_mfma_f32_32x32x16_bf16 v[32:47], v[104:107], a[176:179], v[32:47]\n"
    "ds_read_b64_tr_b16 v[100:101], v12 offset:51200\n"
    "ds_read_b64_tr_b16 v[102:103], v12 offset:51712\n"
    "v_mul_f32 v85, v85, v123\n"
    "v_exp_f32 v114, v114\n"
    "v_fma_f32 v112, -v85, v85, s87\n"
    "v_mfma_f32_32x32x16_bf16 v[48:63], v[104:107], a[180:183], v[48:63]\n"
    "s_waitcnt lgkmcnt(4)\n"
    "buffer_load_dwordx4 v10, s[36:39], s85 offen lds\n"
    "buffer_load_dwordx4 v11, s[36:39], s85 offen offset:1024 lds\n"
    "v_mul_f32 v83, v83, v113\n"
    "v_add_f32 v114, 1.0, v114\n"
    "v_mfma_f32_32x32x16_bf16 v[32:47], v[108:111], a[184:187], v[32:47]\n"
    "ds_read_b64_tr_b16 v[104:105], v12 offset:53248\n"
    "ds_read_b64_tr_b16 v[106:107], v12 offset:53760\n"
    "v_mul_f32 v112, v85, v112\n"
    "v_cvt_pk_bf16_f32 v117, v82, v83\n"
    "v_rcp_f32 v114, v114\n"
    "v_mfma_f32_32x32x16_bf16 v[48:63], v[108:111], a[188:191], v[48:63]\n"
    "s_waitcnt lgkmcnt(4)\n"
    "ds_write_b64 v15, v[116:117] offset:0\n"
    "v_mul_f32 v86, v86, v123\n"
    "v_exp_f32 v112, v112\n"
    "v_mfma_f32_32x32x16_bf16 v[32:47], v[96:99], a[192:195], v[32:47]\n"
    "s_add_u32 s85, s68, s86\n"
    "ds_read_b64_tr_b16 v[108:109], v12 offset:55296\n"
    "ds_read_b64_tr_b16 v[110:111], v12 offset:55808\n"
    "v_fma_f32 v113, -v86, v86, s87\n"
    "v_mul_f32 v84, v84, v114\n"
    "v_add_f32 v112, 1.0, v112\n"
    "v_mul_f32 v113, v86, v113\n"
    "v_mfma_f32_32x32x16_bf16 v[48:63], v[96:99], a[196:199], v[48:63]\n"
    "s_waitcnt lgkmcnt(5)\n"
    "v_rcp_f32 v112, v112\n"
    "v_mul_f32 v87, v87, v123\n"
    "v_exp_f32 v113, v113\n"
    "v_fma_f32 v114, -v87, v87, s87\n"
    "v_mfma_f32_32x32x16_bf16 v[32:47], v[100:103], a[200:203], v[32:47]\n"
    "s_add_u32 m0, s89, 32768\n"
    "ds_read_b64_tr_b16 v[96:97], v12 offset:57344\n"
    "ds_read_b64_tr_b16 v[98:99], v12 offset:57856\n"
    "v_mul_f32 v85, v85, v112\n"
    "v_add_f32 v113, 1.0, v113\n"
    "v_mul_f32 v114, v87, v114\n"
    "v_cvt_pk_bf16_f32 v118, v84, v85\n"
    "v_rcp_f32 v113, v113\n"
    "v_mfma_f32_32x32x16_bf16 v[48:63], v[100:103], a[204:207], v[48:63]\n"
    "s_waitcnt lgkmcnt(5)\n"
    "v_mul_f32 v88, v88, v123\n"
    "v_exp_f32 v114, v114\n"
    "v_fma_f32 v112, -v88, v88, s87\n"
    "v_mul_f32 v86, v86, v113\n"
    "v_add_f32 v114, 1.0, v114\n"
    "v_mfma_f32_32x32x16_bf16 v[32:47], v[104:107], a[208:211], v[32:47]\n"
    "ds_read_b64_tr_b16 v[100:101], v12 offset:59392\n"
    "ds_read_b64_tr_b16 v[102:103], v12 offset:59904\n"
    "v_mul_f32 v112, v88, v112\n"
    "v_rcp_f32 v114, v114\n"
    "v_mul_f32 v89, v89, v123\n"
    "v_mfma_f32_32x32x16_bf16 v[48:63], v[104:107], a[212:215], v[48:63]\n"
    "s_waitcnt lgkmcnt(4)\n"
    "buffer_load_dwordx4 v10, s[36:39], s85 offen lds\n"
    "buffer_load_dwordx4 v11, s[36:39], s85 offen offset:1024 lds\n"
    "v_exp_f32 v112, v112\n"
    "v_mfma_f32_32x32x16_bf16 v[32:47], v[108:111], a[216:219], v[32:47]\n"
    "ds_read_b64_tr_b16 v[104:105], v12 offset:61440\n"
    "ds_read_b64_tr_b16 v[106:107], v12 offset:61952\n"
    "v_fma_f32 v113, -v89, v89, s87\n"
    "v_mul_f32 v87, v87, v114\n"
    "v_add_f32 v112, 1.0, v112\n"
    "v_mul_f32 v113, v89, v113\n"
    "v_mfma_f32_32x32x16_bf16 v[48:63], v[108:111], a[220:223], v[48:63]\n"
    "s_waitcnt lgkmcnt(4)\n"
    "v_cvt_pk_bf16_f32 v119, v86, v87\n"
    "v_rcp_f32 v112, v112\n"
    "v_mfma_f32_32x32x16_bf16 v[32:47], v[96:99], a[224:227], v[32:47]\n"
    "s_add_u32 s85, s69, s86\n"
    "ds_read_b64_tr_b16 v[108:109], v12 offset:63488\n"
    "ds_read_b64_tr_b16 v[110:111], v12 offset:64000\n"
    "v_mul_f32 v90, v90, v123\n"
    "v_exp_f32 v113, v113\n"
    "v_fma_f32 v114, -v90, v90, s87\n"
    "v_mul_f32 v88, v88, v112\n"
    "v_mfma_f32_32x32x16_bf16 v[48:63], v[96:99], a[228:231], v[48:63]\n"
    "s_waitcnt vmcnt(13) lgkmcnt(4)\n"
    "s_barrier\n"
    "v_add_f32 v113, 1.0, v113\n"
    "v_mul_f32 v114, v90, v114\n"
    "v_rcp_f32 v113, v113\n"
    "v_mfma_f32_32x32x16_bf16 v[32:47], v[100:103], a[232:235], v[32:47]\n"
    "s_add_u32 m0, s89, 40960\n"
    "ds_read_b64_tr_b16 v[96:97], v13 offset:0\n"
    "ds_read_b64_tr_b16 v[98:99], v13 offset:512\n"
    "v_mul_f32 v91, v91, v123\n"
    "v_exp_f32 v114, v114\n"
    "v_fma_f32 v112, -v91, v91, s87\n"
    "v_mfma_f32_32x32x16_bf16 v[48:63], v[100:103], a[236:239], v[48:63]\n"
    "s_waitcnt lgkmcnt(4)\n"
    "v_mul_f32 v89, v89, v113\n"
    "v_add_f32 v114, 1.0, v114\n"
    "v_mul_f32 v112, v91, v112\n"
    "v_cvt_pk_bf16_f32 v116, v88, v89\n"
    "v_rcp_f32 v114, v114\n"
    "v_mfma_f32_32x32x16_bf16 v[32:47], v[104:107], a[240:243], v[32:47]\n"
    "ds_read_b64_tr_b16 v[100:101], v13 offset:2048\n"
    "ds_read_b64_tr_b16 v[102:103], v13 offset:2560\n"
    "v_mul_f32 v92, v92, v123\n"
    "v_exp_f32 v112, v112\n"
    "v_fma_f32 v113, -v92, v92, s87\n"
    "v_mfma_f32_32x32x16_bf16 v[48:63], v[104:107], a[244:247], v[48:63]\n"
    "s_waitcnt lgkmcnt(4)\n"
    "buffer_load_dwordx4 v10, s[36:39], s85 offen lds\n"
    "buffer_load_dwordx4 v11, s[36:39], s85 offen offset:1024 lds\n"
    "v_mul_f32 v90, v90, v114\n"
    "v_add_f32 v112, 1.0, v112\n"
    "v_mfma_f32_32x32x16_bf16 v[32:47], v[108:111], a[248:251], v[32:47]\n"
    "ds_read_b64_tr_b16 v[104:105], v13 offset:4096\n"
    "ds_read_b64_tr_b16 v[106:107], v13 offset:4608\n"
    "v_mul_f32 v113, v92, v113\n"
    "v_rcp_f32 v112, v112\n"
    "v_mul_f32 v93, v93, v123\n"
    "v_exp_f32 v113, v113\n"
    "v_mfma_f32_32x32x16_bf16 v[48:63], v[108:111], a[252:255], v[48:63]\n"
    "s_waitcnt lgkmcnt(4)\n"
    "ds_write_b64 v15, v[118:119] offset:8\n"
    "v_fma_f32 v114, -v93, v93, s87\n"
    "v_mul_f32 v91, v91, v112\n"
    "v_add_f32 v113, 1.0, v113\n"
    "v_mfma_f32_32x32x16_bf16 v[32:47], v[96:99], v[224:227], v[32:47]\n"
    "s_add_u32 s85, s70, s86\n"
    "ds_read_b64_tr_b16 v[108:109], v13 offset:6144\n"
    "ds_read_b64_tr_b16 v[110:111], v13 offset:6656\n"
    "v_mul_f32 v114, v93, v114\n"
    "v_cvt_pk_bf16_f32 v117, v90, v91\n"
    "v_rcp_f32 v113, v113\n"
    "v_mfma_f32_32x32x16_bf16 v[48:63], v[96:99], v[228:231], v[48:63]\n"
    "s_waitcnt vmcnt(10) lgkmcnt(5)\n"
    "s_barrier\n"
    "v_mul_f32 v94, v94, v123\n"
    "v_exp_f32 v114, v114\n"
    "v_mfma_f32_32x32x16_bf16 v[32:47], v[100:103], v[232:235], v[32:47]\n"
    "s_add_u32 m0, s89, 49152\n"
    "ds_read_b64_tr_b16 v[96:97], v12 offset:0\n"
    "ds_read_b64_tr_b16 v[98:99], v12 offset:512\n"
    "v_fma_f32 v112, -v94, v94, s87\n"
    "v_mul_f32 v92, v92, v113\n"
    "v_add_f32 v114, 1.0, v114\n"
    "v_mul_f32 v112, v94, v112\n"
    "v_mfma_f32_32x32x16_bf16 v[48:63], v[100:103], v[236:239], v[48:63]\n"
    "s_waitcnt lgkmcnt(5)\n"
    "v_rcp_f32 v114, v114\n"
    "v_mul_f32 v95, v95, v123\n"
    "v_exp_f32 v112, v112\n"
    "v_fma_f32 v113, -v95, v95, s87\n"
    "v_mfma_f32_32x32x16_bf16 v[32:47], v[104:107], v[240:243], v[32:47]\n"
    "ds_read_b64_tr_b16 v[100:101], v12 offset:2048\n"
    "ds_read_b64_tr_b16 v[102:103], v12 offset:2560\n"
    "v_mul_f32 v93, v93, v114\n"
    "v_add_f32 v112, 1.0, v112\n"
    "v_mul_f32 v113, v95, v113\n"
    "v_cvt_pk_bf16_f32 v118, v92, v93\n"
    "v_rcp_f32 v112, v112\n"
    "v_mfma_f32_32x32x16_bf16 v[48:63], v[104:107], v[244:247], v[48:63]\n"
    "s_waitcnt lgkmcnt(4)\n"
    "buffer_load_dwordx4 v10, s[36:39], s85 offen lds\n"
    "buffer_load_dwordx4 v11, s[36:39], s85 offen offset:1024 lds\n"
    "v_exp_f32 v113, v113\n"
    "v_mfma_f32_32x32x16_bf16 v[32:47], v[108:111], v[248:251], v[32:47]\n"
    "s_waitcnt vmcnt(8)\n"
    "ds_read_b64_tr_b16 v[104:105], v12 offset:4096\n"
    "ds_read_b64_tr_b16 v[106:107], v12 offset:4608\n"
    "v_cmp_ne_u32 vcc, s56, v121\n"
    "v_mov_b32 v124, v120\n"
    "s_cbranch_vccnz .LL_CSRETRY1_%=\n"
    ".LL_CSOK1_%=:\n"
    "v_mul_f32 v94, v94, v112\n"
    "v_mfma_f32_32x32x16_bf16 v[48:63], v[108:111], v[252:255], v[48:63]\n"
    "s_waitcnt lgkmcnt(4)\n"
    "ds_write_b64 v15, v[116:117] offset:512\n"
    "v_add_f32 v113, 1.0, v113\n"
    "v_rcp_f32 v113, v113\n"
    "v_mfma_f32_32x32x16_bf16 v[64:79], v[124:127], v[128:131], v[32:47]\n"
    "s_sub_u32 s88, s88, 1\n"
    "s_nop 0\n"
    "v_mul_f32 v95, v95, v113\n"
    "v_cvt_pk_bf16_f32 v119, v94, v95\n"
    "v_mfma_f32_32x32x16_bf16 v[80:95], v[124:127], v[132:135], v[48:63]\n"
    "s_cmp_lg_u32 s88, 0\n"
    "ds_write_b64 v15, v[118:119] offset:520\n"
    "v_add_u32 v14, 0x4000, v14\n"
    "v_add_u32 v15, 0x4000, v15\n"
    "s_cbranch_scc1 .LL_STEP1_%=\n"
    "v_add_u32 v12, 81920, v23\n"
    "v_add_u32 v13, 147456, v23\n"
    "v_mov_b32 v156, s87\n"
    "v_mov_b32 v158, v122\n"
    "v_mov_b32 v160, v123\n"
    "ds_read_b128 a[0:3], v12 offset:0\n"
    "ds_read_b128 a[4:7], v12 offset:1024\n"
    "ds_read_b128 a[8:11], v12 offset:2048\n"
    "ds_read_b128 a[12:15], v12 offset:3072\n"
    "v_add_u32 v16, 65536, v23\n"
    "v_subrev_u32 v14, 98304, v14\n"
    "v_subrev_u32 v15, 98304, v15\n"
    "s_mov_b32 s86, 128\n"
    "s_mov_b32 s92, 81920\n"
    "v_pk_mul_f32 v[64:65], v[64:65], v[158:159] op_sel_hi:[1,0]\n"
    "v_pk_mul_f32 v[66:67], v[66:67], v[158:159] op_sel_hi:[1,0]\n"
    "v_pk_mul_f32 v[68:69], v[68:69], v[158:159] op_sel_hi:[1,0]\n"
    "v_pk_mul_f32 v[70:71], v[70:71], v[158:159] op_sel_hi:[1,0]\n"
    "v_pk_fma_f32 v[32:33], v[64:65], v[64:65], v[156:157] op_sel_hi:[1,1,0] neg_lo:[1,0,0] neg_hi:[1,0,0]\n"
    "v_pk_fma_f32 v[34:35], v[66:67], v[66:67], v[156:157] op_sel_hi:[1,1,0] neg_lo:[1,0,0] neg_hi:[1,0,0]\n"
    "v_pk_fma_f32 v[36:37], v[68:69], v[68:69], v[156:157] op_sel_hi:[1,1,0] neg_lo:[1,0,0] neg_hi:[1,0,0]\n"
    "v_pk_fma_f32 v[38:39], v[70:71], v[70:71], v[156:157] op_sel_hi:[1,1,0] neg_lo:[1,0,0] neg_hi:[1,0,0]\n"
    "v_pk_mul_f32 v[32:33], v[64:65], v[32:33]\n"
    "v_pk_mul_f32 v[34:35], v[66:67], v[34:35]\n"
    "v_pk_mul_f32 v[36:37], v[68:69], v[36:37]\n"
    "v_pk_mul_f32 v[38:39], v[70:71], v[38:39]\n"
    "s_mov_b64 s[48:49], %9\n"
    "s_mov_b32 s50, 1152\n"
    "s_mov_b32 s51, 0x20000\n"
    "v_lshrrev_b32 v3, 4, v1\n"
    "v_and_b32 v2, 3, v1\n"
    "v_lshl_add_u32 v4, v3, 2, v2\n"
    "v_lshlrev_b32 v4, 4, v4\n"
    "v_add_u32 v5, v4, v171\n"
    "ds_read_b128 a[16:19], v12 offset:8192\n"
    "ds_read_b128 a[20:23], v12 offset:9216\n"
    "ds_read_b128 a[24:27], v12 offset:10240\n"
    "ds_read_b128 a[28:31], v12 offset:11264\n"
    "buffer_load_dwordx4 v[132:135], v4, s[48:51], 0 offen offset:0\n"
    "buffer_load_dwordx4 v[136:139], v4, s[48:51], 0 offen offset:256\n"
    "v_exp_f32 v32, v32\n"
    "v_exp_f32 v33, v33\n"
    "v_exp_f32 v34, v34\n"
    "v_exp_f32 v35, v35\n"
    "v_exp_f32 v36, v36\n"
    "v_exp_f32 v37, v37\n"
    "v_exp_f32 v38, v38\n"
    "v_exp_f32 v39, v39\n"
    "v_pk_add_f32 v[32:33], v[32:33], 1.0 op_sel_hi:[1,0]\n"
    "v_pk_add_f32 v[34:35], v[34:35], 1.0 op_sel_hi:[1,0]\n"
    "v_pk_add_f32 v[36:37], v[36:37], 1.0 op_sel_hi:[1,0]\n"
    "v_pk_add_f32 v[38:39], v[38:39], 1.0 op_sel_hi:[1,0]\n"
    "ds_read_b128 a[32:35], v12 offset:16384\n"
    "ds_read_b128 a[36:39], v12 offset:17408\n"
    "ds_read_b128 a[40:43], v12 offset:18432\n"
    "ds_read_b128 a[44:47], v12 offset:19456\n"
    "buffer_load_dwordx4 v[140:143], v4, s[48:51], 0 offen offset:512\n"
    "buffer_load_dwordx4 v[144:147], v4, s[48:51], 0 offen offset:768\n"
    "v_rcp_f32 v32, v32\n"
    "v_rcp_f32 v33, v33\n"
    "v_rcp_f32 v34, v34\n"
    "v_rcp_f32 v35, v35\n"
    "v_rcp_f32 v36, v36\n"
    "v_rcp_f32 v37, v37\n"
    "v_rcp_f32 v38, v38\n"
    "v_rcp_f32 v39, v39\n"
    "v_pk_mul_f32 v[64:65], v[64:65], v[32:33]\n"
    "v_pk_mul_f32 v[66:67], v[66:67], v[34:35]\n"
    "v_pk_mul_f32 v[68:69], v[68:69], v[36:37]\n"
    "v_pk_mul_f32 v[70:71], v[70:71], v[38:39]\n"
    "ds_read_b128 a[48:51], v12 offset:24576\n"
    "ds_read_b128 a[52:55], v12 offset:25600\n"
    "ds_read_b128 a[56:59], v12 offset:26624\n"
    "ds_read_b128 a[60:63], v12 offset:27648\n"
    "buffer_load_dwordx4 v[148:151], v4, s[48:51], 0 offen offset:1024\n"
    "buffer_load_dwordx4 v[112:115], v5, s[40:43], 0 offen offset:0\n"
    "v_cvt_pk_bf16_f32 v64, v64, v65\n"
    "v_cvt_pk_bf16_f32 v65, v66, v67\n"
    "v_cvt_pk_bf16_f32 v66, v68, v69\n"
    "v_cvt_pk_bf16_f32 v67, v70, v71\n"
    "v_pk_mul_f32 v[72:73], v[72:73], v[158:159] op_sel_hi:[1,0]\n"
    "v_pk_mul_f32 v[74:75], v[74:75], v[158:159] op_sel_hi:[1,0]\n"
    "v_pk_mul_f32 v[76:77], v[76:77], v[158:159] op_sel_hi:[1,0]\n"
    "v_pk_mul_f32 v[78:79], v[78:79], v[158:159] op_sel_hi:[1,0]\n"
    "v_pk_fma_f32 v[40:41], v[72:73], v[72:73], v[156:157] op_sel_hi:[1,1,0] neg_lo:[1,0,0] neg_hi:[1,0,0]\n"
    "v_pk_fma_f32 v[42:43], v[74:75], v[74:75], v[156:157] op_sel_hi:[1,1,0] neg_lo:[1,0,0] neg_hi:[1,0,0]\n"
    "v_pk_fma_f32 v[44:45], v[76:77], v[76:77], v[156:157] op_sel_hi:[1,1,0] neg_lo:[1,0,0] neg_hi:[1,0,0]\n"
    "v_pk_fma_f32 v[46:47], v[78:79], v[78:79], v[156:157] op_sel_hi:[1,1,0] neg_lo:[1,0,0] neg_hi:[1,0,0]\n"
    "v_pk_mul_f32 v[40:41], v[72:73], v[40:41]\n"
    "v_pk_mul_f32 v[42:43], v[74:75], v[42:43]\n"
    "v_pk_mul_f32 v[44:45], v[76:77], v[44:45]\n"
    "v_pk_mul_f32 v[46:47], v[78:79], v[46:47]\n"
    "s_waitcnt lgkmcnt(15)\n"
    "s_barrier\n"
    "ds_read_b128 a[64:67], v12 offset:32768\n"
    "ds_read_b128 a[68:71], v12 offset:33792\n"
    "ds_read_b128 a[72:75], v12 offset:34816\n"
    "ds_read_b128 a[76:79], v12 offset:35840\n"
    "buffer_load_dwordx4 v[116:119], v5, s[40:43], 0 offen offset:256\n"
    "buffer_load_dwordx4 v[120:123], v5, s[40:43], 0 offen offset:512\n"
    "v_exp_f32 v40, v40\n"
    "v_exp_f32 v41, v41\n"
    "v_exp_f32 v42, v42\n"
    "v_exp_f32 v43, v43\n"
    "v_exp_f32 v44, v44\n"
    "v_exp_f32 v45, v45\n"
    "v_exp_f32 v46, v46\n"
    "v_exp_f32 v47, v47\n"
    "v_pk_add_f32 v[40:41], v[40:41], 1.0 op_sel_hi:[1,0]\n"
    "v_pk_add_f32 v[42:43], v[42:43], 1.0 op_sel_hi:[1,0]\n"
    "v_pk_add_f32 v[44:45], v[44:45], 1.0 op_sel_hi:[1,0]\n"
    "v_pk_add_f32 v[46:47], v[46:47], 1.0 op_sel_hi:[1,0]\n"
    "ds_read_b128 a[80:83], v12 offset:40960\n"
    "ds_read_b128 a[84:87], v12 offset:41984\n"
    "ds_read_b128 a[88:91], v12 offset:43008\n"
    "ds_read_b128 a[92:95], v12 offset:44032\n"
    "buffer_load_dwordx4 v[124:127], v5, s[40:43], 0 offen offset:768\n"
    "v_rcp_f32 v40, v40\n"
    "v_rcp_f32 v41, v41\n"
    "v_rcp_f32 v42, v42\n"
    "v_rcp_f32 v43, v43\n"
    "v_rcp_f32 v44, v44\n"
    "v_rcp_f32 v45, v45\n"
    "v_rcp_f32 v46, v46\n"
    "v_rcp_f32 v47, v47\n"
    "v_pk_mul_f32 v[72:73], v[72:73], v[40:41]\n"
    "v_pk_mul_f32 v[74:75], v[74:75], v[42:43]\n"
    "v_pk_mul_f32 v[76:77], v[76:77], v[44:45]\n"
    "v_pk_mul_f32 v[78:79], v[78:79], v[46:47]\n"
    "v_cmp_gt_u32 vcc, 32, v1\n"
    "v_mov_b32 v4, 1879048192\n"
    "s_and_b32 s30, s83, 1\n"
    "v_cndmask_b32 v5, v4, v5, vcc\n"
    "s_mov_b64 s[48:49], %8\n"
    "ds_read_b128 a[96:99], v12 offset:49152\n"
    "ds_read_b128 a[100:103], v12 offset:50176\n"
    "ds_read_b128 a[104:107], v12 offset:51200\n"
    "ds_read_b128 a[108:111], v12 offset:52224\n"
    "buffer_load_dwordx4 v[128:131], v5, s[40:43], 0 offen offset:1024\n"
    "v_cvt_pk_bf16_f32 v68, v72, v73\n"
    "v_cvt_pk_bf16_f32 v69, v74, v75\n"
    "v_cvt_pk_bf16_f32 v70, v76, v77\n"
    "v_cvt_pk_bf16_f32 v71, v78, v79\n"
    "v_pk_mul_f32 v[80:81], v[80:81], v[160:161] op_sel_hi:[1,0]\n"
    "v_pk_mul_f32 v[82:83], v[82:83], v[160:161] op_sel_hi:[1,0]\n"
    "v_pk_mul_f32 v[84:85], v[84:85], v[160:161] op_sel_hi:[1,0]\n"
    "v_pk_mul_f32 v[86:87], v[86:87], v[160:161] op_sel_hi:[1,0]\n"
    "v_pk_fma_f32 v[32:33], v[80:81], v[80:81], v[156:157] op_sel_hi:[1,1,0] neg_lo:[1,0,0] neg_hi:[1,0,0]\n"
    "v_pk_fma_f32 v[34:35], v[82:83], v[82:83], v[156:157] op_sel_hi:[1,1,0] neg_lo:[1,0,0] neg_hi:[1,0,0]\n"
    "v_pk_fma_f32 v[36:37], v[84:85], v[84:85], v[156:157] op_sel_hi:[1,1,0] neg_lo:[1,0,0] neg_hi:[1,0,0]\n"
    "v_pk_fma_f32 v[38:39], v[86:87], v[86:87], v[156:157] op_sel_hi:[1,1,0] neg_lo:[1,0,0] neg_hi:[1,0,0]\n"
    "v_pk_mul_f32 v[32:33], v[80:81], v[32:33]\n"
    "v_pk_mul_f32 v[34:35], v[82:83], v[34:35]\n"
    "v_pk_mul_f32 v[36:37], v[84:85], v[36:37]\n"
    "v_pk_mul_f32 v[38:39], v[86:87], v[38:39]\n"
    "s_lshl_b32 s30, s30, 6\n"
    "v_lshlrev_b32 v2, 3, v2\n"
    "v_and_b32 v4, 1, v3\n"
    "v_lshl_add_u32 v2, v4, 5, v2\n"
    "v_lshrrev_b32 v4, 1, v3\n"
    "v_lshl_add_u32 v2, v4, 7, v2\n"
    "v_add_u32 v2, s30, v2\n"
    "v_add_u32 v171, v2, v171\n"
    "ds_read_b128 a[112:115], v12 offset:57344\n"
    "ds_read_b128 a[116:119], v12 offset:58368\n"
    "ds_read_b128 a[120:123], v12 offset:59392\n"
    "ds_read_b128 a[124:127], v12 offset:60416\n"
    "buffer_load_dwordx2 v[200:201], v2, s[48:51], 0 offen offset:0\n"
    "buffer_load_dwordx2 v[202:203], v2, s[48:51], 0 offen offset:256\n"
    "v_exp_f32 v32, v32\n"
    "v_exp_f32 v33, v33\n"
    "v_exp_f32 v34, v34\n"
    "v_exp_f32 v35, v35\n"
    "v_exp_f32 v36, v36\n"
    "v_exp_f32 v37, v37\n"
    "v_exp_f32 v38, v38\n"
    "v_exp_f32 v39, v39\n"
    "v_pk_add_f32 v[32:33], v[32:33], 1.0 op_sel_hi:[1,0]\n"
    "v_pk_add_f32 v[34:35], v[34:35], 1.0 op_sel_hi:[1,0]\n"
    "v_pk_add_f32 v[36:37], v[36:37], 1.0 op_sel_hi:[1,0]\n"
    "v_pk_add_f32 v[38:39], v[38:39], 1.0 op_sel_hi:[1,0]\n"
    "ds_read_b128 a[128:131], v13 offset:0\n"
    "ds_read_b128 a[132:135], v13 offset:1024\n"
    "ds_read_b128 a[136:139], v13 offset:2048\n"
    "ds_read_b128 a[140:143], v13 offset:3072\n"
    "buffer_load_dwordx2 v[204:205], v2, s[48:51], 0 offen offset:512\n"
    "buffer_load_dwordx2 v[206:207], v2, s[48:51], 0 offen offset:768\n"
    "v_rcp_f32 v32, v32\n"
    "v_rcp_f32 v33, v33\n"
    "v_rcp_f32 v34, v34\n"
    "v_rcp_f32 v35, v35\n"
    "v_rcp_f32 v36, v36\n"
    "v_rcp_f32 v37, v37\n"
    "v_rcp_f32 v38, v38\n"
    "v_rcp_f32 v39, v39\n"
    "v_pk_mul_f32 v[80:81], v[80:81], v[32:33]\n"
    "v_pk_mul_f32 v[82:83], v[82:83], v[34:35]\n"
    "v_pk_mul_f32 v[84:85], v[84:85], v[36:37]\n"
    "v_pk_mul_f32 v[86:87], v[86:87], v[38:39]\n"
    "ds_read_b128 a[144:147], v13 offset:8192\n"
    "ds_read_b128 a[148:151], v13 offset:9216\n"
    "ds_read_b128 a[152:155], v13 offset:10240\n"
    "ds_read_b128 a[156:159], v13 offset:11264\n"
    "buffer_load_dwordx2 v[208:209], v2, s[48:51], 0 offen offset:1024\n"
    "v_cvt_pk_bf16_f32 v72, v80, v81\n"
    "v_cvt_pk_bf16_f32 v73, v82, v83\n"
    "v_cvt_pk_bf16_f32 v74, v84, v85\n"
    "v_cvt_pk_bf16_f32 v75, v86, v87\n"
    "v_pk_mul_f32 v[88:89], v[88:89], v[160:161] op_sel_hi:[1,0]\n"
    "v_pk_mul_f32 v[90:91], v[90:91], v[160:161] op_sel_hi:[1,0]\n"
    "v_pk_mul_f32 v[92:93], v[92:93], v[160:161] op_sel_hi:[1,0]\n"
    "v_pk_mul_f32 v[94:95], v[94:95], v[160:161] op_sel_hi:[1,0]\n"
    "v_pk_fma_f32 v[40:41], v[88:89], v[88:89], v[156:157] op_sel_hi:[1,1,0] neg_lo:[1,0,0] neg_hi:[1,0,0]\n"
    "v_pk_fma_f32 v[42:43], v[90:91], v[90:91], v[156:157] op_sel_hi:[1,1,0] neg_lo:[1,0,0] neg_hi:[1,0,0]\n"
    "v_pk_fma_f32 v[44:45], v[92:93], v[92:93], v[156:157] op_sel_hi:[1,1,0] neg_lo:[1,0,0] neg_hi:[1,0,0]\n"
    "v_pk_fma_f32 v[46:47], v[94:95], v[94:95], v[156:157] op_sel_hi:[1,1,0] neg_lo:[1,0,0] neg_hi:[1,0,0]\n"
    "v_pk_mul_f32 v[40:41], v[88:89], v[40:41]\n"
    "v_pk_mul_f32 v[42:43], v[90:91], v[42:43]\n"
    "v_pk_mul_f32 v[44:45], v[92:93], v[44:45]\n"
    "v_pk_mul_f32 v[46:47], v[94:95], v[46:47]\n"
    "ds_read_b64_tr_b16 v[96:97], v21 offset:0\n"
    "ds_read_b64_tr_b16 v[98:99], v21 offset:1024\n"
    "ds_read_b64_tr_b16 v[100:101], v21 offset:4096\n"
    "ds_read_b64_tr_b16 v[102:103], v21 offset:5120\n"
    "v_exp_f32 v40, v40\n"
    "v_exp_f32 v41, v41\n"
    "v_exp_f32 v42, v42\n"
    "v_exp_f32 v43, v43\n"
    "v_exp_f32 v44, v44\n"
    "v_exp_f32 v45, v45\n"
    "v_exp_f32 v46, v46\n"
    "v_exp_f32 v47, v47\n"
    "v_pk_add_f32 v[40:41], v[40:41], 1.0 op_sel_hi:[1,0]\n"
    "v_pk_add_f32 v[42:43], v[42:43], 1.0 op_sel_hi:[1,0]\n"
    "v_pk_add_f32 v[44:45], v[44:45], 1.0 op_sel_hi:[1,0]\n"
    "v_pk_add_f32 v[46:47], v[46:47], 1.0 op_sel_hi:[1,0]\n"
    "v_rcp_f32 v40, v40\n"
    "v_rcp_f32 v41, v41\n"
    "v_rcp_f32 v42, v42\n"
    "v_rcp_f32 v43, v43\n"
    "v_rcp_f32 v44, v44\n"
    "v_rcp_f32 v45, v45\n"
    "v_rcp_f32 v46, v46\n"
    "v_rcp_f32 v47, v47\n"
    "v_pk_mul_f32 v[88:89], v[88:89], v[40:41]\n"
    "v_pk_mul_f32 v[90:91], v[90:91], v[42:43]\n"
    "v_pk_mul_f32 v[92:93], v[92:93], v[44:45]\n"
    "v_pk_mul_f32 v[94:95], v[94:95], v[46:47]\n"
    "v_cvt_pk_bf16_f32 v76, v88, v89\n"
    "v_cvt_pk_bf16_f32 v77, v90, v91\n"
    "v_cvt_pk_bf16_f32 v78, v92, v93\n"
    "v_cvt_pk_bf16_f32 v79, v94, v95\n"
    "s_waitcnt lgkmcnt(0)\n"
    "s_barrier\n"
    "s_add_u32 s85, s64, s86\n"
    "s_add_u32 m0, s89, 81920\n"
    "v_mfma_f32_16x16x32_bf16 v[32:35], v[96:99], a[0:3], 0\n"
    "ds_read_b64_tr_b16 v[104:105], v22 offset:0\n"
    "v_and_b32 v184, 15, v1\n"
    "v_mfma_f32_16x16x32_bf16 v[32:35], v[100:103], a[16:19], v[32:35]\n"
    "buffer_load_dwordx4 v10, s[36:39], s85 offen lds\n"
    "v_mfma_f32_16x16x32_bf16 v[36:39], v[96:99], a[4:7], 0\n"
    "ds_read_b64_tr_b16 v[106:107], v22 offset:1024\n"
    "v_lshrrev_b32 v185, 4, v1\n"
    "v_mfma_f32_16x16x32_bf16 v[36:39], v[100:103], a[20:23], v[36:39]\n"
    "ds_read_b64_tr_b16 v[108:109], v22 offset:4096\n"
    "v_lshrrev_b32 v6, 2, v184\n"
    "v_mfma_f32_16x16x32_bf16 v[40:43], v[96:99], a[8:11], 0\n"
    "ds_read_b64_tr_b16 v[110:111], v22 offset:5120\n"
    "v_lshlrev_b32 v6, 8, v6\n"
    "v_mfma_f32_16x16x32_bf16 v[40:43], v[100:103], a[24:27], v[40:43]\n"
    "buffer_load_dwordx4 v11, s[36:39], s85 offen offset:1024 lds\n"
    "v_mfma_f32_16x16x32_bf16 v[44:47], v[96:99], a[12:15], 0\n"
    "s_add_u32 s85, s65, s86\n"
    "s_add_u32 m0, s89, 90112\n"
    "ds_write_b64 v14, v[64:65] offset:0\n"
    "v_mfma_f32_16x16x32_bf16 v[44:47], v[100:103], a[28:31], v[44:47]\n"
    "s_waitcnt lgkmcnt(3)\n"
    "ds_write_b64 v14, v[66:67] offset:8\n"
    "v_mfma_f32_16x16x32_bf16 v[48:51], v[104:107], a[0:3], 0\n"
    "s_waitcnt lgkmcnt(2)\n"
    "ds_read_b64_tr_b16 v[96:97], v21 offset:8192\n"
    "v_and_b32 v184, 3, v184\n"
    "v_mfma_f32_16x16x32_bf16 v[48:51], v[108:111], a[16:19], v[48:51]\n"
    "buffer_load_dwordx4 v10, s[36:39], s85 offen lds\n"
    "v_mfma_f32_16x16x32_bf16 v[52:55], v[104:107], a[4:7], 0\n"
    "ds_read_b64_tr_b16 v[98:99], v21 offset:9216\n"
    "v_lshl_add_u32 v6, v184, 4, v6\n"
    "v_mfma_f32_16x16x32_bf16 v[52:55], v[108:111], a[20:23], v[52:55]\n"
    "ds_read_b64_tr_b16 v[100:101], v21 offset:12288\n"
    "v_lshl_add_u32 v6, v185, 6, v6\n"
    "v_mfma_f32_16x16x32_bf16 v[56:59], v[104:107], a[8:11], 0\n"
    "ds_read_b64_tr_b16 v[102:103], v21 offset:13312\n"
    "v_add_u32 v7, 294912, v6\n"
    "v_mfma_f32_16x16x32_bf16 v[56:59], v[108:111], a[24:27], v[56:59]\n"
    "buffer_load_dwordx4 v11, s[36:39], s85 offen offset:1024 lds\n"
    "v_mfma_f32_16x16x32_bf16 v[60:63], v[104:107], a[12:15], 0\n"
    "s_add_u32 s85, s66, s86\n"
    "s_add_u32 m0, s89, 98304\n"
    "ds_write_b64 v14, v[68:69] offset:512\n"
    "v_mfma_f32_16x16x32_bf16 v[60:63], v[108:111], a[28:31], v[60:63]\n"
    "s_waitcnt lgkmcnt(3)\n"
    "ds_write_b64 v14, v[70:71] offset:520\n"
    "v_mfma_f32_16x16x32_bf16 v[32:35], v[96:99], a[32:35], v[32:35]\n"
    "s_waitcnt lgkmcnt(2)\n"
    "ds_read_b64_tr_b16 v[104:105], v22 offset:8192\n"
    "v_add_u32 v8, 589824, v6\n"
    "v_mfma_f32_16x16x32_bf16 v[32:35], v[100:103], a[48:51], v[32:35]\n"
    "buffer_load_dwordx4 v10, s[36:39], s85 offen lds\n"
    "v_mfma_f32_16x16x32_bf16 v[36:39], v[96:99], a[36:39], v[36:39]\n"
    "ds_read_b64_tr_b16 v[106:107], v22 offset:9216\n"
    "v_add_u32 v9, 884736, v6\n"
    "v_mfma_f32_16x16x32_bf16 v[36:39], v[100:103], a[52:55], v[36:39]\n"
    "ds_read_b64_tr_b16 v[108:109], v22 offset:12288\n"
    "s_mov_b32 s88, 8\n"
    "v_mfma_f32_16x16x32_bf16 v[40:43], v[96:99], a[40:43], v[40:43]\n"
    "ds_read_b64_tr_b16 v[110:111], v22 offset:13312\n"
    "s_mov_b32 s58, 1879048192\n"
    "v_mfma_f32_16x16x32_bf16 v[40:43], v[100:103], a[56:59], v[40:43]\n"
    "buffer_load_dwordx4 v11, s[36:39], s85 offen offset:1024 lds\n"
    "v_mfma_f32_16x16x32_bf16 v[44:47], v[96:99], a[44:47], v[44:47]\n"
    "s_add_u32 s85, s67, s86\n"
    "s_add_u32 m0, s89, 106496\n"
    "ds_write_b64 v15, v[72:73] offset:0\n"
    "v_mfma_f32_16x16x32_bf16 v[44:47], v[100:103], a[60:63], v[44:47]\n"
    "s_waitcnt vmcnt(27) lgkmcnt(1)\n"
    "s_barrier\n"
    "v_mfma_f32_16x16x32_bf16 v[48:51], v[104:107], a[32:35], v[48:51]\n"
    "s_waitcnt lgkmcnt(1)\n"
    "ds_read_b64_tr_b16 v[96:97], v21 offset:16384\n"
    "s_mov_b32 s59, 1879048192\n"
    "v_mfma_f32_16x16x32_bf16 v[48:51], v[108:111], a[48:51], v[48:51]\n"
    "buffer_load_dwordx4 v10, s[36:39], s85 offen lds\n"
    "v_mfma_f32_16x16x32_bf16 v[52:55], v[104:107], a[36:39], v[52:55]\n"
    "ds_read_b64_tr_b16 v[98:99], v21 offset:17408\n"
    "s_mov_b32 s60, 1879048192\n"
    "v_mfma_f32_16x16x32_bf16 v[52:55], v[108:111], a[52:55], v[52:55]\n"
    "ds_read_b64_tr_b16 v[100:101], v21 offset:20480\n"
    "s_mov_b64 s[62:63], 0\n"
    "v_mfma_f32_16x16x32_bf16 v[56:59], v[104:107], a[40:43], v[56:59]\n"
    "ds_read_b64_tr_b16 v[102:103], v21 offset:21504\n"
    "v_mfma_f32_16x16x32_bf16 v[56:59], v[108:111], a[56:59], v[56:59]\n"
    "buffer_load_dwordx4 v11, s[36:39], s85 offen offset:1024 lds\n"
    "v_mfma_f32_16x16x32_bf16 v[60:63], v[104:107], a[44:47], v[60:63]\n"
    "s_add_u32 s85, s68, s86\n"
    "s_add_u32 m0, s89, 114688\n"
    "ds_write_b64 v15, v[74:75] offset:8\n"
    "v_mfma_f32_16x16x32_bf16 v[60:63], v[108:111], a[60:63], v[60:63]\n"
    "s_waitcnt lgkmcnt(3)\n"
    "ds_write_b64 v15, v[76:77] offset:512\n"
    "v_mfma_f32_16x16x32_bf16 v[32:35], v[96:99], a[64:67], v[32:35]\n"
    "s_waitcnt lgkmcnt(2)\n"
    "ds_read_b64_tr_b16 v[104:105], v22 offset:16384\n"
    "v_mfma_f32_16x16x32_bf16 v[32:35], v[100:103], a[80:83], v[32:35]\n"
    "buffer_load_dwordx4 v10, s[36:39], s85 offen lds\n"
    "v_mfma_f32_16x16x32_bf16 v[36:39], v[96:99], a[68:71], v[36:39]\n"
    "ds_read_b64_tr_b16 v[106:107], v22 offset:17408\n"
    "v_mfma_f32_16x16x32_bf16 v[36:39], v[100:103], a[84:87], v[36:39]\n"
    "ds_read_b64_tr_b16 v[108:109], v22 offset:20480\n"
    "v_mfma_f32_16x16x32_bf16 v[40:43], v[96:99], a[72:75], v[40:43]\n"
    "ds_read_b64_tr_b16 v[110:111], v22 offset:21504\n"
    "v_mfma_f32_16x16x32_bf16 v[40:43], v[100:103], a[88:91], v[40:43]\n"
    "buffer_load_dwordx4 v11, s[36:39], s85 offen offset:1024 lds\n"
    "v_mfma_f32_16x16x32_bf16 v[44:47], v[96:99], a[76:79], v[44:47]\n"
    "s_add_u32 s85, s69, s86\n"
    "s_add_u32 m0, s89, 122880\n"
    "ds_write_b64 v15, v[78:79] offset:520\n"
    "v_mfma_f32_16x16x32_bf16 v[44:47], v[100:103], a[92:95], v[44:47]\n"
    "s_waitcnt lgkmcnt(3)\n"
    "v_mfma_f32_16x16x32_bf16 v[48:51], v[104:107], a[64:67], v[48:51]\n"
    "s_waitcnt lgkmcnt(1)\n"
    "ds_read_b64_tr_b16 v[96:97], v21 offset:24576\n"
    "v_mfma_f32_16x16x32_bf16 v[48:51], v[108:111], a[80:83], v[48:51]\n"
    "buffer_load_dwordx4 v10, s[36:39], s85 offen lds\n"
    "v_mfma_f32_16x16x32_bf16 v[52:55], v[104:107], a[68:71], v[52:55]\n"
    "ds_read_b64_tr_b16 v[98:99], v21 offset:25600\n"
    "v_mfma_f32_16x16x32_bf16 v[52:55], v[108:111], a[84:87], v[52:55]\n"
    "ds_read_b64_tr_b16 v[100:101], v21 offset:28672\n"
    "v_mfma_f32_16x16x32_bf16 v[56:59], v[104:107], a[72:75], v[56:59]\n"
    "ds_read_b64_tr_b16 v[102:103], v21 offset:29696\n"
    "v_mfma_f32_16x16x32_bf16 v[56:59], v[108:111], a[88:91], v[56:59]\n"
    "buffer_load_dwordx4 v11, s[36:39], s85 offen offset:1024 lds\n"
    "v_mfma_f32_16x16x32_bf16 v[60:63], v[104:107], a[76:79], v[60:63]\n"
    "s_mov_b32 s86, 256\n"
    "s_add_u32 s85, s64, s86\n"
    "s_add_u32 m0, s89, 0\n"
    "v_mfma_f32_16x16x32_bf16 v[60:63], v[108:111], a[92:95], v[60:63]\n"
    "s_waitcnt lgkmcnt(2)\n"
    "v_mfma_f32_16x16x32_bf16 v[32:35], v[96:99], a[96:99], v[32:35]\n"
    "s_waitcnt lgkmcnt(0)\n"
    "ds_read_b64_tr_b16 v[104:105], v22 offset:24576\n"
    "v_mfma_f32_16x16x32_bf16 v[32:35], v[100:103], a[112:115], v[32:35]\n"
    "buffer_load_dwordx4 v10, s[36:39], s85 offen lds\n"
    "v_mfma_f32_16x16x32_bf16 v[36:39], v[96:99], a[100:103], v[36:39]\n"
    "ds_read_b64_tr_b16 v[106:107], v22 offset:25600\n"
    "v_mfma_f32_16x16x32_bf16 v[36:39], v[100:103], a[116:119], v[36:39]\n"
    "ds_read_b64_tr_b16 v[108:109], v22 offset:28672\n"
    "v_mfma_f32_16x16x32_bf16 v[40:43], v[96:99], a[104:107], v[40:43]\n"
    "ds_read_b64_tr_b16 v[110:111], v22 offset:29696\n"
    "v_mfma_f32_16x16x32_bf16 v[40:43], v[100:103], a[120:123], v[40:43]\n"
    "buffer_load_dwordx4 v11, s[36:39], s85 offen offset:1024 lds\n"
    "v_mfma_f32_16x16x32_bf16 v[44:47], v[96:99], a[108:111], v[44:47]\n"
    "s_add_u32 s85, s65, s86\n"
    "s_add_u32 m0, s89, 8192\n"
    "v_mfma_f32_16x16x32_bf16 v[44:47], v[100:103], a[124:127], v[44:47]\n"
    "s_waitcnt vmcnt(31) lgkmcnt(0)\n"
    "s_barrier\n"
    "v_mfma_f32_16x16x32_bf16 v[48:51], v[104:107], a[96:99], v[48:51]\n"
    "s_waitcnt lgkmcnt(0)\n"
    "ds_read_b64_tr_b16 v[96:97], v21 offset:32768\n"
    "ds_read_b128 a[160:163], v16 offset:0\n"
    "v_mfma_f32_16x16x32_bf16 v[48:51], v[108:111], a[112:115], v[48:51]\n"
    "buffer_load_dwordx4 v10, s[36:39], s85 offen lds\n"
    "v_mfma_f32_16x16x32_bf16 v[52:55], v[104:107], a[100:103], v[52:55]\n"
    "ds_read_b64_tr_b16 v[98:99], v21 offset:33792\n"
    "ds_read_b128 a[164:167], v16 offset:1024\n"
    "v_mfma_f32_16x16x32_bf16 v[52:55], v[108:111], a[116:119], v[52:55]\n"
    "ds_read_b64_tr_b16 v[100:101], v21 offset:36864\n"
    "ds_read_b128 a[168:171], v16 offset:2048\n"
    "v_mfma_f32_16x16x32_bf16 v[56:59], v[104:107], a[104:107], v[56:59]\n"
    "ds_read_b64_tr_b16 v[102:103], v21 offset:37888\n"
    "ds_read_b128 a[172:175], v16 offset:3072\n"
    "v_mfma_f32_16x16x32_bf16 v[56:59], v[108:111], a[120:123], v[56:59]\n"
    "buffer_load_dwordx4 v11, s[36:39], s85 offen offset:1024 lds\n"
    "v_mfma_f32_16x16x32_bf16 v[60:63], v[104:107], a[108:111], v[60:63]\n"
    "s_add_u32 s85, s66, s86\n"
    "s_add_u32 m0, s89, 16384\n"
    "ds_read_b128 a[176:179], v16 offset:8192\n"
    "v_mfma_f32_16x16x32_bf16 v[60:63], v[108:111], a[124:127], v[60:63]\n"
    "s_waitcnt lgkmcnt(6)\n"
    "ds_read_b128 a[180:183], v16 offset:9216\n"
    "v_mfma_f32_16x16x32_bf16 v[32:35], v[96:99], a[128:131], v[32:35]\n"
    "s_waitcnt lgkmcnt(3)\n"
    "ds_read_b64_tr_b16 v[104:105], v22 offset:32768\n"
    "ds_read_b128 a[184:187], v16 offset:10240\n"
    "v_mfma_f32_16x16x32_bf16 v[32:35], v[100:103], a[144:147], v[32:35]\n"
    "buffer_load_dwordx4 v10, s[36:39], s85 offen lds\n"
    "v_mfma_f32_16x16x32_bf16 v[36:39], v[96:99], a[132:135], v[36:39]\n"
    "ds_read_b64_tr_b16 v[106:107], v22 offset:33792\n"
    "ds_read_b128 a[188:191], v16 offset:11264\n"
    "v_mfma_f32_16x16x32_bf16 v[36:39], v[100:103], a[148:151], v[36:39]\n"
    "ds_read_b64_tr_b16 v[108:109], v22 offset:36864\n"
    "v_mfma_f32_16x16x32_bf16 v[40:43], v[96:99], a[136:139], v[40:43]\n"
    "ds_read_b64_tr_b16 v[110:111], v22 offset:37888\n"
    "v_mfma_f32_16x16x32_bf16 v[40:43], v[100:103], a[152:155], v[40:43]\n"
    "buffer_load_dwordx4 v11, s[36:39], s85 offen offset:1024 lds\n"
    "v_mfma_f32_16x16x32_bf16 v[44:47], v[96:99], a[140:143], v[44:47]\n"
    "s_add_u32 s85, s67, s86\n"
    "s_add_u32 m0, s89, 24576\n"
    "v_mfma_f32_16x16x32_bf16 v[44:47], v[100:103], a[156:159], v[44:47]\n"
    "s_waitcnt lgkmcnt(3)\n"
    "v_mfma_f32_16x16x32_bf16 v[48:51], v[104:107], a[128:131], v[48:51]\n"
    "s_waitcnt lgkmcnt(0)\n"
    "ds_read_b64_tr_b16 v[96:97], v21 offset:40960\n"
    "v_mfma_f32_16x16x32_bf16 v[48:51], v[108:111], a[144:147], v[48:51]\n"
    "buffer_load_dwordx4 v10, s[36:39], s85 offen lds\n"
    "v_mfma_f32_16x16x32_bf16 v[52:55], v[104:107], a[132:135], v[52:55]\n"
    "ds_read_b64_tr_b16 v[98:99], v21 offset:41984\n"
    "v_mfma_f32_16x16x32_bf16 v[52:55], v[108:111], a[148:151], v[52:55]\n"
    "ds_read_b64_tr_b16 v[100:101], v21 offset:45056\n"
    "v_mfma_f32_16x16x32_bf16 v[56:59], v[104:107], a[136:139], v[56:59]\n"
    "ds_read_b64_tr_b16 v[102:103], v21 offset:46080\n"
    "v_mfma_f32_16x16x32_bf16 v[56:59], v[108:111], a[152:155], v[56:59]\n"
    "buffer_load_dwordx4 v11, s[36:39], s85 offen offset:1024 lds\n"
    "v_mfma_f32_16x16x32_bf16 v[60:63], v[104:107], a[140:143], v[60:63]\n"
    "s_add_u32 s85, s68, s86\n"
    "v_mfma_f32_16x16x32_bf16 v[60:63], v[108:111], a[156:159], v[60:63]\n"
    "s_waitcnt lgkmcnt(2)\n"
    "v_mfma_f32_16x16x32_bf16 v[32:35], v[96:99], a[160:163], v[32:35]\n"
    "s_waitcnt lgkmcnt(0)\n"
    "ds_read_b64_tr_b16 v[104:105], v22 offset:40960\n"
    "v_add_u32 v21, s92, v21\n"
    "v_mfma_f32_16x16x32_bf16 v[32:35], v[100:103], a[176:179], v[32:35]\n"
    "s_add_u32 m0, s89, 32768\n"
    "ds_read_b64_tr_b16 v[106:107], v22 offset:41984\n"
    "v_mfma_f32_16x16x32_bf16 v[36:39], v[96:99], a[164:167], v[36:39]\n"
    "ds_read_b64_tr_b16 v[108:109], v22 offset:45056\n"
    "v_mfma_f32_16x16x32_bf16 v[36:39], v[100:103], a[180:183], v[36:39]\n"
    "ds_read_b64_tr_b16 v[110:111], v22 offset:46080\n"
    "v_mfma_f32_16x16x32_bf16 v[40:43], v[96:99], a[168:171], v[40:43]\n"
    "v_mfma_f32_16x16x32_bf16 v[40:43], v[100:103], a[184:187], v[40:43]\n"
    "v_mfma_f32_16x16x32_bf16 v[44:47], v[96:99], a[172:175], v[44:47]\n"
    "v_add_u32 v22, s92, v22\n"
    "s_sub_u32 s92, 0, s92\n"
    "v_mfma_f32_16x16x32_bf16 v[44:47], v[100:103], a[188:191], v[44:47]\n"
    "s_waitcnt vmcnt(16) lgkmcnt(0)\n"
    "s_barrier\n"
    "v_mfma_f32_16x16x32_bf16 v[48:51], v[104:107], a[160:163], v[48:51]\n"
    "s_waitcnt lgkmcnt(0)\n"
    "ds_read_b64_tr_b16 v[96:97], v21 offset:0\n"
    "v_cvt_pk_bf16_f32 v80, v32, v33\n"
    "v_mfma_f32_16x16x32_bf16 v[48:51], v[108:111], a[176:179], v[48:51]\n"
    "buffer_load_dwordx4 v10, s[36:39], s85 offen lds\n"
    "v_mfma_f32_16x16x32_bf16 v[52:55], v[104:107], a[164:167], v[52:55]\n"
    "ds_read_b64_tr_b16 v[98:99], v21 offset:1024\n"
    "v_cvt_pk_bf16_f32 v81, v34, v35\n"
    "v_mfma_f32_16x16x32_bf16 v[52:55], v[108:111], a[180:183], v[52:55]\n"
    "ds_read_b64_tr_b16 v[100:101], v21 offset:4096\n"
    "v_cvt_pk_bf16_f32 v84, v36, v37\n"
    "v_mfma_f32_16x16x32_bf16 v[56:59], v[104:107], a[168:171], v[56:59]\n"
    "ds_read_b64_tr_b16 v[102:103], v21 offset:5120\n"
    "v_cvt_pk_bf16_f32 v85, v38, v39\n"
    "v_mfma_f32_16x16x32_bf16 v[56:59], v[108:111], a[184:187], v[56:59]\n"
    "v_cvt_pk_bf16_f32 v88, v40, v41\n"
    "v_cvt_pk_bf16_f32 v89, v42, v43\n"
    "v_mfma_f32_16x16x32_bf16 v[60:63], v[104:107], a[172:175], v[60:63]\n"
    "v_cvt_pk_bf16_f32 v92, v44, v45\n"
    "v_cvt_pk_bf16_f32 v93, v46, v47\n"
    "v_mfma_f32_16x16x32_bf16 v[60:63], v[108:111], a[188:191], v[60:63]\n"
    "s_waitcnt lgkmcnt(2)\n"
    ".LL_CHUNK1_%=:\n"
    "v_mfma_f32_16x16x32_bf16 v[32:35], v[96:99], a[0:3], 0\n"
    "s_waitcnt lgkmcnt(0)\n"
    "ds_read_b64_tr_b16 v[104:105], v22 offset:0\n"
    "v_cvt_pk_bf16_f32 v82, v48, v49\n"
    "v_mfma_f32_16x16x32_bf16 v[32:35], v[100:103], a[16:19], v[32:35]\n"
    "buffer_load_dwordx4 v11, s[36:39], s85 offen offset:1024 lds\n"
    "v_mfma_f32_16x16x32_bf16 v[36:39], v[96:99], a[4:7], 0\n"
    "s_add_u32 s85, s69, s86\n"
    "ds_read_b64_tr_b16 v[106:107], v22 offset:1024\n"
    "v_cvt_pk_bf16_f32 v83, v50, v51\n"
    "v_mfma_f32_16x16x32_bf16 v[36:39], v[100:103], a[20:23], v[36:39]\n"
    "s_add_u32 m0, s89, 40960\n"
    "ds_read_b64_tr_b16 v[108:109], v22 offset:4096\n"
    "v_cvt_pk_bf16_f32 v86, v52, v53\n"
    "v_mfma_f32_16x16x32_bf16 v[40:43], v[96:99], a[8:11], 0\n"
    "ds_read_b64_tr_b16 v[110:111], v22 offset:5120\n"
    "v_cvt_pk_bf16_f32 v87, v54, v55\n"
    "v_mfma_f32_16x16x32_bf16 v[40:43], v[100:103], a[24:27], v[40:43]\n"
    "buffer_load_dwordx2 v[180:181], v210, s[44:47], s59 offen sc1\n"
    "v_mfma_f32_16x16x32_bf16 v[44:47], v[96:99], a[12:15], 0\n"
    "v_cvt_pk_bf16_f32 v90, v56, v57\n"
    "v_cvt_pk_bf16_f32 v91, v58, v59\n"
    "v_mfma_f32_16x16x32_bf16 v[44:47], v[100:103], a[28:31], v[44:47]\n"
    "s_waitcnt lgkmcnt(2)\n"
    "v_cvt_pk_bf16_f32 v94, v60, v61\n"
    "v_cvt_pk_bf16_f32 v95, v62, v63\n"
    "v_mfma_f32_16x16x32_bf16 v[48:51], v[104:107], a[0:3], 0\n"
    "s_waitcnt lgkmcnt(0)\n"
    "ds_read_b64_tr_b16 v[96:97], v21 offset:8192\n"
    "v_mfma_f32_16x16x32_bf16 v[48:51], v[108:111], a[16:19], v[48:51]\n"
    "buffer_load_dwordx4 v10, s[36:39], s85 offen lds\n"
    "v_mfma_f32_16x16x32_bf16 v[52:55], v[104:107], a[4:7], 0\n"
    "ds_read_b64_tr_b16 v[98:99], v21 offset:9216\n"
    "v_mfma_f32_16x16x32_bf16 v[52:55], v[108:111], a[20:23], v[52:55]\n"
    "ds_read_b64_tr_b16 v[100:101], v21 offset:12288\n"
    "v_mfma_f32_16x16x32_bf16 v[56:59], v[104:107], a[8:11], 0\n"
    "ds_read_b64_tr_b16 v[102:103], v21 offset:13312\n"
    "v_mfma_f32_16x16x32_bf16 v[56:59], v[108:111], a[24:27], v[56:59]\n"
    "buffer_store_dwordx4 v[80:83], v6, s[44:47], s95 offen sc1\n"
    "v_mfma_f32_16x16x32_bf16 v[60:63], v[104:107], a[12:15], 0\n"
    "v_mfma_f32_16x16x32_bf16 v[60:63], v[108:111], a[28:31], v[60:63]\n"
    "s_waitcnt lgkmcnt(2)\n"
    "v_mfma_f32_16x16x32_bf16 v[32:35], v[96:99], a[32:35], v[32:35]\n"
    "s_waitcnt lgkmcnt(0)\n"
    "ds_read_b64_tr_b16 v[104:105], v22 offset:8192\n"
    "v_mfma_f32_16x16x32_bf16 v[32:35], v[100:103], a[48:51], v[32:35]\n"
    "buffer_load_dwordx4 v11, s[36:39], s85 offen offset:1024 lds\n"
    "v_mfma_f32_16x16x32_bf16 v[36:39], v[96:99], a[36:39], v[36:39]\n"
    "s_sub_u32 s89, s89, s92\n"
    "ds_read_b64_tr_b16 v[106:107], v22 offset:9216\n"
    "v_mfma_f32_16x16x32_bf16 v[36:39], v[100:103], a[52:55], v[36:39]\n"
    "s_add_u32 s86, s86, 128\n"
    "ds_read_b64_tr_b16 v[108:109], v22 offset:12288\n"
    "v_mfma_f32_16x16x32_bf16 v[40:43], v[96:99], a[40:43], v[40:43]\n"
    "s_cmp_le_u32 s88, 2\n"
    "s_cselect_b32 s86, 1879048192, s86\n"
    "ds_read_b64_tr_b16 v[110:111], v22 offset:13312\n"
    "v_mfma_f32_16x16x32_bf16 v[40:43], v[100:103], a[56:59], v[40:43]\n"
    "s_add_u32 s85, s64, s86\n"
    "s_add_u32 s60, s60, 16384\n"
    "s_cmp_eq_u32 s88, 6\n"
    "s_cselect_b32 s60, s99, s60\n"
    "v_mfma_f32_16x16x32_bf16 v[44:47], v[96:99], a[44:47], v[44:47]\n"
    "s_add_u32 m0, s89, 0\n"
    "v_mfma_f32_16x16x32_bf16 v[44:47], v[100:103], a[60:63], v[44:47]\n"
    "s_waitcnt vmcnt(14) lgkmcnt(0)\n"
    "s_barrier\n"
    "v_mfma_f32_16x16x32_bf16 v[48:51], v[104:107], a[32:35], v[48:51]\n"
    "s_waitcnt lgkmcnt(0)\n"
    "ds_read_b64_tr_b16 v[96:97], v21 offset:16384\n"
    "v_mfma_f32_16x16x32_bf16 v[48:51], v[108:111], a[48:51], v[48:51]\n"
    "buffer_load_dwordx4 v10, s[36:39], s85 offen lds\n"
    "v_mfma_f32_16x16x32_bf16 v[52:55], v[104:107], a[36:39], v[52:55]\n"
    "ds_read_b64_tr_b16 v[98:99], v21 offset:17408\n"
    "v_mfma_f32_16x16x32_bf16 v[52:55], v[108:111], a[52:55], v[52:55]\n"
    "ds_read_b64_tr_b16 v[100:101], v21 offset:20480\n"
    "v_mfma_f32_16x16x32_bf16 v[56:59], v[104:107], a[40:43], v[56:59]\n"
    "ds_read_b64_tr_b16 v[102:103], v21 offset:21504\n"
    "v_mfma_f32_16x16x32_bf16 v[56:59], v[108:111], a[56:59], v[56:59]\n"
    "buffer_store_dwordx4 v[84:87], v7, s[44:47], s95 offen sc1\n"
    "v_mfma_f32_16x16x32_bf16 v[60:63], v[104:107], a[44:47], v[60:63]\n"
    "v_mfma_f32_16x16x32_bf16 v[60:63], v[108:111], a[60:63], v[60:63]\n"
    "s_waitcnt lgkmcnt(2)\n"
    "v_mfma_f32_16x16x32_bf16 v[32:35], v[96:99], a[64:67], v[32:35]\n"
    "s_waitcnt lgkmcnt(0)\n"
    "ds_read_b64_tr_b16 v[104:105], v22 offset:16384\n"
    "v_mfma_f32_16x16x32_bf16 v[32:35], v[100:103], a[80:83], v[32:35]\n"
    "buffer_load_dwordx4 v11, s[36:39], s85 offen offset:1024 lds\n"
    "v_mfma_f32_16x16x32_bf16 v[36:39], v[96:99], a[68:71], v[36:39]\n"
    "s_add_u32 s85, s65, s86\n"
    "ds_read_b64_tr_b16 v[106:107], v22 offset:17408\n"
    "v_mfma_f32_16x16x32_bf16 v[36:39], v[100:103], a[84:87], v[36:39]\n"
    "s_add_u32 m0, s89, 8192\n"
    "ds_read_b64_tr_b16 v[108:109], v22 offset:20480\n"
    "v_mfma_f32_16x16x32_bf16 v[40:43], v[96:99], a[72:75], v[40:43]\n"
    "ds_read_b64_tr_b16 v[110:111], v22 offset:21504\n"
    "v_mfma_f32_16x16x32_bf16 v[40:43], v[100:103], a[88:91], v[40:43]\n"
    "buffer_store_dwordx4 v[88:91], v8, s[44:47], s95 offen sc1\n"
    "v_mfma_f32_16x16x32_bf16 v[44:47], v[96:99], a[76:79], v[44:47]\n"
    "v_mfma_f32_16x16x32_bf16 v[44:47], v[100:103], a[92:95], v[44:47]\n"
    "s_waitcnt lgkmcnt(2)\n"
    "v_mfma_f32_16x16x32_bf16 v[48:51], v[104:107], a[64:67], v[48:51]\n"
    "s_waitcnt lgkmcnt(0)\n"
    "ds_read_b64_tr_b16 v[96:97], v21 offset:24576\n"
    "v_mfma_f32_16x16x32_bf16 v[48:51], v[108:111], a[80:83], v[48:51]\n"
    "buffer_load_dwordx4 v10, s[36:39], s85 offen lds\n"
    "v_mfma_f32_16x16x32_bf16 v[52:55], v[104:107], a[68:71], v[52:55]\n"
    "ds_read_b64_tr_b16 v[98:99], v21 offset:25600\n"
    "v_mfma_f32_16x16x32_bf16 v[52:55], v[108:111], a[84:87], v[52:55]\n"
    "ds_read_b64_tr_b16 v[100:101], v21 offset:28672\n"
    "v_mfma_f32_16x16x32_bf16 v[56:59], v[104:107], a[72:75], v[56:59]\n"
    "ds_read_b64_tr_b16 v[102:103], v21 offset:29696\n"
    "v_mfma_f32_16x16x32_bf16 v[56:59], v[108:111], a[88:91], v[56:59]\n"
    "buffer_store_dwordx4 v[92:95], v9, s[44:47], s95 offen sc1\n"
    "v_mfma_f32_16x16x32_bf16 v[60:63], v[104:107], a[76:79], v[60:63]\n"
    "s_waitcnt vmcnt(13)\n"
    "v_mfma_f32_16x16x32_bf16 v[184:187], v[172:175], v[188:191], 0\n"
    "v_mfma_f32_16x16x32_bf16 v[184:187], v[172:175], v[192:195], v[184:187]\n"
    "v_mfma_f32_16x16x32_bf16 v[184:187], v[172:175], v[196:199], v[184:187]\n"
    "v_mfma_f32_16x16x32_bf16 v[60:63], v[108:111], a[92:95], v[60:63]\n"
    "s_waitcnt lgkmcnt(2)\n"
    "v_mfma_f32_16x16x32_bf16 v[32:35], v[96:99], a[96:99], v[32:35]\n"
    "s_waitcnt lgkmcnt(0)\n"
    "ds_read_b64_tr_b16 v[104:105], v22 offset:24576\n"
    "v_mfma_f32_16x16x32_bf16 v[32:35], v[100:103], a[112:115], v[32:35]\n"
    "buffer_load_dwordx4 v11, s[36:39], s85 offen offset:1024 lds\n"
    "v_mfma_f32_16x16x32_bf16 v[36:39], v[96:99], a[100:103], v[36:39]\n"
    "s_add_u32 s85, s66, s86\n"
    "ds_read_b64_tr_b16 v[106:107], v22 offset:25600\n"
    "v_mfma_f32_16x16x32_bf16 v[36:39], v[100:103], a[116:119], v[36:39]\n"
    "s_add_u32 m0, s89, 16384\n"
    "ds_read_b64_tr_b16 v[108:109], v22 offset:28672\n"
    "v_mfma_f32_16x16x32_bf16 v[40:43], v[96:99], a[104:107], v[40:43]\n"
    "ds_read_b64_tr_b16 v[110:111], v22 offset:29696\n"
    "v_mfma_f32_16x16x32_bf16 v[40:43], v[100:103], a[120:123], v[40:43]\n"
    "s_waitcnt vmcnt(10)\n"
    "v_cmp_ne_u64 vcc, v[164:165], v[180:181]\n"
    "s_and_b64 vcc, vcc, s[62:63]\n"
    "s_cbranch_vccnz .LL_PORETRY1_%=\n"
    ".LL_POOK1_%=:\n"
    "v_mfma_f32_16x16x32_bf16 v[44:47], v[96:99], a[108:111], v[44:47]\n"
    "v_mfma_f32_16x16x32_bf16 v[44:47], v[100:103], a[124:127], v[44:47]\n"
    "s_waitcnt vmcnt(21) lgkmcnt(0)\n"
    "s_barrier\n"
    "v_mfma_f32_16x16x32_bf16 v[48:51], v[104:107], a[96:99], v[48:51]\n"
    "s_waitcnt lgkmcnt(0)\n"
    "ds_read_b64_tr_b16 v[96:97], v21 offset:32768\n"
    "v_mfma_f32_16x16x32_bf16 v[48:51], v[108:111], a[112:115], v[48:51]\n"
    "buffer_load_dwordx4 v10, s[36:39], s85 offen lds\n"
    "v_mfma_f32_16x16x32_bf16 v[52:55], v[104:107], a[100:103], v[52:55]\n"
    "ds_read_b64_tr_b16 v[98:99], v21 offset:33792\n"
    "v_mfma_f32_16x16x32_bf16 v[52:55], v[108:111], a[116:119], v[52:55]\n"
    "ds_read_b64_tr_b16 v[100:101], v21 offset:36864\n"
    "v_mfma_f32_16x16x32_bf16 v[56:59], v[104:107], a[104:107], v[56:59]\n"
    "ds_read_b64_tr_b16 v[102:103], v21 offset:37888\n"
    "v_mfma_f32_16x16x32_bf16 v[56:59], v[108:111], a[120:123], v[56:59]\n"
    "buffer_load_dwordx4 v[188:191], v212, s[44:47], s60 offen sc1\n"
    "v_mfma_f32_16x16x32_bf16 v[60:63], v[104:107], a[108:111], v[60:63]\n"
    "ds_write_b64 v170, v[184:185]\n"
    "v_mfma_f32_16x16x32_bf16 v[60:63], v[108:111], a[124:127], v[60:63]\n"
    "s_waitcnt lgkmcnt(3)\n"
    "v_mfma_f32_16x16x32_bf16 v[32:35], v[96:99], a[128:131], v[32:35]\n"
    "s_waitcnt lgkmcnt(1)\n"
    "ds_read_b64_tr_b16 v[104:105], v22 offset:32768\n"
    "v_mfma_f32_16x16x32_bf16 v[32:35], v[100:103], a[144:147], v[32:35]\n"
    "buffer_load_dwordx4 v11, s[36:39], s85 offen offset:1024 lds\n"
    "v_mfma_f32_16x16x32_bf16 v[36:39], v[96:99], a[132:135], v[36:39]\n"
    "s_add_u32 s85, s67, s86\n"
    "ds_read_b64_tr_b16 v[106:107], v22 offset:33792\n"
    "v_mfma_f32_16x16x32_bf16 v[36:39], v[100:103], a[148:151], v[36:39]\n"
    "s_add_u32 m0, s89, 24576\n"
    "ds_read_b64_tr_b16 v[108:109], v22 offset:36864\n"
    "v_mfma_f32_16x16x32_bf16 v[40:43], v[96:99], a[136:139], v[40:43]\n"
    "ds_read_b64_tr_b16 v[110:111], v22 offset:37888\n"
    "v_mfma_f32_16x16x32_bf16 v[40:43], v[100:103], a[152:155], v[40:43]\n"
    "buffer_load_dwordx4 v[192:195], v213, s[44:47], s60 offen sc1\n"
    "v_mfma_f32_16x16x32_bf16 v[44:47], v[96:99], a[140:143], v[44:47]\n"
    "ds_write_b64 v170, v[186:187] offset:8\n"
    "v_add_u32 v170, 0x200, v170\n"
    "v_mfma_f32_16x16x32_bf16 v[44:47], v[100:103], a[156:159], v[44:47]\n"
    "s_waitcnt lgkmcnt(3)\n"
    "s_add_u32 s95, s95, 16384\n"
    "s_mov_b32 s58, s97\n"
    "s_add_u32 s97, s97, 32768\n"
    "v_mfma_f32_16x16x32_bf16 v[48:51], v[104:107], a[128:131], v[48:51]\n"
    "s_waitcnt lgkmcnt(1)\n"
    "ds_read_b64_tr_b16 v[96:97], v21 offset:40960\n"
    "v_mfma_f32_16x16x32_bf16 v[48:51], v[108:111], a[144:147], v[48:51]\n"
    "buffer_load_dwordx4 v10, s[36:39], s85 offen lds\n"
    "v_mfma_f32_16x16x32_bf16 v[52:55], v[104:107], a[132:135], v[52:55]\n"
    "ds_read_b64_tr_b16 v[98:99], v21 offset:41984\n"
    "v_mfma_f32_16x16x32_bf16 v[52:55], v[108:111], a[148:151], v[52:55]\n"
    "ds_read_b64_tr_b16 v[100:101], v21 offset:45056\n"
    "v_mfma_f32_16x16x32_bf16 v[56:59], v[104:107], a[136:139], v[56:59]\n"
    "ds_read_b64_tr_b16 v[102:103], v21 offset:46080\n"
    "v_mfma_f32_16x16x32_bf16 v[56:59], v[108:111], a[152:155], v[56:59]\n"
    "buffer_load_dwordx4 v[196:199], v214, s[44:47], s60 offen sc1\n"
    "v_mfma_f32_16x16x32_bf16 v[60:63], v[104:107], a[140:143], v[60:63]\n"
    "s_sub_u32 s88, s88, 1\n"
    "v_mfma_f32_16x16x32_bf16 v[60:63], v[108:111], a[156:159], v[60:63]\n"
    "s_waitcnt lgkmcnt(2)\n"
    "v_mfma_f32_16x16x32_bf16 v[32:35], v[96:99], a[160:163], v[32:35]\n"
    "s_waitcnt lgkmcnt(0)\n"
    "ds_read_b64_tr_b16 v[104:105], v22 offset:40960\n"
    "v_add_u32 v21, s92, v21\n"
    "v_mfma_f32_16x16x32_bf16 v[32:35], v[100:103], a[176:179], v[32:35]\n"
    "buffer_load_dwordx4 v11, s[36:39], s85 offen offset:1024 lds\n"
    "v_mfma_f32_16x16x32_bf16 v[36:39], v[96:99], a[164:167], v[36:39]\n"
    "s_add_u32 s85, s68, s86\n"
    "ds_read_b64_tr_b16 v[106:107], v22 offset:41984\n"
    "v_mfma_f32_16x16x32_bf16 v[36:39], v[100:103], a[180:183], v[36:39]\n"
    "s_add_u32 m0, s89, 32768\n"
    "ds_read_b64_tr_b16 v[108:109], v22 offset:45056\n"
    "v_mfma_f32_16x16x32_bf16 v[40:43], v[96:99], a[168:171], v[40:43]\n"
    "ds_read_b64_tr_b16 v[110:111], v22 offset:46080\n"
    "v_mfma_f32_16x16x32_bf16 v[40:43], v[100:103], a[184:187], v[40:43]\n"
    "s_add_u32 s59, s59, 32768\n"
    "s_cmp_eq_u32 s88, 6\n"
    "s_cselect_b32 s59, s98, s59\n"
    "s_cselect_b64 s[62:63], -1, s[62:63]\n"
    "v_mfma_f32_16x16x32_bf16 v[44:47], v[96:99], a[172:175], v[44:47]\n"
    "v_add_u32 v22, s92, v22\n"
    "s_sub_u32 s92, 0, s92\n"
    "v_mfma_f32_16x16x32_bf16 v[44:47], v[100:103], a[188:191], v[44:47]\n"
    "s_waitcnt vmcnt(24) lgkmcnt(0)\n"
    "s_barrier\n"
    "v_mfma_f32_16x16x32_bf16 v[48:51], v[104:107], a[160:163], v[48:51]\n"
    "s_waitcnt lgkmcnt(0)\n"
    "ds_read_b64_tr_b16 v[96:97], v21 offset:0\n"
    "v_cvt_pk_bf16_f32 v80, v32, v33\n"
    "v_mfma_f32_16x16x32_bf16 v[48:51], v[108:111], a[176:179], v[48:51]\n"
    "buffer_load_dwordx4 v10, s[36:39], s85 offen lds\n"
    "v_mfma_f32_16x16x32_bf16 v[52:55], v[104:107], a[164:167], v[52:55]\n"
    "ds_read_b64_tr_b16 v[98:99], v21 offset:1024\n"
    "v_cvt_pk_bf16_f32 v81, v34, v35\n"
    "v_mfma_f32_16x16x32_bf16 v[52:55], v[108:111], a[180:183], v[52:55]\n"
    "ds_read_b64_tr_b16 v[100:101], v21 offset:4096\n"
    "v_cvt_pk_bf16_f32 v84, v36, v37\n"
    "v_mfma_f32_16x16x32_bf16 v[56:59], v[104:107], a[168:171], v[56:59]\n"
    "ds_read_b64_tr_b16 v[102:103], v21 offset:5120\n"
    "v_cvt_pk_bf16_f32 v85, v38, v39\n"
    "v_mfma_f32_16x16x32_bf16 v[56:59], v[108:111], a[184:187], v[56:59]\n"
    "v_cvt_pk_bf16_f32 v88, v40, v41\n"
    "v_cvt_pk_bf16_f32 v89, v42, v43\n"
    "v_mfma_f32_16x16x32_bf16 v[60:63], v[104:107], a[172:175], v[60:63]\n"
    "v_cvt_pk_bf16_f32 v92, v44, v45\n"
    "v_cvt_pk_bf16_f32 v93, v46, v47\n"
    "v_mfma_f32_16x16x32_bf16 v[60:63], v[108:111], a[188:191], v[60:63]\n"
    "s_waitcnt lgkmcnt(2)\n"
    "s_cmp_lg_u32 s88, 0\n"
    "s_cbranch_scc1 .LL_CHUNK1_%=\n"
    "s_branch .LL_TAIL_%=\n"
    ".LL_CSRETRY1_%=:\n"
    "s_mov_b32 s58, 0\n"
    ".LL_CSR1_%=:\n"
    "s_sleep 1\n"
    "buffer_load_dwordx2 v[120:121], v16, s[44:47], s60 offen sc1\n"
    "s_waitcnt vmcnt(0)\n"
    "v_cmp_ne_u32 vcc, s56, v121\n"
    "v_mov_b32 v124, v120\n"
    "s_add_u32 s58, s58, 1\n"
    "s_cmp_gt_u32 s58, 32768\n"
    "s_cbranch_scc1 .LL_CSOK1_%=\n"
    "s_cbranch_vccnz .LL_CSR1_%=\n"
    "s_branch .LL_CSOK1_%=\n"
    ".LL_PORETRY1_%=:\n"
    "s_mov_b32 s28, 0\n"
    ".LL_POR1_%=:\n"
    "s_sleep 1\n"
    "buffer_load_dwordx2 v[180:181], v210, s[44:47], s59 offen sc1\n"
    "s_waitcnt vmcnt(0)\n"
    "v_cmp_ne_u64 vcc, v[164:165], v[180:181]\n"
    "s_add_u32 s28, s28, 1\n"
    "s_cmp_gt_u32 s28, 32768\n"
    "s_cbranch_scc1 .LL_POOK1_%=\n"
    "s_cbranch_vccnz .LL_POR1_%=\n"
    "s_branch .LL_POOK1_%=\n"
    ".LL_STEP2_%=:\n"
    "v_mfma_f32_32x32x16_bf16 v[32:47], v[96:99], a[0:3], 0\n"
    "s_add_u32 s85, s71, s86\n"
    "ds_read_b64_tr_b16 v[108:109], v12 offset:6144\n"
    "ds_read_b64_tr_b16 v[110:111], v12 offset:6656\n"
    "v_mfma_f32_32x32x16_bf16 v[48:63], v[96:99], a[4:7], 0\n"
    "s_waitcnt lgkmcnt(5)\n"
    "v_mul_f32 v64, v64, v122\n"
    "v_fma_f32 v112, -v64, v64, s87\n"
    "v_mul_f32 v112, v64, v112\n"
    "v_mul_f32 v65, v65, v122\n"
    "v_exp_f32 v112, v112\n"
    "v_mfma_f32_32x32x16_bf16 v[32:47], v[100:103], a[8:11], v[32:47]\n"
    "s_add_u32 m0, s89, 57344\n"
    "ds_read_b64_tr_b16 v[96:97], v12 offset:8192\n"
    "ds_read_b64_tr_b16 v[98:99], v12 offset:8704\n"
    "v_fma_f32 v113, -v65, v65, s87\n"
    "v_add_f32 v112, 1.0, v112\n"
    "v_mul_f32 v113, v65, v113\n"
    "v_rcp_f32 v112, v112\n"
    "v_mfma_f32_32x32x16_bf16 v[48:63], v[100:103], a[12:15], v[48:63]\n"
    "s_waitcnt lgkmcnt(5)\n"
    "v_mul_f32 v66, v66, v122\n"
    "v_exp_f32 v113, v113\n"
    "v_fma_f32 v114, -v66, v66, s87\n"
    "v_mul_f32 v64, v64, v112\n"
    "v_add_f32 v113, 1.0, v113\n"
    "v_mul_f32 v114, v66, v114\n"
    "v_mfma_f32_32x32x16_bf16 v[32:47], v[104:107], a[16:19], v[32:47]\n"
    "s_lshl_b32 s60, s86, 2\n"
    "ds_read_b64_tr_b16 v[100:101], v12 offset:10240\n"
    "ds_read_b64_tr_b16 v[102:103], v12 offset:10752\n"
    "v_rcp_f32 v113, v113\n"
    "v_mul_f32 v67, v67, v122\n"
    "v_exp_f32 v114, v114\n"
    "v_mfma_f32_32x32x16_bf16 v[48:63], v[104:107], a[20:23], v[48:63]\n"
    "s_waitcnt lgkmcnt(4)\n"
    "v_fma_f32 v112, -v67, v67, s87\n"
    "v_mul_f32 v65, v65, v113\n"
    "v_add_f32 v114, 1.0, v114\n"
    "v_mul_f32 v112, v67, v112\n"
    "v_cvt_pk_bf16_f32 v116, v64, v65\n"
    "v_rcp_f32 v114, v114\n"
    "v_mfma_f32_32x32x16_bf16 v[32:47], v[108:111], a[24:27], v[32:47]\n"
    "s_add_u32 s60, s60, s92\n"
    "ds_read_b64_tr_b16 v[104:105], v12 offset:12288\n"
    "ds_read_b64_tr_b16 v[106:107], v12 offset:12800\n"
    "v_mul_f32 v68, v68, v122\n"
    "v_exp_f32 v112, v112\n"
    "v_fma_f32 v113, -v68, v68, s87\n"
    "v_mul_f32 v66, v66, v114\n"
    "v_mfma_f32_32x32x16_bf16 v[48:63], v[108:111], a[28:31], v[48:63]\n"
    "s_waitcnt lgkmcnt(4)\n"
    "buffer_load_dwordx4 v10, s[36:39], s85 offen lds\n"
    "buffer_load_dwordx4 v11, s[36:39], s85 offen offset:1024 lds\n"
    "v_add_f32 v112, 1.0, v112\n"
    "v_mul_f32 v113, v68, v113\n"
    "v_rcp_f32 v112, v112\n"
    "v_mfma_f32_32x32x16_bf16 v[32:47], v[96:99], a[32:35], v[32:47]\n"
    "s_add_u32 s85, s72, s86\n"
    "ds_read_b64_tr_b16 v[108:109], v12 offset:14336\n"
    "ds_read_b64_tr_b16 v[110:111], v12 offset:14848\n"
    "v_mul_f32 v69, v69, v122\n"
    "v_exp_f32 v113, v113\n"
    "v_fma_f32 v114, -v69, v69, s87\n"
    "v_mul_f32 v67, v67, v112\n"
    "v_mfma_f32_32x32x16_bf16 v[48:63], v[96:99], a[36:39], v[48:63]\n"
    "s_waitcnt vmcnt(8) lgkmcnt(4)\n"
    "s_barrier\n"
    "v_add_f32 v113, 1.0, v113\n"
    "v_mul_f32 v114, v69, v114\n"
    "v_cvt_pk_bf16_f32 v117, v66, v67\n"
    "v_rcp_f32 v113, v113\n"
    "v_mfma_f32_32x32x16_bf16 v[32:47], v[100:103], a[40:43], v[32:47]\n"
    "s_add_u32 m0, s89, 65536\n"
    "ds_read_b64_tr_b16 v[96:97], v12 offset:16384\n"
    "ds_read_b64_tr_b16 v[98:99], v12 offset:16896\n"
    "v_mul_f32 v70, v70, v122\n"
    "v_exp_f32 v114, v114\n"
    "v_fma_f32 v112, -v70, v70, s87\n"
    "v_mul_f32 v68, v68, v113\n"
    "v_mfma_f32_32x32x16_bf16 v[48:63], v[100:103], a[44:47], v[48:63]\n"
    "s_waitcnt lgkmcnt(4)\n"
    "ds_write_b64 v14, v[116:117] offset:0\n"
    "v_add_f32 v114, 1.0, v114\n"
    "v_mul_f32 v112, v70, v112\n"
    "v_rcp_f32 v114, v114\n"
    "v_mfma_f32_32x32x16_bf16 v[32:47], v[104:107], a[48:51], v[32:47]\n"
    "s_mov_b32 s61, s86\n"
    "s_add_u32 s86, s86, 128\n"
    "ds_read_b64_tr_b16 v[100:101], v12 offset:18432\n"
    "ds_read_b64_tr_b16 v[102:103], v12 offset:18944\n"
    "v_mul_f32 v71, v71, v122\n"
    "v_exp_f32 v112, v112\n"
    "v_fma_f32 v113, -v71, v71, s87\n"
    "v_mfma_f32_32x32x16_bf16 v[48:63], v[104:107], a[52:55], v[48:63]\n"
    "s_waitcnt lgkmcnt(5)\n"
    "v_mul_f32 v69, v69, v114\n"
    "v_add_f32 v112, 1.0, v112\n"
    "v_mul_f32 v113, v71, v113\n"
    "v_cvt_pk_bf16_f32 v118, v68, v69\n"
    "v_rcp_f32 v112, v112\n"
    "v_mul_f32 v72, v72, v122\n"
    "v_mfma_f32_32x32x16_bf16 v[32:47], v[108:111], a[56:59], v[32:47]\n"
    "s_cmp_eq_u32 s86, 768\n"
    "s_cselect_b32 s86, 0, s86\n"
    "s_cmp_eq_u32 s88, 1\n"
    "ds_read_b64_tr_b16 v[104:105], v12 offset:20480\n"
    "ds_read_b64_tr_b16 v[106:107], v12 offset:20992\n"
    "v_exp_f32 v113, v113\n"
    "v_mfma_f32_32x32x16_bf16 v[48:63], v[108:111], a[60:63], v[48:63]\n"
    "s_waitcnt lgkmcnt(5)\n"
    "buffer_load_dwordx4 v10, s[36:39], s85 offen lds\n"
    "buffer_load_dwordx4 v11, s[36:39], s85 offen offset:1024 lds\n"
    "v_fma_f32 v114, -v72, v72, s87\n"
    "v_mul_f32 v70, v70, v112\n"
    "v_add_f32 v113, 1.0, v113\n"
    "v_mfma_f32_32x32x16_bf16 v[32:47], v[96:99], a[64:67], v[32:47]\n"
    "s_cbranch_scc0 .LL_SKSW2_%=\n"
    "s_mov_b64 s[36:37], s[52:53]\n"
    "s_mov_b32 s38, s54\n"
    "s_mov_b32 s86, 0\n"
    "s_mov_b32 s64, s73\n"
    "s_mov_b32 s65, s74\n"
    "s_mov_b32 s66, s75\n"
    "s_mov_b32 s67, s76\n"
    "s_mov_b32 s68, s77\n"
    "s_mov_b32 s69, s78\n"
    "s_mov_b32 s70, 1879048192\n"
    "v_mov_b32 v10, v19\n"
    "v_mov_b32 v11, v20\n"
    ".LL_SKSW2_%=:\n"
    "s_add_u32 s85, s64, s86\n"
    "ds_read_b64_tr_b16 v[108:109], v12 offset:22528\n"
    "ds_read_b64_tr_b16 v[110:111], v12 offset:23040\n"
    "v_mul_f32 v114, v72, v114\n"
    "v_rcp_f32 v113, v113\n"
    "v_mfma_f32_32x32x16_bf16 v[48:63], v[96:99], a[68:71], v[48:63]\n"
    "s_waitcnt lgkmcnt(4)\n"
    "v_mul_f32 v73, v73, v122\n"
    "v_exp_f32 v114, v114\n"
    "v_fma_f32 v112, -v73, v73, s87\n"
    "v_mul_f32 v71, v71, v113\n"
    "v_add_f32 v114, 1.0, v114\n"
    "v_mfma_f32_32x32x16_bf16 v[32:47], v[100:103], a[72:75], v[32:47]\n"
    "s_add_u32 m0, s89, 0\n"
    "ds_read_b64_tr_b16 v[96:97], v12 offset:24576\n"
    "ds_read_b64_tr_b16 v[98:99], v12 offset:25088\n"
    "v_mul_f32 v112, v73, v112\n"
    "v_cvt_pk_bf16_f32 v119, v70, v71\n"
    "v_rcp_f32 v114, v114\n"
    "v_mul_f32 v74, v74, v122\n"
    "v_mfma_f32_32x32x16_bf16 v[48:63], v[100:103], a[76:79], v[48:63]\n"
    "s_waitcnt lgkmcnt(4)\n"
    "ds_write_b64 v14, v[118:119] offset:8\n"
    "v_exp_f32 v112, v112\n"
    "v_fma_f32 v113, -v74, v74, s87\n"
    "v_mfma_f32_32x32x16_bf16 v[32:47], v[104:107], a[80:83], v[32:47]\n"
    "ds_read_b64_tr_b16 v[100:101], v12 offset:26624\n"
    "ds_read_b64_tr_b16 v[102:103], v12 offset:27136\n"
    "v_mul_f32 v72, v72, v114\n"
    "v_add_f32 v112, 1.0, v112\n"
    "v_mul_f32 v113, v74, v113\n"
    "v_rcp_f32 v112, v112\n"
    "v_mfma_f32_32x32x16_bf16 v[48:63], v[104:107], a[84:87], v[48:63]\n"
    "s_waitcnt lgkmcnt(5)\n"
    "buffer_load_dword v31, v18, s[52:55], s93 offen\n"
    "v_mul_f32 v75, v75, v122\n"
    "v_exp_f32 v113, v113\n"
    "v_fma_f32 v114, -v75, v75, s87\n"
    "v_mfma_f32_32x32x16_bf16 v[32:47], v[108:111], a[88:91], v[32:47]\n"
    "ds_read_b64_tr_b16 v[104:105], v12 offset:28672\n"
    "ds_read_b64_tr_b16 v[106:107], v12 offset:29184\n"
    "v_mul_f32 v73, v73, v112\n"
    "v_add_f32 v113, 1.0, v113\n"
    "v_mul_f32 v114, v75, v114\n"
    "v_cvt_pk_bf16_f32 v116, v72, v73\n"
    "v_mfma_f32_32x32x16_bf16 v[48:63], v[108:111], a[92:95], v[48:63]\n"
    "s_waitcnt lgkmcnt(5)\n"
    "buffer_load_dwordx4 v10, s[36:39], s85 offen lds\n"
    "buffer_load_dwordx4 v11, s[36:39], s85 offen offset:1024 lds\n"
    "v_rcp_f32 v113, v113\n"
    "v_mfma_f32_32x32x16_bf16 v[32:47], v[96:99], a[96:99], v[32:47]\n"
    "s_add_u32 s85, s65, s86\n"
    "ds_read_b64_tr_b16 v[108:109], v12 offset:30720\n"
    "ds_read_b64_tr_b16 v[110:111], v12 offset:31232\n"
    "v_mul_f32 v76, v76, v122\n"
    "v_exp_f32 v114, v114\n"
    "v_fma_f32 v112, -v76, v76, s87\n"
    "v_mul_f32 v74, v74, v113\n"
    "v_mfma_f32_32x32x16_bf16 v[48:63], v[96:99], a[100:103], v[48:63]\n"
    "s_waitcnt vmcnt(9) lgkmcnt(4)\n"
    "s_barrier\n"
    "v_add_f32 v114, 1.0, v114\n"
    "v_mul_f32 v112, v76, v112\n"
    "v_rcp_f32 v114, v114\n"
    "v_mfma_f32_32x32x16_bf16 v[32:47], v[100:103], a[104:107], v[32:47]\n"
    "s_add_u32 m0, s89, 8192\n"
    "ds_read_b64_tr_b16 v[96:97], v12 offset:32768\n"
    "ds_read_b64_tr_b16 v[98:99], v12 offset:33280\n"
    "v_mul_f32 v77, v77, v122\n"
    "v_exp_f32 v112, v112\n"
    "v_fma_f32 v113, -v77, v77, s87\n"
    "v_mfma_f32_32x32x16_bf16 v[48:63], v[100:103], a[108:111], v[48:63]\n"
    "s_waitcnt lgkmcnt(4)\n"
    "v_mul_f32 v75, v75, v114\n"
    "v_add_f32 v112, 1.0, v112\n"
    "v_mul_f32 v113, v77, v113\n"
    "v_mfma_f32_32x32x16_bf16 v[32:47], v[104:107], a[112:115], v[32:47]\n"
    "s_add_u32 s93, s93, 11264\n"
    "ds_read_b64_tr_b16 v[100:101], v12 offset:34816\n"
    "ds_read_b64_tr_b16 v[102:103], v12 offset:35328\n"
    "v_cvt_pk_bf16_f32 v117, v74, v75\n"
    "v_rcp_f32 v112, v112\n"
    "v_mul_f32 v78, v78, v122\n"
    "v_mfma_f32_32x32x16_bf16 v[48:63], v[104:107], a[116:119], v[48:63]\n"
    "s_waitcnt lgkmcnt(4)\n"
    "v_exp_f32 v113, v113\n"
    "v_fma_f32 v114, -v78, v78, s87\n"
    "v_mul_f32 v76, v76, v112\n"
    "v_add_f32 v113, 1.0, v113\n"
    "v_mul_f32 v114, v78, v114\n"
    "v_rcp_f32 v113, v113\n"
    "v_mfma_f32_32x32x16_bf16 v[32:47], v[108:111], a[120:123], v[32:47]\n"
    "ds_read_b64_tr_b16 v[104:105], v12 offset:36864\n"
    "ds_read_b64_tr_b16 v[106:107], v12 offset:37376\n"
    "v_mul_f32 v79, v79, v122\n"
    "v_exp_f32 v114, v114\n"
    "v_fma_f32 v112, -v79, v79, s87\n"
    "v_mfma_f32_32x32x16_bf16 v[48:63], v[108:111], a[124:127], v[48:63]\n"
    "s_waitcnt lgkmcnt(4)\n"
    "buffer_load_dwordx4 v10, s[36:39], s85 offen lds\n"
    "buffer_load_dwordx4 v11, s[36:39], s85 offen offset:1024 lds\n"
    "v_mul_f32 v77, v77, v113\n"
    "v_add_f32 v114, 1.0, v114\n"
    "v_mfma_f32_32x32x16_bf16 v[32:47], v[96:99], a[128:131], v[32:47]\n"
    "s_add_u32 s85, s66, s86\n"
    "ds_read_b64_tr_b16 v[108:109], v12 offset:38912\n"
    "ds_read_b64_tr_b16 v[110:111], v12 offset:39424\n"
    "v_mul_f32 v112, v79, v112\n"
    "v_cvt_pk_bf16_f32 v118, v76, v77\n"
    "v_rcp_f32 v114, v114\n"
    "v_mfma_f32_32x32x16_bf16 v[48:63], v[96:99], a[132:135], v[48:63]\n"
    "s_waitcnt lgkmcnt(4)\n"
    "v_mul_f32 v80, v80, v123\n"
    "v_exp_f32 v112, v112\n"
    "v_fma_f32 v113, -v80, v80, s87\n"
    "v_mul_f32 v78, v78, v114\n"
    "v_add_f32 v112, 1.0, v112\n"
    "v_mfma_f32_32x32x16_bf16 v[32:47], v[100:103], a[136:139], v[32:47]\n"
    "s_add_u32 m0, s89, 16384\n"
    "ds_read_b64_tr_b16 v[96:97], v12 offset:40960\n"
    "ds_read_b64_tr_b16 v[98:99], v12 offset:41472\n"
    "v_mul_f32 v113, v80, v113\n"
    "v_rcp_f32 v112, v112\n"
    "v_mul_f32 v81, v81, v123\n"
    "v_mfma_f32_32x32x16_bf16 v[48:63], v[100:103], a[140:143], v[48:63]\n"
    "s_waitcnt lgkmcnt(4)\n"
    "ds_write_b64 v14, v[116:117] offset:512\n"
    "v_exp_f32 v113, v113\n"
    "v_fma_f32 v114, -v81, v81, s87\n"
    "v_mul_f32 v79, v79, v112\n"
    "v_mfma_f32_32x32x16_bf16 v[32:47], v[104:107], a[144:147], v[32:47]\n"
    "ds_read_b64_tr_b16 v[100:101], v12 offset:43008\n"
    "ds_read_b64_tr_b16 v[102:103], v12 offset:43520\n"
    "v_add_f32 v113, 1.0, v113\n"
    "v_mul_f32 v114, v81, v114\n"
    "v_cvt_pk_bf16_f32 v119, v78, v79\n"
    "v_rcp_f32 v113, v113\n"
    "v_mfma_f32_32x32x16_bf16 v[48:63], v[104:107], a[148:151], v[48:63]\n"
    "s_waitcnt lgkmcnt(5)\n"
    "buffer_load_ushort v125, v17, s[48:51], s61 offen\n"
    "v_mul_f32 v82, v82, v123\n"
    "v_exp_f32 v114, v114\n"
    "v_fma_f32 v112, -v82, v82, s87\n"
    "v_mfma_f32_32x32x16_bf16 v[32:47], v[108:111], a[152:155], v[32:47]\n"
    "ds_read_b64_tr_b16 v[104:105], v12 offset:45056\n"
    "ds_read_b64_tr_b16 v[106:107], v12 offset:45568\n"
    "v_mul_f32 v80, v80, v113\n"
    "v_add_f32 v114, 1.0, v114\n"
    "v_mul_f32 v112, v82, v112\n"
    "v_rcp_f32 v114, v114\n"
    "v_mfma_f32_32x32x16_bf16 v[48:63], v[108:111], a[156:159], v[48:63]\n"
    "s_waitcnt lgkmcnt(5)\n"
    "buffer_load_dwordx4 v10, s[36:39], s85 offen lds\n"
    "buffer_load_dwordx4 v11, s[36:39], s85 offen offset:1024 lds\n"
    "v_mul_f32 v83, v83, v123\n"
    "v_exp_f32 v112, v112\n"
    "v_mfma_f32_32x32x16_bf16 v[32:47], v[96:99], a[160:163], v[32:47]\n"
    "s_add_u32 s85, s67, s86\n"
    "ds_read_b64_tr_b16 v[108:109], v12 offset:47104\n"
    "ds_read_b64_tr_b16 v[110:111], v12 offset:47616\n"
    "v_fma_f32 v113, -v83, v83, s87\n"
    "v_mul_f32 v81, v81, v114\n"
    "v_add_f32 v112, 1.0, v112\n"
    "v_mul_f32 v113, v83, v113\n"
    "v_mfma_f32_32x32x16_bf16 v[48:63], v[96:99], a[164:167], v[48:63]\n"
    "s_waitcnt vmcnt(10) lgkmcnt(4)\n"
    "s_barrier\n"
    "v_cvt_pk_bf16_f32 v116, v80, v81\n"
    "v_rcp_f32 v112, v112\n"
    "v_mfma_f32_32x32x16_bf16 v[32:47], v[100:103], a[168:171], v[32:47]\n"
    "s_add_u32 m0, s89, 24576\n"
    "ds_read_b64_tr_b16 v[96:97], v12 offset:49152\n"
    "ds_read_b64_tr_b16 v[98:99], v12 offset:49664\n"
    "v_mul_f32 v84, v84, v123\n"
    "v_exp_f32 v113, v113\n"
    "v_fma_f32 v114, -v84, v84, s87\n"
    "v_mfma_f32_32x32x16_bf16 v[48:63], v[100:103], a[172:175], v[48:63]\n"
    "s_waitcnt lgkmcnt(4)\n"
    "ds_write_b64 v14, v[118:119] offset:520\n"
    "v_mul_f32 v82, v82, v112\n"
    "v_add_f32 v113, 1.0, v113\n"
    "v_mul_f32 v114, v84, v114\n"
    "v_mfma_f32_32x32x16_bf16 v[32:47], v[104:107], a[176:179], v[32:47]\n"
    "ds_read_b64_tr_b16 v[100:101], v12 offset:51200\n"
    "ds_read_b64_tr_b16 v[102:103], v12 offset:51712\n"
    "v_rcp_f32 v113, v113\n"
    "v_mul_f32 v85, v85, v123\n"
    "v_exp_f32 v114, v114\n"
    "v_mfma_f32_32x32x16_bf16 v[48:63], v[104:107], a[180:183], v[48:63]\n"
    "s_waitcnt lgkmcnt(5)\n"
    "buffer_load_dwordx2 v[120:121], v16, s[44:47], s60 offen sc1\n"
    "v_fma_f32 v112, -v85, v85, s87\n"
    "v_mul_f32 v83, v83, v113\n"
    "v_add_f32 v114, 1.0, v114\n"
    "v_mul_f32 v112, v85, v112\n"
    "v_mfma_f32_32x32x16_bf16 v[32:47], v[108:111], a[184:187], v[32:47]\n"
    "ds_read_b64_tr_b16 v[104:105], v12 offset:53248\n"
    "ds_read_b64_tr_b16 v[106:107], v12 offset:53760\n"
    "v_cvt_pk_bf16_f32 v117, v82, v83\n"
    "v_rcp_f32 v114, v114\n"
    "v_mul_f32 v86, v86, v123\n"
    "v_mfma_f32_32x32x16_bf16 v[48:63], v[108:111], a[188:191], v[48:63]\n"
    "s_waitcnt lgkmcnt(5)\n"
    "buffer_load_dwordx4 v10, s[36:39], s85 offen lds\n"
    "buffer_load_dwordx4 v11, s[36:39], s85 offen offset:1024 lds\n"
    "v_exp_f32 v112, v112\n"
    "v_mfma_f32_32x32x16_bf16 v[32:47], v[96:99], a[192:195], v[32:47]\n"
    "s_add_u32 s85, s68, s86\n"
    "ds_read_b64_tr_b16 v[108:109], v12 offset:55296\n"
    "ds_read_b64_tr_b16 v[110:111], v12 offset:55808\n"
    "v_fma_f32 v113, -v86, v86, s87\n"
    "v_mul_f32 v84, v84, v114\n"
    "v_add_f32 v112, 1.0, v112\n"
    "v_mul_f32 v113, v86, v113\n"
    "v_mfma_f32_32x32x16_bf16 v[48:63], v[96:99], a[196:199], v[48:63]\n"
    "s_waitcnt lgkmcnt(4)\n"
    "v_rcp_f32 v112, v112\n"
    "v_mul_f32 v87, v87, v123\n"
    "v_exp_f32 v113, v113\n"
    "v_fma_f32 v114, -v87, v87, s87\n"
    "v_mfma_f32_32x32x16_bf16 v[32:47], v[100:103], a[200:203], v[32:47]\n"
    "s_add_u32 m0, s89, 32768\n"
    "ds_read_b64_tr_b16 v[96:97], v12 offset:57344\n"
    "ds_read_b64_tr_b16 v[98:99], v12 offset:57856\n"
    "v_mul_f32 v85, v85, v112\n"
    "v_add_f32 v113, 1.0, v113\n"
    "v_mul_f32 v114, v87, v114\n"
    "v_cvt_pk_bf16_f32 v118, v84, v85\n"
    "v_rcp_f32 v113, v113\n"
    "v_mfma_f32_32x32x16_bf16 v[48:63], v[100:103], a[204:207], v[48:63]\n"
    "s_waitcnt lgkmcnt(4)\n"
    "ds_write_b64 v15, v[116:117] offset:0\n"
    "v_mul_f32 v88, v88, v123\n"
    "v_exp_f32 v114, v114\n"
    "v_mfma_f32_32x32x16_bf16 v[32:47], v[104:107], a[208:211], v[32:47]\n"
    "ds_read_b64_tr_b16 v[100:101], v12 offset:59392\n"
    "ds_read_b64_tr_b16 v[102:103], v12 offset:59904\n"
    "v_fma_f32 v112, -v88, v88, s87\n"
    "v_mul_f32 v86, v86, v113\n"
    "v_add_f32 v114, 1.0, v114\n"
    "v_mul_f32 v112, v88, v112\n"
    "v_mfma_f32_32x32x16_bf16 v[48:63], v[104:107], a[212:215], v[48:63]\n"
    "s_waitcnt lgkmcnt(5)\n"
    "v_rcp_f32 v114, v114\n"
    "v_mul_f32 v89, v89, v123\n"
    "v_exp_f32 v112, v112\n"
    "v_fma_f32 v113, -v89, v89, s87\n"
    "v_mfma_f32_32x32x16_bf16 v[32:47], v[108:111], a[216:219], v[32:47]\n"
    "ds_read_b64_tr_b16 v[104:105], v12 offset:61440\n"
    "ds_read_b64_tr_b16 v[106:107], v12 offset:61952\n"
    "v_mul_f32 v87, v87, v114\n"
    "v_add_f32 v112, 1.0, v112\n"
    "v_mul_f32 v113, v89, v113\n"
    "v_cvt_pk_bf16_f32 v119, v86, v87\n"
    "v_mfma_f32_32x32x16_bf16 v[48:63], v[108:111], a[220:223], v[48:63]\n"
    "s_waitcnt lgkmcnt(5)\n"
    "buffer_load_dwordx4 v10, s[36:39], s85 offen lds\n"
    "buffer_load_dwordx4 v11, s[36:39], s85 offen offset:1024 lds\n"
    "v_rcp_f32 v112, v112\n"
    "v_mfma_f32_32x32x16_bf16 v[32:47], v[96:99], a[224:227], v[32:47]\n"
    "s_add_u32 s85, s69, s86\n"
    "ds_read_b64_tr_b16 v[108:109], v12 offset:63488\n"
    "ds_read_b64_tr_b16 v[110:111], v12 offset:64000\n"
    "v_mul_f32 v90, v90, v123\n"
    "v_exp_f32 v113, v113\n"
    "v_fma_f32 v114, -v90, v90, s87\n"
    "v_mul_f32 v88, v88, v112\n"
    "v_mfma_f32_32x32x16_bf16 v[48:63], v[96:99], a[228:231], v[48:63]\n"
    "s_waitcnt vmcnt(13) lgkmcnt(4)\n"
    "s_barrier\n"
    "v_add_f32 v113, 1.0, v113\n"
    "v_mul_f32 v114, v90, v114\n"
    "v_rcp_f32 v113, v113\n"
    "v_mfma_f32_32x32x16_bf16 v[32:47], v[100:103], a[232:235], v[32:47]\n"
    "s_add_u32 m0, s89, 40960\n"
    "ds_read_b64_tr_b16 v[96:97], v13 offset:0\n"
    "ds_read_b64_tr_b16 v[98:99], v13 offset:512\n"
    "v_mul_f32 v91, v91, v123\n"
    "v_exp_f32 v114, v114\n"
    "v_fma_f32 v112, -v91, v91, s87\n"
    "v_mfma_f32_32x32x16_bf16 v[48:63], v[100:103], a[236:239], v[48:63]\n"
    "s_waitcnt lgkmcnt(4)\n"
    "ds_write_b64 v15, v[118:119] offset:8\n"
    "v_mul_f32 v89, v89, v113\n"
    "v_add_f32 v114, 1.0, v114\n"
    "v_mul_f32 v112, v91, v112\n"
    "v_mfma_f32_32x32x16_bf16 v[32:47], v[104:107], a[240:243], v[32:47]\n"
    "ds_read_b64_tr_b16 v[100:101], v13 offset:2048\n"
    "ds_read_b64_tr_b16 v[102:103], v13 offset:2560\n"
    "v_cvt_pk_bf16_f32 v116, v88, v89\n"
    "v_rcp_f32 v114, v114\n"
    "v_mul_f32 v92, v92, v123\n"
    "v_mfma_f32_32x32x16_bf16 v[48:63], v[104:107], a[244:247], v[48:63]\n"
    "s_waitcnt lgkmcnt(5)\n"
    "v_exp_f32 v112, v112\n"
    "v_fma_f32 v113, -v92, v92, s87\n"
    "v_mul_f32 v90, v90, v114\n"
    "v_add_f32 v112, 1.0, v112\n"
    "v_mul_f32 v113, v92, v113\n"
    "v_mfma_f32_32x32x16_bf16 v[32:47], v[108:111], a[248:251], v[32:47]\n"
    "ds_read_b64_tr_b16 v[104:105], v13 offset:4096\n"
    "ds_read_b64_tr_b16 v[106:107], v13 offset:4608\n"
    "v_rcp_f32 v112, v112\n"
    "v_mul_f32 v93, v93, v123\n"
    "v_exp_f32 v113, v113\n"
    "v_mfma_f32_32x32x16_bf16 v[48:63], v[108:111], a[252:255], v[48:63]\n"
    "s_waitcnt lgkmcnt(5)\n"
    "buffer_load_dwordx4 v10, s[36:39], s85 offen lds\n"
    "buffer_load_dwordx4 v11, s[36:39], s85 offen offset:1024 lds\n"
    "v_fma_f32 v114, -v93, v93, s87\n"
    "v_mul_f32 v91, v91, v112\n"
    "v_mfma_f32_32x32x16_bf16 v[32:47], v[96:99], v[224:227], v[32:47]\n"
    "s_add_u32 s85, s70, s86\n"
    "ds_read_b64_tr_b16 v[108:109], v13 offset:6144\n"
    "ds_read_b64_tr_b16 v[110:111], v13 offset:6656\n"
    "v_add_f32 v113, 1.0, v113\n"
    "v_mul_f32 v114, v93, v114\n"
    "v_cvt_pk_bf16_f32 v117, v90, v91\n"
    "v_rcp_f32 v113, v113\n"
    "v_mfma_f32_32x32x16_bf16 v[48:63], v[96:99], v[228:231], v[48:63]\n"
    "s_waitcnt vmcnt(10) lgkmcnt(4)\n"
    "s_barrier\n"
    "v_mul_f32 v94, v94, v123\n"
    "v_exp_f32 v114, v114\n"
    "v_mfma_f32_32x32x16_bf16 v[32:47], v[100:103], v[232:235], v[32:47]\n"
    "s_add_u32 m0, s89, 49152\n"
    "ds_read_b64_tr_b16 v[96:97], v12 offset:0\n"
    "ds_read_b64_tr_b16 v[98:99], v12 offset:512\n"
    "v_fma_f32 v112, -v94, v94, s87\n"
    "v_mul_f32 v92, v92, v113\n"
    "v_add_f32 v114, 1.0, v114\n"
    "v_mul_f32 v112, v94, v112\n"
    "v_mfma_f32_32x32x16_bf16 v[48:63], v[100:103], v[236:239], v[48:63]\n"
    "s_waitcnt lgkmcnt(4)\n"
    "ds_write_b64 v15, v[116:117] offset:512\n"
    "v_rcp_f32 v114, v114\n"
    "v_mul_f32 v95, v95, v123\n"
    "v_mfma_f32_32x32x16_bf16 v[32:47], v[104:107], v[240:243], v[32:47]\n"
    "ds_read_b64_tr_b16 v[100:101], v12 offset:2048\n"
    "ds_read_b64_tr_b16 v[102:103], v12 offset:2560\n"
    "v_exp_f32 v112, v112\n"
    "v_fma_f32 v113, -v95, v95, s87\n"
    "v_mul_f32 v93, v93, v114\n"
    "v_add_f32 v112, 1.0, v112\n"
    "v_mfma_f32_32x32x16_bf16 v[48:63], v[104:107], v[244:247], v[48:63]\n"
    "s_waitcnt lgkmcnt(5)\n"
    "v_mul_f32 v113, v95, v113\n"
    "v_cvt_pk_bf16_f32 v118, v92, v93\n"
    "v_rcp_f32 v112, v112\n"
    "v_exp_f32 v113, v113\n"
    "v_mfma_f32_32x32x16_bf16 v[32:47], v[108:111], v[248:251], v[32:47]\n"
    "s_waitcnt vmcnt(6)\n"
    "ds_read_b64_tr_b16 v[104:105], v12 offset:4096\n"
    "ds_read_b64_tr_b16 v[106:107], v12 offset:4608\n"
    "v_cmp_ne_u32 vcc, s56, v121\n"
    "v_mov_b32 v124, v120\n"
    "s_cbranch_vccnz .LL_CSRETRY2_%=\n"
    ".LL_CSOK2_%=:\n"
    "v_mul_f32 v94, v94, v112\n"
    "v_mfma_f32_32x32x16_bf16 v[48:63], v[108:111], v[252:255], v[48:63]\n"
    "s_waitcnt lgkmcnt(5)\n"
    "buffer_load_dwordx4 v10, s[36:39], s85 offen lds\n"
    "buffer_load_dwordx4 v11, s[36:39], s85 offen offset:1024 lds\n"
    "v_add_f32 v113, 1.0, v113\n"
    "v_rcp_f32 v113, v113\n"
    "v_mfma_f32_32x32x16_bf16 v[64:79], v[124:127], v[128:131], v[32:47]\n"
    "s_sub_u32 s88, s88, 1\n"
    "s_nop 0\n"
    "v_mul_f32 v95, v95, v113\n"
    "v_cvt_pk_bf16_f32 v119, v94, v95\n"
    "v_mfma_f32_32x32x16_bf16 v[80:95], v[124:127], v[132:135], v[48:63]\n"
    "s_cmp_lg_u32 s88, 0\n"
    "ds_write_b64 v15, v[118:119] offset:520\n"
    "v_add_u32 v14, 0x4000, v14\n"
    "v_add_u32 v15, 0x4000, v15\n"
    "s_cbranch_scc1 .LL_STEP2_%=\n"
    "v_add_u32 v12, 81920, v23\n"
    "v_add_u32 v13, 147456, v23\n"
    "v_mov_b32 v156, s87\n"
    "v_mov_b32 v158, v122\n"
    "v_mov_b32 v160, v123\n"
    "ds_read_b128 a[0:3], v12 offset:0\n"
    "ds_read_b128 a[4:7], v12 offset:1024\n"
    "ds_read_b128 a[8:11], v12 offset:2048\n"
    "ds_read_b128 a[12:15], v12 offset:3072\n"
    "v_add_u32 v16, 65536, v23\n"
    "v_subrev_u32 v14, 98304, v14\n"
    "v_subrev_u32 v15, 98304, v15\n"
    "s_mov_b32 s86, 128\n"
    "s_mov_b32 s92, 81920\n"
    "v_pk_mul_f32 v[64:65], v[64:65], v[158:159] op_sel_hi:[1,0]\n"
    "v_pk_mul_f32 v[66:67], v[66:67], v[158:159] op_sel_hi:[1,0]\n"
    "v_pk_mul_f32 v[68:69], v[68:69], v[158:159] op_sel_hi:[1,0]\n"
    "v_pk_mul_f32 v[70:71], v[70:71], v[158:159] op_sel_hi:[1,0]\n"
    "v_pk_fma_f32 v[32:33], v[64:65], v[64:65], v[156:157] op_sel_hi:[1,1,0] neg_lo:[1,0,0] neg_hi:[1,0,0]\n"
    "v_pk_fma_f32 v[34:35], v[66:67], v[66:67], v[156:157] op_sel_hi:[1,1,0] neg_lo:[1,0,0] neg_hi:[1,0,0]\n"
    "v_pk_fma_f32 v[36:37], v[68:69], v[68:69], v[156:157] op_sel_hi:[1,1,0] neg_lo:[1,0,0] neg_hi:[1,0,0]\n"
    "v_pk_fma_f32 v[38:39], v[70:71], v[70:71], v[156:157] op_sel_hi:[1,1,0] neg_lo:[1,0,0] neg_hi:[1,0,0]\n"
    "v_pk_mul_f32 v[32:33], v[64:65], v[32:33]\n"
    "v_pk_mul_f32 v[34:35], v[66:67], v[34:35]\n"
    "v_pk_mul_f32 v[36:37], v[68:69], v[36:37]\n"
    "v_pk_mul_f32 v[38:39], v[70:71], v[38:39]\n"
    "s_mov_b64 s[48:49], %9\n"
    "s_mov_b32 s50, 1152\n"
    "s_mov_b32 s51, 0x20000\n"
    "v_lshrrev_b32 v3, 4, v1\n"
    "v_and_b32 v2, 3, v1\n"
    "v_lshl_add_u32 v4, v3, 2, v2\n"
    "v_lshlrev_b32 v4, 4, v4\n"
    "v_add_u32 v5, v4, v171\n"
    "ds_read_b128 a[16:19], v12 offset:8192\n"
    "ds_read_b128 a[20:23], v12 offset:9216\n"
    "ds_read_b128 a[24:27], v12 offset:10240\n"
    "ds_read_b128 a[28:31], v12 offset:11264\n"
    "buffer_load_dwordx4 v[132:135], v4, s[48:51], 0 offen offset:0\n"
    "buffer_load_dwordx4 v[136:139], v4, s[48:51], 0 offen offset:256\n"
    "v_exp_f32 v32, v32\n"
    "v_exp_f32 v33, v33\n"
    "v_exp_f32 v34, v34\n"
    "v_exp_f32 v35, v35\n"
    "v_exp_f32 v36, v36\n"
    "v_exp_f32 v37, v37\n"
    "v_exp_f32 v38, v38\n"
    "v_exp_f32 v39, v39\n"
    "v_pk_add_f32 v[32:33], v[32:33], 1.0 op_sel_hi:[1,0]\n"
    "v_pk_add_f32 v[34:35], v[34:35], 1.0 op_sel_hi:[1,0]\n"
    "v_pk_add_f32 v[36:37], v[36:37], 1.0 op_sel_hi:[1,0]\n"
    "v_pk_add_f32 v[38:39], v[38:39], 1.0 op_sel_hi:[1,0]\n"
    "ds_read_b128 a[32:35], v12 offset:16384\n"
    "ds_read_b128 a[36:39], v12 offset:17408\n"
    "ds_read_b128 a[40:43], v12 offset:18432\n"
    "ds_read_b128 a[44:47], v12 offset:19456\n"
    "buffer_load_dwordx4 v[140:143], v4, s[48:51], 0 offen offset:512\n"
    "buffer_load_dwordx4 v[144:147], v4, s[48:51], 0 offen offset:768\n"
    "v_rcp_f32 v32, v32\n"
    "v_rcp_f32 v33, v33\n"
    "v_rcp_f32 v34, v34\n"
    "v_rcp_f32 v35, v35\n"
    "v_rcp_f32 v36, v36\n"
    "v_rcp_f32 v37, v37\n"
    "v_rcp_f32 v38, v38\n"
    "v_rcp_f32 v39, v39\n"
    "v_pk_mul_f32 v[64:65], v[64:65], v[32:33]\n"
    "v_pk_mul_f32 v[66:67], v[66:67], v[34:35]\n"
    "v_pk_mul_f32 v[68:69], v[68:69], v[36:37]\n"
    "v_pk_mul_f32 v[70:71], v[70:71], v[38:39]\n"
    "ds_read_b128 a[48:51], v12 offset:24576\n"
    "ds_read_b128 a[52:55], v12 offset:25600\n"
    "ds_read_b128 a[56:59], v12 offset:26624\n"
    "ds_read_b128 a[60:63], v12 offset:27648\n"
    "buffer_load_dwordx4 v[148:151], v4, s[48:51], 0 offen offset:1024\n"
    "buffer_load_dwordx4 v[112:115], v5, s[40:43], 0 offen offset:0\n"
    "v_cvt_pk_bf16_f32 v64, v64, v65\n"
    "v_cvt_pk_bf16_f32 v65, v66, v67\n"
    "v_cvt_pk_bf16_f32 v66, v68, v69\n"
    "v_cvt_pk_bf16_f32 v67, v70, v71\n"
    "v_pk_mul_f32 v[72:73], v[72:73], v[158:159] op_sel_hi:[1,0]\n"
    "v_pk_mul_f32 v[74:75], v[74:75], v[158:159] op_sel_hi:[1,0]\n"
    "v_pk_mul_f32 v[76:77], v[76:77], v[158:159] op_sel_hi:[1,0]\n"
    "v_pk_mul_f32 v[78:79], v[78:79], v[158:159] op_sel_hi:[1,0]\n"
    "v_pk_fma_f32 v[40:41], v[72:73], v[72:73], v[156:157] op_sel_hi:[1,1,0] neg_lo:[1,0,0] neg_hi:[1,0,0]\n"
    "v_pk_fma_f32 v[42:43], v[74:75], v[74:75], v[156:157] op_sel_hi:[1,1,0] neg_lo:[1,0,0] neg_hi:[1,0,0]\n"
    "v_pk_fma_f32 v[44:45], v[76:77], v[76:77], v[156:157] op_sel_hi:[1,1,0] neg_lo:[1,0,0] neg_hi:[1,0,0]\n"
    "v_pk_fma_f32 v[46:47], v[78:79], v[78:79], v[156:157] op_sel_hi:[1,1,0] neg_lo:[1,0,0] neg_hi:[1,0,0]\n"
    "v_pk_mul_f32 v[40:41], v[72:73], v[40:41]\n"
    "v_pk_mul_f32 v[42:43], v[74:75], v[42:43]\n"
    "v_pk_mul_f32 v[44:45], v[76:77], v[44:45]\n"
    "v_pk_mul_f32 v[46:47], v[78:79], v[46:47]\n"
    "s_waitcnt lgkmcnt(15)\n"
    "s_barrier\n"
    "ds_read_b128 a[64:67], v12 offset:32768\n"
    "ds_read_b128 a[68:71], v12 offset:33792\n"
    "ds_read_b128 a[72:75], v12 offset:34816\n"
    "ds_read_b128 a[76:79], v12 offset:35840\n"
    "buffer_load_dwordx4 v[116:119], v5, s[40:43], 0 offen offset:256\n"
    "buffer_load_dwordx4 v[120:123], v5, s[40:43], 0 offen offset:512\n"
    "v_exp_f32 v40, v40\n"
    "v_exp_f32 v41, v41\n"
    "v_exp_f32 v42, v42\n"
    "v_exp_f32 v43, v43\n"
    "v_exp_f32 v44, v44\n"
    "v_exp_f32 v45, v45\n"
    "v_exp_f32 v46, v46\n"
    "v_exp_f32 v47, v47\n"
    "v_pk_add_f32 v[40:41], v[40:41], 1.0 op_sel_hi:[1,0]\n"
    "v_pk_add_f32 v[42:43], v[42:43], 1.0 op_sel_hi:[1,0]\n"
    "v_pk_add_f32 v[44:45], v[44:45], 1.0 op_sel_hi:[1,0]\n"
    "v_pk_add_f32 v[46:47], v[46:47], 1.0 op_sel_hi:[1,0]\n"
    "ds_read_b128 a[80:83], v12 offset:40960\n"
    "ds_read_b128 a[84:87], v12 offset:41984\n"
    "ds_read_b128 a[88:91], v12 offset:43008\n"
    "ds_read_b128 a[92:95], v12 offset:44032\n"
    "buffer_load_dwordx4 v[124:127], v5, s[40:43], 0 offen offset:768\n"
    "v_rcp_f32 v40, v40\n"
    "v_rcp_f32 v41, v41\n"
    "v_rcp_f32 v42, v42\n"
    "v_rcp_f32 v43, v43\n"
    "v_rcp_f32 v44, v44\n"
    "v_rcp_f32 v45, v45\n"
    "v_rcp_f32 v46, v46\n"
    "v_rcp_f32 v47, v47\n"
    "v_pk_mul_f32 v[72:73], v[72:73], v[40:41]\n"
    "v_pk_mul_f32 v[74:75], v[74:75], v[42:43]\n"
    "v_pk_mul_f32 v[76:77], v[76:77], v[44:45]\n"
    "v_pk_mul_f32 v[78:79], v[78:79], v[46:47]\n"
    "v_cmp_gt_u32 vcc, 32, v1\n"
    "v_mov_b32 v4, 1879048192\n"
    "s_and_b32 s30, s83, 1\n"
    "v_cndmask_b32 v5, v4, v5, vcc\n"
    "s_mov_b64 s[48:49], %8\n"
    "ds_read_b128 a[96:99], v12 offset:49152\n"
    "ds_read_b128 a[100:103], v12 offset:50176\n"
    "ds_read_b128 a[104:107], v12 offset:51200\n"
    "ds_read_b128 a[108:111], v12 offset:52224\n"
    "buffer_load_dwordx4 v[128:131], v5, s[40:43], 0 offen offset:1024\n"
    "v_cvt_pk_bf16_f32 v68, v72, v73\n"
    "v_cvt_pk_bf16_f32 v69, v74, v75\n"
    "v_cvt_pk_bf16_f32 v70, v76, v77\n"
    "v_cvt_pk_bf16_f32 v71, v78, v79\n"
    "v_pk_mul_f32 v[80:81], v[80:81], v[160:161] op_sel_hi:[1,0]\n"
    "v_pk_mul_f32 v[82:83], v[82:83], v[160:161] op_sel_hi:[1,0]\n"
    "v_pk_mul_f32 v[84:85], v[84:85], v[160:161] op_sel_hi:[1,0]\n"
    "v_pk_mul_f32 v[86:87], v[86:87], v[160:161] op_sel_hi:[1,0]\n"
    "v_pk_fma_f32 v[32:33], v[80:81], v[80:81], v[156:157] op_sel_hi:[1,1,0] neg_lo:[1,0,0] neg_hi:[1,0,0]\n"
    "v_pk_fma_f32 v[34:35], v[82:83], v[82:83], v[156:157] op_sel_hi:[1,1,0] neg_lo:[1,0,0] neg_hi:[1,0,0]\n"
    "v_pk_fma_f32 v[36:37], v[84:85], v[84:85], v[156:157] op_sel_hi:[1,1,0] neg_lo:[1,0,0] neg_hi:[1,0,0]\n"
    "v_pk_fma_f32 v[38:39], v[86:87], v[86:87], v[156:157] op_sel_hi:[1,1,0] neg_lo:[1,0,0] neg_hi:[1,0,0]\n"
    "v_pk_mul_f32 v[32:33], v[80:81], v[32:33]\n"
    "v_pk_mul_f32 v[34:35], v[82:83], v[34:35]\n"
    "v_pk_mul_f32 v[36:37], v[84:85], v[36:37]\n"
    "v_pk_mul_f32 v[38:39], v[86:87], v[38:39]\n"
    "s_lshl_b32 s30, s30, 6\n"
    "v_lshlrev_b32 v2, 3, v2\n"
    "v_and_b32 v4, 1, v3\n"
    "v_lshl_add_u32 v2, v4, 5, v2\n"
    "v_lshrrev_b32 v4, 1, v3\n"
    "v_lshl_add_u32 v2, v4, 7, v2\n"
    "v_add_u32 v2, s30, v2\n"
    "v_add_u32 v171, v2, v171\n"
    "ds_read_b128 a[112:115], v12 offset:57344\n"
    "ds_read_b128 a[116:119], v12 offset:58368\n"
    "ds_read_b128 a[120:123], v12 offset:59392\n"
    "ds_read_b128 a[124:127], v12 offset:60416\n"
    "buffer_load_dwordx2 v[200:201], v2, s[48:51], 0 offen offset:0\n"
    "buffer_load_dwordx2 v[202:203], v2, s[48:51], 0 offen offset:256\n"
    "v_exp_f32 v32, v32\n"
    "v_exp_f32 v33, v33\n"
    "v_exp_f32 v34, v34\n"
    "v_exp_f32 v35, v35\n"
    "v_exp_f32 v36, v36\n"
    "v_exp_f32 v37, v37\n"
    "v_exp_f32 v38, v38\n"
    "v_exp_f32 v39, v39\n"
    "v_pk_add_f32 v[32:33], v[32:33], 1.0 op_sel_hi:[1,0]\n"
    "v_pk_add_f32 v[34:35], v[34:35], 1.0 op_sel_hi:[1,0]\n"
    "v_pk_add_f32 v[36:37], v[36:37], 1.0 op_sel_hi:[1,0]\n"
    "v_pk_add_f32 v[38:39], v[38:39], 1.0 op_sel_hi:[1,0]\n"
    "ds_read_b128 a[128:131], v13 offset:0\n"
    "ds_read_b128 a[132:135], v13 offset:1024\n"
    "ds_read_b128 a[136:139], v13 offset:2048\n"
    "ds_read_b128 a[140:143], v13 offset:3072\n"
    "buffer_load_dwordx2 v[204:205], v2, s[48:51], 0 offen offset:512\n"
    "buffer_load_dwordx2 v[206:207], v2, s[48:51], 0 offen offset:768\n"
    "v_rcp_f32 v32, v32\n"
    "v_rcp_f32 v33, v33\n"
    "v_rcp_f32 v34, v34\n"
    "v_rcp_f32 v35, v35\n"
    "v_rcp_f32 v36, v36\n"
    "v_rcp_f32 v37, v37\n"
    "v_rcp_f32 v38, v38\n"
    "v_rcp_f32 v39, v39\n"
    "v_pk_mul_f32 v[80:81], v[80:81], v[32:33]\n"
    "v_pk_mul_f32 v[82:83], v[82:83], v[34:35]\n"
    "v_pk_mul_f32 v[84:85], v[84:85], v[36:37]\n"
    "v_pk_mul_f32 v[86:87], v[86:87], v[38:39]\n"
    "ds_read_b128 a[144:147], v13 offset:8192\n"
    "ds_read_b128 a[148:151], v13 offset:9216\n"
    "ds_read_b128 a[152:155], v13 offset:10240\n"
    "ds_read_b128 a[156:159], v13 offset:11264\n"
    "buffer_load_dwordx2 v[208:209], v2, s[48:51], 0 offen offset:1024\n"
    "v_cvt_pk_bf16_f32 v72, v80, v81\n"
    "v_cvt_pk_bf16_f32 v73, v82, v83\n"
    "v_cvt_pk_bf16_f32 v74, v84, v85\n"
    "v_cvt_pk_bf16_f32 v75, v86, v87\n"
    "v_pk_mul_f32 v[88:89], v[88:89], v[160:161] op_sel_hi:[1,0]\n"
    "v_pk_mul_f32 v[90:91], v[90:91], v[160:161] op_sel_hi:[1,0]\n"
    "v_pk_mul_f32 v[92:93], v[92:93], v[160:161] op_sel_hi:[1,0]\n"
    "v_pk_mul_f32 v[94:95], v[94:95], v[160:161] op_sel_hi:[1,0]\n"
    "v_pk_fma_f32 v[40:41], v[88:89], v[88:89], v[156:157] op_sel_hi:[1,1,0] neg_lo:[1,0,0] neg_hi:[1,0,0]\n"
    "v_pk_fma_f32 v[42:43], v[90:91], v[90:91], v[156:157] op_sel_hi:[1,1,0] neg_lo:[1,0,0] neg_hi:[1,0,0]\n"
    "v_pk_fma_f32 v[44:45], v[92:93], v[92:93], v[156:157] op_sel_hi:[1,1,0] neg_lo:[1,0,0] neg_hi:[1,0,0]\n"
    "v_pk_fma_f32 v[46:47], v[94:95], v[94:95], v[156:157] op_sel_hi:[1,1,0] neg_lo:[1,0,0] neg_hi:[1,0,0]\n"
    "v_pk_mul_f32 v[40:41], v[88:89], v[40:41]\n"
    "v_pk_mul_f32 v[42:43], v[90:91], v[42:43]\n"
    "v_pk_mul_f32 v[44:45], v[92:93], v[44:45]\n"
    "v_pk_mul_f32 v[46:47], v[94:95], v[46:47]\n"
    "ds_read_b64_tr_b16 v[96:97], v21 offset:0\n"
    "ds_read_b64_tr_b16 v[98:99], v21 offset:1024\n"
    "ds_read_b64_tr_b16 v[100:101], v21 offset:4096\n"
    "ds_read_b64_tr_b16 v[102:103], v21 offset:5120\n"
    "v_exp_f32 v40, v40\n"
    "v_exp_f32 v41, v41\n"
    "v_exp_f32 v42, v42\n"
    "v_exp_f32 v43, v43\n"
    "v_exp_f32 v44, v44\n"
    "v_exp_f32 v45, v45\n"
    "v_exp_f32 v46, v46\n"
    "v_exp_f32 v47, v47\n"
    "v_pk_add_f32 v[40:41], v[40:41], 1.0 op_sel_hi:[1,0]\n"
    "v_pk_add_f32 v[42:43], v[42:43], 1.0 op_sel_hi:[1,0]\n"
    "v_pk_add_f32 v[44:45], v[44:45], 1.0 op_sel_hi:[1,0]\n"
    "v_pk_add_f32 v[46:47], v[46:47], 1.0 op_sel_hi:[1,0]\n"
    "v_rcp_f32 v40, v40\n"
    "v_rcp_f32 v41, v41\n"
    "v_rcp_f32 v42, v42\n"
    "v_rcp_f32 v43, v43\n"
    "v_rcp_f32 v44, v44\n"
    "v_rcp_f32 v45, v45\n"
    "v_rcp_f32 v46, v46\n"
    "v_rcp_f32 v47, v47\n"
    "v_pk_mul_f32 v[88:89], v[88:89], v[40:41]\n"
    "v_pk_mul_f32 v[90:91], v[90:91], v[42:43]\n"
    "v_pk_mul_f32 v[92:93], v[92:93], v[44:45]\n"
    "v_pk_mul_f32 v[94:95], v[94:95], v[46:47]\n"
    "v_cvt_pk_bf16_f32 v76, v88, v89\n"
    "v_cvt_pk_bf16_f32 v77, v90, v91\n"
    "v_cvt_pk_bf16_f32 v78, v92, v93\n"
    "v_cvt_pk_bf16_f32 v79, v94, v95\n"
    "s_waitcnt lgkmcnt(0)\n"
    "s_barrier\n"
    "s_add_u32 s85, s64, s86\n"
    "s_add_u32 m0, s89, 81920\n"
    "v_mfma_f32_16x16x32_bf16 v[32:35], v[96:99], a[0:3], 0\n"
    "ds_read_b64_tr_b16 v[104:105], v22 offset:0\n"
    "v_and_b32 v184, 15, v1\n"
    "v_mfma_f32_16x16x32_bf16 v[32:35], v[100:103], a[16:19], v[32:35]\n"
    "ds_read_b64_tr_b16 v[106:107], v22 offset:1024\n"
    "v_lshrrev_b32 v185, 4, v1\n"
    "v_mfma_f32_16x16x32_bf16 v[36:39], v[96:99], a[4:7], 0\n"
    "buffer_load_dwordx4 v10, s[36:39], s85 offen lds\n"
    "v_mfma_f32_16x16x32_bf16 v[36:39], v[100:103], a[20:23], v[36:39]\n"
    "ds_read_b64_tr_b16 v[108:109], v22 offset:4096\n"
    "v_lshrrev_b32 v6, 2, v184\n"
    "v_mfma_f32_16x16x32_bf16 v[40:43], v[96:99], a[8:11], 0\n"
    "ds_read_b64_tr_b16 v[110:111], v22 offset:5120\n"
    "v_lshlrev_b32 v6, 8, v6\n"
    "v_mfma_f32_16x16x32_bf16 v[40:43], v[100:103], a[24:27], v[40:43]\n"
    "ds_write_b64 v14, v[64:65] offset:0\n"
    "v_mfma_f32_16x16x32_bf16 v[44:47], v[96:99], a[12:15], 0\n"
    "buffer_load_dwordx4 v11, s[36:39], s85 offen offset:1024 lds\n"
    "v_mfma_f32_16x16x32_bf16 v[44:47], v[100:103], a[28:31], v[44:47]\n"
    "s_waitcnt lgkmcnt(3)\n"
    "ds_write_b64 v14, v[66:67] offset:8\n"
    "v_mfma_f32_16x16x32_bf16 v[48:51], v[104:107], a[0:3], 0\n"
    "s_waitcnt lgkmcnt(2)\n"
    "ds_read_b64_tr_b16 v[96:97], v21 offset:8192\n"
    "v_and_b32 v184, 3, v184\n"
    "v_mfma_f32_16x16x32_bf16 v[48:51], v[108:111], a[16:19], v[48:51]\n"
    "s_add_u32 s85, s65, s86\n"
    "s_add_u32 m0, s89, 90112\n"
    "ds_read_b64_tr_b16 v[98:99], v21 offset:9216\n"
    "v_mfma_f32_16x16x32_bf16 v[52:55], v[104:107], a[4:7], 0\n"
    "buffer_load_dwordx4 v10, s[36:39], s85 offen lds\n"
    "v_mfma_f32_16x16x32_bf16 v[52:55], v[108:111], a[20:23], v[52:55]\n"
    "ds_read_b64_tr_b16 v[100:101], v21 offset:12288\n"
    "v_lshl_add_u32 v6, v184, 4, v6\n"
    "v_mfma_f32_16x16x32_bf16 v[56:59], v[104:107], a[8:11], 0\n"
    "ds_read_b64_tr_b16 v[102:103], v21 offset:13312\n"
    "v_lshl_add_u32 v6, v185, 6, v6\n"
    "v_mfma_f32_16x16x32_bf16 v[56:59], v[108:111], a[24:27], v[56:59]\n"
    "ds_write_b64 v14, v[68:69] offset:512\n"
    "v_mfma_f32_16x16x32_bf16 v[60:63], v[104:107], a[12:15], 0\n"
    "buffer_load_dwordx4 v11, s[36:39], s85 offen offset:1024 lds\n"
    "v_mfma_f32_16x16x32_bf16 v[60:63], v[108:111], a[28:31], v[60:63]\n"
    "s_waitcnt lgkmcnt(3)\n"
    "ds_write_b64 v14, v[70:71] offset:520\n"
    "v_mfma_f32_16x16x32_bf16 v[32:35], v[96:99], a[32:35], v[32:35]\n"
    "s_waitcnt lgkmcnt(2)\n"
    "ds_read_b64_tr_b16 v[104:105], v22 offset:8192\n"
    "v_add_u32 v7, 294912, v6\n"
    "v_mfma_f32_16x16x32_bf16 v[32:35], v[100:103], a[48:51], v[32:35]\n"
    "s_add_u32 s85, s66, s86\n"
    "s_add_u32 m0, s89, 98304\n"
    "ds_read_b64_tr_b16 v[106:107], v22 offset:9216\n"
    "v_mfma_f32_16x16x32_bf16 v[36:39], v[96:99], a[36:39], v[36:39]\n"
    "buffer_load_dwordx4 v10, s[36:39], s85 offen lds\n"
    "v_mfma_f32_16x16x32_bf16 v[36:39], v[100:103], a[52:55], v[36:39]\n"
    "ds_read_b64_tr_b16 v[108:109], v22 offset:12288\n"
    "v_add_u32 v8, 589824, v6\n"
    "v_mfma_f32_16x16x32_bf16 v[40:43], v[96:99], a[40:43], v[40:43]\n"
    "ds_read_b64_tr_b16 v[110:111], v22 offset:13312\n"
    "v_add_u32 v9, 884736, v6\n"
    "v_mfma_f32_16x16x32_bf16 v[40:43], v[100:103], a[56:59], v[40:43]\n"
    "ds_write_b64 v15, v[72:73] offset:0\n"
    "v_mfma_f32_16x16x32_bf16 v[44:47], v[96:99], a[44:47], v[44:47]\n"
    "buffer_load_dwordx4 v11, s[36:39], s85 offen offset:1024 lds\n"
    "v_mfma_f32_16x16x32_bf16 v[44:47], v[100:103], a[60:63], v[44:47]\n"
    "s_waitcnt vmcnt(27) lgkmcnt(1)\n"
    "s_barrier\n"
    "v_mfma_f32_16x16x32_bf16 v[48:51], v[104:107], a[32:35], v[48:51]\n"
    "s_waitcnt lgkmcnt(1)\n"
    "ds_read_b64_tr_b16 v[96:97], v21 offset:16384\n"
    "s_mov_b32 s88, 8\n"
    "v_mfma_f32_16x16x32_bf16 v[48:51], v[108:111], a[48:51], v[48:51]\n"
    "s_add_u32 s85, s67, s86\n"
    "s_add_u32 m0, s89, 106496\n"
    "ds_read_b64_tr_b16 v[98:99], v21 offset:17408\n"
    "v_mfma_f32_16x16x32_bf16 v[52:55], v[104:107], a[36:39], v[52:55]\n"
    "buffer_load_dwordx4 v10, s[36:39], s85 offen lds\n"
    "v_mfma_f32_16x16x32_bf16 v[52:55], v[108:111], a[52:55], v[52:55]\n"
    "ds_read_b64_tr_b16 v[100:101], v21 offset:20480\n"
    "s_mov_b32 s58, 1879048192\n"
    "v_mfma_f32_16x16x32_bf16 v[56:59], v[104:107], a[40:43], v[56:59]\n"
    "ds_read_b64_tr_b16 v[102:103], v21 offset:21504\n"
    "s_mov_b32 s59, 1879048192\n"
    "v_mfma_f32_16x16x32_bf16 v[56:59], v[108:111], a[56:59], v[56:59]\n"
    "ds_write_b64 v15, v[74:75] offset:8\n"
    "v_mfma_f32_16x16x32_bf16 v[60:63], v[104:107], a[44:47], v[60:63]\n"
    "buffer_load_dwordx4 v11, s[36:39], s85 offen offset:1024 lds\n"
    "v_mfma_f32_16x16x32_bf16 v[60:63], v[108:111], a[60:63], v[60:63]\n"
    "s_waitcnt lgkmcnt(3)\n"
    "ds_write_b64 v15, v[76:77] offset:512\n"
    "v_mfma_f32_16x16x32_bf16 v[32:35], v[96:99], a[64:67], v[32:35]\n"
    "s_waitcnt lgkmcnt(2)\n"
    "ds_read_b64_tr_b16 v[104:105], v22 offset:16384\n"
    "s_mov_b32 s60, 1879048192\n"
    "v_mfma_f32_16x16x32_bf16 v[32:35], v[100:103], a[80:83], v[32:35]\n"
    "s_add_u32 s85, s68, s86\n"
    "s_add_u32 m0, s89, 114688\n"
    "ds_read_b64_tr_b16 v[106:107], v22 offset:17408\n"
    "v_mfma_f32_16x16x32_bf16 v[36:39], v[96:99], a[68:71], v[36:39]\n"
    "buffer_load_dwordx4 v10, s[36:39], s85 offen lds\n"
    "v_mfma_f32_16x16x32_bf16 v[36:39], v[100:103], a[84:87], v[36:39]\n"
    "ds_read_b64_tr_b16 v[108:109], v22 offset:20480\n"
    "s_mov_b64 s[62:63], 0\n"
    "v_mfma_f32_16x16x32_bf16 v[40:43], v[96:99], a[72:75], v[40:43]\n"
    "ds_read_b64_tr_b16 v[110:111], v22 offset:21504\n"
    "v_mfma_f32_16x16x32_bf16 v[40:43], v[100:103], a[88:91], v[40:43]\n"
    "ds_write_b64 v15, v[78:79] offset:520\n"
    "v_mfma_f32_16x16x32_bf16 v[44:47], v[96:99], a[76:79], v[44:47]\n"
    "buffer_load_dwordx4 v11, s[36:39], s85 offen offset:1024 lds\n"
    "v_mfma_f32_16x16x32_bf16 v[44:47], v[100:103], a[92:95], v[44:47]\n"
    "s_waitcnt lgkmcnt(3)\n"
    "v_mfma_f32_16x16x32_bf16 v[48:51], v[104:107], a[64:67], v[48:51]\n"
    "s_waitcnt lgkmcnt(1)\n"
    "ds_read_b64_tr_b16 v[96:97], v21 offset:24576\n"
    "v_mfma_f32_16x16x32_bf16 v[48:51], v[108:111], a[80:83], v[48:51]\n"
    "s_add_u32 s85, s69, s86\n"
    "s_add_u32 m0, s89, 122880\n"
    "ds_read_b64_tr_b16 v[98:99], v21 offset:25600\n"
    "v_mfma_f32_16x16x32_bf16 v[52:55], v[104:107], a[68:71], v[52:55]\n"
    "buffer_load_dwordx4 v10, s[36:39], s85 offen lds\n"
    "v_mfma_f32_16x16x32_bf16 v[52:55], v[108:111], a[84:87], v[52:55]\n"
    "ds_read_b64_tr_b16 v[100:101], v21 offset:28672\n"
    "v_mfma_f32_16x16x32_bf16 v[56:59], v[104:107], a[72:75], v[56:59]\n"
    "ds_read_b64_tr_b16 v[102:103], v21 offset:29696\n"
    "v_mfma_f32_16x16x32_bf16 v[56:59], v[108:111], a[88:91], v[56:59]\n"
    "v_mfma_f32_16x16x32_bf16 v[60:63], v[104:107], a[76:79], v[60:63]\n"
    "buffer_load_dwordx4 v11, s[36:39], s85 offen offset:1024 lds\n"
    "v_mfma_f32_16x16x32_bf16 v[60:63], v[108:111], a[92:95], v[60:63]\n"
    "s_waitcnt lgkmcnt(2)\n"
    "v_mfma_f32_16x16x32_bf16 v[32:35], v[96:99], a[96:99], v[32:35]\n"
    "s_waitcnt lgkmcnt(0)\n"
    "ds_read_b64_tr_b16 v[104:105], v22 offset:24576\n"
    "v_mfma_f32_16x16x32_bf16 v[32:35], v[100:103], a[112:115], v[32:35]\n"
    "s_mov_b32 s86, 256\n"
    "s_add_u32 s85, s64, s86\n"
    "s_add_u32 m0, s89, 0\n"
    "ds_read_b64_tr_b16 v[106:107], v22 offset:25600\n"
    "v_mfma_f32_16x16x32_bf16 v[36:39], v[96:99], a[100:103], v[36:39]\n"
    "buffer_load_dwordx4 v10, s[36:39], s85 offen lds\n"
    "v_mfma_f32_16x16x32_bf16 v[36:39], v[100:103], a[116:119], v[36:39]\n"
    "ds_read_b64_tr_b16 v[108:109], v22 offset:28672\n"
    "v_mfma_f32_16x16x32_bf16 v[40:43], v[96:99], a[104:107], v[40:43]\n"
    "ds_read_b64_tr_b16 v[110:111], v22 offset:29696\n"
    "v_mfma_f32_16x16x32_bf16 v[40:43], v[100:103], a[120:123], v[40:43]\n"
    "v_mfma_f32_16x16x32_bf16 v[44:47], v[96:99], a[108:111], v[44:47]\n"
    "buffer_load_dwordx4 v11, s[36:39], s85 offen offset:1024 lds\n"
    "v_mfma_f32_16x16x32_bf16 v[44:47], v[100:103], a[124:127], v[44:47]\n"
    "s_waitcnt vmcnt(31) lgkmcnt(0)\n"
    "s_barrier\n"
    "v_mfma_f32_16x16x32_bf16 v[48:51], v[104:107], a[96:99], v[48:51]\n"
    "s_waitcnt lgkmcnt(0)\n"
    "ds_read_b64_tr_b16 v[96:97], v21 offset:32768\n"
    "ds_read_b128 a[160:163], v16 offset:0\n"
    "v_mfma_f32_16x16x32_bf16 v[48:51], v[108:111], a[112:115], v[48:51]\n"
    "s_add_u32 s85, s65, s86\n"
    "s_add_u32 m0, s89, 8192\n"
    "ds_read_b64_tr_b16 v[98:99], v21 offset:33792\n"
    "ds_read_b128 a[164:167], v16 offset:1024\n"
    "v_mfma_f32_16x16x32_bf16 v[52:55], v[104:107], a[100:103], v[52:55]\n"
    "buffer_load_dwordx4 v10, s[36:39], s85 offen lds\n"
    "v_mfma_f32_16x16x32_bf16 v[52:55], v[108:111], a[116:119], v[52:55]\n"
    "ds_read_b64_tr_b16 v[100:101], v21 offset:36864\n"
    "ds_read_b128 a[168:171], v16 offset:2048\n"
    "v_mfma_f32_16x16x32_bf16 v[56:59], v[104:107], a[104:107], v[56:59]\n"
    "ds_read_b64_tr_b16 v[102:103], v21 offset:37888\n"
    "ds_read_b128 a[172:175], v16 offset:3072\n"
    "v_mfma_f32_16x16x32_bf16 v[56:59], v[108:111], a[120:123], v[56:59]\n"
    "ds_read_b128 a[176:179], v16 offset:8192\n"
    "v_mfma_f32_16x16x32_bf16 v[60:63], v[104:107], a[108:111], v[60:63]\n"
    "buffer_load_dwordx4 v11, s[36:39], s85 offen offset:1024 lds\n"
    "v_mfma_f32_16x16x32_bf16 v[60:63], v[108:111], a[124:127], v[60:63]\n"
    "s_waitcnt lgkmcnt(6)\n"
    "ds_read_b128 a[180:183], v16 offset:9216\n"
    "v_mfma_f32_16x16x32_bf16 v[32:35], v[96:99], a[128:131], v[32:35]\n"
    "s_waitcnt lgkmcnt(3)\n"
    "ds_read_b64_tr_b16 v[104:105], v22 offset:32768\n"
    "ds_read_b128 a[184:187], v16 offset:10240\n"
    "v_mfma_f32_16x16x32_bf16 v[32:35], v[100:103], a[144:147], v[32:35]\n"
    "s_add_u32 s85, s66, s86\n"
    "s_add_u32 m0, s89, 16384\n"
    "ds_read_b64_tr_b16 v[106:107], v22 offset:33792\n"
    "ds_read_b128 a[188:191], v16 offset:11264\n"
    "v_mfma_f32_16x16x32_bf16 v[36:39], v[96:99], a[132:135], v[36:39]\n"
    "buffer_load_dwordx4 v10, s[36:39], s85 offen lds\n"
    "v_mfma_f32_16x16x32_bf16 v[36:39], v[100:103], a[148:151], v[36:39]\n"
    "ds_read_b64_tr_b16 v[108:109], v22 offset:36864\n"
    "v_mfma_f32_16x16x32_bf16 v[40:43], v[96:99], a[136:139], v[40:43]\n"
    "ds_read_b64_tr_b16 v[110:111], v22 offset:37888\n"
    "v_mfma_f32_16x16x32_bf16 v[40:43], v[100:103], a[152:155], v[40:43]\n"
    "v_mfma_f32_16x16x32_bf16 v[44:47], v[96:99], a[140:143], v[44:47]\n"
    "buffer_load_dwordx4 v11, s[36:39], s85 offen offset:1024 lds\n"
    "v_mfma_f32_16x16x32_bf16 v[44:47], v[100:103], a[156:159], v[44:47]\n"
    "s_waitcnt lgkmcnt(3)\n"
    "v_mfma_f32_16x16x32_bf16 v[48:51], v[104:107], a[128:131], v[48:51]\n"
    "s_waitcnt lgkmcnt(0)\n"
    "ds_read_b64_tr_b16 v[96:97], v21 offset:40960\n"
    "v_mfma_f32_16x16x32_bf16 v[48:51], v[108:111], a[144:147], v[48:51]\n"
    "s_add_u32 s85, s67, s86\n"
    "s_add_u32 m0, s89, 24576\n"
    "ds_read_b64_tr_b16 v[98:99], v21 offset:41984\n"
    "v_mfma_f32_16x16x32_bf16 v[52:55], v[104:107], a[132:135], v[52:55]\n"
    "buffer_load_dwordx4 v10, s[36:39], s85 offen lds\n"
    "v_mfma_f32_16x16x32_bf16 v[52:55], v[108:111], a[148:151], v[52:55]\n"
    "ds_read_b64_tr_b16 v[100:101], v21 offset:45056\n"
    "v_mfma_f32_16x16x32_bf16 v[56:59], v[104:107], a[136:139], v[56:59]\n"
    "ds_read_b64_tr_b16 v[102:103], v21 offset:46080\n"
    "v_mfma_f32_16x16x32_bf16 v[56:59], v[108:111], a[152:155], v[56:59]\n"
    "v_mfma_f32_16x16x32_bf16 v[60:63], v[104:107], a[140:143], v[60:63]\n"
    "buffer_load_dwordx4 v11, s[36:39], s85 offen offset:1024 lds\n"
    "v_mfma_f32_16x16x32_bf16 v[60:63], v[108:111], a[156:159], v[60:63]\n"
    "s_waitcnt lgkmcnt(2)\n"
    "v_mfma_f32_16x16x32_bf16 v[32:35], v[96:99], a[160:163], v[32:35]\n"
    "s_waitcnt lgkmcnt(0)\n"
    "ds_read_b64_tr_b16 v[104:105], v22 offset:40960\n"
    "v_add_u32 v21, s92, v21\n"
    "v_mfma_f32_16x16x32_bf16 v[32:35], v[100:103], a[176:179], v[32:35]\n"
    "s_add_u32 s85, s68, s86\n"
    "ds_read_b64_tr_b16 v[106:107], v22 offset:41984\n"
    "v_mfma_f32_16x16x32_bf16 v[36:39], v[96:99], a[164:167], v[36:39]\n"
    "s_add_u32 m0, s89, 32768\n"
    "ds_read_b64_tr_b16 v[108:109], v22 offset:45056\n"
    "v_mfma_f32_16x16x32_bf16 v[36:39], v[100:103], a[180:183], v[36:39]\n"
    "ds_read_b64_tr_b16 v[110:111], v22 offset:46080\n"
    "v_mfma_f32_16x16x32_bf16 v[40:43], v[96:99], a[168:171], v[40:43]\n"
    "v_mfma_f32_16x16x32_bf16 v[40:43], v[100:103], a[184:187], v[40:43]\n"
    "v_mfma_f32_16x16x32_bf16 v[44:47], v[96:99], a[172:175], v[44:47]\n"
    "v_add_u32 v22, s92, v22\n"
    "s_sub_u32 s92, 0, s92\n"
    "v_mfma_f32_16x16x32_bf16 v[44:47], v[100:103], a[188:191], v[44:47]\n"
    "s_waitcnt vmcnt(16) lgkmcnt(0)\n"
    "s_barrier\n"
    "v_mfma_f32_16x16x32_bf16 v[48:51], v[104:107], a[160:163], v[48:51]\n"
    "s_waitcnt lgkmcnt(0)\n"
    "ds_read_b64_tr_b16 v[96:97], v21 offset:0\n"
    "v_cvt_pk_bf16_f32 v80, v32, v33\n"
    "v_mfma_f32_16x16x32_bf16 v[48:51], v[108:111], a[176:179], v[48:51]\n"
    "ds_read_b64_tr_b16 v[98:99], v21 offset:1024\n"
    "v_cvt_pk_bf16_f32 v81, v34, v35\n"
    "v_mfma_f32_16x16x32_bf16 v[52:55], v[104:107], a[164:167], v[52:55]\n"
    "buffer_load_dwordx4 v10, s[36:39], s85 offen lds\n"
    "v_mfma_f32_16x16x32_bf16 v[52:55], v[108:111], a[180:183], v[52:55]\n"
    "ds_read_b64_tr_b16 v[100:101], v21 offset:4096\n"
    "v_cvt_pk_bf16_f32 v84, v36, v37\n"
    "v_mfma_f32_16x16x32_bf16 v[56:59], v[104:107], a[168:171], v[56:59]\n"
    "ds_read_b64_tr_b16 v[102:103], v21 offset:5120\n"
    "v_cvt_pk_bf16_f32 v85, v38, v39\n"
    "v_mfma_f32_16x16x32_bf16 v[56:59], v[108:111], a[184:187], v[56:59]\n"
    "v_cvt_pk_bf16_f32 v88, v40, v41\n"
    "v_cvt_pk_bf16_f32 v89, v42, v43\n"
    "v_mfma_f32_16x16x32_bf16 v[60:63], v[104:107], a[172:175], v[60:63]\n"
    "v_cvt_pk_bf16_f32 v92, v44, v45\n"
    "v_cvt_pk_bf16_f32 v93, v46, v47\n"
    "v_mfma_f32_16x16x32_bf16 v[60:63], v[108:111], a[188:191], v[60:63]\n"
    "s_waitcnt lgkmcnt(2)\n"
    ".LL_CHUNK2_%=:\n"
    "v_mfma_f32_16x16x32_bf16 v[32:35], v[96:99], a[0:3], 0\n"
    "s_waitcnt lgkmcnt(0)\n"
    "ds_read_b64_tr_b16 v[104:105], v22 offset:0\n"
    "v_cvt_pk_bf16_f32 v82, v48, v49\n"
    "v_mfma_f32_16x16x32_bf16 v[32:35], v[100:103], a[16:19], v[32:35]\n"
    "ds_read_b64_tr_b16 v[106:107], v22 offset:1024\n"
    "v_cvt_pk_bf16_f32 v83, v50, v51\n"
    "v_mfma_f32_16x16x32_bf16 v[36:39], v[96:99], a[4:7], 0\n"
    "buffer_load_dwordx4 v11, s[36:39], s85 offen offset:1024 lds\n"
    "v_mfma_f32_16x16x32_bf16 v[36:39], v[100:103], a[20:23], v[36:39]\n"
    "s_add_u32 s85, s69, s86\n"
    "ds_read_b64_tr_b16 v[108:109], v22 offset:4096\n"
    "v_cvt_pk_bf16_f32 v86, v52, v53\n"
    "v_mfma_f32_16x16x32_bf16 v[40:43], v[96:99], a[8:11], 0\n"
    "s_add_u32 m0, s89, 40960\n"
    "ds_read_b64_tr_b16 v[110:111], v22 offset:5120\n"
    "v_cvt_pk_bf16_f32 v87, v54, v55\n"
    "v_mfma_f32_16x16x32_bf16 v[40:43], v[100:103], a[24:27], v[40:43]\n"
    "v_cvt_pk_bf16_f32 v90, v56, v57\n"
    "v_cvt_pk_bf16_f32 v91, v58, v59\n"
    "v_mfma_f32_16x16x32_bf16 v[44:47], v[96:99], a[12:15], 0\n"
    "v_cvt_pk_bf16_f32 v94, v60, v61\n"
    "v_cvt_pk_bf16_f32 v95, v62, v63\n"
    "v_mfma_f32_16x16x32_bf16 v[44:47], v[100:103], a[28:31], v[44:47]\n"
    "s_waitcnt lgkmcnt(2)\n"
    "v_mfma_f32_16x16x32_bf16 v[48:51], v[104:107], a[0:3], 0\n"
    "s_waitcnt lgkmcnt(0)\n"
    "ds_read_b64_tr_b16 v[96:97], v21 offset:8192\n"
    "v_mfma_f32_16x16x32_bf16 v[48:51], v[108:111], a[16:19], v[48:51]\n"
    "ds_read_b64_tr_b16 v[98:99], v21 offset:9216\n"
    "v_mfma_f32_16x16x32_bf16 v[52:55], v[104:107], a[4:7], 0\n"
    "buffer_load_dwordx4 v10, s[36:39], s85 offen lds\n"
    "v_mfma_f32_16x16x32_bf16 v[52:55], v[108:111], a[20:23], v[52:55]\n"
    "ds_read_b64_tr_b16 v[100:101], v21 offset:12288\n"
    "v_mfma_f32_16x16x32_bf16 v[56:59], v[104:107], a[8:11], 0\n"
    "ds_read_b64_tr_b16 v[102:103], v21 offset:13312\n"
    "v_mfma_f32_16x16x32_bf16 v[56:59], v[108:111], a[24:27], v[56:59]\n"
    "v_mfma_f32_16x16x32_bf16 v[60:63], v[104:107], a[12:15], 0\n"
    "buffer_store_dwordx4 v[80:83], v6, s[44:47], s95 offen sc1\n"
    "v_mfma_f32_16x16x32_bf16 v[60:63], v[108:111], a[28:31], v[60:63]\n"
    "s_waitcnt lgkmcnt(2)\n"
    "v_mfma_f32_16x16x32_bf16 v[32:35], v[96:99], a[32:35], v[32:35]\n"
    "s_waitcnt lgkmcnt(0)\n"
    "ds_read_b64_tr_b16 v[104:105], v22 offset:8192\n"
    "v_mfma_f32_16x16x32_bf16 v[32:35], v[100:103], a[48:51], v[32:35]\n"
    "ds_read_b64_tr_b16 v[106:107], v22 offset:9216\n"
    "v_mfma_f32_16x16x32_bf16 v[36:39], v[96:99], a[36:39], v[36:39]\n"
    "buffer_load_dwordx4 v11, s[36:39], s85 offen offset:1024 lds\n"
    "v_mfma_f32_16x16x32_bf16 v[36:39], v[100:103], a[52:55], v[36:39]\n"
    "s_sub_u32 s89, s89, s92\n"
    "ds_read_b64_tr_b16 v[108:109], v22 offset:12288\n"
    "v_mfma_f32_16x16x32_bf16 v[40:43], v[96:99], a[40:43], v[40:43]\n"
    "s_add_u32 s86, s86, 128\n"
    "ds_read_b64_tr_b16 v[110:111], v22 offset:13312\n"
    "v_mfma_f32_16x16x32_bf16 v[40:43], v[100:103], a[56:59], v[40:43]\n"
    "s_cmp_le_u32 s88, 2\n"
    "s_cselect_b32 s86, 1879048192, s86\n"
    "s_add_u32 s60, s60, 16384\n"
    "s_cmp_eq_u32 s88, 6\n"
    "s_cselect_b32 s60, s99, s60\n"
    "v_mfma_f32_16x16x32_bf16 v[44:47], v[96:99], a[44:47], v[44:47]\n"
    "s_add_u32 s85, s64, s86\n"
    "v_mfma_f32_16x16x32_bf16 v[44:47], v[100:103], a[60:63], v[44:47]\n"
    "s_waitcnt vmcnt(13) lgkmcnt(0)\n"
    "s_barrier\n"
    "v_mfma_f32_16x16x32_bf16 v[48:51], v[104:107], a[32:35], v[48:51]\n"
    "s_waitcnt lgkmcnt(0)\n"
    "ds_read_b64_tr_b16 v[96:97], v21 offset:16384\n"
    "v_mfma_f32_16x16x32_bf16 v[48:51], v[108:111], a[48:51], v[48:51]\n"
    "s_add_u32 m0, s89, 0\n"
    "ds_read_b64_tr_b16 v[98:99], v21 offset:17408\n"
    "v_mfma_f32_16x16x32_bf16 v[52:55], v[104:107], a[36:39], v[52:55]\n"
    "buffer_load_dwordx4 v10, s[36:39], s85 offen lds\n"
    "v_mfma_f32_16x16x32_bf16 v[52:55], v[108:111], a[52:55], v[52:55]\n"
    "ds_read_b64_tr_b16 v[100:101], v21 offset:20480\n"
    "v_mfma_f32_16x16x32_bf16 v[56:59], v[104:107], a[40:43], v[56:59]\n"
    "ds_read_b64_tr_b16 v[102:103], v21 offset:21504\n"
    "v_mfma_f32_16x16x32_bf16 v[56:59], v[108:111], a[56:59], v[56:59]\n"
    "v_mfma_f32_16x16x32_bf16 v[60:63], v[104:107], a[44:47], v[60:63]\n"
    "buffer_store_dwordx4 v[84:87], v7, s[44:47], s95 offen sc1\n"
    "v_mfma_f32_16x16x32_bf16 v[60:63], v[108:111], a[60:63], v[60:63]\n"
    "s_waitcnt lgkmcnt(2)\n"
    "v_mfma_f32_16x16x32_bf16 v[32:35], v[96:99], a[64:67], v[32:35]\n"
    "s_waitcnt lgkmcnt(0)\n"
    "ds_read_b64_tr_b16 v[104:105], v22 offset:16384\n"
    "v_mfma_f32_16x16x32_bf16 v[32:35], v[100:103], a[80:83], v[32:35]\n"
    "ds_read_b64_tr_b16 v[106:107], v22 offset:17408\n"
    "v_mfma_f32_16x16x32_bf16 v[36:39], v[96:99], a[68:71], v[36:39]\n"
    "buffer_load_dwordx4 v11, s[36:39], s85 offen offset:1024 lds\n"
    "v_mfma_f32_16x16x32_bf16 v[36:39], v[100:103], a[84:87], v[36:39]\n"
    "s_add_u32 s85, s65, s86\n"
    "ds_read_b64_tr_b16 v[108:109], v22 offset:20480\n"
    "v_mfma_f32_16x16x32_bf16 v[40:43], v[96:99], a[72:75], v[40:43]\n"
    "s_add_u32 m0, s89, 8192\n"
    "ds_read_b64_tr_b16 v[110:111], v22 offset:21504\n"
    "v_mfma_f32_16x16x32_bf16 v[40:43], v[100:103], a[88:91], v[40:43]\n"
    "v_mfma_f32_16x16x32_bf16 v[44:47], v[96:99], a[76:79], v[44:47]\n"
    "buffer_store_dwordx4 v[88:91], v8, s[44:47], s95 offen sc1\n"
    "v_mfma_f32_16x16x32_bf16 v[44:47], v[100:103], a[92:95], v[44:47]\n"
    "s_waitcnt lgkmcnt(2)\n"
    "v_mfma_f32_16x16x32_bf16 v[48:51], v[104:107], a[64:67], v[48:51]\n"
    "s_waitcnt lgkmcnt(0)\n"
    "ds_read_b64_tr_b16 v[96:97], v21 offset:24576\n"
    "v_mfma_f32_16x16x32_bf16 v[48:51], v[108:111], a[80:83], v[48:51]\n"
    "ds_read_b64_tr_b16 v[98:99], v21 offset:25600\n"
    "v_mfma_f32_16x16x32_bf16 v[52:55], v[104:107], a[68:71], v[52:55]\n"
    "buffer_load_dwordx4 v10, s[36:39], s85 offen lds\n"
    "v_mfma_f32_16x16x32_bf16 v[52:55], v[108:111], a[84:87], v[52:55]\n"
    "ds_read_b64_tr_b16 v[100:101], v21 offset:28672\n"
    "v_mfma_f32_16x16x32_bf16 v[56:59], v[104:107], a[72:75], v[56:59]\n"
    "ds_read_b64_tr_b16 v[102:103], v21 offset:29696\n"
    "v_mfma_f32_16x16x32_bf16 v[56:59], v[108:111], a[88:91], v[56:59]\n"
    "s_waitcnt vmcnt(11)\n"
    "v_mfma_f32_16x16x32_bf16 v[184:187], v[172:175], v[188:191], 0\n"
    "v_mfma_f32_16x16x32_bf16 v[184:187], v[172:175], v[192:195], v[184:187]\n"
    "v_mfma_f32_16x16x32_bf16 v[184:187], v[172:175], v[196:199], v[184:187]\n"
    "v_mfma_f32_16x16x32_bf16 v[60:63], v[104:107], a[76:79], v[60:63]\n"
    "buffer_store_dwordx4 v[92:95], v9, s[44:47], s95 offen sc1\n"
    "v_mfma_f32_16x16x32_bf16 v[60:63], v[108:111], a[92:95], v[60:63]\n"
    "s_waitcnt lgkmcnt(2)\n"
    "v_mfma_f32_16x16x32_bf16 v[32:35], v[96:99], a[96:99], v[32:35]\n"
    "s_waitcnt lgkmcnt(0)\n"
    "ds_read_b64_tr_b16 v[104:105], v22 offset:24576\n"
    "v_mfma_f32_16x16x32_bf16 v[32:35], v[100:103], a[112:115], v[32:35]\n"
    "ds_read_b64_tr_b16 v[106:107], v22 offset:25600\n"
    "v_mfma_f32_16x16x32_bf16 v[36:39], v[96:99], a[100:103], v[36:39]\n"
    "buffer_load_dwordx4 v11, s[36:39], s85 offen offset:1024 lds\n"
    "v_mfma_f32_16x16x32_bf16 v[36:39], v[100:103], a[116:119], v[36:39]\n"
    "s_add_u32 s85, s66, s86\n"
    "ds_read_b64_tr_b16 v[108:109], v22 offset:28672\n"
    "v_mfma_f32_16x16x32_bf16 v[40:43], v[96:99], a[104:107], v[40:43]\n"
    "s_add_u32 m0, s89, 16384\n"
    "ds_read_b64_tr_b16 v[110:111], v22 offset:29696\n"
    "v_mfma_f32_16x16x32_bf16 v[40:43], v[100:103], a[120:123], v[40:43]\n"
    "v_mfma_f32_16x16x32_bf16 v[44:47], v[96:99], a[108:111], v[44:47]\n"
    "v_mfma_f32_16x16x32_bf16 v[44:47], v[100:103], a[124:127], v[44:47]\n"
    "s_waitcnt vmcnt(20) lgkmcnt(0)\n"
    "s_barrier\n"
    "v_mfma_f32_16x16x32_bf16 v[48:51], v[104:107], a[96:99], v[48:51]\n"
    "s_waitcnt lgkmcnt(0)\n"
    "ds_read_b64_tr_b16 v[96:97], v21 offset:32768\n"
    "v_mfma_f32_16x16x32_bf16 v[48:51], v[108:111], a[112:115], v[48:51]\n"
    "ds_read_b64_tr_b16 v[98:99], v21 offset:33792\n"
    "v_mfma_f32_16x16x32_bf16 v[52:55], v[104:107], a[100:103], v[52:55]\n"
    "buffer_load_dwordx4 v10, s[36:39], s85 offen lds\n"
    "v_mfma_f32_16x16x32_bf16 v[52:55], v[108:111], a[116:119], v[52:55]\n"
    "ds_read_b64_tr_b16 v[100:101], v21 offset:36864\n"
    "v_mfma_f32_16x16x32_bf16 v[56:59], v[104:107], a[104:107], v[56:59]\n"
    "ds_read_b64_tr_b16 v[102:103], v21 offset:37888\n"
    "v_mfma_f32_16x16x32_bf16 v[56:59], v[108:111], a[120:123], v[56:59]\n"
    "ds_write_b64 v170, v[184:185]\n"
    "v_mfma_f32_16x16x32_bf16 v[60:63], v[104:107], a[108:111], v[60:63]\n"
    "buffer_load_dwordx4 v[188:191], v212, s[44:47], s60 offen sc1\n"
    "v_mfma_f32_16x16x32_bf16 v[60:63], v[108:111], a[124:127], v[60:63]\n"
    "s_waitcnt lgkmcnt(3)\n"
    "v_mfma_f32_16x16x32_bf16 v[32:35], v[96:99], a[128:131], v[32:35]\n"
    "s_waitcnt lgkmcnt(1)\n"
    "ds_read_b64_tr_b16 v[104:105], v22 offset:32768\n"
    "v_mfma_f32_16x16x32_bf16 v[32:35], v[100:103], a[144:147], v[32:35]\n"
    "ds_read_b64_tr_b16 v[106:107], v22 offset:33792\n"
    "v_mfma_f32_16x16x32_bf16 v[36:39], v[96:99], a[132:135], v[36:39]\n"
    "buffer_load_dwordx4 v11, s[36:39], s85 offen offset:1024 lds\n"
    "v_mfma_f32_16x16x32_bf16 v[36:39], v[100:103], a[148:151], v[36:39]\n"
    "s_add_u32 s85, s67, s86\n"
    "ds_read_b64_tr_b16 v[108:109], v22 offset:36864\n"
    "v_mfma_f32_16x16x32_bf16 v[40:43], v[96:99], a[136:139], v[40:43]\n"
    "s_add_u32 m0, s89, 24576\n"
    "ds_read_b64_tr_b16 v[110:111], v22 offset:37888\n"
    "v_mfma_f32_16x16x32_bf16 v[40:43], v[100:103], a[152:155], v[40:43]\n"
    "ds_write_b64 v170, v[186:187] offset:8\n"
    "v_add_u32 v170, 0x200, v170\n"
    "v_mfma_f32_16x16x32_bf16 v[44:47], v[96:99], a[140:143], v[44:47]\n"
    "buffer_load_dwordx4 v[192:195], v213, s[44:47], s60 offen sc1\n"
    "v_mfma_f32_16x16x32_bf16 v[44:47], v[100:103], a[156:159], v[44:47]\n"
    "s_waitcnt lgkmcnt(3)\n"
    "s_add_u32 s95, s95, 16384\n"
    "s_mov_b32 s58, s97\n"
    "s_add_u32 s97, s97, 32768\n"
    "v_mfma_f32_16x16x32_bf16 v[48:51], v[104:107], a[128:131], v[48:51]\n"
    "s_waitcnt lgkmcnt(1)\n"
    "ds_read_b64_tr_b16 v[96:97], v21 offset:40960\n"
    "v_mfma_f32_16x16x32_bf16 v[48:51], v[108:111], a[144:147], v[48:51]\n"
    "ds_read_b64_tr_b16 v[98:99], v21 offset:41984\n"
    "v_mfma_f32_16x16x32_bf16 v[52:55], v[104:107], a[132:135], v[52:55]\n"
    "buffer_load_dwordx4 v10, s[36:39], s85 offen lds\n"
    "v_mfma_f32_16x16x32_bf16 v[52:55], v[108:111], a[148:151], v[52:55]\n"
    "ds_read_b64_tr_b16 v[100:101], v21 offset:45056\n"
    "v_mfma_f32_16x16x32_bf16 v[56:59], v[104:107], a[136:139], v[56:59]\n"
    "ds_read_b64_tr_b16 v[102:103], v21 offset:46080\n"
    "v_mfma_f32_16x16x32_bf16 v[56:59], v[108:111], a[152:155], v[56:59]\n"
    "s_sub_u32 s88, s88, 1\n"
    "v_mfma_f32_16x16x32_bf16 v[60:63], v[104:107], a[140:143], v[60:63]\n"
    "buffer_load_dwordx4 v[196:199], v214, s[44:47], s60 offen sc1\n"
    "v_mfma_f32_16x16x32_bf16 v[60:63], v[108:111], a[156:159], v[60:63]\n"
    "s_waitcnt lgkmcnt(2)\n"
    "v_mfma_f32_16x16x32_bf16 v[32:35], v[96:99], a[160:163], v[32:35]\n"
    "s_waitcnt lgkmcnt(0)\n"
    "ds_read_b64_tr_b16 v[104:105], v22 offset:40960\n"
    "v_add_u32 v21, s92, v21\n"
    "v_mfma_f32_16x16x32_bf16 v[32:35], v[100:103], a[176:179], v[32:35]\n"
    "ds_read_b64_tr_b16 v[106:107], v22 offset:41984\n"
    "v_mfma_f32_16x16x32_bf16 v[36:39], v[96:99], a[164:167], v[36:39]\n"
    "buffer_load_dwordx4 v11, s[36:39], s85 offen offset:1024 lds\n"
    "v_mfma_f32_16x16x32_bf16 v[36:39], v[100:103], a[180:183], v[36:39]\n"
    "s_add_u32 s85, s68, s86\n"
    "ds_read_b64_tr_b16 v[108:109], v22 offset:45056\n"
    "v_mfma_f32_16x16x32_bf16 v[40:43], v[96:99], a[168:171], v[40:43]\n"
    "s_add_u32 m0, s89, 32768\n"
    "ds_read_b64_tr_b16 v[110:111], v22 offset:46080\n"
    "v_mfma_f32_16x16x32_bf16 v[40:43], v[100:103], a[184:187], v[40:43]\n"
    "s_add_u32 s59, s59, 32768\n"
    "s_cmp_eq_u32 s88, 6\n"
    "s_cselect_b32 s59, s98, s59\n"
    "s_cselect_b64 s[62:63], -1, s[62:63]\n"
    "v_mfma_f32_16x16x32_bf16 v[44:47], v[96:99], a[172:175], v[44:47]\n"
    "v_add_u32 v22, s92, v22\n"
    "s_sub_u32 s92, 0, s92\n"
    "v_mfma_f32_16x16x32_bf16 v[44:47], v[100:103], a[188:191], v[44:47]\n"
    "s_waitcnt vmcnt(23) lgkmcnt(0)\n"
    "s_barrier\n"
    "v_mfma_f32_16x16x32_bf16 v[48:51], v[104:107], a[160:163], v[48:51]\n"
    "s_waitcnt lgkmcnt(0)\n"
    "ds_read_b64_tr_b16 v[96:97], v21 offset:0\n"
    "v_cvt_pk_bf16_f32 v80, v32, v33\n"
    "v_mfma_f32_16x16x32_bf16 v[48:51], v[108:111], a[176:179], v[48:51]\n"
    "ds_read_b64_tr_b16 v[98:99], v21 offset:1024\n"
    "v_cvt_pk_bf16_f32 v81, v34, v35\n"
    "v_mfma_f32_16x16x32_bf16 v[52:55], v[104:107], a[164:167], v[52:55]\n"
    "buffer_load_dwordx4 v10, s[36:39], s85 offen lds\n"
    "v_mfma_f32_16x16x32_bf16 v[52:55], v[108:111], a[180:183], v[52:55]\n"
    "ds_read_b64_tr_b16 v[100:101], v21 offset:4096\n"
    "v_cvt_pk_bf16_f32 v84, v36, v37\n"
    "v_mfma_f32_16x16x32_bf16 v[56:59], v[104:107], a[168:171], v[56:59]\n"
    "ds_read_b64_tr_b16 v[102:103], v21 offset:5120\n"
    "v_cvt_pk_bf16_f32 v85, v38, v39\n"
    "v_mfma_f32_16x16x32_bf16 v[56:59], v[108:111], a[184:187], v[56:59]\n"
    "v_cvt_pk_bf16_f32 v88, v40, v41\n"
    "v_cvt_pk_bf16_f32 v89, v42, v43\n"
    "v_mfma_f32_16x16x32_bf16 v[60:63], v[104:107], a[172:175], v[60:63]\n"
    "v_cvt_pk_bf16_f32 v92, v44, v45\n"
    "v_cvt_pk_bf16_f32 v93, v46, v47\n"
    "v_mfma_f32_16x16x32_bf16 v[60:63], v[108:111], a[188:191], v[60:63]\n"
    "s_waitcnt lgkmcnt(2)\n"
    "s_cmp_lg_u32 s88, 0\n"
    "s_cbranch_scc1 .LL_CHUNK2_%=\n"
    "s_branch .LL_TAIL_%=\n"
    ".LL_CSRETRY2_%=:\n"
    "s_mov_b32 s58, 0\n"
    ".LL_CSR2_%=:\n"
    "s_sleep 1\n"
    "buffer_load_dwordx2 v[120:121], v16, s[44:47], s60 offen sc1\n"
    "s_waitcnt vmcnt(0)\n"
    "v_cmp_ne_u32 vcc, s56, v121\n"
    "v_mov_b32 v124, v120\n"
    "s_add_u32 s58, s58, 1\n"
    "s_cmp_gt_u32 s58, 32768\n"
    "s_cbranch_scc1 .LL_CSOK2_%=\n"
    "s_cbranch_vccnz .LL_CSR2_%=\n"
    "s_branch .LL_CSOK2_%=\n"
    ".LL_STEP3_%=:\n"
    "v_mfma_f32_32x32x16_bf16 v[32:47], v[96:99], a[0:3], 0\n"
    "s_add_u32 s85, s71, s86\n"
    "s_add_u32 m0, s89, 57344\n"
    "ds_read_b64_tr_b16 v[108:109], v12 offset:6144\n"
    "ds_read_b64_tr_b16 v[110:111], v12 offset:6656\n"
    "v_mfma_f32_32x32x16_bf16 v[48:63], v[96:99], a[4:7], 0\n"
    "s_waitcnt lgkmcnt(6)\n"
    "v_mul_f32 v64, v64, v122\n"
    "v_fma_f32 v112, -v64, v64, s87\n"
    "v_mul_f32 v112, v64, v112\n"
    "v_mul_f32 v65, v65, v122\n"
    "v_exp_f32 v112, v112\n"
    "v_mfma_f32_32x32x16_bf16 v[32:47], v[100:103], a[8:11], v[32:47]\n"
    "buffer_load_dwordx4 v10, s[36:39], s85 offen lds\n"
    "buffer_load_dwordx4 v11, s[36:39], s85 offen offset:1024 lds\n"
    "v_fma_f32 v113, -v65, v65, s87\n"
    "v_add_f32 v112, 1.0, v112\n"
    "v_mul_f32 v113, v65, v113\n"
    "v_mfma_f32_32x32x16_bf16 v[48:63], v[100:103], a[12:15], v[48:63]\n"
    "s_waitcnt lgkmcnt(3)\n"
    "ds_read_b64_tr_b16 v[96:97], v12 offset:8192\n"
    "ds_read_b64_tr_b16 v[98:99], v12 offset:8704\n"
    "v_rcp_f32 v112, v112\n"
    "v_mul_f32 v66, v66, v122\n"
    "v_exp_f32 v113, v113\n"
    "v_mfma_f32_32x32x16_bf16 v[32:47], v[104:107], a[16:19], v[32:47]\n"
    "s_lshl_b32 s60, s86, 2\n"
    "ds_read_b64_tr_b16 v[100:101], v12 offset:10240\n"
    "ds_read_b64_tr_b16 v[102:103], v12 offset:10752\n"
    "v_fma_f32 v114, -v66, v66, s87\n"
    "v_mul_f32 v64, v64, v112\n"
    "v_add_f32 v113, 1.0, v113\n"
    "v_mul_f32 v114, v66, v114\n"
    "v_rcp_f32 v113, v113\n"
    "v_mfma_f32_32x32x16_bf16 v[48:63], v[104:107], a[20:23], v[48:63]\n"
    "s_waitcnt lgkmcnt(4)\n"
    "v_mul_f32 v67, v67, v122\n"
    "v_exp_f32 v114, v114\n"
    "v_fma_f32 v112, -v67, v67, s87\n"
    "v_mul_f32 v65, v65, v113\n"
    "v_add_f32 v114, 1.0, v114\n"
    "v_mul_f32 v112, v67, v112\n"
    "v_mfma_f32_32x32x16_bf16 v[32:47], v[108:111], a[24:27], v[32:47]\n"
    "s_add_u32 s60, s60, s92\n"
    "ds_read_b64_tr_b16 v[104:105], v12 offset:12288\n"
    "ds_read_b64_tr_b16 v[106:107], v12 offset:12800\n"
    "v_cvt_pk_bf16_f32 v116, v64, v65\n"
    "v_rcp_f32 v114, v114\n"
    "v_mul_f32 v68, v68, v122\n"
    "v_exp_f32 v112, v112\n"
    "v_mfma_f32_32x32x16_bf16 v[48:63], v[108:111], a[28:31], v[48:63]\n"
    "s_waitcnt lgkmcnt(4)\n"
    "v_fma_f32 v113, -v68, v68, s87\n"
    "v_mul_f32 v66, v66, v114\n"
    "v_add_f32 v112, 1.0, v112\n"
    "v_mul_f32 v113, v68, v113\n"
    "v_rcp_f32 v112, v112\n"
    "v_mul_f32 v69, v69, v122\n"
    "v_mfma_f32_32x32x16_bf16 v[32:47], v[96:99], a[32:35], v[32:47]\n"
    "s_add_u32 s85, s72, s86\n"
    "s_add_u32 m0, s89, 65536\n"
    "ds_read_b64_tr_b16 v[108:109], v12 offset:14336\n"
    "ds_read_b64_tr_b16 v[110:111], v12 offset:14848\n"
    "v_exp_f32 v113, v113\n"
    "v_fma_f32 v114, -v69, v69, s87\n"
    "v_mul_f32 v67, v67, v112\n"
    "v_mfma_f32_32x32x16_bf16 v[48:63], v[96:99], a[36:39], v[48:63]\n"
    "s_waitcnt vmcnt(9) lgkmcnt(4)\n"
    "s_barrier\n"
    "v_add_f32 v113, 1.0, v113\n"
    "v_mul_f32 v114, v69, v114\n"
    "v_cvt_pk_bf16_f32 v117, v66, v67\n"
    "v_rcp_f32 v113, v113\n"
    "v_mfma_f32_32x32x16_bf16 v[32:47], v[100:103], a[40:43], v[32:47]\n"
    "buffer_load_dwordx4 v10, s[36:39], s85 offen lds\n"
    "buffer_load_dwordx4 v11, s[36:39], s85 offen offset:1024 lds\n"
    "v_mul_f32 v70, v70, v122\n"
    "v_exp_f32 v114, v114\n"
    "v_mfma_f32_32x32x16_bf16 v[48:63], v[100:103], a[44:47], v[48:63]\n"
    "s_waitcnt lgkmcnt(2)\n"
    "ds_read_b64_tr_b16 v[96:97], v12 offset:16384\n"
    "ds_read_b64_tr_b16 v[98:99], v12 offset:16896\n"
    "v_fma_f32 v112, -v70, v70, s87\n"
    "v_mul_f32 v68, v68, v113\n"
    "v_add_f32 v114, 1.0, v114\n"
    "v_mul_f32 v112, v70, v112\n"
    "v_rcp_f32 v114, v114\n"
    "v_mfma_f32_32x32x16_bf16 v[32:47], v[104:107], a[48:51], v[32:47]\n"
    "s_mov_b32 s61, s86\n"
    "s_add_u32 s86, s86, 128\n"
    "ds_read_b64_tr_b16 v[100:101], v12 offset:18432\n"
    "ds_read_b64_tr_b16 v[102:103], v12 offset:18944\n"
    "v_mul_f32 v71, v71, v122\n"
    "v_exp_f32 v112, v112\n"
    "v_fma_f32 v113, -v71, v71, s87\n"
    "v_mfma_f32_32x32x16_bf16 v[48:63], v[104:107], a[52:55], v[48:63]\n"
    "s_waitcnt lgkmcnt(4)\n"
    "ds_write_b64 v14, v[116:117] offset:0\n"
    "v_mul_f32 v69, v69, v114\n"
    "v_add_f32 v112, 1.0, v112\n"
    "v_mul_f32 v113, v71, v113\n"
    "v_cvt_pk_bf16_f32 v118, v68, v69\n"
    "v_mfma_f32_32x32x16_bf16 v[32:47], v[108:111], a[56:59], v[32:47]\n"
    "s_cmp_eq_u32 s86, 768\n"
    "s_cselect_b32 s86, 0, s86\n"
    "s_cmp_eq_u32 s88, 1\n"
    "ds_read_b64_tr_b16 v[104:105], v12 offset:20480\n"
    "ds_read_b64_tr_b16 v[106:107], v12 offset:20992\n"
    "v_rcp_f32 v112, v112\n"
    "v_mul_f32 v72, v72, v122\n"
    "v_mfma_f32_32x32x16_bf16 v[48:63], v[108:111], a[60:63], v[48:63]\n"
    "s_waitcnt lgkmcnt(5)\n"
    "v_exp_f32 v113, v113\n"
    "v_fma_f32 v114, -v72, v72, s87\n"
    "v_mul_f32 v70, v70, v112\n"
    "v_add_f32 v113, 1.0, v113\n"
    "v_mul_f32 v114, v72, v114\n"
    "v_rcp_f32 v113, v113\n"
    "v_mfma_f32_32x32x16_bf16 v[32:47], v[96:99], a[64:67], v[32:47]\n"
    "s_cbranch_scc0 .LL_SKSW3_%=\n"
    "s_mov_b64 s[36:37], s[52:53]\n"
    "s_mov_b32 s38, s54\n"
    "s_mov_b32 s86, 0\n"
    "s_mov_b32 s64, s73\n"
    "s_mov_b32 s65, s74\n"
    "s_mov_b32 s66, s75\n"
    "s_mov_b32 s67, s76\n"
    "s_mov_b32 s68, s77\n"
    "s_mov_b32 s69, s78\n"
    "s_mov_b32 s70, 1879048192\n"
    "v_mov_b32 v10, v19\n"
    "v_mov_b32 v11, v20\n"
    ".LL_SKSW3_%=:\n"
    "s_add_u32 s85, s64, s86\n"
    "s_add_u32 m0, s89, 0\n"
    "ds_read_b64_tr_b16 v[108:109], v12 offset:22528\n"
    "ds_read_b64_tr_b16 v[110:111], v12 offset:23040\n"
    "v_mul_f32 v73, v73, v122\n"
    "v_exp_f32 v114, v114\n"
    "v_mfma_f32_32x32x16_bf16 v[48:63], v[96:99], a[68:71], v[48:63]\n"
    "s_waitcnt lgkmcnt(5)\n"
    "v_fma_f32 v112, -v73, v73, s87\n"
    "v_mul_f32 v71, v71, v113\n"
    "v_add_f32 v114, 1.0, v114\n"
    "v_mul_f32 v112, v73, v112\n"
    "v_cvt_pk_bf16_f32 v119, v70, v71\n"
    "v_rcp_f32 v114, v114\n"
    "v_mfma_f32_32x32x16_bf16 v[32:47], v[100:103], a[72:75], v[32:47]\n"
    "buffer_load_dwordx4 v10, s[36:39], s85 offen lds\n"
    "buffer_load_dwordx4 v11, s[36:39], s85 offen offset:1024 lds\n"
    "v_mul_f32 v74, v74, v122\n"
    "v_exp_f32 v112, v112\n"
    "v_mfma_f32_32x32x16_bf16 v[48:63], v[100:103], a[76:79], v[48:63]\n"
    "s_waitcnt lgkmcnt(2)\n"
    "ds_read_b64_tr_b16 v[96:97], v12 offset:24576\n"
    "ds_read_b64_tr_b16 v[98:99], v12 offset:25088\n"
    "v_fma_f32 v113, -v74, v74, s87\n"
    "v_mul_f32 v72, v72, v114\n"
    "v_add_f32 v112, 1.0, v112\n"
    "v_mul_f32 v113, v74, v113\n"
    "v_rcp_f32 v112, v112\n"
    "v_mfma_f32_32x32x16_bf16 v[32:47], v[104:107], a[80:83], v[32:47]\n"
    "ds_read_b64_tr_b16 v[100:101], v12 offset:26624\n"
    "ds_read_b64_tr_b16 v[102:103], v12 offset:27136\n"
    "v_mul_f32 v75, v75, v122\n"
    "v_exp_f32 v113, v113\n"
    "v_fma_f32 v114, -v75, v75, s87\n"
    "v_mfma_f32_32x32x16_bf16 v[48:63], v[104:107], a[84:87], v[48:63]\n"
    "s_waitcnt lgkmcnt(4)\n"
    "ds_write_b64 v14, v[118:119] offset:8\n"
    "v_mul_f32 v73, v73, v112\n"
    "v_add_f32 v113, 1.0, v113\n"
    "v_mul_f32 v114, v75, v114\n"
    "v_cvt_pk_bf16_f32 v116, v72, v73\n"
    "v_mfma_f32_32x32x16_bf16 v[32:47], v[108:111], a[88:91], v[32:47]\n"
    "ds_read_b64_tr_b16 v[104:105], v12 offset:28672\n"
    "ds_read_b64_tr_b16 v[106:107], v12 offset:29184\n"
    "v_rcp_f32 v113, v113\n"
    "v_mul_f32 v76, v76, v122\n"
    "v_exp_f32 v114, v114\n"
    "v_mfma_f32_32x32x16_bf16 v[48:63], v[108:111], a[92:95], v[48:63]\n"
    "s_waitcnt lgkmcnt(5)\n"
    "buffer_load_dword v31, v18, s[52:55], s93 offen\n"
    "v_fma_f32 v112, -v76, v76, s87\n"
    "v_mul_f32 v74, v74, v113\n"
    "v_add_f32 v114, 1.0, v114\n"
    "v_mul_f32 v112, v76, v112\n"
    "v_mfma_f32_32x32x16_bf16 v[32:47], v[96:99], a[96:99], v[32:47]\n"
    "s_add_u32 s85, s65, s86\n"
    "s_add_u32 m0, s89, 8192\n"
    "ds_read_b64_tr_b16 v[108:109], v12 offset:30720\n"
    "ds_read_b64_tr_b16 v[110:111], v12 offset:31232\n"
    "v_rcp_f32 v114, v114\n"
    "v_mul_f32 v77, v77, v122\n"
    "v_mfma_f32_32x32x16_bf16 v[48:63], v[96:99], a[100:103], v[48:63]\n"
    "s_waitcnt vmcnt(9) lgkmcnt(5)\n"
    "s_barrier\n"
    "v_exp_f32 v112, v112\n"
    "v_fma_f32 v113, -v77, v77, s87\n"
    "v_mfma_f32_32x32x16_bf16 v[32:47], v[100:103], a[104:107], v[32:47]\n"
    "buffer_load_dwordx4 v10, s[36:39], s85 offen lds\n"
    "buffer_load_dwordx4 v11, s[36:39], s85 offen offset:1024 lds\n"
    "v_mul_f32 v75, v75, v114\n"
    "v_add_f32 v112, 1.0, v112\n"
    "v_mul_f32 v113, v77, v113\n"
    "v_mfma_f32_32x32x16_bf16 v[48:63], v[100:103], a[108:111], v[48:63]\n"
    "s_waitcnt lgkmcnt(2)\n"
    "ds_read_b64_tr_b16 v[96:97], v12 offset:32768\n"
    "ds_read_b64_tr_b16 v[98:99], v12 offset:33280\n"
    "v_cvt_pk_bf16_f32 v117, v74, v75\n"
    "v_rcp_f32 v112, v112\n"
    "v_mul_f32 v78, v78, v122\n"
    "v_mfma_f32_32x32x16_bf16 v[32:47], v[104:107], a[112:115], v[32:47]\n"
    "s_add_u32 s93, s93, 11264\n"
    "ds_read_b64_tr_b16 v[100:101], v12 offset:34816\n"
    "ds_read_b64_tr_b16 v[102:103], v12 offset:35328\n"
    "v_exp_f32 v113, v113\n"
    "v_fma_f32 v114, -v78, v78, s87\n"
    "v_mul_f32 v76, v76, v112\n"
    "v_mfma_f32_32x32x16_bf16 v[48:63], v[104:107], a[116:119], v[48:63]\n"
    "s_waitcnt lgkmcnt(4)\n"
    "ds_write_b64 v14, v[116:117] offset:512\n"
    "v_add_f32 v113, 1.0, v113\n"
    "v_mul_f32 v114, v78, v114\n"
    "v_rcp_f32 v113, v113\n"
    "v_mfma_f32_32x32x16_bf16 v[32:47], v[108:111], a[120:123], v[32:47]\n"
    "ds_read_b64_tr_b16 v[104:105], v12 offset:36864\n"
    "ds_read_b64_tr_b16 v[106:107], v12 offset:37376\n"
    "v_mul_f32 v79, v79, v122\n"
    "v_exp_f32 v114, v114\n"
    "v_fma_f32 v112, -v79, v79, s87\n"
    "v_mfma_f32_32x32x16_bf16 v[48:63], v[108:111], a[124:127], v[48:63]\n"
    "s_waitcnt lgkmcnt(5)\n"
    "v_mul_f32 v77, v77, v113\n"
    "v_add_f32 v114, 1.0, v114\n"
    "v_mul_f32 v112, v79, v112\n"
    "v_cvt_pk_bf16_f32 v118, v76, v77\n"
    "v_rcp_f32 v114, v114\n"
    "v_mfma_f32_32x32x16_bf16 v[32:47], v[96:99], a[128:131], v[32:47]\n"
    "s_add_u32 s85, s66, s86\n"
    "s_add_u32 m0, s89, 16384\n"
    "ds_read_b64_tr_b16 v[108:109], v12 offset:38912\n"
    "ds_read_b64_tr_b16 v[110:111], v12 offset:39424\n"
    "v_mul_f32 v80, v80, v123\n"
    "v_exp_f32 v112, v112\n"
    "v_fma_f32 v113, -v80, v80, s87\n"
    "v_mfma_f32_32x32x16_bf16 v[48:63], v[96:99], a[132:135], v[48:63]\n"
    "s_waitcnt lgkmcnt(5)\n"
    "v_mul_f32 v78, v78, v114\n"
    "v_add_f32 v112, 1.0, v112\n"
    "v_mul_f32 v113, v80, v113\n"
    "v_rcp_f32 v112, v112\n"
    "v_mul_f32 v81, v81, v123\n"
    "v_mfma_f32_32x32x16_bf16 v[32:47], v[100:103], a[136:139], v[32:47]\n"
    "buffer_load_dwordx4 v10, s[36:39], s85 offen lds\n"
    "buffer_load_dwordx4 v11, s[36:39], s85 offen offset:1024 lds\n"
    "v_exp_f32 v113, v113\n"
    "v_mfma_f32_32x32x16_bf16 v[48:63], v[100:103], a[140:143], v[48:63]\n"
    "s_waitcnt lgkmcnt(2)\n"
    "ds_read_b64_tr_b16 v[96:97], v12 offset:40960\n"
    "ds_read_b64_tr_b16 v[98:99], v12 offset:41472\n"
    "v_fma_f32 v114, -v81, v81, s87\n"
    "v_mul_f32 v79, v79, v112\n"
    "v_add_f32 v113, 1.0, v113\n"
    "v_mul_f32 v114, v81, v114\n"
    "v_cvt_pk_bf16_f32 v119, v78, v79\n"
    "v_mfma_f32_32x32x16_bf16 v[32:47], v[104:107], a[144:147], v[32:47]\n"
    "ds_read_b64_tr_b16 v[100:101], v12 offset:43008\n"
    "ds_read_b64_tr_b16 v[102:103], v12 offset:43520\n"
    "v_rcp_f32 v113, v113\n"
    "v_mul_f32 v82, v82, v123\n"
    "v_exp_f32 v114, v114\n"
    "v_mfma_f32_32x32x16_bf16 v[48:63], v[104:107], a[148:151], v[48:63]\n"
    "s_waitcnt lgkmcnt(4)\n"
    "ds_write_b64 v14, v[118:119] offset:520\n"
    "v_fma_f32 v112, -v82, v82, s87\n"
    "v_mul_f32 v80, v80, v113\n"
    "v_add_f32 v114, 1.0, v114\n"
    "v_mfma_f32_32x32x16_bf16 v[32:47], v[108:111], a[152:155], v[32:47]\n"
    "ds_read_b64_tr_b16 v[104:105], v12 offset:45056\n"
    "ds_read_b64_tr_b16 v[106:107], v12 offset:45568\n"
    "v_mul_f32 v112, v82, v112\n"
    "v_rcp_f32 v114, v114\n"
    "v_mul_f32 v83, v83, v123\n"
    "v_mfma_f32_32x32x16_bf16 v[48:63], v[108:111], a[156:159], v[48:63]\n"
    "s_waitcnt lgkmcnt(5)\n"
    "buffer_load_ushort v125, v17, s[48:51], s61 offen\n"
    "v_exp_f32 v112, v112\n"
    "v_fma_f32 v113, -v83, v83, s87\n"
    "v_mul_f32 v81, v81, v114\n"
    "v_mfma_f32_32x32x16_bf16 v[32:47], v[96:99], a[160:163], v[32:47]\n"
    "s_add_u32 s85, s67, s86\n"
    "s_add_u32 m0, s89, 24576\n"
    "ds_read_b64_tr_b16 v[108:109], v12 offset:47104\n"
    "ds_read_b64_tr_b16 v[110:111], v12 offset:47616\n"
    "v_add_f32 v112, 1.0, v112\n"
    "v_mul_f32 v113, v83, v113\n"
    "v_cvt_pk_bf16_f32 v116, v80, v81\n"
    "v_mfma_f32_32x32x16_bf16 v[48:63], v[96:99], a[164:167], v[48:63]\n"
    "s_waitcnt vmcnt(10) lgkmcnt(5)\n"
    "s_barrier\n"
    "v_rcp_f32 v112, v112\n"
    "v_mul_f32 v84, v84, v123\n"
    "v_exp_f32 v113, v113\n"
    "v_mfma_f32_32x32x16_bf16 v[32:47], v[100:103], a[168:171], v[32:47]\n"
    "buffer_load_dwordx4 v10, s[36:39], s85 offen lds\n"
    "buffer_load_dwordx4 v11, s[36:39], s85 offen offset:1024 lds\n"
    "v_fma_f32 v114, -v84, v84, s87\n"
    "v_mul_f32 v82, v82, v112\n"
    "v_mfma_f32_32x32x16_bf16 v[48:63], v[100:103], a[172:175], v[48:63]\n"
    "s_waitcnt lgkmcnt(2)\n"
    "ds_read_b64_tr_b16 v[96:97], v12 offset:49152\n"
    "ds_read_b64_tr_b16 v[98:99], v12 offset:49664\n"
    "v_add_f32 v113, 1.0, v113\n"
    "v_mul_f32 v114, v84, v114\n"
    "v_rcp_f32 v113, v113\n"
    "v_mfma_f32_32x32x16_bf16 v[32:47], v[104:107], a[176:179], v[32:47]\n"
    "ds_read_b64_tr_b16 v[100:101], v12 offset:51200\n"
    "ds_read_b64_tr_b16 v[102:103], v12 offset:51712\n"
    "v_mul_f32 v85, v85, v123\n"
    "v_exp_f32 v114, v114\n"
    "v_fma_f32 v112, -v85, v85, s87\n"
    "v_mfma_f32_32x32x16_bf16 v[48:63], v[104:107], a[180:183], v[48:63]\n"
    "s_waitcnt lgkmcnt(4)\n"
    "v_mul_f32 v83, v83, v113\n"
    "v_add_f32 v114, 1.0, v114\n"
    "v_mul_f32 v112, v85, v112\n"
    "v_mfma_f32_32x32x16_bf16 v[32:47], v[108:111], a[184:187], v[32:47]\n"
    "ds_read_b64_tr_b16 v[104:105], v12 offset:53248\n"
    "ds_read_b64_tr_b16 v[106:107], v12 offset:53760\n"
    "v_cvt_pk_bf16_f32 v117, v82, v83\n"
    "v_rcp_f32 v114, v114\n"
    "v_mul_f32 v86, v86, v123\n"
    "v_mfma_f32_32x32x16_bf16 v[48:63], v[108:111], a[188:191], v[48:63]\n"
    "s_waitcnt lgkmcnt(4)\n"
    "buffer_load_dwordx2 v[120:121], v16, s[44:47], s60 offen sc1\n"
    "v_exp_f32 v112, v112\n"
    "v_fma_f32 v113, -v86, v86, s87\n"
    "v_mul_f32 v84, v84, v114\n"
    "v_add_f32 v112, 1.0, v112\n"
    "v_mfma_f32_32x32x16_bf16 v[32:47], v[96:99], a[192:195], v[32:47]\n"
    "s_add_u32 s85, s68, s86\n"
    "s_add_u32 m0, s89, 32768\n"
    "ds_read_b64_tr_b16 v[108:109], v12 offset:55296\n"
    "ds_read_b64_tr_b16 v[110:111], v12 offset:55808\n"
    "v_mul_f32 v113, v86, v113\n"
    "v_rcp_f32 v112, v112\n"
    "v_mfma_f32_32x32x16_bf16 v[48:63], v[96:99], a[196:199], v[48:63]\n"
    "s_waitcnt lgkmcnt(4)\n"
    "v_mul_f32 v87, v87, v123\n"
    "v_exp_f32 v113, v113\n"
    "v_fma_f32 v114, -v87, v87, s87\n"
    "v_mul_f32 v85, v85, v112\n"
    "v_add_f32 v113, 1.0, v113\n"
    "v_mfma_f32_32x32x16_bf16 v[32:47], v[100:103], a[200:203], v[32:47]\n"
    "buffer_load_dwordx4 v10, s[36:39], s85 offen lds\n"
    "buffer_load_dwordx4 v11, s[36:39], s85 offen offset:1024 lds\n"
    "v_mul_f32 v114, v87, v114\n"
    "v_cvt_pk_bf16_f32 v118, v84, v85\n"
    "v_rcp_f32 v113, v113\n"
    "v_mfma_f32_32x32x16_bf16 v[48:63], v[100:103], a[204:207], v[48:63]\n"
    "s_waitcnt lgkmcnt(2)\n"
    "ds_read_b64_tr_b16 v[96:97], v12 offset:57344\n"
    "ds_read_b64_tr_b16 v[98:99], v12 offset:57856\n"
    "v_mul_f32 v88, v88, v123\n"
    "v_exp_f32 v114, v114\n"
    "v_fma_f32 v112, -v88, v88, s87\n"
    "v_mfma_f32_32x32x16_bf16 v[32:47], v[104:107], a[208:211], v[32:47]\n"
    "ds_read_b64_tr_b16 v[100:101], v12 offset:59392\n"
    "ds_read_b64_tr_b16 v[102:103], v12 offset:59904\n"
    "v_mul_f32 v86, v86, v113\n"
    "v_add_f32 v114, 1.0, v114\n"
    "v_mul_f32 v112, v88, v112\n"
    "v_rcp_f32 v114, v114\n"
    "v_mfma_f32_32x32x16_bf16 v[48:63], v[104:107], a[212:215], v[48:63]\n"
    "s_waitcnt lgkmcnt(4)\n"
    "ds_write_b64 v15, v[116:117] offset:0\n"
    "v_mul_f32 v89, v89, v123\n"
    "v_exp_f32 v112, v112\n"
    "v_mfma_f32_32x32x16_bf16 v[32:47], v[108:111], a[216:219], v[32:47]\n"
    "ds_read_b64_tr_b16 v[104:105], v12 offset:61440\n"
    "ds_read_b64_tr_b16 v[106:107], v12 offset:61952\n"
    "v_fma_f32 v113, -v89, v89, s87\n"
    "v_mul_f32 v87, v87, v114\n"
    "v_add_f32 v112, 1.0, v112\n"
    "v_mul_f32 v113, v89, v113\n"
    "v_mfma_f32_32x32x16_bf16 v[48:63], v[108:111], a[220:223], v[48:63]\n"
    "s_waitcnt lgkmcnt(5)\n"
    "v_cvt_pk_bf16_f32 v119, v86, v87\n"
    "v_rcp_f32 v112, v112\n"
    "v_mul_f32 v90, v90, v123\n"
    "v_exp_f32 v113, v113\n"
    "v_mfma_f32_32x32x16_bf16 v[32:47], v[96:99], a[224:227], v[32:47]\n"
    "s_add_u32 s85, s69, s86\n"
    "s_add_u32 m0, s89, 40960\n"
    "ds_read_b64_tr_b16 v[108:109], v12 offset:63488\n"
    "ds_read_b64_tr_b16 v[110:111], v12 offset:64000\n"
    "v_fma_f32 v114, -v90, v90, s87\n"
    "v_mul_f32 v88, v88, v112\n"
    "v_add_f32 v113, 1.0, v113\n"
    "v_mfma_f32_32x32x16_bf16 v[48:63], v[96:99], a[228:231], v[48:63]\n"
    "s_waitcnt vmcnt(13) lgkmcnt(5)\n"
    "s_barrier\n"
    "v_mul_f32 v114, v90, v114\n"
    "v_rcp_f32 v113, v113\n"
    "v_mfma_f32_32x32x16_bf16 v[32:47], v[100:103], a[232:235], v[32:47]\n"
    "buffer_load_dwordx4 v10, s[36:39], s85 offen lds\n"
    "buffer_load_dwordx4 v11, s[36:39], s85 offen offset:1024 lds\n"
    "v_mul_f32 v91, v91, v123\n"
    "v_exp_f32 v114, v114\n"
    "v_mfma_f32_32x32x16_bf16 v[48:63], v[100:103], a[236:239], v[48:63]\n"
    "s_waitcnt lgkmcnt(2)\n"
    "ds_read_b64_tr_b16 v[96:97], v13 offset:0\n"
    "ds_read_b64_tr_b16 v[98:99], v13 offset:512\n"
    "v_fma_f32 v112, -v91, v91, s87\n"
    "v_mul_f32 v89, v89, v113\n"
    "v_add_f32 v114, 1.0, v114\n"
    "v_mul_f32 v112, v91, v112\n"
    "v_mfma_f32_32x32x16_bf16 v[32:47], v[104:107], a[240:243], v[32:47]\n"
    "ds_read_b64_tr_b16 v[100:101], v13 offset:2048\n"
    "ds_read_b64_tr_b16 v[102:103], v13 offset:2560\n"
    "v_cvt_pk_bf16_f32 v116, v88, v89\n"
    "v_rcp_f32 v114, v114\n"
    "v_mul_f32 v92, v92, v123\n"
    "v_mfma_f32_32x32x16_bf16 v[48:63], v[104:107], a[244:247], v[48:63]\n"
    "s_waitcnt lgkmcnt(4)\n"
    "ds_write_b64 v15, v[118:119] offset:8\n"
    "v_exp_f32 v112, v112\n"
    "v_fma_f32 v113, -v92, v92, s87\n"
    "v_mfma_f32_32x32x16_bf16 v[32:47], v[108:111], a[248:251], v[32:47]\n"
    "ds_read_b64_tr_b16 v[104:105], v13 offset:4096\n"
    "ds_read_b64_tr_b16 v[106:107], v13 offset:4608\n"
    "v_mul_f32 v90, v90, v114\n"
    "v_add_f32 v112, 1.0, v112\n"
    "v_mul_f32 v113, v92, v113\n"
    "v_rcp_f32 v112, v112\n"
    "v_mfma_f32_32x32x16_bf16 v[48:63], v[108:111], a[252:255], v[48:63]\n"
    "s_waitcnt lgkmcnt(5)\n"
    "v_mul_f32 v93, v93, v123\n"
    "v_exp_f32 v113, v113\n"
    "v_fma_f32 v114, -v93, v93, s87\n"
    "v_mul_f32 v91, v91, v112\n"
    "v_add_f32 v113, 1.0, v113\n"
    "v_mfma_f32_32x32x16_bf16 v[32:47], v[96:99], v[224:227], v[32:47]\n"
    "s_add_u32 s85, s70, s86\n"
    "s_add_u32 m0, s89, 49152\n"
    "ds_read_b64_tr_b16 v[108:109], v13 offset:6144\n"
    "ds_read_b64_tr_b16 v[110:111], v13 offset:6656\n"
    "v_mul_f32 v114, v93, v114\n"
    "v_cvt_pk_bf16_f32 v117, v90, v91\n"
    "v_rcp_f32 v113, v113\n"
    "v_mfma_f32_32x32x16_bf16 v[48:63], v[96:99], v[228:231], v[48:63]\n"
    "s_waitcnt vmcnt(10) lgkmcnt(5)\n"
    "s_barrier\n"
    "v_mul_f32 v94, v94, v123\n"
    "v_exp_f32 v114, v114\n"
    "v_mfma_f32_32x32x16_bf16 v[32:47], v[100:103], v[232:235], v[32:47]\n"
    "buffer_load_dwordx4 v10, s[36:39], s85 offen lds\n"
    "buffer_load_dwordx4 v11, s[36:39], s85 offen offset:1024 lds\n"
    "v_fma_f32 v112, -v94, v94, s87\n"
    "v_mul_f32 v92, v92, v113\n"
    "v_mfma_f32_32x32x16_bf16 v[48:63], v[100:103], v[236:239], v[48:63]\n"
    "s_waitcnt lgkmcnt(2)\n"
    "ds_read_b64_tr_b16 v[96:97], v12 offset:0\n"
    "ds_read_b64_tr_b16 v[98:99], v12 offset:512\n"
    "v_add_f32 v114, 1.0, v114\n"
    "v_mul_f32 v112, v94, v112\n"
    "v_rcp_f32 v114, v114\n"
    "v_mfma_f32_32x32x16_bf16 v[32:47], v[104:107], v[240:243], v[32:47]\n"
    "ds_read_b64_tr_b16 v[100:101], v12 offset:2048\n"
    "ds_read_b64_tr_b16 v[102:103], v12 offset:2560\n"
    "v_mul_f32 v95, v95, v123\n"
    "v_exp_f32 v112, v112\n"
    "v_fma_f32 v113, -v95, v95, s87\n"
    "v_mul_f32 v93, v93, v114\n"
    "v_mfma_f32_32x32x16_bf16 v[48:63], v[104:107], v[244:247], v[48:63]\n"
    "s_waitcnt lgkmcnt(4)\n"
    "ds_write_b64 v15, v[116:117] offset:512\n"
    "v_add_f32 v112, 1.0, v112\n"
    "v_mul_f32 v113, v95, v113\n"
    "v_cvt_pk_bf16_f32 v118, v92, v93\n"
    "v_mfma_f32_32x32x16_bf16 v[32:47], v[108:111], v[248:251], v[32:47]\n"
    "s_waitcnt vmcnt(6)\n"
    "ds_read_b64_tr_b16 v[104:105], v12 offset:4096\n"
    "ds_read_b64_tr_b16 v[106:107], v12 offset:4608\n"
    "v_cmp_ne_u32 vcc, s56, v121\n"
    "v_mov_b32 v124, v120\n"
    "s_cbranch_vccnz .LL_CSRETRY3_%=\n"
    ".LL_CSOK3_%=:\n"
    "v_rcp_f32 v112, v112\n"
    "v_mfma_f32_32x32x16_bf16 v[48:63], v[108:111], v[252:255], v[48:63]\n"
    "s_waitcnt lgkmcnt(5)\n"
    "v_exp_f32 v113, v113\n"
    "v_mul_f32 v94, v94, v112\n"
    "v_add_f32 v113, 1.0, v113\n"
    "v_rcp_f32 v113, v113\n"
    "v_mfma_f32_32x32x16_bf16 v[64:79], v[124:127], v[128:131], v[32:47]\n"
    "s_sub_u32 s88, s88, 1\n"
    "s_nop 0\n"
    "v_mul_f32 v95, v95, v113\n"
    "v_cvt_pk_bf16_f32 v119, v94, v95\n"
    "v_mfma_f32_32x32x16_bf16 v[80:95], v[124:127], v[132:135], v[48:63]\n"
    "s_cmp_lg_u32 s88, 0\n"
    "ds_write_b64 v15, v[118:119] offset:520\n"
    "v_add_u32 v14, 0x4000, v14\n"
    "v_add_u32 v15, 0x4000, v15\n"
    "s_cbranch_scc1 .LL_STEP3_%=\n"
    "v_add_u32 v12, 81920, v23\n"
    "v_add_u32 v13, 147456, v23\n"
    "v_mov_b32 v156, s87\n"
    "v_mov_b32 v158, v122\n"
    "v_mov_b32 v160, v123\n"
    "ds_read_b128 a[0:3], v12 offset:0\n"
    "ds_read_b128 a[4:7], v12 offset:1024\n"
    "ds_read_b128 a[8:11], v12 offset:2048\n"
    "ds_read_b128 a[12:15], v12 offset:3072\n"
    "v_add_u32 v16, 65536, v23\n"
    "v_subrev_u32 v14, 98304, v14\n"
    "v_subrev_u32 v15, 98304, v15\n"
    "s_mov_b32 s86, 128\n"
    "s_mov_b32 s92, 81920\n"
    "v_pk_mul_f32 v[64:65], v[64:65], v[158:159] op_sel_hi:[1,0]\n"
    "v_pk_mul_f32 v[66:67], v[66:67], v[158:159] op_sel_hi:[1,0]\n"
    "v_pk_mul_f32 v[68:69], v[68:69], v[158:159] op_sel_hi:[1,0]\n"
    "v_pk_mul_f32 v[70:71], v[70:71], v[158:159] op_sel_hi:[1,0]\n"
    "v_pk_fma_f32 v[32:33], v[64:65], v[64:65], v[156:157] op_sel_hi:[1,1,0] neg_lo:[1,0,0] neg_hi:[1,0,0]\n"
    "v_pk_fma_f32 v[34:35], v[66:67], v[66:67], v[156:157] op_sel_hi:[1,1,0] neg_lo:[1,0,0] neg_hi:[1,0,0]\n"
    "v_pk_fma_f32 v[36:37], v[68:69], v[68:69], v[156:157] op_sel_hi:[1,1,0] neg_lo:[1,0,0] neg_hi:[1,0,0]\n"
    "v_pk_fma_f32 v[38:39], v[70:71], v[70:71], v[156:157] op_sel_hi:[1,1,0] neg_lo:[1,0,0] neg_hi:[1,0,0]\n"
    "v_pk_mul_f32 v[32:33], v[64:65], v[32:33]\n"
    "v_pk_mul_f32 v[34:35], v[66:67], v[34:35]\n"
    "v_pk_mul_f32 v[36:37], v[68:69], v[36:37]\n"
    "v_pk_mul_f32 v[38:39], v[70:71], v[38:39]\n"
    "s_mov_b64 s[48:49], %9\n"
    "s_mov_b32 s50, 1152\n"
    "s_mov_b32 s51, 0x20000\n"
    "v_lshrrev_b32 v3, 4, v1\n"
    "v_and_b32 v2, 3, v1\n"
    "v_lshl_add_u32 v4, v3, 2, v2\n"
    "v_lshlrev_b32 v4, 4, v4\n"
    "v_add_u32 v5, v4, v171\n"
    "ds_read_b128 a[16:19], v12 offset:8192\n"
    "ds_read_b128 a[20:23], v12 offset:9216\n"
    "ds_read_b128 a[24:27], v12 offset:10240\n"
    "ds_read_b128 a[28:31], v12 offset:11264\n"
    "buffer_load_dwordx4 v[132:135], v4, s[48:51], 0 offen offset:0\n"
    "buffer_load_dwordx4 v[136:139], v4, s[48:51], 0 offen offset:256\n"
    "v_exp_f32 v32, v32\n"
    "v_exp_f32 v33, v33\n"
    "v_exp_f32 v34, v34\n"
    "v_exp_f32 v35, v35\n"
    "v_exp_f32 v36, v36\n"
    "v_exp_f32 v37, v37\n"
    "v_exp_f32 v38, v38\n"
    "v_exp_f32 v39, v39\n"
    "v_pk_add_f32 v[32:33], v[32:33], 1.0 op_sel_hi:[1,0]\n"
    "v_pk_add_f32 v[34:35], v[34:35], 1.0 op_sel_hi:[1,0]\n"
    "v_pk_add_f32 v[36:37], v[36:37], 1.0 op_sel_hi:[1,0]\n"
    "v_pk_add_f32 v[38:39], v[38:39], 1.0 op_sel_hi:[1,0]\n"
    "ds_read_b128 a[32:35], v12 offset:16384\n"
    "ds_read_b128 a[36:39], v12 offset:17408\n"
    "ds_read_b128 a[40:43], v12 offset:18432\n"
    "ds_read_b128 a[44:47], v12 offset:19456\n"
    "buffer_load_dwordx4 v[140:143], v4, s[48:51], 0 offen offset:512\n"
    "buffer_load_dwordx4 v[144:147], v4, s[48:51], 0 offen offset:768\n"
    "v_rcp_f32 v32, v32\n"
    "v_rcp_f32 v33, v33\n"
    "v_rcp_f32 v34, v34\n"
    "v_rcp_f32 v35, v35\n"
    "v_rcp_f32 v36, v36\n"
    "v_rcp_f32 v37, v37\n"
    "v_rcp_f32 v38, v38\n"
    "v_rcp_f32 v39, v39\n"
    "v_pk_mul_f32 v[64:65], v[64:65], v[32:33]\n"
    "v_pk_mul_f32 v[66:67], v[66:67], v[34:35]\n"
    "v_pk_mul_f32 v[68:69], v[68:69], v[36:37]\n"
    "v_pk_mul_f32 v[70:71], v[70:71], v[38:39]\n"
    "ds_read_b128 a[48:51], v12 offset:24576\n"
    "ds_read_b128 a[52:55], v12 offset:25600\n"
    "ds_read_b128 a[56:59], v12 offset:26624\n"
    "ds_read_b128 a[60:63], v12 offset:27648\n"
    "buffer_load_dwordx4 v[148:151], v4, s[48:51], 0 offen offset:1024\n"
    "buffer_load_dwordx4 v[112:115], v5, s[40:43], 0 offen offset:0\n"
    "v_cvt_pk_bf16_f32 v64, v64, v65\n"
    "v_cvt_pk_bf16_f32 v65, v66, v67\n"
    "v_cvt_pk_bf16_f32 v66, v68, v69\n"
    "v_cvt_pk_bf16_f32 v67, v70, v71\n"
    "v_pk_mul_f32 v[72:73], v[72:73], v[158:159] op_sel_hi:[1,0]\n"
    "v_pk_mul_f32 v[74:75], v[74:75], v[158:159] op_sel_hi:[1,0]\n"
    "v_pk_mul_f32 v[76:77], v[76:77], v[158:159] op_sel_hi:[1,0]\n"
    "v_pk_mul_f32 v[78:79], v[78:79], v[158:159] op_sel_hi:[1,0]\n"
    "v_pk_fma_f32 v[40:41], v[72:73], v[72:73], v[156:157] op_sel_hi:[1,1,0] neg_lo:[1,0,0] neg_hi:[1,0,0]\n"
    "v_pk_fma_f32 v[42:43], v[74:75], v[74:75], v[156:157] op_sel_hi:[1,1,0] neg_lo:[1,0,0] neg_hi:[1,0,0]\n"
    "v_pk_fma_f32 v[44:45], v[76:77], v[76:77], v[156:157] op_sel_hi:[1,1,0] neg_lo:[1,0,0] neg_hi:[1,0,0]\n"
    "v_pk_fma_f32 v[46:47], v[78:79], v[78:79], v[156:157] op_sel_hi:[1,1,0] neg_lo:[1,0,0] neg_hi:[1,0,0]\n"
    "v_pk_mul_f32 v[40:41], v[72:73], v[40:41]\n"
    "v_pk_mul_f32 v[42:43], v[74:75], v[42:43]\n"
    "v_pk_mul_f32 v[44:45], v[76:77], v[44:45]\n"
    "v_pk_mul_f32 v[46:47], v[78:79], v[46:47]\n"
    "s_waitcnt lgkmcnt(15)\n"
    "s_barrier\n"
    "ds_read_b128 a[64:67], v12 offset:32768\n"
    "ds_read_b128 a[68:71], v12 offset:33792\n"
    "ds_read_b128 a[72:75], v12 offset:34816\n"
    "ds_read_b128 a[76:79], v12 offset:35840\n"
    "buffer_load_dwordx4 v[116:119], v5, s[40:43], 0 offen offset:256\n"
    "buffer_load_dwordx4 v[120:123], v5, s[40:43], 0 offen offset:512\n"
    "v_exp_f32 v40, v40\n"
    "v_exp_f32 v41, v41\n"
    "v_exp_f32 v42, v42\n"
    "v_exp_f32 v43, v43\n"
    "v_exp_f32 v44, v44\n"
    "v_exp_f32 v45, v45\n"
    "v_exp_f32 v46, v46\n"
    "v_exp_f32 v47, v47\n"
    "v_pk_add_f32 v[40:41], v[40:41], 1.0 op_sel_hi:[1,0]\n"
    "v_pk_add_f32 v[42:43], v[42:43], 1.0 op_sel_hi:[1,0]\n"
    "v_pk_add_f32 v[44:45], v[44:45], 1.0 op_sel_hi:[1,0]\n"
    "v_pk_add_f32 v[46:47], v[46:47], 1.0 op_sel_hi:[1,0]\n"
    "ds_read_b128 a[80:83], v12 offset:40960\n"
    "ds_read_b128 a[84:87], v12 offset:41984\n"
    "ds_read_b128 a[88:91], v12 offset:43008\n"
    "ds_read_b128 a[92:95], v12 offset:44032\n"
    "buffer_load_dwordx4 v[124:127], v5, s[40:43], 0 offen offset:768\n"
    "v_rcp_f32 v40, v40\n"
    "v_rcp_f32 v41, v41\n"
    "v_rcp_f32 v42, v42\n"
    "v_rcp_f32 v43, v43\n"
    "v_rcp_f32 v44, v44\n"
    "v_rcp_f32 v45, v45\n"
    "v_rcp_f32 v46, v46\n"
    "v_rcp_f32 v47, v47\n"
    "v_pk_mul_f32 v[72:73], v[72:73], v[40:41]\n"
    "v_pk_mul_f32 v[74:75], v[74:75], v[42:43]\n"
    "v_pk_mul_f32 v[76:77], v[76:77], v[44:45]\n"
    "v_pk_mul_f32 v[78:79], v[78:79], v[46:47]\n"
    "v_cmp_gt_u32 vcc, 32, v1\n"
    "v_mov_b32 v4, 1879048192\n"
    "s_and_b32 s30, s83, 1\n"
    "v_cndmask_b32 v5, v4, v5, vcc\n"
    "s_mov_b64 s[48:49], %8\n"
    "ds_read_b128 a[96:99], v12 offset:49152\n"
    "ds_read_b128 a[100:103], v12 offset:50176\n"
    "ds_read_b128 a[104:107], v12 offset:51200\n"
    "ds_read_b128 a[108:111], v12 offset:52224\n"
    "buffer_load_dwordx4 v[128:131], v5, s[40:43], 0 offen offset:1024\n"
    "v_cvt_pk_bf16_f32 v68, v72, v73\n"
    "v_cvt_pk_bf16_f32 v69, v74, v75\n"
    "v_cvt_pk_bf16_f32 v70, v76, v77\n"
    "v_cvt_pk_bf16_f32 v71, v78, v79\n"
    "v_pk_mul_f32 v[80:81], v[80:81], v[160:161] op_sel_hi:[1,0]\n"
    "v_pk_mul_f32 v[82:83], v[82:83], v[160:161] op_sel_hi:[1,0]\n"
    "v_pk_mul_f32 v[84:85], v[84:85], v[160:161] op_sel_hi:[1,0]\n"
    "v_pk_mul_f32 v[86:87], v[86:87], v[160:161] op_sel_hi:[1,0]\n"
    "v_pk_fma_f32 v[32:33], v[80:81], v[80:81], v[156:157] op_sel_hi:[1,1,0] neg_lo:[1,0,0] neg_hi:[1,0,0]\n"
    "v_pk_fma_f32 v[34:35], v[82:83], v[82:83], v[156:157] op_sel_hi:[1,1,0] neg_lo:[1,0,0] neg_hi:[1,0,0]\n"
    "v_pk_fma_f32 v[36:37], v[84:85], v[84:85], v[156:157] op_sel_hi:[1,1,0] neg_lo:[1,0,0] neg_hi:[1,0,0]\n"
    "v_pk_fma_f32 v[38:39], v[86:87], v[86:87], v[156:157] op_sel_hi:[1,1,0] neg_lo:[1,0,0] neg_hi:[1,0,0]\n"
    "v_pk_mul_f32 v[32:33], v[80:81], v[32:33]\n"
    "v_pk_mul_f32 v[34:35], v[82:83], v[34:35]\n"
    "v_pk_mul_f32 v[36:37], v[84:85], v[36:37]\n"
    "v_pk_mul_f32 v[38:39], v[86:87], v[38:39]\n"
    "s_lshl_b32 s30, s30, 6\n"
    "v_lshlrev_b32 v2, 3, v2\n"
    "v_and_b32 v4, 1, v3\n"
    "v_lshl_add_u32 v2, v4, 5, v2\n"
    "v_lshrrev_b32 v4, 1, v3\n"
    "v_lshl_add_u32 v2, v4, 7, v2\n"
    "v_add_u32 v2, s30, v2\n"
    "v_add_u32 v171, v2, v171\n"
    "ds_read_b128 a[112:115], v12 offset:57344\n"
    "ds_read_b128 a[116:119], v12 offset:58368\n"
    "ds_read_b128 a[120:123], v12 offset:59392\n"
    "ds_read_b128 a[124:127], v12 offset:60416\n"
    "buffer_load_dwordx2 v[200:201], v2, s[48:51], 0 offen offset:0\n"
    "buffer_load_dwordx2 v[202:203], v2, s[48:51], 0 offen offset:256\n"
    "v_exp_f32 v32, v32\n"
    "v_exp_f32 v33, v33\n"
    "v_exp_f32 v34, v34\n"
    "v_exp_f32 v35, v35\n"
    "v_exp_f32 v36, v36\n"
    "v_exp_f32 v37, v37\n"
    "v_exp_f32 v38, v38\n"
    "v_exp_f32 v39, v39\n"
    "v_pk_add_f32 v[32:33], v[32:33], 1.0 op_sel_hi:[1,0]\n"
    "v_pk_add_f32 v[34:35], v[34:35], 1.0 op_sel_hi:[1,0]\n"
    "v_pk_add_f32 v[36:37], v[36:37], 1.0 op_sel_hi:[1,0]\n"
    "v_pk_add_f32 v[38:39], v[38:39], 1.0 op_sel_hi:[1,0]\n"
    "ds_read_b128 a[128:131], v13 offset:0\n"
    "ds_read_b128 a[132:135], v13 offset:1024\n"
    "ds_read_b128 a[136:139], v13 offset:2048\n"
    "ds_read_b128 a[140:143], v13 offset:3072\n"
    "buffer_load_dwordx2 v[204:205], v2, s[48:51], 0 offen offset:512\n"
    "buffer_load_dwordx2 v[206:207], v2, s[48:51], 0 offen offset:768\n"
    "v_rcp_f32 v32, v32\n"
    "v_rcp_f32 v33, v33\n"
    "v_rcp_f32 v34, v34\n"
    "v_rcp_f32 v35, v35\n"
    "v_rcp_f32 v36, v36\n"
    "v_rcp_f32 v37, v37\n"
    "v_rcp_f32 v38, v38\n"
    "v_rcp_f32 v39, v39\n"
    "v_pk_mul_f32 v[80:81], v[80:81], v[32:33]\n"
    "v_pk_mul_f32 v[82:83], v[82:83], v[34:35]\n"
    "v_pk_mul_f32 v[84:85], v[84:85], v[36:37]\n"
    "v_pk_mul_f32 v[86:87], v[86:87], v[38:39]\n"
    "ds_read_b128 a[144:147], v13 offset:8192\n"
    "ds_read_b128 a[148:151], v13 offset:9216\n"
    "ds_read_b128 a[152:155], v13 offset:10240\n"
    "ds_read_b128 a[156:159], v13 offset:11264\n"
    "buffer_load_dwordx2 v[208:209], v2, s[48:51], 0 offen offset:1024\n"
    "v_cvt_pk_bf16_f32 v72, v80, v81\n"
    "v_cvt_pk_bf16_f32 v73, v82, v83\n"
    "v_cvt_pk_bf16_f32 v74, v84, v85\n"
    "v_cvt_pk_bf16_f32 v75, v86, v87\n"
    "v_pk_mul_f32 v[88:89], v[88:89], v[160:161] op_sel_hi:[1,0]\n"
    "v_pk_mul_f32 v[90:91], v[90:91], v[160:161] op_sel_hi:[1,0]\n"
    "v_pk_mul_f32 v[92:93], v[92:93], v[160:161] op_sel_hi:[1,0]\n"
    "v_pk_mul_f32 v[94:95], v[94:95], v[160:161] op_sel_hi:[1,0]\n"
    "v_pk_fma_f32 v[40:41], v[88:89], v[88:89], v[156:157] op_sel_hi:[1,1,0] neg_lo:[1,0,0] neg_hi:[1,0,0]\n"
    "v_pk_fma_f32 v[42:43], v[90:91], v[90:91], v[156:157] op_sel_hi:[1,1,0] neg_lo:[1,0,0] neg_hi:[1,0,0]\n"
    "v_pk_fma_f32 v[44:45], v[92:93], v[92:93], v[156:157] op_sel_hi:[1,1,0] neg_lo:[1,0,0] neg_hi:[1,0,0]\n"
    "v_pk_fma_f32 v[46:47], v[94:95], v[94:95], v[156:157] op_sel_hi:[1,1,0] neg_lo:[1,0,0] neg_hi:[1,0,0]\n"
    "v_pk_mul_f32 v[40:41], v[88:89], v[40:41]\n"
    "v_pk_mul_f32 v[42:43], v[90:91], v[42:43]\n"
    "v_pk_mul_f32 v[44:45], v[92:93], v[44:45]\n"
    "v_pk_mul_f32 v[46:47], v[94:95], v[46:47]\n"
    "ds_read_b64_tr_b16 v[96:97], v21 offset:0\n"
    "ds_read_b64_tr_b16 v[98:99], v21 offset:1024\n"
    "ds_read_b64_tr_b16 v[100:101], v21 offset:4096\n"
    "ds_read_b64_tr_b16 v[102:103], v21 offset:5120\n"
    "v_exp_f32 v40, v40\n"
    "v_exp_f32 v41, v41\n"
    "v_exp_f32 v42, v42\n"
    "v_exp_f32 v43, v43\n"
    "v_exp_f32 v44, v44\n"
    "v_exp_f32 v45, v45\n"
    "v_exp_f32 v46, v46\n"
    "v_exp_f32 v47, v47\n"
    "v_pk_add_f32 v[40:41], v[40:41], 1.0 op_sel_hi:[1,0]\n"
    "v_pk_add_f32 v[42:43], v[42:43], 1.0 op_sel_hi:[1,0]\n"
    "v_pk_add_f32 v[44:45], v[44:45], 1.0 op_sel_hi:[1,0]\n"
    "v_pk_add_f32 v[46:47], v[46:47], 1.0 op_sel_hi:[1,0]\n"
    "v_rcp_f32 v40, v40\n"
    "v_rcp_f32 v41, v41\n"
    "v_rcp_f32 v42, v42\n"
    "v_rcp_f32 v43, v43\n"
    "v_rcp_f32 v44, v44\n"
    "v_rcp_f32 v45, v45\n"
    "v_rcp_f32 v46, v46\n"
    "v_rcp_f32 v47, v47\n"
    "v_pk_mul_f32 v[88:89], v[88:89], v[40:41]\n"
    "v_pk_mul_f32 v[90:91], v[90:91], v[42:43]\n"
    "v_pk_mul_f32 v[92:93], v[92:93], v[44:45]\n"
    "v_pk_mul_f32 v[94:95], v[94:95], v[46:47]\n"
    "v_cvt_pk_bf16_f32 v76, v88, v89\n"
    "v_cvt_pk_bf16_f32 v77, v90, v91\n"
    "v_cvt_pk_bf16_f32 v78, v92, v93\n"
    "v_cvt_pk_bf16_f32 v79, v94, v95\n"
    "s_waitcnt lgkmcnt(0)\n"
    "s_barrier\n"
    "s_add_u32 s85, s64, s86\n"
    "s_add_u32 m0, s89, 81920\n"
    "v_mfma_f32_16x16x32_bf16 v[32:35], v[96:99], a[0:3], 0\n"
    "ds_read_b64_tr_b16 v[104:105], v22 offset:0\n"
    "v_and_b32 v184, 15, v1\n"
    "v_mfma_f32_16x16x32_bf16 v[32:35], v[100:103], a[16:19], v[32:35]\n"
    "ds_read_b64_tr_b16 v[106:107], v22 offset:1024\n"
    "v_lshrrev_b32 v185, 4, v1\n"
    "v_mfma_f32_16x16x32_bf16 v[36:39], v[96:99], a[4:7], 0\n"
    "ds_read_b64_tr_b16 v[108:109], v22 offset:4096\n"
    "v_lshrrev_b32 v6, 2, v184\n"
    "v_mfma_f32_16x16x32_bf16 v[36:39], v[100:103], a[20:23], v[36:39]\n"
    "buffer_load_dwordx4 v10, s[36:39], s85 offen lds\n"
    "v_mfma_f32_16x16x32_bf16 v[40:43], v[96:99], a[8:11], 0\n"
    "ds_read_b64_tr_b16 v[110:111], v22 offset:5120\n"
    "v_lshlrev_b32 v6, 8, v6\n"
    "v_mfma_f32_16x16x32_bf16 v[40:43], v[100:103], a[24:27], v[40:43]\n"
    "ds_write_b64 v14, v[64:65] offset:0\n"
    "v_mfma_f32_16x16x32_bf16 v[44:47], v[96:99], a[12:15], 0\n"
    "ds_write_b64 v14, v[66:67] offset:8\n"
    "v_mfma_f32_16x16x32_bf16 v[44:47], v[100:103], a[28:31], v[44:47]\n"
    "s_waitcnt lgkmcnt(4)\n"
    "buffer_load_dwordx4 v11, s[36:39], s85 offen offset:1024 lds\n"
    "v_mfma_f32_16x16x32_bf16 v[48:51], v[104:107], a[0:3], 0\n"
    "s_waitcnt lgkmcnt(2)\n"
    "ds_read_b64_tr_b16 v[96:97], v21 offset:8192\n"
    "v_and_b32 v184, 3, v184\n"
    "v_mfma_f32_16x16x32_bf16 v[48:51], v[108:111], a[16:19], v[48:51]\n"
    "s_add_u32 s85, s65, s86\n"
    "ds_read_b64_tr_b16 v[98:99], v21 offset:9216\n"
    "v_lshl_add_u32 v6, v184, 4, v6\n"
    "v_mfma_f32_16x16x32_bf16 v[52:55], v[104:107], a[4:7], 0\n"
    "s_add_u32 m0, s89, 90112\n"
    "ds_read_b64_tr_b16 v[100:101], v21 offset:12288\n"
    "v_lshl_add_u32 v6, v185, 6, v6\n"
    "v_mfma_f32_16x16x32_bf16 v[52:55], v[108:111], a[20:23], v[52:55]\n"
    "buffer_load_dwordx4 v10, s[36:39], s85 offen lds\n"
    "v_mfma_f32_16x16x32_bf16 v[56:59], v[104:107], a[8:11], 0\n"
    "ds_read_b64_tr_b16 v[102:103], v21 offset:13312\n"
    "v_add_u32 v7, 294912, v6\n"
    "v_mfma_f32_16x16x32_bf16 v[56:59], v[108:111], a[24:27], v[56:59]\n"
    "ds_write_b64 v14, v[68:69] offset:512\n"
    "v_mfma_f32_16x16x32_bf16 v[60:63], v[104:107], a[12:15], 0\n"
    "ds_write_b64 v14, v[70:71] offset:520\n"
    "v_mfma_f32_16x16x32_bf16 v[60:63], v[108:111], a[28:31], v[60:63]\n"
    "s_waitcnt lgkmcnt(4)\n"
    "buffer_load_dwordx4 v11, s[36:39], s85 offen offset:1024 lds\n"
    "v_mfma_f32_16x16x32_bf16 v[32:35], v[96:99], a[32:35], v[32:35]\n"
    "s_waitcnt lgkmcnt(2)\n"
    "ds_read_b64_tr_b16 v[104:105], v22 offset:8192\n"
    "v_add_u32 v8, 589824, v6\n"
    "v_mfma_f32_16x16x32_bf16 v[32:35], v[100:103], a[48:51], v[32:35]\n"
    "s_add_u32 s85, s66, s86\n"
    "ds_read_b64_tr_b16 v[106:107], v22 offset:9216\n"
    "v_add_u32 v9, 884736, v6\n"
    "v_mfma_f32_16x16x32_bf16 v[36:39], v[96:99], a[36:39], v[36:39]\n"
    "s_add_u32 m0, s89, 98304\n"
    "ds_read_b64_tr_b16 v[108:109], v22 offset:12288\n"
    "s_mov_b32 s88, 8\n"
    "v_mfma_f32_16x16x32_bf16 v[36:39], v[100:103], a[52:55], v[36:39]\n"
    "buffer_load_dwordx4 v10, s[36:39], s85 offen lds\n"
    "v_mfma_f32_16x16x32_bf16 v[40:43], v[96:99], a[40:43], v[40:43]\n"
    "ds_read_b64_tr_b16 v[110:111], v22 offset:13312\n"
    "s_mov_b32 s58, 1879048192\n"
    "v_mfma_f32_16x16x32_bf16 v[40:43], v[100:103], a[56:59], v[40:43]\n"
    "ds_write_b64 v15, v[72:73] offset:0\n"
    "v_mfma_f32_16x16x32_bf16 v[44:47], v[96:99], a[44:47], v[44:47]\n"
    "buffer_load_dwordx4 v11, s[36:39], s85 offen offset:1024 lds\n"
    "v_mfma_f32_16x16x32_bf16 v[44:47], v[100:103], a[60:63], v[44:47]\n"
    "s_waitcnt vmcnt(28) lgkmcnt(1)\n"
    "s_barrier\n"
    "v_mfma_f32_16x16x32_bf16 v[48:51], v[104:107], a[32:35], v[48:51]\n"
    "s_waitcnt lgkmcnt(1)\n"
    "ds_read_b64_tr_b16 v[96:97], v21 offset:16384\n"
    "s_mov_b32 s59, 1879048192\n"
    "v_mfma_f32_16x16x32_bf16 v[48:51], v[108:111], a[48:51], v[48:51]\n"
    "s_add_u32 s85, s67, s86\n"
    "ds_read_b64_tr_b16 v[98:99], v21 offset:17408\n"
    "s_mov_b32 s60, 1879048192\n"
    "v_mfma_f32_16x16x32_bf16 v[52:55], v[104:107], a[36:39], v[52:55]\n"
    "s_add_u32 m0, s89, 106496\n"
    "ds_read_b64_tr_b16 v[100:101], v21 offset:20480\n"
    "s_mov_b64 s[62:63], 0\n"
    "v_mfma_f32_16x16x32_bf16 v[52:55], v[108:111], a[52:55], v[52:55]\n"
    "buffer_load_dwordx4 v10, s[36:39], s85 offen lds\n"
    "v_mfma_f32_16x16x32_bf16 v[56:59], v[104:107], a[40:43], v[56:59]\n"
    "ds_read_b64_tr_b16 v[102:103], v21 offset:21504\n"
    "v_mfma_f32_16x16x32_bf16 v[56:59], v[108:111], a[56:59], v[56:59]\n"
    "ds_write_b64 v15, v[74:75] offset:8\n"
    "v_mfma_f32_16x16x32_bf16 v[60:63], v[104:107], a[44:47], v[60:63]\n"
    "ds_write_b64 v15, v[76:77] offset:512\n"
    "v_mfma_f32_16x16x32_bf16 v[60:63], v[108:111], a[60:63], v[60:63]\n"
    "s_waitcnt lgkmcnt(4)\n"
    "buffer_load_dwordx4 v11, s[36:39], s85 offen offset:1024 lds\n"
    "v_mfma_f32_16x16x32_bf16 v[32:35], v[96:99], a[64:67], v[32:35]\n"
    "s_waitcnt lgkmcnt(2)\n"
    "ds_read_b64_tr_b16 v[104:105], v22 offset:16384\n"
    "v_mfma_f32_16x16x32_bf16 v[32:35], v[100:103], a[80:83], v[32:35]\n"
    "s_add_u32 s85, s68, s86\n"
    "ds_read_b64_tr_b16 v[106:107], v22 offset:17408\n"
    "v_mfma_f32_16x16x32_bf16 v[36:39], v[96:99], a[68:71], v[36:39]\n"
    "s_add_u32 m0, s89, 114688\n"
    "ds_read_b64_tr_b16 v[108:109], v22 offset:20480\n"
    "v_mfma_f32_16x16x32_bf16 v[36:39], v[100:103], a[84:87], v[36:39]\n"
    "buffer_load_dwordx4 v10, s[36:39], s85 offen lds\n"
    "v_mfma_f32_16x16x32_bf16 v[40:43], v[96:99], a[72:75], v[40:43]\n"
    "ds_read_b64_tr_b16 v[110:111], v22 offset:21504\n"
    "v_mfma_f32_16x16x32_bf16 v[40:43], v[100:103], a[88:91], v[40:43]\n"
    "ds_write_b64 v15, v[78:79] offset:520\n"
    "v_mfma_f32_16x16x32_bf16 v[44:47], v[96:99], a[76:79], v[44:47]\n"
    "v_mfma_f32_16x16x32_bf16 v[44:47], v[100:103], a[92:95], v[44:47]\n"
    "s_waitcnt lgkmcnt(3)\n"
    "buffer_load_dwordx4 v11, s[36:39], s85 offen offset:1024 lds\n"
    "v_mfma_f32_16x16x32_bf16 v[48:51], v[104:107], a[64:67], v[48:51]\n"
    "s_waitcnt lgkmcnt(1)\n"
    "ds_read_b64_tr_b16 v[96:97], v21 offset:24576\n"
    "v_mfma_f32_16x16x32_bf16 v[48:51], v[108:111], a[80:83], v[48:51]\n"
    "s_add_u32 s85, s69, s86\n"
    "ds_read_b64_tr_b16 v[98:99], v21 offset:25600\n"
    "v_mfma_f32_16x16x32_bf16 v[52:55], v[104:107], a[68:71], v[52:55]\n"
    "s_add_u32 m0, s89, 122880\n"
    "ds_read_b64_tr_b16 v[100:101], v21 offset:28672\n"
    "v_mfma_f32_16x16x32_bf16 v[52:55], v[108:111], a[84:87], v[52:55]\n"
    "buffer_load_dwordx4 v10, s[36:39], s85 offen lds\n"
    "v_mfma_f32_16x16x32_bf16 v[56:59], v[104:107], a[72:75], v[56:59]\n"
    "ds_read_b64_tr_b16 v[102:103], v21 offset:29696\n"
    "v_mfma_f32_16x16x32_bf16 v[56:59], v[108:111], a[88:91], v[56:59]\n"
    "v_mfma_f32_16x16x32_bf16 v[60:63], v[104:107], a[76:79], v[60:63]\n"
    "v_mfma_f32_16x16x32_bf16 v[60:63], v[108:111], a[92:95], v[60:63]\n"
    "s_waitcnt lgkmcnt(2)\n"
    "buffer_load_dwordx4 v11, s[36:39], s85 offen offset:1024 lds\n"
    "v_mfma_f32_16x16x32_bf16 v[32:35], v[96:99], a[96:99], v[32:35]\n"
    "s_waitcnt lgkmcnt(0)\n"
    "ds_read_b64_tr_b16 v[104:105], v22 offset:24576\n"
    "v_mfma_f32_16x16x32_bf16 v[32:35], v[100:103], a[112:115], v[32:35]\n"
    "s_mov_b32 s86, 256\n"
    "ds_read_b64_tr_b16 v[106:107], v22 offset:25600\n"
    "v_mfma_f32_16x16x32_bf16 v[36:39], v[96:99], a[100:103], v[36:39]\n"
    "s_add_u32 s85, s64, s86\n"
    "s_add_u32 m0, s89, 0\n"
    "ds_read_b64_tr_b16 v[108:109], v22 offset:28672\n"
    "v_mfma_f32_16x16x32_bf16 v[36:39], v[100:103], a[116:119], v[36:39]\n"
    "buffer_load_dwordx4 v10, s[36:39], s85 offen lds\n"
    "v_mfma_f32_16x16x32_bf16 v[40:43], v[96:99], a[104:107], v[40:43]\n"
    "ds_read_b64_tr_b16 v[110:111], v22 offset:29696\n"
    "v_mfma_f32_16x16x32_bf16 v[40:43], v[100:103], a[120:123], v[40:43]\n"
    "v_mfma_f32_16x16x32_bf16 v[44:47], v[96:99], a[108:111], v[44:47]\n"
    "buffer_load_dwordx4 v11, s[36:39], s85 offen offset:1024 lds\n"
    "v_mfma_f32_16x16x32_bf16 v[44:47], v[100:103], a[124:127], v[44:47]\n"
    "s_waitcnt vmcnt(31) lgkmcnt(0)\n"
    "s_barrier\n"
    "v_mfma_f32_16x16x32_bf16 v[48:51], v[104:107], a[96:99], v[48:51]\n"
    "s_waitcnt lgkmcnt(0)\n"
    "ds_read_b64_tr_b16 v[96:97], v21 offset:32768\n"
    "ds_read_b128 a[160:163], v16 offset:0\n"
    "v_mfma_f32_16x16x32_bf16 v[48:51], v[108:111], a[112:115], v[48:51]\n"
    "s_add_u32 s85, s65, s86\n"
    "ds_read_b64_tr_b16 v[98:99], v21 offset:33792\n"
    "ds_read_b128 a[164:167], v16 offset:1024\n"
    "v_mfma_f32_16x16x32_bf16 v[52:55], v[104:107], a[100:103], v[52:55]\n"
    "s_add_u32 m0, s89, 8192\n"
    "ds_read_b64_tr_b16 v[100:101], v21 offset:36864\n"
    "ds_read_b128 a[168:171], v16 offset:2048\n"
    "v_mfma_f32_16x16x32_bf16 v[52:55], v[108:111], a[116:119], v[52:55]\n"
    "buffer_load_dwordx4 v10, s[36:39], s85 offen lds\n"
    "v_mfma_f32_16x16x32_bf16 v[56:59], v[104:107], a[104:107], v[56:59]\n"
    "ds_read_b64_tr_b16 v[102:103], v21 offset:37888\n"
    "ds_read_b128 a[172:175], v16 offset:3072\n"
    "v_mfma_f32_16x16x32_bf16 v[56:59], v[108:111], a[120:123], v[56:59]\n"
    "ds_read_b128 a[176:179], v16 offset:8192\n"
    "v_mfma_f32_16x16x32_bf16 v[60:63], v[104:107], a[108:111], v[60:63]\n"
    "ds_read_b128 a[180:183], v16 offset:9216\n"
    "v_mfma_f32_16x16x32_bf16 v[60:63], v[108:111], a[124:127], v[60:63]\n"
    "s_waitcnt lgkmcnt(7)\n"
    "buffer_load_dwordx4 v11, s[36:39], s85 offen offset:1024 lds\n"
    "v_mfma_f32_16x16x32_bf16 v[32:35], v[96:99], a[128:131], v[32:35]\n"
    "s_waitcnt lgkmcnt(3)\n"
    "ds_read_b64_tr_b16 v[104:105], v22 offset:32768\n"
    "ds_read_b128 a[184:187], v16 offset:10240\n"
    "v_mfma_f32_16x16x32_bf16 v[32:35], v[100:103], a[144:147], v[32:35]\n"
    "s_add_u32 s85, s66, s86\n"
    "ds_read_b64_tr_b16 v[106:107], v22 offset:33792\n"
    "ds_read_b128 a[188:191], v16 offset:11264\n"
    "v_mfma_f32_16x16x32_bf16 v[36:39], v[96:99], a[132:135], v[36:39]\n"
    "s_add_u32 m0, s89, 16384\n"
    "ds_read_b64_tr_b16 v[108:109], v22 offset:36864\n"
    "v_mfma_f32_16x16x32_bf16 v[36:39], v[100:103], a[148:151], v[36:39]\n"
    "buffer_load_dwordx4 v10, s[36:39], s85 offen lds\n"
    "v_mfma_f32_16x16x32_bf16 v[40:43], v[96:99], a[136:139], v[40:43]\n"
    "ds_read_b64_tr_b16 v[110:111], v22 offset:37888\n"
    "v_mfma_f32_16x16x32_bf16 v[40:43], v[100:103], a[152:155], v[40:43]\n"
    "v_mfma_f32_16x16x32_bf16 v[44:47], v[96:99], a[140:143], v[44:47]\n"
    "v_mfma_f32_16x16x32_bf16 v[44:47], v[100:103], a[156:159], v[44:47]\n"
    "s_waitcnt lgkmcnt(3)\n"
    "buffer_load_dwordx4 v11, s[36:39], s85 offen offset:1024 lds\n"
    "v_mfma_f32_16x16x32_bf16 v[48:51], v[104:107], a[128:131], v[48:51]\n"
    "s_waitcnt lgkmcnt(0)\n"
    "ds_read_b64_tr_b16 v[96:97], v21 offset:40960\n"
    "v_mfma_f32_16x16x32_bf16 v[48:51], v[108:111], a[144:147], v[48:51]\n"
    "s_add_u32 s85, s67, s86\n"
    "ds_read_b64_tr_b16 v[98:99], v21 offset:41984\n"
    "v_mfma_f32_16x16x32_bf16 v[52:55], v[104:107], a[132:135], v[52:55]\n"
    "s_add_u32 m0, s89, 24576\n"
    "ds_read_b64_tr_b16 v[100:101], v21 offset:45056\n"
    "v_mfma_f32_16x16x32_bf16 v[52:55], v[108:111], a[148:151], v[52:55]\n"
    "buffer_load_dwordx4 v10, s[36:39], s85 offen lds\n"
    "v_mfma_f32_16x16x32_bf16 v[56:59], v[104:107], a[136:139], v[56:59]\n"
    "ds_read_b64_tr_b16 v[102:103], v21 offset:46080\n"
    "v_mfma_f32_16x16x32_bf16 v[56:59], v[108:111], a[152:155], v[56:59]\n"
    "v_mfma_f32_16x16x32_bf16 v[60:63], v[104:107], a[140:143], v[60:63]\n"
    "v_mfma_f32_16x16x32_bf16 v[60:63], v[108:111], a[156:159], v[60:63]\n"
    "s_waitcnt lgkmcnt(2)\n"
    "buffer_load_dwordx4 v11, s[36:39], s85 offen offset:1024 lds\n"
    "v_mfma_f32_16x16x32_bf16 v[32:35], v[96:99], a[160:163], v[32:35]\n"
    "s_waitcnt lgkmcnt(0)\n"
    "ds_read_b64_tr_b16 v[104:105], v22 offset:40960\n"
    "v_add_u32 v21, s92, v21\n"
    "v_mfma_f32_16x16x32_bf16 v[32:35], v[100:103], a[176:179], v[32:35]\n"
    "s_add_u32 s85, s68, s86\n"
    "ds_read_b64_tr_b16 v[106:107], v22 offset:41984\n"
    "v_mfma_f32_16x16x32_bf16 v[36:39], v[96:99], a[164:167], v[36:39]\n"
    "s_add_u32 m0, s89, 32768\n"
    "ds_read_b64_tr_b16 v[108:109], v22 offset:45056\n"
    "v_mfma_f32_16x16x32_bf16 v[36:39], v[100:103], a[180:183], v[36:39]\n"
    "ds_read_b64_tr_b16 v[110:111], v22 offset:46080\n"
    "v_mfma_f32_16x16x32_bf16 v[40:43], v[96:99], a[168:171], v[40:43]\n"
    "v_mfma_f32_16x16x32_bf16 v[40:43], v[100:103], a[184:187], v[40:43]\n"
    "v_mfma_f32_16x16x32_bf16 v[44:47], v[96:99], a[172:175], v[44:47]\n"
    "v_add_u32 v22, s92, v22\n"
    "s_sub_u32 s92, 0, s92\n"
    "v_mfma_f32_16x16x32_bf16 v[44:47], v[100:103], a[188:191], v[44:47]\n"
    "s_waitcnt vmcnt(16) lgkmcnt(0)\n"
    "s_barrier\n"
    "v_mfma_f32_16x16x32_bf16 v[48:51], v[104:107], a[160:163], v[48:51]\n"
    "s_waitcnt lgkmcnt(0)\n"
    "ds_read_b64_tr_b16 v[96:97], v21 offset:0\n"
    "v_cvt_pk_bf16_f32 v80, v32, v33\n"
    "v_mfma_f32_16x16x32_bf16 v[48:51], v[108:111], a[176:179], v[48:51]\n"
    "ds_read_b64_tr_b16 v[98:99], v21 offset:1024\n"
    "v_cvt_pk_bf16_f32 v81, v34, v35\n"
    "v_mfma_f32_16x16x32_bf16 v[52:55], v[104:107], a[164:167], v[52:55]\n"
    "ds_read_b64_tr_b16 v[100:101], v21 offset:4096\n"
    "v_cvt_pk_bf16_f32 v84, v36, v37\n"
    "v_mfma_f32_16x16x32_bf16 v[52:55], v[108:111], a[180:183], v[52:55]\n"
    "buffer_load_dwordx4 v10, s[36:39], s85 offen lds\n"
    "v_mfma_f32_16x16x32_bf16 v[56:59], v[104:107], a[168:171], v[56:59]\n"
    "ds_read_b64_tr_b16 v[102:103], v21 offset:5120\n"
    "v_cvt_pk_bf16_f32 v85, v38, v39\n"
    "v_mfma_f32_16x16x32_bf16 v[56:59], v[108:111], a[184:187], v[56:59]\n"
    "v_cvt_pk_bf16_f32 v88, v40, v41\n"
    "v_cvt_pk_bf16_f32 v89, v42, v43\n"
    "v_mfma_f32_16x16x32_bf16 v[60:63], v[104:107], a[172:175], v[60:63]\n"
    "v_cvt_pk_bf16_f32 v92, v44, v45\n"
    "v_cvt_pk_bf16_f32 v93, v46, v47\n"
    "v_mfma_f32_16x16x32_bf16 v[60:63], v[108:111], a[188:191], v[60:63]\n"
    "s_waitcnt lgkmcnt(2)\n"
    ".LL_CHUNK3_%=:\n"
    "v_mfma_f32_16x16x32_bf16 v[32:35], v[96:99], a[0:3], 0\n"
    "s_waitcnt lgkmcnt(0)\n"
    "ds_read_b64_tr_b16 v[104:105], v22 offset:0\n"
    "v_cvt_pk_bf16_f32 v82, v48, v49\n"
    "v_mfma_f32_16x16x32_bf16 v[32:35], v[100:103], a[16:19], v[32:35]\n"
    "ds_read_b64_tr_b16 v[106:107], v22 offset:1024\n"
    "v_cvt_pk_bf16_f32 v83, v50, v51\n"
    "v_mfma_f32_16x16x32_bf16 v[36:39], v[96:99], a[4:7], 0\n"
    "ds_read_b64_tr_b16 v[108:109], v22 offset:4096\n"
    "v_cvt_pk_bf16_f32 v86, v52, v53\n"
    "v_mfma_f32_16x16x32_bf16 v[36:39], v[100:103], a[20:23], v[36:39]\n"
    "buffer_load_dwordx4 v11, s[36:39], s85 offen offset:1024 lds\n"
    "v_mfma_f32_16x16x32_bf16 v[40:43], v[96:99], a[8:11], 0\n"
    "s_add_u32 s85, s69, s86\n"
    "ds_read_b64_tr_b16 v[110:111], v22 offset:5120\n"
    "v_cvt_pk_bf16_f32 v87, v54, v55\n"
    "v_mfma_f32_16x16x32_bf16 v[40:43], v[100:103], a[24:27], v[40:43]\n"
    "s_add_u32 m0, s89, 40960\n"
    "v_cvt_pk_bf16_f32 v90, v56, v57\n"
    "v_cvt_pk_bf16_f32 v91, v58, v59\n"
    "v_mfma_f32_16x16x32_bf16 v[44:47], v[96:99], a[12:15], 0\n"
    "v_cvt_pk_bf16_f32 v94, v60, v61\n"
    "v_cvt_pk_bf16_f32 v95, v62, v63\n"
    "v_mfma_f32_16x16x32_bf16 v[44:47], v[100:103], a[28:31], v[44:47]\n"
    "s_waitcnt lgkmcnt(2)\n"
    "v_mfma_f32_16x16x32_bf16 v[48:51], v[104:107], a[0:3], 0\n"
    "s_waitcnt lgkmcnt(0)\n"
    "ds_read_b64_tr_b16 v[96:97], v21 offset:8192\n"
    "v_mfma_f32_16x16x32_bf16 v[48:51], v[108:111], a[16:19], v[48:51]\n"
    "ds_read_b64_tr_b16 v[98:99], v21 offset:9216\n"
    "v_mfma_f32_16x16x32_bf16 v[52:55], v[104:107], a[4:7], 0\n"
    "ds_read_b64_tr_b16 v[100:101], v21 offset:12288\n"
    "v_mfma_f32_16x16x32_bf16 v[52:55], v[108:111], a[20:23], v[52:55]\n"
    "buffer_load_dwordx4 v10, s[36:39], s85 offen lds\n"
    "v_mfma_f32_16x16x32_bf16 v[56:59], v[104:107], a[8:11], 0\n"
    "ds_read_b64_tr_b16 v[102:103], v21 offset:13312\n"
    "v_mfma_f32_16x16x32_bf16 v[56:59], v[108:111], a[24:27], v[56:59]\n"
    "v_mfma_f32_16x16x32_bf16 v[60:63], v[104:107], a[12:15], 0\n"
    "v_mfma_f32_16x16x32_bf16 v[60:63], v[108:111], a[28:31], v[60:63]\n"
    "s_waitcnt lgkmcnt(2)\n"
    "buffer_store_dwordx4 v[80:83], v6, s[44:47], s95 offen sc1\n"
    "v_mfma_f32_16x16x32_bf16 v[32:35], v[96:99], a[32:35], v[32:35]\n"
    "s_waitcnt lgkmcnt(0)\n"
    "ds_read_b64_tr_b16 v[104:105], v22 offset:8192\n"
    "v_mfma_f32_16x16x32_bf16 v[32:35], v[100:103], a[48:51], v[32:35]\n"
    "ds_read_b64_tr_b16 v[106:107], v22 offset:9216\n"
    "v_mfma_f32_16x16x32_bf16 v[36:39], v[96:99], a[36:39], v[36:39]\n"
    "ds_read_b64_tr_b16 v[108:109], v22 offset:12288\n"
    "v_mfma_f32_16x16x32_bf16 v[36:39], v[100:103], a[52:55], v[36:39]\n"
    "buffer_load_dwordx4 v11, s[36:39], s85 offen offset:1024 lds\n"
    "v_mfma_f32_16x16x32_bf16 v[40:43], v[96:99], a[40:43], v[40:43]\n"
    "s_sub_u32 s89, s89, s92\n"
    "ds_read_b64_tr_b16 v[110:111], v22 offset:13312\n"
    "v_mfma_f32_16x16x32_bf16 v[40:43], v[100:103], a[56:59], v[40:43]\n"
    "s_add_u32 s86, s86, 128\n"
    "s_add_u32 s60, s60, 16384\n"
    "s_cmp_eq_u32 s88, 6\n"
    "s_cselect_b32 s60, s99, s60\n"
    "v_mfma_f32_16x16x32_bf16 v[44:47], v[96:99], a[44:47], v[44:47]\n"
    "s_cmp_le_u32 s88, 2\n"
    "s_cselect_b32 s86, 1879048192, s86\n"
    "v_mfma_f32_16x16x32_bf16 v[44:47], v[100:103], a[60:63], v[44:47]\n"
    "s_waitcnt vmcnt(13) lgkmcnt(0)\n"
    "s_barrier\n"
    "v_mfma_f32_16x16x32_bf16 v[48:51], v[104:107], a[32:35], v[48:51]\n"
    "s_waitcnt lgkmcnt(0)\n"
    "ds_read_b64_tr_b16 v[96:97], v21 offset:16384\n"
    "v_mfma_f32_16x16x32_bf16 v[48:51], v[108:111], a[48:51], v[48:51]\n"
    "s_add_u32 s85, s64, s86\n"
    "ds_read_b64_tr_b16 v[98:99], v21 offset:17408\n"
    "v_mfma_f32_16x16x32_bf16 v[52:55], v[104:107], a[36:39], v[52:55]\n"
    "s_add_u32 m0, s89, 0\n"
    "ds_read_b64_tr_b16 v[100:101], v21 offset:20480\n"
    "v_mfma_f32_16x16x32_bf16 v[52:55], v[108:111], a[52:55], v[52:55]\n"
    "buffer_load_dwordx4 v10, s[36:39], s85 offen lds\n"
    "v_mfma_f32_16x16x32_bf16 v[56:59], v[104:107], a[40:43], v[56:59]\n"
    "ds_read_b64_tr_b16 v[102:103], v21 offset:21504\n"
    "v_mfma_f32_16x16x32_bf16 v[56:59], v[108:111], a[56:59], v[56:59]\n"
    "v_mfma_f32_16x16x32_bf16 v[60:63], v[104:107], a[44:47], v[60:63]\n"
    "v_mfma_f32_16x16x32_bf16 v[60:63], v[108:111], a[60:63], v[60:63]\n"
    "s_waitcnt lgkmcnt(2)\n"
    "buffer_store_dwordx4 v[84:87], v7, s[44:47], s95 offen sc1\n"
    "v_mfma_f32_16x16x32_bf16 v[32:35], v[96:99], a[64:67], v[32:35]\n"
    "s_waitcnt lgkmcnt(0)\n"
    "ds_read_b64_tr_b16 v[104:105], v22 offset:16384\n"
    "v_mfma_f32_16x16x32_bf16 v[32:35], v[100:103], a[80:83], v[32:35]\n"
    "ds_read_b64_tr_b16 v[106:107], v22 offset:17408\n"
    "v_mfma_f32_16x16x32_bf16 v[36:39], v[96:99], a[68:71], v[36:39]\n"
    "ds_read_b64_tr_b16 v[108:109], v22 offset:20480\n"
    "v_mfma_f32_16x16x32_bf16 v[36:39], v[100:103], a[84:87], v[36:39]\n"
    "buffer_load_dwordx4 v11, s[36:39], s85 offen offset:1024 lds\n"
    "v_mfma_f32_16x16x32_bf16 v[40:43], v[96:99], a[72:75], v[40:43]\n"
    "s_add_u32 s85, s65, s86\n"
    "ds_read_b64_tr_b16 v[110:111], v22 offset:21504\n"
    "v_mfma_f32_16x16x32_bf16 v[40:43], v[100:103], a[88:91], v[40:43]\n"
    "s_add_u32 m0, s89, 8192\n"
    "v_mfma_f32_16x16x32_bf16 v[44:47], v[96:99], a[76:79], v[44:47]\n"
    "v_mfma_f32_16x16x32_bf16 v[44:47], v[100:103], a[92:95], v[44:47]\n"
    "s_waitcnt lgkmcnt(2)\n"
    "buffer_store_dwordx4 v[88:91], v8, s[44:47], s95 offen sc1\n"
    "v_mfma_f32_16x16x32_bf16 v[48:51], v[104:107], a[64:67], v[48:51]\n"
    "s_waitcnt lgkmcnt(0)\n"
    "ds_read_b64_tr_b16 v[96:97], v21 offset:24576\n"
    "v_mfma_f32_16x16x32_bf16 v[48:51], v[108:111], a[80:83], v[48:51]\n"
    "ds_read_b64_tr_b16 v[98:99], v21 offset:25600\n"
    "v_mfma_f32_16x16x32_bf16 v[52:55], v[104:107], a[68:71], v[52:55]\n"
    "ds_read_b64_tr_b16 v[100:101], v21 offset:28672\n"
    "v_mfma_f32_16x16x32_bf16 v[52:55], v[108:111], a[84:87], v[52:55]\n"
    "buffer_load_dwordx4 v10, s[36:39], s85 offen lds\n"
    "v_mfma_f32_16x16x32_bf16 v[56:59], v[104:107], a[72:75], v[56:59]\n"
    "ds_read_b64_tr_b16 v[102:103], v21 offset:29696\n"
    "v_mfma_f32_16x16x32_bf16 v[56:59], v[108:111], a[88:91], v[56:59]\n"
    "s_waitcnt vmcnt(11)\n"
    "v_mfma_f32_16x16x32_bf16 v[184:187], v[172:175], v[188:191], 0\n"
    "v_mfma_f32_16x16x32_bf16 v[184:187], v[172:175], v[192:195], v[184:187]\n"
    "v_mfma_f32_16x16x32_bf16 v[184:187], v[172:175], v[196:199], v[184:187]\n"
    "v_mfma_f32_16x16x32_bf16 v[60:63], v[104:107], a[76:79], v[60:63]\n"
    "v_mfma_f32_16x16x32_bf16 v[60:63], v[108:111], a[92:95], v[60:63]\n"
    "s_waitcnt lgkmcnt(2)\n"
    "buffer_store_dwordx4 v[92:95], v9, s[44:47], s95 offen sc1\n"
    "v_mfma_f32_16x16x32_bf16 v[32:35], v[96:99], a[96:99], v[32:35]\n"
    "s_waitcnt lgkmcnt(0)\n"
    "ds_read_b64_tr_b16 v[104:105], v22 offset:24576\n"
    "v_mfma_f32_16x16x32_bf16 v[32:35], v[100:103], a[112:115], v[32:35]\n"
    "ds_read_b64_tr_b16 v[106:107], v22 offset:25600\n"
    "v_mfma_f32_16x16x32_bf16 v[36:39], v[96:99], a[100:103], v[36:39]\n"
    "ds_read_b64_tr_b16 v[108:109], v22 offset:28672\n"
    "v_mfma_f32_16x16x32_bf16 v[36:39], v[100:103], a[116:119], v[36:39]\n"
    "buffer_load_dwordx4 v11, s[36:39], s85 offen offset:1024 lds\n"
    "v_mfma_f32_16x16x32_bf16 v[40:43], v[96:99], a[104:107], v[40:43]\n"
    "s_add_u32 s85, s66, s86\n"
    "ds_read_b64_tr_b16 v[110:111], v22 offset:29696\n"
    "v_mfma_f32_16x16x32_bf16 v[40:43], v[100:103], a[120:123], v[40:43]\n"
    "s_add_u32 m0, s89, 16384\n"
    "v_mfma_f32_16x16x32_bf16 v[44:47], v[96:99], a[108:111], v[44:47]\n"
    "v_mfma_f32_16x16x32_bf16 v[44:47], v[100:103], a[124:127], v[44:47]\n"
    "s_waitcnt vmcnt(20) lgkmcnt(0)\n"
    "s_barrier\n"
    "v_mfma_f32_16x16x32_bf16 v[48:51], v[104:107], a[96:99], v[48:51]\n"
    "s_waitcnt lgkmcnt(0)\n"
    "ds_read_b64_tr_b16 v[96:97], v21 offset:32768\n"
    "v_mfma_f32_16x16x32_bf16 v[48:51], v[108:111], a[112:115], v[48:51]\n"
    "ds_read_b64_tr_b16 v[98:99], v21 offset:33792\n"
    "v_mfma_f32_16x16x32_bf16 v[52:55], v[104:107], a[100:103], v[52:55]\n"
    "ds_read_b64_tr_b16 v[100:101], v21 offset:36864\n"
    "v_mfma_f32_16x16x32_bf16 v[52:55], v[108:111], a[116:119], v[52:55]\n"
    "buffer_load_dwordx4 v10, s[36:39], s85 offen lds\n"
    "v_mfma_f32_16x16x32_bf16 v[56:59], v[104:107], a[104:107], v[56:59]\n"
    "ds_read_b64_tr_b16 v[102:103], v21 offset:37888\n"
    "v_mfma_f32_16x16x32_bf16 v[56:59], v[108:111], a[120:123], v[56:59]\n"
    "ds_write_b64 v170, v[184:185]\n"
    "v_mfma_f32_16x16x32_bf16 v[60:63], v[104:107], a[108:111], v[60:63]\n"
    "v_mfma_f32_16x16x32_bf16 v[60:63], v[108:111], a[124:127], v[60:63]\n"
    "s_waitcnt lgkmcnt(3)\n"
    "buffer_load_dwordx4 v[188:191], v212, s[44:47], s60 offen sc1\n"
    "v_mfma_f32_16x16x32_bf16 v[32:35], v[96:99], a[128:131], v[32:35]\n"
    "s_waitcnt lgkmcnt(1)\n"
    "ds_read_b64_tr_b16 v[104:105], v22 offset:32768\n"
    "v_mfma_f32_16x16x32_bf16 v[32:35], v[100:103], a[144:147], v[32:35]\n"
    "ds_read_b64_tr_b16 v[106:107], v22 offset:33792\n"
    "v_mfma_f32_16x16x32_bf16 v[36:39], v[96:99], a[132:135], v[36:39]\n"
    "ds_read_b64_tr_b16 v[108:109], v22 offset:36864\n"
    "v_mfma_f32_16x16x32_bf16 v[36:39], v[100:103], a[148:151], v[36:39]\n"
    "buffer_load_dwordx4 v11, s[36:39], s85 offen offset:1024 lds\n"
    "v_mfma_f32_16x16x32_bf16 v[40:43], v[96:99], a[136:139], v[40:43]\n"
    "s_add_u32 s85, s67, s86\n"
    "ds_read_b64_tr_b16 v[110:111], v22 offset:37888\n"
    "v_mfma_f32_16x16x32_bf16 v[40:43], v[100:103], a[152:155], v[40:43]\n"
    "s_add_u32 m0, s89, 24576\n"
    "ds_write_b64 v170, v[186:187] offset:8\n"
    "v_add_u32 v170, 0x200, v170\n"
    "v_mfma_f32_16x16x32_bf16 v[44:47], v[96:99], a[140:143], v[44:47]\n"
    "s_add_u32 s95, s95, 16384\n"
    "s_mov_b32 s58, s97\n"
    "s_add_u32 s97, s97, 32768\n"
    "v_mfma_f32_16x16x32_bf16 v[44:47], v[100:103], a[156:159], v[44:47]\n"
    "s_waitcnt lgkmcnt(3)\n"
    "buffer_load_dwordx4 v[192:195], v213, s[44:47], s60 offen sc1\n"
    "v_mfma_f32_16x16x32_bf16 v[48:51], v[104:107], a[128:131], v[48:51]\n"
    "s_waitcnt lgkmcnt(1)\n"
    "ds_read_b64_tr_b16 v[96:97], v21 offset:40960\n"
    "v_mfma_f32_16x16x32_bf16 v[48:51], v[108:111], a[144:147], v[48:51]\n"
    "ds_read_b64_tr_b16 v[98:99], v21 offset:41984\n"
    "v_mfma_f32_16x16x32_bf16 v[52:55], v[104:107], a[132:135], v[52:55]\n"
    "ds_read_b64_tr_b16 v[100:101], v21 offset:45056\n"
    "v_mfma_f32_16x16x32_bf16 v[52:55], v[108:111], a[148:151], v[52:55]\n"
    "buffer_load_dwordx4 v10, s[36:39], s85 offen lds\n"
    "v_mfma_f32_16x16x32_bf16 v[56:59], v[104:107], a[136:139], v[56:59]\n"
    "ds_read_b64_tr_b16 v[102:103], v21 offset:46080\n"
    "v_mfma_f32_16x16x32_bf16 v[56:59], v[108:111], a[152:155], v[56:59]\n"
    "v_mfma_f32_16x16x32_bf16 v[60:63], v[104:107], a[140:143], v[60:63]\n"
    "s_sub_u32 s88, s88, 1\n"
    "v_mfma_f32_16x16x32_bf16 v[60:63], v[108:111], a[156:159], v[60:63]\n"
    "s_waitcnt lgkmcnt(2)\n"
    "buffer_load_dwordx4 v[196:199], v214, s[44:47], s60 offen sc1\n"
    "v_mfma_f32_16x16x32_bf16 v[32:35], v[96:99], a[160:163], v[32:35]\n"
    "s_waitcnt lgkmcnt(0)\n"
    "ds_read_b64_tr_b16 v[104:105], v22 offset:40960\n"
    "v_add_u32 v21, s92, v21\n"
    "v_mfma_f32_16x16x32_bf16 v[32:35], v[100:103], a[176:179], v[32:35]\n"
    "ds_read_b64_tr_b16 v[106:107], v22 offset:41984\n"
    "v_mfma_f32_16x16x32_bf16 v[36:39], v[96:99], a[164:167], v[36:39]\n"
    "ds_read_b64_tr_b16 v[108:109], v22 offset:45056\n"
    "v_mfma_f32_16x16x32_bf16 v[36:39], v[100:103], a[180:183], v[36:39]\n"
    "buffer_load_dwordx4 v11, s[36:39], s85 offen offset:1024 lds\n"
    "v_mfma_f32_16x16x32_bf16 v[40:43], v[96:99], a[168:171], v[40:43]\n"
    "s_add_u32 s85, s68, s86\n"
    "ds_read_b64_tr_b16 v[110:111], v22 offset:46080\n"
    "v_mfma_f32_16x16x32_bf16 v[40:43], v[100:103], a[184:187], v[40:43]\n"
    "s_add_u32 m0, s89, 32768\n"
    "s_add_u32 s59, s59, 32768\n"
    "s_cmp_eq_u32 s88, 6\n"
    "s_cselect_b32 s59, s98, s59\n"
    "s_cselect_b64 s[62:63], -1, s[62:63]\n"
    "v_mfma_f32_16x16x32_bf16 v[44:47], v[96:99], a[172:175], v[44:47]\n"
    "v_add_u32 v22, s92, v22\n"
    "s_sub_u32 s92, 0, s92\n"
    "v_mfma_f32_16x16x32_bf16 v[44:47], v[100:103], a[188:191], v[44:47]\n"
    "s_waitcnt vmcnt(23) lgkmcnt(0)\n"
    "s_barrier\n"
    "v_mfma_f32_16x16x32_bf16 v[48:51], v[104:107], a[160:163], v[48:51]\n"
    "s_waitcnt lgkmcnt(0)\n"
    "ds_read_b64_tr_b16 v[96:97], v21 offset:0\n"
    "v_cvt_pk_bf16_f32 v80, v32, v33\n"
    "v_mfma_f32_16x16x32_bf16 v[48:51], v[108:111], a[176:179], v[48:51]\n"
    "ds_read_b64_tr_b16 v[98:99], v21 offset:1024\n"
    "v_cvt_pk_bf16_f32 v81, v34, v35\n"
    "v_mfma_f32_16x16x32_bf16 v[52:55], v[104:107], a[164:167], v[52:55]\n"
    "ds_read_b64_tr_b16 v[100:101], v21 offset:4096\n"
    "v_cvt_pk_bf16_f32 v84, v36, v37\n"
    "v_mfma_f32_16x16x32_bf16 v[52:55], v[108:111], a[180:183], v[52:55]\n"
    "buffer_load_dwordx4 v10, s[36:39], s85 offen lds\n"
    "v_mfma_f32_16x16x32_bf16 v[56:59], v[104:107], a[168:171], v[56:59]\n"
    "ds_read_b64_tr_b16 v[102:103], v21 offset:5120\n"
    "v_cvt_pk_bf16_f32 v85, v38, v39\n"
    "v_mfma_f32_16x16x32_bf16 v[56:59], v[108:111], a[184:187], v[56:59]\n"
    "v_cvt_pk_bf16_f32 v88, v40, v41\n"
    "v_cvt_pk_bf16_f32 v89, v42, v43\n"
    "v_mfma_f32_16x16x32_bf16 v[60:63], v[104:107], a[172:175], v[60:63]\n"
    "v_cvt_pk_bf16_f32 v92, v44, v45\n"
    "v_cvt_pk_bf16_f32 v93, v46, v47\n"
    "v_mfma_f32_16x16x32_bf16 v[60:63], v[108:111], a[188:191], v[60:63]\n"
    "s_waitcnt lgkmcnt(2)\n"
    "s_cmp_lg_u32 s88, 0\n"
    "s_cbranch_scc1 .LL_CHUNK3_%=\n"
    "s_branch .LL_TAIL_%=\n"
    ".LL_CSRETRY3_%=:\n"
    "s_mov_b32 s58, 0\n"
    ".LL_CSR3_%=:\n"
    "s_sleep 1\n"
    "buffer_load_dwordx2 v[120:121], v16, s[44:47], s60 offen sc1\n"
    "s_waitcnt vmcnt(0)\n"
    "v_cmp_ne_u32 vcc, s56, v121\n"
    "v_mov_b32 v124, v120\n"
    "s_add_u32 s58, s58, 1\n"
    "s_cmp_gt_u32 s58, 32768\n"
    "s_cbranch_scc1 .LL_CSOK3_%=\n"
    "s_cbranch_vccnz .LL_CSR3_%=\n"
    "s_branch .LL_CSOK3_%=\n"
    ".LL_TAIL_%=:\n"
    "s_add_u32 s30, s98, 196608\n"
    "buffer_load_dwordx2 v[180:181], v210, s[44:47], s30 offen sc1\n"
    "s_add_u32 s30, s98, 296448\n"
    "buffer_load_dwordx2 v[182:183], v211, s[44:47], s30 offen sc1\n"
    "v_cvt_pk_bf16_f32 v82, v48, v49\n"
    "v_cvt_pk_bf16_f32 v83, v50, v51\n"
    "v_cvt_pk_bf16_f32 v86, v52, v53\n"
    "v_cvt_pk_bf16_f32 v87, v54, v55\n"
    "v_cvt_pk_bf16_f32 v90, v56, v57\n"
    "v_cvt_pk_bf16_f32 v91, v58, v59\n"
    "v_cvt_pk_bf16_f32 v94, v60, v61\n"
    "v_cvt_pk_bf16_f32 v95, v62, v63\n"
    "buffer_store_dwordx4 v[80:83], v6, s[44:47], s95 offen sc1\n"
    "buffer_store_dwordx4 v[84:87], v7, s[44:47], s95 offen sc1\n"
    "buffer_store_dwordx4 v[88:91], v8, s[44:47], s95 offen sc1\n"
    "buffer_store_dwordx4 v[92:95], v9, s[44:47], s95 offen sc1\n"
    "s_waitcnt vmcnt(8) lgkmcnt(0)\n"
    "s_barrier\n"
    "buffer_store_dwordx4 v[164:167], v168, s[44:47], s58 offen sc1\n"
    "v_mfma_f32_16x16x32_bf16 v[184:187], v[172:175], v[188:191], 0\n"
    "v_mfma_f32_16x16x32_bf16 v[184:187], v[172:175], v[192:195], v[184:187]\n"
    "v_mfma_f32_16x16x32_bf16 v[184:187], v[172:175], v[196:199], v[184:187]\n"
    "v_dot2_f32_bf16 v152, v112, v132, 0\n"
    "v_dot2_f32_bf16 v153, v113, v133, 0\n"
    "v_dot2_f32_bf16 v152, v114, v134, v152\n"
    "v_dot2_f32_bf16 v153, v115, v135, v153\n"
    "v_dot2_f32_bf16 v152, v116, v136, v152\n"
    "v_dot2_f32_bf16 v153, v117, v137, v153\n"
    "v_dot2_f32_bf16 v152, v118, v138, v152\n"
    "v_dot2_f32_bf16 v153, v119, v139, v153\n"
    "v_dot2_f32_bf16 v152, v120, v140, v152\n"
    "v_dot2_f32_bf16 v153, v121, v141, v153\n"
    "v_dot2_f32_bf16 v152, v122, v142, v152\n"
    "v_dot2_f32_bf16 v153, v123, v143, v153\n"
    "v_dot2_f32_bf16 v152, v124, v144, v152\n"
    "v_dot2_f32_bf16 v153, v125, v145, v153\n"
    "v_dot2_f32_bf16 v152, v126, v146, v152\n"
    "v_dot2_f32_bf16 v153, v127, v147, v153\n"
    "v_dot2_f32_bf16 v152, v128, v148, v152\n"
    "v_dot2_f32_bf16 v153, v129, v149, v153\n"
    "v_dot2_f32_bf16 v152, v130, v150, v152\n"
    "v_dot2_f32_bf16 v153, v131, v151, v153\n"
    "s_mov_b32 s60, 0xffff0000\n"
    "s_nop 2\n"
    "v_add_f32 v152, v152, v153\n"
    "v_lshlrev_b32 v32, 16, v200\n"
    "v_and_b32 v33, s60, v200\n"
    "v_add_f32_dpp v152, v152, v152 quad_perm:[1,0,3,2] row_mask:0xf bank_mask:0xf\n"
    "v_lshlrev_b32 v34, 16, v201\n"
    "v_and_b32 v35, s60, v201\n"
    "v_add_f32_dpp v152, v152, v152 quad_perm:[2,3,0,1] row_mask:0xf bank_mask:0xf\n"
    "v_lshlrev_b32 v36, 16, v202\n"
    "v_mov_b32 v153, v152\n"
    "v_and_b32 v37, s60, v202\n"
    "v_lshlrev_b32 v38, 16, v203\n"
    "v_permlane16_swap_b32 v152, v153\n"
    "v_and_b32 v39, s60, v203\n"
    "v_add_f32 v152, v152, v153\n"
    "v_lshlrev_b32 v40, 16, v204\n"
    "v_mov_b32 v153, v152\n"
    "v_and_b32 v41, s60, v204\n"
    "v_lshlrev_b32 v42, 16, v205\n"
    "v_permlane32_swap_b32 v152, v153\n"
    "v_and_b32 v43, s60, v205\n"
    "v_add_f32 v152, v152, v153\n"
    "v_lshlrev_b32 v44, 16, v206\n"
    "v_mul_f32 v152, 0x3fb8aa3b, v152\n"
    "v_and_b32 v45, s60, v206\n"
    "v_exp_f32 v153, v152\n"
    "v_exp_f32 v152, -v152\n"
    "v_lshlrev_b32 v46, 16, v207\n"
    "v_add_f32 v153, 1.0, v153\n"
    "v_add_f32 v152, 1.0, v152\n"
    "v_and_b32 v47, s60, v207\n"
    "v_rcp_f32 v154, v153\n"
    "v_rcp_f32 v152, v152\n"
    "v_lshlrev_b32 v48, 16, v208\n"
    "v_and_b32 v49, s60, v208\n"
    "v_lshlrev_b32 v50, 16, v209\n"
    "v_and_b32 v51, s60, v209\n"
    "v_mul_f32 v153, 0x40088e99, v154\n"
    "v_mul_f32 v32, v32, v154\n"
    "v_mul_f32 v33, v33, v154\n"
    "v_mul_f32 v34, v34, v154\n"
    "v_mul_f32 v35, v35, v154\n"
    "v_mul_f32 v36, v36, v154\n"
    "v_mul_f32 v37, v37, v154\n"
    "v_mul_f32 v38, v38, v154\n"
    "v_mul_f32 v39, v39, v154\n"
    "v_mul_f32 v40, v40, v154\n"
    "v_mul_f32 v41, v41, v154\n"
    "v_mul_f32 v42, v42, v154\n"
    "v_mul_f32 v43, v43, v154\n"
    "v_mul_f32 v44, v44, v154\n"
    "v_mul_f32 v45, v45, v154\n"
    "v_mul_f32 v46, v46, v154\n"
    "v_mul_f32 v47, v47, v154\n"
    "v_mul_f32 v48, v48, v154\n"
    "v_mul_f32 v49, v49, v154\n"
    "v_mul_f32 v50, v50, v154\n"
    "v_mul_f32 v51, v51, v154\n"
    "ds_write_b128 v170, v[184:187]\n"
    "s_waitcnt vmcnt(5)\n"
    "v_cmp_ne_u64 vcc, v[164:165], v[180:181]\n"
    "v_cmp_ne_u64 s[62:63], v[164:165], v[182:183]\n"
    "s_or_b64 vcc, vcc, s[62:63]\n"
    "s_cbranch_vccnz .LL_SLOW1_%=\n"
    "s_add_u32 s29, s99, 98304\n"
    "buffer_load_dwordx4 v[52:55], v212, s[44:47], s29 offen sc1\n"
    "buffer_load_dwordx4 v[56:59], v213, s[44:47], s29 offen sc1\n"
    "buffer_load_dwordx4 v[60:63], v214, s[44:47], s29 offen sc1\n"
    "buffer_load_dwordx4 v[216:219], v215, s[44:47], s99 offen sc1\n"
    "s_add_u32 s29, s99, 16384\n"
    "buffer_load_dwordx4 v[220:223], v215, s[44:47], s29 offen sc1\n"
    "s_add_u32 s29, s99, 32768\n"
    "buffer_load_dwordx4 v[224:227], v215, s[44:47], s29 offen sc1\n"
    "s_add_u32 s29, s99, 49152\n"
    "buffer_load_dwordx4 v[228:231], v215, s[44:47], s29 offen sc1\n"
    "s_add_u32 s29, s99, 65536\n"
    "buffer_load_dwordx4 v[232:235], v215, s[44:47], s29 offen sc1\n"
    "s_add_u32 s29, s99, 81920\n"
    "buffer_load_dwordx4 v[236:239], v215, s[44:47], s29 offen sc1\n"
    "s_add_u32 s29, s99, 98304\n"
    "buffer_load_dwordx4 v[240:243], v215, s[44:47], s29 offen sc1\n"
    "s_add_u32 s29, s99, 114688\n"
    "buffer_load_dwordx4 v[244:247], v215, s[44:47], s29 offen sc1\n"
    "s_add_u32 s29, s99, 131072\n"
    "buffer_load_dwordx4 v[248:251], v215, s[44:47], s29 offen sc1\n"
    "s_waitcnt vmcnt(13)\n"
    "s_barrier\n"
    "buffer_store_dwordx4 v[164:167], v168, s[44:47], s97 offen sc1\n"
    ".LL_JOIN_%=:\n"
    "s_add_u32 s30, s98, 229376\n"
    "buffer_load_dwordx2 v[180:181], v210, s[44:47], s30 offen sc1\n"
    "s_mul_i32 s61, s79, 147456\n"
    "s_mov_b64 s[40:41], %10\n"
    "s_add_u32 s40, s40, s61\n"
    "s_addc_u32 s41, s41, 0\n"
    "s_mov_b32 s42, 147456\n"
    "s_mov_b32 s43, 0x20000\n"
    "v_subrev_u32 v170, 2560, v170\n"
    "v_cmp_gt_u32 vcc, 32, v1\n"
    "v_mov_b32 v2, 1879048192\n"
    "ds_read_b128 v[116:119], v170 offset:0\n"
    "ds_read_b128 v[120:123], v170 offset:1024\n"
    "ds_read_b128 v[124:127], v170 offset:2048\n"
    "v_cndmask_b32 v2, v2, v171, vcc\n"
    "s_waitcnt vmcnt(0)\n"
    "s_mov_b32 s28, 0\n"
    "s_branch .LL_SPB2_%=\n"
    ".LL_SPA2_%=:\n"
    "s_sleep 1\n"
    "s_add_u32 s30, s98, 229376\n"
    "buffer_load_dwordx2 v[180:181], v210, s[44:47], s30 offen sc1\n"
    "s_waitcnt vmcnt(0)\n"
    ".LL_SPB2_%=:\n"
    "v_cmp_ne_u64 vcc, v[164:165], v[180:181]\n"
    "s_add_u32 s28, s28, 1\n"
    "s_cmp_gt_u32 s28, 32768\n"
    "s_cbranch_scc1 .LL_SPC2_%=\n"
    "s_cbranch_vccnz .LL_SPA2_%=\n"
    ".LL_SPC2_%=:\n"
    "s_add_u32 s29, s99, 114688\n"
    "buffer_load_dwordx4 v[64:67], v212, s[44:47], s29 offen sc1\n"
    "buffer_load_dwordx4 v[68:71], v213, s[44:47], s29 offen sc1\n"
    "buffer_load_dwordx4 v[72:75], v214, s[44:47], s29 offen sc1\n"
    "s_add_u32 s30, s98, 262144\n"
    "buffer_load_dwordx2 v[180:181], v210, s[44:47], s30 offen sc1\n"
    "v_mfma_f32_16x16x32_bf16 v[96:99], v[172:175], v[216:219], 0\n"
    "v_mfma_f32_16x16x32_bf16 v[96:99], v[176:179], v[220:223], v[96:99]\n"
    "v_mfma_f32_16x16x32_bf16 v[100:103], v[172:175], v[224:227], 0\n"
    "v_mfma_f32_16x16x32_bf16 v[100:103], v[176:179], v[228:231], v[100:103]\n"
    "v_mfma_f32_16x16x32_bf16 v[104:107], v[172:175], v[232:235], 0\n"
    "v_mfma_f32_16x16x32_bf16 v[104:107], v[176:179], v[236:239], v[104:107]\n"
    "v_mfma_f32_16x16x32_bf16 v[108:111], v[172:175], v[240:243], 0\n"
    "v_mfma_f32_16x16x32_bf16 v[108:111], v[176:179], v[244:247], v[108:111]\n"
    "v_mfma_f32_16x16x32_bf16 v[112:115], v[172:175], v[248:251], 0\n"
    "v_mfma_f32_16x16x32_bf16 v[128:131], v[172:175], v[52:55], 0\n"
    "v_mfma_f32_16x16x32_bf16 v[128:131], v[172:175], v[56:59], v[128:131]\n"
    "v_mfma_f32_16x16x32_bf16 v[128:131], v[172:175], v[60:63], v[128:131]\n"
    "s_waitcnt lgkmcnt(0)\n"
    "v_fmac_f32 v32, v152, v96\n"
    "v_fmac_f32 v33, v152, v97\n"
    "v_fmac_f32 v34, v152, v98\n"
    "v_fmac_f32 v35, v152, v99\n"
    "v_fmac_f32 v32, v153, v116\n"
    "v_fmac_f32 v33, v153, v117\n"
    "v_fmac_f32 v34, v153, v118\n"
    "v_fmac_f32 v35, v153, v119\n"
    "v_cvt_pk_bf16_f32 v132, v32, v33\n"
    "v_cvt_pk_bf16_f32 v133, v34, v35\n"
    "v_fmac_f32 v36, v152, v100\n"
    "v_fmac_f32 v37, v152, v101\n"
    "v_fmac_f32 v38, v152, v102\n"
    "v_fmac_f32 v39, v152, v103\n"
    "v_fmac_f32 v36, v153, v120\n"
    "v_fmac_f32 v37, v153, v121\n"
    "v_fmac_f32 v38, v153, v122\n"
    "v_fmac_f32 v39, v153, v123\n"
    "v_cvt_pk_bf16_f32 v134, v36, v37\n"
    "v_cvt_pk_bf16_f32 v135, v38, v39\n"
    "v_fmac_f32 v40, v152, v104\n"
    "v_fmac_f32 v41, v152, v105\n"
    "v_fmac_f32 v42, v152, v106\n"
    "v_fmac_f32 v43, v152, v107\n"
    "v_fmac_f32 v40, v153, v124\n"
    "v_fmac_f32 v41, v153, v125\n"
    "v_fmac_f32 v42, v153, v126\n"
    "v_fmac_f32 v43, v153, v127\n"
    "v_cvt_pk_bf16_f32 v136, v40, v41\n"
    "v_cvt_pk_bf16_f32 v137, v42, v43\n"
    "s_waitcnt vmcnt(0)\n"
    "s_mov_b32 s28, 0\n"
    "s_branch .LL_SPB3_%=\n"
    ".LL_SPA3_%=:\n"
    "s_sleep 1\n"
    "s_add_u32 s30, s98, 262144\n"
    "buffer_load_dwordx2 v[180:181], v210, s[44:47], s30 offen sc1\n"
    "s_waitcnt vmcnt(0)\n"
    ".LL_SPB3_%=:\n"
    "v_cmp_ne_u64 vcc, v[164:165], v[180:181]\n"
    "s_add_u32 s28, s28, 1\n"
    "s_cmp_gt_u32 s28, 32768\n"
    "s_cbranch_scc1 .LL_SPC3_%=\n"
    "s_cbranch_vccnz .LL_SPA3_%=\n"
    ".LL_SPC3_%=:\n"
    "s_add_u32 s29, s99, 131072\n"
    "buffer_load_dwordx4 v[188:191], v212, s[44:47], s29 offen sc1\n"
    "buffer_load_dwordx4 v[192:195], v213, s[44:47], s29 offen sc1\n"
    "buffer_load_dwordx4 v[196:199], v214, s[44:47], s29 offen sc1\n"
    "v_mfma_f32_16x16x32_bf16 v[128:131], v[176:179], v[64:67], v[128:131]\n"
    "v_mfma_f32_16x16x32_bf16 v[128:131], v[176:179], v[68:71], v[128:131]\n"
    "v_mfma_f32_16x16x32_bf16 v[128:131], v[176:179], v[72:75], v[128:131]\n"
    "buffer_store_dwordx2 v[132:133], v171, s[40:43], 0 offen offset:0 sc1\n"
    "buffer_store_dwordx2 v[134:135], v171, s[40:43], 0 offen offset:256 sc1\n"
    "buffer_store_dwordx2 v[136:137], v171, s[40:43], 0 offen offset:512 sc1\n"
    "s_nop 4\n"
    "v_fmac_f32 v44, v152, v108\n"
    "v_fmac_f32 v45, v152, v109\n"
    "v_fmac_f32 v46, v152, v110\n"
    "v_fmac_f32 v47, v152, v111\n"
    "v_fmac_f32 v44, v153, v128\n"
    "v_fmac_f32 v45, v153, v129\n"
    "v_fmac_f32 v46, v153, v130\n"
    "v_fmac_f32 v47, v153, v131\n"
    "v_cvt_pk_bf16_f32 v138, v44, v45\n"
    "v_cvt_pk_bf16_f32 v139, v46, v47\n"
    "buffer_store_dwordx2 v[138:139], v171, s[40:43], 0 offen offset:768 sc1\n"
    "v_fmac_f32 v48, v152, v112\n"
    "v_fmac_f32 v49, v152, v113\n"
    "v_fmac_f32 v50, v152, v114\n"
    "v_fmac_f32 v51, v152, v115\n"
    "s_waitcnt vmcnt(4)\n"
    "v_mfma_f32_16x16x32_bf16 v[184:187], v[172:175], v[188:191], 0\n"
    "v_mfma_f32_16x16x32_bf16 v[184:187], v[172:175], v[192:195], v[184:187]\n"
    "v_mfma_f32_16x16x32_bf16 v[184:187], v[172:175], v[196:199], v[184:187]\n"
    "s_nop 7\n"
    "v_fmac_f32 v48, v153, v184\n"
    "v_fmac_f32 v49, v153, v185\n"
    "v_fmac_f32 v50, v153, v186\n"
    "v_fmac_f32 v51, v153, v187\n"
    "v_cvt_pk_bf16_f32 v140, v48, v49\n"
    "v_cvt_pk_bf16_f32 v141, v50, v51\n"
    "buffer_store_dwordx2 v[140:141], v2, s[40:43], 0 offen offset:1024 sc1\n"
    "s_endpgm\n"
    ".LL_SLOW1_%=:\n"
    "s_waitcnt vmcnt(0)\n"
    "s_barrier\n"
    "buffer_store_dwordx4 v[164:167], v168, s[44:47], s97 offen sc1\n"
    ".LL_SLOW2_%=:\n"
    "s_mov_b32 s28, 0\n"
    ".LL_SPA4_%=:\n"
    "s_sleep 1\n"
    "s_add_u32 s30, s98, 196608\n"
    "buffer_load_dwordx2 v[180:181], v210, s[44:47], s30 offen sc1\n"
    "s_waitcnt vmcnt(0)\n"
    ".LL_SPB4_%=:\n"
    "v_cmp_ne_u64 vcc, v[164:165], v[180:181]\n"
    "s_add_u32 s28, s28, 1\n"
    "s_cmp_gt_u32 s28, 32768\n"
    "s_cbranch_scc1 .LL_SPC4_%=\n"
    "s_cbranch_vccnz .LL_SPA4_%=\n"
    ".LL_SPC4_%=:\n"
    "s_mov_b32 s28, 0\n"
    ".LL_SPA5_%=:\n"
    "s_sleep 1\n"
    "s_add_u32 s30, s98, 296448\n"
    "buffer_load_dwordx2 v[182:183], v211, s[44:47], s30 offen sc1\n"
    "s_waitcnt vmcnt(0)\n"
    ".LL_SPB5_%=:\n"
    "v_cmp_ne_u64 vcc, v[164:165], v[182:183]\n"
    "s_add_u32 s28, s28, 1\n"
    "s_cmp_gt_u32 s28, 32768\n"
    "s_cbranch_scc1 .LL_SPC5_%=\n"
    "s_cbranch_vccnz .LL_SPA5_%=\n"
    ".LL_SPC5_%=:\n"
    "s_add_u32 s29, s99, 98304\n"
    "buffer_load_dwordx4 v[52:55], v212, s[44:47], s29 offen sc1\n"
    "buffer_load_dwordx4 v[56:59], v213, s[44:47], s29 offen sc1\n"
    "buffer_load_dwordx4 v[60:63], v214, s[44:47], s29 offen sc1\n"
    "buffer_load_dwordx4 v[216:219], v215, s[44:47], s99 offen sc1\n"
    "s_add_u32 s29, s99, 16384\n"
    "buffer_load_dwordx4 v[220:223], v215, s[44:47], s29 offen sc1\n"
    "s_add_u32 s29, s99, 32768\n"
    "buffer_load_dwordx4 v[224:227], v215, s[44:47], s29 offen sc1\n"
    "s_add_u32 s29, s99, 49152\n"
    "buffer_load_dwordx4 v[228:231], v215, s[44:47], s29 offen sc1\n"
    "s_add_u32 s29, s99, 65536\n"
    "buffer_load_dwordx4 v[232:235], v215, s[44:47], s29 offen sc1\n"
    "s_add_u32 s29, s99, 81920\n"
    "buffer_load_dwordx4 v[236:239], v215, s[44:47], s29 offen sc1\n"
    "s_add_u32 s29, s99, 98304\n"
    "buffer_load_dwordx4 v[240:243], v215, s[44:47], s29 offen sc1\n"
    "s_add_u32 s29, s99, 114688\n"
    "buffer_load_dwordx4 v[244:247], v215, s[44:47], s29 offen sc1\n"
    "s_add_u32 s29, s99, 131072\n"
    "buffer_load_dwordx4 v[248:251], v215, s[44:47], s29 offen sc1\n"
    "s_branch .LL_JOIN_%=\n"
    ".LL_ATT_%=:\n"
    "s_add_u32 m0, s90, 16384\n"
    "s_nop 0\n"
    "buffer_load_dwordx4 v6, s[40:43], s94 offen lds\n"
    "buffer_load_dwordx4 v7, s[40:43], s94 offen offset:1024 lds\n"
    "buffer_load_dwordx4 v8, s[40:43], s94 offen offset:2048 lds\n"
    "buffer_load_dwordx4 v9, s[40:43], s94 offen offset:3072 lds\n"
    "s_add_u32 s94, s94, 128\n"
    "s_cmp_eq_u32 s94, 1152\n"
    "s_cselect_b32 s94, 0, s94\n"
    "s_mov_b32 s84, s60\n"
    "s_add_u32 s83, s60, 12\n"
    "s_mul_i32 s61, s84, 288\n"
    "s_sub_u32 s60, 1990656, s61\n"
    "s_mov_b64 s[36:37], %3\n"
    "s_add_u32 s36, s36, s61\n"
    "s_addc_u32 s37, s37, 0\n"
    "s_mov_b32 s38, s60\n"
    "s_mov_b32 s39, 0x20000\n"
    "s_mul_i32 s64, s88, 221184\n"
    "s_mul_i32 s89, s80, 5120\n"
    "s_mul_i32 s60, s80, 55296\n"
    "s_movk_i32 s61, 3641\n"
    "v_add_u32 v128, 0, v1\n"
    "v_mul_u32_u24 v129, s61, v128\n"
    "v_lshrrev_b32 v129, 16, v129\n"
    "v_mul_u32_u24 v130, 18, v129\n"
    "v_sub_u32 v128, v128, v130\n"
    "v_and_b32 v130, 3, v129\n"
    "v_bfe_u32 v10, v129, 2, 1\n"
    "v_lshl_add_u32 v130, v10, 3, v130\n"
    "v_bfe_u32 v10, v129, 3, 1\n"
    "v_lshl_add_u32 v130, v10, 2, v130\n"
    "v_mul_u32_u24 v130, 3456, v130\n"
    "v_lshl_add_u32 v128, v128, 4, v130\n"
    "s_add_u32 s30, s60, 0\n"
    "v_add_u32 v10, s30, v128\n"
    "v_add_u32 v128, 64, v1\n"
    "v_mul_u32_u24 v129, s61, v128\n"
    "v_lshrrev_b32 v129, 16, v129\n"
    "v_mul_u32_u24 v130, 18, v129\n"
    "v_sub_u32 v128, v128, v130\n"
    "v_and_b32 v130, 3, v129\n"
    "v_bfe_u32 v11, v129, 2, 1\n"
    "v_lshl_add_u32 v130, v11, 3, v130\n"
    "v_bfe_u32 v11, v129, 3, 1\n"
    "v_lshl_add_u32 v130, v11, 2, v130\n"
    "v_mul_u32_u24 v130, 3456, v130\n"
    "v_lshl_add_u32 v128, v128, 4, v130\n"
    "s_add_u32 s30, s60, 4294966272\n"
    "v_add_u32 v11, s30, v128\n"
    "v_add_u32 v128, 128, v1\n"
    "v_mul_u32_u24 v129, s61, v128\n"
    "v_lshrrev_b32 v129, 16, v129\n"
    "v_mul_u32_u24 v130, 18, v129\n"
    "v_sub_u32 v128, v128, v130\n"
    "v_and_b32 v130, 3, v129\n"
    "v_bfe_u32 v12, v129, 2, 1\n"
    "v_lshl_add_u32 v130, v12, 3, v130\n"
    "v_bfe_u32 v12, v129, 3, 1\n"
    "v_lshl_add_u32 v130, v12, 2, v130\n"
    "v_mul_u32_u24 v130, 3456, v130\n"
    "v_lshl_add_u32 v128, v128, 4, v130\n"
    "s_add_u32 s30, s60, 4294965248\n"
    "v_add_u32 v12, s30, v128\n"
    "v_add_u32 v128, 192, v1\n"
    "v_mul_u32_u24 v129, s61, v128\n"
    "v_lshrrev_b32 v129, 16, v129\n"
    "v_mul_u32_u24 v130, 18, v129\n"
    "v_sub_u32 v128, v128, v130\n"
    "v_and_b32 v130, 3, v129\n"
    "v_bfe_u32 v13, v129, 2, 1\n"
    "v_lshl_add_u32 v130, v13, 3, v130\n"
    "v_bfe_u32 v13, v129, 3, 1\n"
    "v_lshl_add_u32 v130, v13, 2, v130\n"
    "v_mul_u32_u24 v130, 3456, v130\n"
    "v_lshl_add_u32 v128, v128, 4, v130\n"
    "s_add_u32 s30, s60, 4294964224\n"
    "v_add_u32 v13, s30, v128\n"
    "v_add_u32 v128, 256, v1\n"
    "v_mul_u32_u24 v129, s61, v128\n"
    "v_lshrrev_b32 v129, 16, v129\n"
    "v_mul_u32_u24 v130, 18, v129\n"
    "v_sub_u32 v128, v128, v130\n"
    "v_and_b32 v130, 3, v129\n"
    "v_bfe_u32 v14, v129, 2, 1\n"
    "v_lshl_add_u32 v130, v14, 3, v130\n"
    "v_bfe_u32 v14, v129, 3, 1\n"
    "v_lshl_add_u32 v130, v14, 2, v130\n"
    "v_mul_u32_u24 v130, 3456, v130\n"
    "v_lshl_add_u32 v128, v128, 4, v130\n"
    "s_add_u32 s30, s60, 0\n"
    "v_add_u32 v14, s30, v128\n"
    "v_cmp_gt_u32 vcc, 32, v1\n"
    "v_mov_b32 v128, 1879048192\n"
    "s_nop 0\n"
    "v_cndmask_b32 v14, v128, v14, vcc\n"
    "s_add_u32 s85, s64, 1152\n"
    "s_add_u32 m0, s89, 0\n"
    "s_nop 0\n"
    "buffer_load_dwordx4 v10, s[36:39], s85 offen lds\n"
    "buffer_load_dwordx4 v11, s[36:39], s85 offen offset:1024 lds\n"
    "buffer_load_dwordx4 v12, s[36:39], s85 offen offset:2048 lds\n"
    "buffer_load_dwordx4 v13, s[36:39], s85 offen offset:3072 lds\n"
    "s_add_u32 m0, s89, 4096\n"
    "s_nop 0\n"
    "buffer_load_dwordx4 v14, s[36:39], s85 offen lds\n"
    "s_getpc_b64 s[28:29]\n"
    ".LL_PCREF1_%=:\n"
    "s_sub_u32 s28, s28, .LL_PCREF1_%=-.LL_START_%=\n"
    "s_subb_u32 s29, s29, 0\n"
    "s_lshr_b32 s30, %1, 3\n"
    "s_lshl_b32 s30, s30, 2\n"
    "s_add_u32 s30, s30, s80\n"
    "s_mul_i32 s30, s30, 5\n"
    "s_mov_b32 s31, .LL_CODEEND_%=-.LL_START_%=-4\n"
    "v_min_u32 v31, 4, v1\n"
    "v_add_lshl_u32 v31, v31, s30, 7\n"
    "v_min_u32 v31, s31, v31\n"
    "global_load_dword v31, v31, s[28:29]\n"
    "s_mov_b64 s[44:45], %11\n"
    "s_mov_b32 s46, 50331648\n"
    "s_mov_b32 s47, 0x20000\n"
    "s_add_u32 s65, s64, 221184\n"
    "s_cmp_eq_u32 s65, 1990656\n"
    "s_cselect_b32 s65, 0, s65\n"
    "s_add_u32 s66, s65, 221184\n"
    "s_cmp_eq_u32 s66, 1990656\n"
    "s_cselect_b32 s66, 0, s66\n"
    "s_add_u32 s67, s66, 221184\n"
    "s_cmp_eq_u32 s67, 1990656\n"
    "s_cselect_b32 s67, 0, s67\n"
    "s_add_u32 s68, s67, 221184\n"
    "s_cmp_eq_u32 s68, 1990656\n"
    "s_cselect_b32 s68, 0, s68\n"
    "s_add_u32 s69, s68, 221184\n"
    "s_cmp_eq_u32 s69, 1990656\n"
    "s_cselect_b32 s69, 0, s69\n"
    "s_add_u32 s70, s69, 221184\n"
    "s_cmp_eq_u32 s70, 1990656\n"
    "s_cselect_b32 s70, 0, s70\n"
    "s_add_u32 s71, s70, 221184\n"
    "s_cmp_eq_u32 s71, 1990656\n"
    "s_cselect_b32 s71, 0, s71\n"
    "s_add_u32 s72, s71, 221184\n"
    "s_cmp_eq_u32 s72, 1990656\n"
    "s_cselect_b32 s72, 0, s72\n"
    "s_mul_i32 s61, s84, 165888\n"
    "s_mov_b64 s[52:53], %4\n"
    "s_add_u32 s52, s52, s61\n"
    "s_addc_u32 s53, s53, 0\n"
    "s_mov_b32 s54, 165888\n"
    "s_mov_b32 s55, 0x20000\n"
    "s_mul_i32 s60, s80, 40\n"
    "v_add_u32 v128, s60, v2\n"
    "v_mul_u32_u24 v128, 1152, v128\n"
    "v_lshlrev_b32 v5, 1, v3\n"
    "v_xor_b32 v5, v5, v4\n"
    "v_lshl_add_u32 v15, v5, 4, v128\n"
    "v_add_u32 v16, 8192, v15\n"
    "v_add_u32 v17, 16384, v15\n"
    "v_add_u32 v18, 24576, v15\n"
    "v_add_u32 v19, 36864, v15\n"
    "v_bfe_u32 v2, v1, 2, 3\n"
    "v_mul_u32_u24 v20, 288, v2\n"
    "v_lshrrev_b32 v4, 5, v1\n"
    "v_mul_u32_u24 v4, 5120, v4\n"
    "v_and_b32 v5, 3, v1\n"
    "v_lshl_add_u32 v4, v5, 3, v4\n"
    "v_add_u32 v20, v20, v4\n"
    "v_add_u32 v21, 61440, v20\n"
    "v_and_b32 v4, 15, v1\n"
    "v_lshrrev_b32 v5, 1, v4\n"
    "v_xor_b32 v5, v5, v3\n"
    "v_lshlrev_b32 v5, 4, v5\n"
    "v_lshl_add_u32 v5, v4, 7, v5\n"
    "v_add_u32 v22, s90, v5\n"
    "v_xor_b32 v23, 64, v22\n"
    "v_add_u32 v24, 0x10000, v22\n"
    "v_add_u32 v25, 0x10000, v23\n"
    "v_lshlrev_b32 v154, 4, v1\n"
    "v_lshlrev_b32 v155, 3, v1\n"
    "v_lshlrev_b32 v29, 11, v4\n"
    "s_mul_i32 s60, s80, 9216\n"
    "s_add_u32 s60, s60, 81920\n"
    "v_add_u32 v26, s60, v154\n"
    "s_lshl_b32 s60, s80, 11\n"
    "s_add_u32 s60, s60, 81920\n"
    "v_add_u32 v27, s60, v154\n"
    "v_add_u32 v28, 35840, v27\n"
    "s_mov_b32 s87, 0x358637bd\n"
    "s_waitcnt lgkmcnt(0)\n"
    "s_add_u32 s56, s56, 1\n"
    "s_addc_u32 s57, s57, 0\n"
    "v_mov_b32 v136, s87\n"
    "v_mov_b32 v140, 0x3f803f80\n"
    "v_mov_b32 v164, s56\n"
    "v_mov_b32 v137, s87\n"
    "v_mov_b32 v141, 0x3f803f80\n"
    "v_mov_b32 v165, s57\n"
    "v_mov_b32 v138, s87\n"
    "v_mov_b32 v142, 0x3f803f80\n"
    "v_mov_b32 v166, s56\n"
    "v_mov_b32 v139, s87\n"
    "v_mov_b32 v143, 0x3f803f80\n"
    "v_mov_b32 v167, s57\n"
    "s_lshl_b32 s30, s80, 4\n"
    "s_add_u32 s30, s30, s79\n"
    "s_mul_i32 s81, s84, 36\n"
    "s_add_u32 s81, s81, s30\n"
    "s_lshl_b32 s81, s81, 9\n"
    "s_add_u32 s81, s81, 655360\n"
    "s_cmp_eq_u32 s30, 0\n"
    "s_cselect_b32 s31, 19, s30\n"
    "s_cselect_b32 s58, 4, 0\n"
    "s_lshr_b32 s28, s31, 2\n"
    "s_and_b32 s29, s31, 3\n"
    "s_mul_i32 s92, s84, 589824\n"
    "s_add_u32 s92, s92, 1048576\n"
    "s_lshl_b32 s31, s31, 14\n"
    "s_add_u32 s93, s92, s31\n"
    "s_cmp_ge_u32 s30, 36\n"
    "s_cselect_b32 s93, 1879048192, s93\n"
    "s_cselect_b32 s81, 1879048192, s81\n"
    "s_cmp_ge_u32 s28, 5\n"
    "s_cselect_b32 s58, 4, s58\n"
    "s_add_u32 s31, s28, s29\n"
    "s_sub_u32 s31, s31, 1\n"
    "s_lshl_b32 s31, s31, 4\n"
    "s_movk_i32 s59, 3641\n"
    "v_add_u32 v128, s31, v4\n"
    "v_mul_u32_u24 v128, s59, v128\n"
    "v_lshrrev_b32 v128, 16, v128\n"
    "v_and_b32 v129, 3, v4\n"
    "v_add_u32 v129, s58, v129\n"
    "s_cmp_eq_u32 s29, 3\n"
    "s_cselect_b64 vcc, -1, 0\n"
    "s_lshl_b32 s28, s28, 4\n"
    "v_cndmask_b32 v128, v128, v129, vcc\n"
    "v_lshl_add_u32 v129, v3, 2, s28\n"
    "v_add_u32 v130, 2, v129\n"
    "v_mul_u32_u24 v129, s59, v129\n"
    "v_mul_u32_u24 v130, s59, v130\n"
    "v_lshrrev_b32 v129, 16, v129\n"
    "v_lshrrev_b32 v130, 16, v130\n"
    "v_cmp_eq_u32 vcc, v128, v129\n"
    "v_cmp_eq_u32 s[62:63], v128, v130\n"
    "s_lshl_b32 s30, s79, 10\n"
    "v_cndmask_b32 v152, 0, -1, vcc\n"
    "v_cndmask_b32 v153, 0, -1, s[62:63]\n"
    "s_add_u32 s92, s92, s30\n"
    "s_lshl_b32 s97, s79, 11\n"
    "s_lshl_b32 s30, s83, 7\n"
    "s_add_u32 s98, s30, 524288\n"
    "s_add_u32 s97, s97, s30\n"
    "s_add_u32 s97, s97, 491520\n"
    "v_cmp_gt_u32 vcc, 8, %0\n"
    "v_mov_b32 v168, 1879048192\n"
    "s_mul_i32 s30, s80, 41\n"
    "v_cndmask_b32 v168, v168, v154, vcc\n"
    "v_min_u32 v156, 40, v1\n"
    "v_add_lshl_u32 v156, v156, s30, 7\n"
    "s_mul_i32 s99, s91, 20736\n"
    "s_lshl_b32 s31, %1, 7\n"
    "s_add_u32 s31, s31, 65536\n"
    "v_lshrrev_b32 v2, 3, %0\n"
    "v_and_b32 v3, 7, %0\n"
    "v_cmp_gt_u32 vcc, 24, %0\n"
    "v_lshlrev_b32 v2, 15, v2\n"
    "v_lshl_add_u32 v2, v3, 4, v2\n"
    "v_mov_b32 v192, 1879048192\n"
    "v_cndmask_b32 v192, v192, v2, vcc\n"
    "v_not_b32 v188, v164\n"
    "v_not_b32 v189, v164\n"
    "v_not_b32 v190, v164\n"
    "v_not_b32 v191, v164\n"
    "s_add_u32 s85, s65, 1152\n"
    "s_add_u32 m0, s89, 20480\n"
    "s_nop 0\n"
    "buffer_load_dwordx4 v10, s[36:39], s85 offen lds\n"
    "buffer_load_dwordx4 v11, s[36:39], s85 offen offset:1024 lds\n"
    "buffer_load_dwordx4 v12, s[36:39], s85 offen offset:2048 lds\n"
    "buffer_load_dwordx4 v13, s[36:39], s85 offen offset:3072 lds\n"
    "s_add_u32 m0, s89, 24576\n"
    "s_nop 0\n"
    "buffer_load_dwordx4 v14, s[36:39], s85 offen lds\n"
    "s_waitcnt vmcnt(6)\n"
    "s_barrier\n"
    "ds_read_b128 a[0:3], v22 offset:0\n"
    "ds_read_b128 a[4:7], v22 offset:2048\n"
    "ds_read_b128 a[8:11], v23 offset:0\n"
    "ds_read_b128 a[12:15], v23 offset:2048\n"
    "ds_read_b64_tr_b16 v[104:105], v20 offset:0\n"
    "ds_read_b64_tr_b16 v[106:107], v20 offset:2304\n"
    "ds_read_b64_tr_b16 v[108:109], v20 offset:10240\n"
    "ds_read_b64_tr_b16 v[110:111], v20 offset:12544\n"
    "ds_read_b64_tr_b16 v[112:113], v20 offset:32\n"
    "ds_read_b64_tr_b16 v[114:115], v20 offset:2336\n"
    "ds_read_b64_tr_b16 v[116:117], v20 offset:10272\n"
    "ds_read_b64_tr_b16 v[118:119], v20 offset:12576\n"
    "s_add_u32 m0, s90, 32768\n"
    "s_nop 0\n"
    "buffer_load_dwordx4 v6, s[40:43], s94 offen lds\n"
    "buffer_load_dwordx4 v7, s[40:43], s94 offen offset:1024 lds\n"
    "buffer_load_dwordx4 v8, s[40:43], s94 offen offset:2048 lds\n"
    "buffer_load_dwordx4 v9, s[40:43], s94 offen offset:3072 lds\n"
    "s_add_u32 s94, s94, 128\n"
    "s_cmp_eq_u32 s94, 1152\n"
    "s_cselect_b32 s94, 0, s94\n"
    "s_add_u32 s85, s66, 1152\n"
    "s_add_u32 m0, s89, 40960\n"
    "s_nop 0\n"
    "buffer_load_dwordx4 v10, s[36:39], s85 offen lds\n"
    "buffer_load_dwordx4 v11, s[36:39], s85 offen offset:1024 lds\n"
    "buffer_load_dwordx4 v12, s[36:39], s85 offen offset:2048 lds\n"
    "buffer_load_dwordx4 v13, s[36:39], s85 offen offset:3072 lds\n"
    "s_add_u32 m0, s89, 45056\n"
    "s_nop 0\n"
    "buffer_load_dwordx4 v14, s[36:39], s85 offen lds\n"
    "s_waitcnt lgkmcnt(4)\n"
    "v_mfma_f32_16x16x32_bf16 v[32:35], a[0:3], v[104:107], v[136:139]\n"
    "ds_read_b64_tr_b16 v[120:121], v20 offset:64\n"
    "v_mfma_f32_16x16x32_bf16 v[32:35], a[8:11], v[108:111], v[32:35]\n"
    "ds_read_b64_tr_b16 v[122:123], v20 offset:2368\n"
    "v_mfma_f32_16x16x32_bf16 v[36:39], a[4:7], v[104:107], v[136:139]\n"
    "ds_read_b64_tr_b16 v[124:125], v20 offset:10304\n"
    "v_mfma_f32_16x16x32_bf16 v[36:39], a[12:15], v[108:111], v[36:39]\n"
    "s_waitcnt lgkmcnt(3)\n"
    "ds_read_b64_tr_b16 v[126:127], v20 offset:12608\n"
    "v_mfma_f32_16x16x32_bf16 v[40:43], a[0:3], v[112:115], v[136:139]\n"
    "ds_read_b64_tr_b16 v[104:105], v20 offset:96\n"
    "v_mfma_f32_16x16x32_bf16 v[40:43], a[8:11], v[116:119], v[40:43]\n"
    "ds_read_b64_tr_b16 v[106:107], v20 offset:2400\n"
    "v_mfma_f32_16x16x32_bf16 v[44:47], a[4:7], v[112:115], v[136:139]\n"
    "ds_read_b64_tr_b16 v[108:109], v20 offset:10336\n"
    "v_mfma_f32_16x16x32_bf16 v[44:47], a[12:15], v[116:119], v[44:47]\n"
    "s_waitcnt lgkmcnt(3)\n"
    "ds_read_b64_tr_b16 v[110:111], v20 offset:12640\n"
    "v_mfma_f32_16x16x32_bf16 v[48:51], a[0:3], v[120:123], v[136:139]\n"
    "ds_read_b64_tr_b16 v[112:113], v20 offset:128\n"
    "v_mfma_f32_16x16x32_bf16 v[48:51], a[8:11], v[124:127], v[48:51]\n"
    "ds_read_b64_tr_b16 v[114:115], v20 offset:2432\n"
    "v_mfma_f32_16x16x32_bf16 v[52:55], a[4:7], v[120:123], v[136:139]\n"
    "ds_read_b64_tr_b16 v[116:117], v20 offset:10368\n"
    "v_mfma_f32_16x16x32_bf16 v[52:55], a[12:15], v[124:127], v[52:55]\n"
    "s_waitcnt lgkmcnt(3)\n"
    "ds_read_b64_tr_b16 v[118:119], v20 offset:12672\n"
    "v_mfma_f32_16x16x32_bf16 v[56:59], a[0:3], v[104:107], v[136:139]\n"
    "ds_read_b64_tr_b16 v[120:121], v20 offset:160\n"
    "v_mfma_f32_16x16x32_bf16 v[56:59], a[8:11], v[108:111], v[56:59]\n"
    "ds_read_b64_tr_b16 v[122:123], v20 offset:2464\n"
    "v_mfma_f32_16x16x32_bf16 v[60:63], a[4:7], v[104:107], v[136:139]\n"
    "ds_read_b64_tr_b16 v[124:125], v20 offset:10400\n"
    "v_mfma_f32_16x16x32_bf16 v[60:63], a[12:15], v[108:111], v[60:63]\n"
    "s_waitcnt lgkmcnt(3)\n"
    "ds_read_b64_tr_b16 v[126:127], v20 offset:12704\n"
    "v_mfma_f32_16x16x32_bf16 v[64:67], a[0:3], v[112:115], v[136:139]\n"
    "ds_read_b64_tr_b16 v[104:105], v20 offset:192\n"
    "v_mfma_f32_16x16x32_bf16 v[64:67], a[8:11], v[116:119], v[64:67]\n"
    "ds_read_b64_tr_b16 v[106:107], v20 offset:2496\n"
    "v_mfma_f32_16x16x32_bf16 v[68:71], a[4:7], v[112:115], v[136:139]\n"
    "ds_read_b64_tr_b16 v[108:109], v20 offset:10432\n"
    "v_mfma_f32_16x16x32_bf16 v[68:71], a[12:15], v[116:119], v[68:71]\n"
    "s_waitcnt lgkmcnt(3)\n"
    "ds_read_b64_tr_b16 v[110:111], v20 offset:12736\n"
    "v_mfma_f32_16x16x32_bf16 v[72:75], a[0:3], v[120:123], v[136:139]\n"
    "ds_read_b64_tr_b16 v[112:113], v20 offset:224\n"
    "v_mfma_f32_16x16x32_bf16 v[72:75], a[8:11], v[124:127], v[72:75]\n"
    "ds_read_b64_tr_b16 v[114:115], v20 offset:2528\n"
    "v_mfma_f32_16x16x32_bf16 v[76:79], a[4:7], v[120:123], v[136:139]\n"
    "ds_read_b64_tr_b16 v[116:117], v20 offset:10464\n"
    "v_mfma_f32_16x16x32_bf16 v[76:79], a[12:15], v[124:127], v[76:79]\n"
    "s_waitcnt lgkmcnt(3)\n"
    "ds_read_b64_tr_b16 v[118:119], v20 offset:12768\n"
    "v_mfma_f32_16x16x32_bf16 v[80:83], a[0:3], v[104:107], v[136:139]\n"
    "ds_read_b64_tr_b16 v[120:121], v20 offset:256\n"
    "v_mfma_f32_16x16x32_bf16 v[80:83], a[8:11], v[108:111], v[80:83]\n"
    "ds_read_b64_tr_b16 v[122:123], v20 offset:2560\n"
    "v_mfma_f32_16x16x32_bf16 v[84:87], a[4:7], v[104:107], v[136:139]\n"
    "ds_read_b64_tr_b16 v[124:125], v20 offset:10496\n"
    "v_mfma_f32_16x16x32_bf16 v[84:87], a[12:15], v[108:111], v[84:87]\n"
    "s_waitcnt lgkmcnt(3)\n"
    "ds_read_b64_tr_b16 v[126:127], v20 offset:12800\n"
    "v_mfma_f32_16x16x32_bf16 v[88:91], a[0:3], v[112:115], v[136:139]\n"
    "s_waitcnt vmcnt(9)\n"
    "s_barrier\n"
    "ds_read_b64_tr_b16 v[104:105], v20 offset:20480\n"
    "ds_read_b128 a[16:19], v22 offset:16384\n"
    "v_mfma_f32_16x16x32_bf16 v[88:91], a[8:11], v[116:119], v[88:91]\n"
    "ds_read_b64_tr_b16 v[106:107], v20 offset:22784\n"
    "ds_read_b128 a[20:23], v22 offset:18432\n"
    "v_mfma_f32_16x16x32_bf16 v[92:95], a[4:7], v[112:115], v[136:139]\n"
    "ds_read_b64_tr_b16 v[108:109], v20 offset:30720\n"
    "ds_read_b128 a[24:27], v23 offset:16384\n"
    "v_mfma_f32_16x16x32_bf16 v[92:95], a[12:15], v[116:119], v[92:95]\n"
    "s_waitcnt lgkmcnt(6)\n"
    "ds_read_b64_tr_b16 v[110:111], v20 offset:33024\n"
    "ds_read_b128 a[28:31], v23 offset:18432\n"
    "v_mfma_f32_16x16x32_bf16 v[96:99], a[0:3], v[120:123], v[136:139]\n"
    "s_add_u32 m0, s90, 49152\n"
    "ds_read_b64_tr_b16 v[112:113], v20 offset:20512\n"
    "buffer_load_dwordx4 v6, s[40:43], s94 offen lds\n"
    "v_mfma_f32_16x16x32_bf16 v[96:99], a[8:11], v[124:127], v[96:99]\n"
    "ds_read_b64_tr_b16 v[114:115], v20 offset:22816\n"
    "v_mfma_f32_16x16x32_bf16 v[100:103], a[4:7], v[120:123], v[136:139]\n"
    "ds_read_b64_tr_b16 v[116:117], v20 offset:30752\n"
    "buffer_load_dwordx4 v7, s[40:43], s94 offen offset:1024 lds\n"
    "v_mfma_f32_16x16x32_bf16 v[100:103], a[12:15], v[124:127], v[100:103]\n"
    "s_waitcnt lgkmcnt(3)\n"
    "ds_read_b64_tr_b16 v[118:119], v20 offset:33056\n"
    "v_mfma_f32_16x16x32_bf16 v[32:35], a[16:19], v[104:107], v[32:35]\n"
    "ds_read_b64_tr_b16 v[120:121], v20 offset:20544\n"
    "buffer_load_dwordx4 v8, s[40:43], s94 offen offset:2048 lds\n"
    "v_mfma_f32_16x16x32_bf16 v[32:35], a[24:27], v[108:111], v[32:35]\n"
    "ds_read_b64_tr_b16 v[122:123], v20 offset:22848\n"
    "v_mfma_f32_16x16x32_bf16 v[36:39], a[20:23], v[104:107], v[36:39]\n"
    "ds_read_b64_tr_b16 v[124:125], v20 offset:30784\n"
    "buffer_load_dwordx4 v9, s[40:43], s94 offen offset:3072 lds\n"
    "v_mfma_f32_16x16x32_bf16 v[36:39], a[28:31], v[108:111], v[36:39]\n"
    "s_waitcnt lgkmcnt(3)\n"
    "ds_read_b64_tr_b16 v[126:127], v20 offset:33088\n"
    "v_mfma_f32_16x16x32_bf16 v[40:43], a[16:19], v[112:115], v[40:43]\n"
    "s_add_u32 s94, s94, 128\n"
    "s_cmp_eq_u32 s94, 1152\n"
    "s_cselect_b32 s94, 0, s94\n"
    "ds_read_b64_tr_b16 v[104:105], v20 offset:20576\n"
    "v_mfma_f32_16x16x32_bf16 v[40:43], a[24:27], v[116:119], v[40:43]\n"
    "ds_read_b64_tr_b16 v[106:107], v20 offset:22880\n"
    "v_mfma_f32_16x16x32_bf16 v[44:47], a[20:23], v[112:115], v[44:47]\n"
    "s_add_u32 s85, s67, 1152\n"
    "s_add_u32 m0, s89, 61440\n"
    "ds_read_b64_tr_b16 v[108:109], v20 offset:30816\n"
    "buffer_load_dwordx4 v10, s[36:39], s85 offen lds\n"
    "v_mfma_f32_16x16x32_bf16 v[44:47], a[28:31], v[116:119], v[44:47]\n"
    "s_waitcnt lgkmcnt(3)\n"
    "ds_read_b64_tr_b16 v[110:111], v20 offset:33120\n"
    "v_mfma_f32_16x16x32_bf16 v[48:51], a[16:19], v[120:123], v[48:51]\n"
    "ds_read_b64_tr_b16 v[112:113], v20 offset:20608\n"
    "buffer_load_dwordx4 v11, s[36:39], s85 offen offset:1024 lds\n"
    "v_mfma_f32_16x16x32_bf16 v[48:51], a[24:27], v[124:127], v[48:51]\n"
    "ds_read_b64_tr_b16 v[114:115], v20 offset:22912\n"
    "v_mfma_f32_16x16x32_bf16 v[52:55], a[20:23], v[120:123], v[52:55]\n"
    "ds_read_b64_tr_b16 v[116:117], v20 offset:30848\n"
    "buffer_load_dwordx4 v12, s[36:39], s85 offen offset:2048 lds\n"
    "v_mfma_f32_16x16x32_bf16 v[52:55], a[28:31], v[124:127], v[52:55]\n"
    "s_waitcnt lgkmcnt(3)\n"
    "ds_read_b64_tr_b16 v[118:119], v20 offset:33152\n"
    "v_mfma_f32_16x16x32_bf16 v[56:59], a[16:19], v[104:107], v[56:59]\n"
    "ds_read_b64_tr_b16 v[120:121], v20 offset:20640\n"
    "buffer_load_dwordx4 v13, s[36:39], s85 offen offset:3072 lds\n"
    "v_mfma_f32_16x16x32_bf16 v[56:59], a[24:27], v[108:111], v[56:59]\n"
    "ds_read_b64_tr_b16 v[122:123], v20 offset:22944\n"
    "v_mfma_f32_16x16x32_bf16 v[60:63], a[20:23], v[104:107], v[60:63]\n"
    "s_add_u32 m0, s89, 65536\n"
    "ds_read_b64_tr_b16 v[124:125], v20 offset:30880\n"
    "buffer_load_dwordx4 v14, s[36:39], s85 offen lds\n"
    "v_mfma_f32_16x16x32_bf16 v[60:63], a[28:31], v[108:111], v[60:63]\n"
    "s_waitcnt lgkmcnt(3)\n"
    "ds_read_b64_tr_b16 v[126:127], v20 offset:33184\n"
    "v_mfma_f32_16x16x32_bf16 v[64:67], a[16:19], v[112:115], v[64:67]\n"
    "ds_read_b64_tr_b16 v[104:105], v20 offset:20672\n"
    "v_mfma_f32_16x16x32_bf16 v[64:67], a[24:27], v[116:119], v[64:67]\n"
    "ds_read_b64_tr_b16 v[106:107], v20 offset:22976\n"
    "v_mfma_f32_16x16x32_bf16 v[68:71], a[20:23], v[112:115], v[68:71]\n"
    "ds_read_b64_tr_b16 v[108:109], v20 offset:30912\n"
    "v_mfma_f32_16x16x32_bf16 v[68:71], a[28:31], v[116:119], v[68:71]\n"
    "s_waitcnt lgkmcnt(3)\n"
    "ds_read_b64_tr_b16 v[110:111], v20 offset:33216\n"
    "v_mfma_f32_16x16x32_bf16 v[72:75], a[16:19], v[120:123], v[72:75]\n"
    "ds_read_b64_tr_b16 v[112:113], v20 offset:20704\n"
    "v_mfma_f32_16x16x32_bf16 v[72:75], a[24:27], v[124:127], v[72:75]\n"
    "ds_read_b64_tr_b16 v[114:115], v20 offset:23008\n"
    "v_mfma_f32_16x16x32_bf16 v[76:79], a[20:23], v[120:123], v[76:79]\n"
    "ds_read_b64_tr_b16 v[116:117], v20 offset:30944\n"
    "v_mfma_f32_16x16x32_bf16 v[76:79], a[28:31], v[124:127], v[76:79]\n"
    "s_waitcnt lgkmcnt(3)\n"
    "ds_read_b64_tr_b16 v[118:119], v20 offset:33248\n"
    "v_mfma_f32_16x16x32_bf16 v[80:83], a[16:19], v[104:107], v[80:83]\n"
    "ds_read_b64_tr_b16 v[120:121], v20 offset:20736\n"
    "v_mfma_f32_16x16x32_bf16 v[80:83], a[24:27], v[108:111], v[80:83]\n"
    "ds_read_b64_tr_b16 v[122:123], v20 offset:23040\n"
    "v_mfma_f32_16x16x32_bf16 v[84:87], a[20:23], v[104:107], v[84:87]\n"
    "ds_read_b64_tr_b16 v[124:125], v20 offset:30976\n"
    "v_mfma_f32_16x16x32_bf16 v[84:87], a[28:31], v[108:111], v[84:87]\n"
    "s_waitcnt lgkmcnt(3)\n"
    "ds_read_b64_tr_b16 v[126:127], v20 offset:33280\n"
    "v_mfma_f32_16x16x32_bf16 v[88:91], a[16:19], v[112:115], v[88:91]\n"
    "s_waitcnt vmcnt(9)\n"
    "s_barrier\n"
    "ds_read_b64_tr_b16 v[104:105], v20 offset:40960\n"
    "ds_read_b128 a[32:35], v22 offset:32768\n"
    "v_mfma_f32_16x16x32_bf16 v[88:91], a[24:27], v[116:119], v[88:91]\n"
    "ds_read_b64_tr_b16 v[106:107], v20 offset:43264\n"
    "ds_read_b128 a[36:39], v22 offset:34816\n"
    "v_mfma_f32_16x16x32_bf16 v[92:95], a[20:23], v[112:115], v[92:95]\n"
    "ds_read_b64_tr_b16 v[108:109], v20 offset:51200\n"
    "ds_read_b128 a[40:43], v23 offset:32768\n"
    "v_mfma_f32_16x16x32_bf16 v[92:95], a[28:31], v[116:119], v[92:95]\n"
    "s_waitcnt lgkmcnt(6)\n"
    "ds_read_b64_tr_b16 v[110:111], v20 offset:53504\n"
    "ds_read_b128 a[44:47], v23 offset:34816\n"
    "v_mfma_f32_16x16x32_bf16 v[96:99], a[16:19], v[120:123], v[96:99]\n"
    "s_add_u32 m0, s90, 65536\n"
    "ds_read_b64_tr_b16 v[112:113], v20 offset:40992\n"
    "buffer_load_dwordx4 v6, s[40:43], s94 offen lds\n"
    "v_mfma_f32_16x16x32_bf16 v[96:99], a[24:27], v[124:127], v[96:99]\n"
    "ds_read_b64_tr_b16 v[114:115], v20 offset:43296\n"
    "v_mfma_f32_16x16x32_bf16 v[100:103], a[20:23], v[120:123], v[100:103]\n"
    "ds_read_b64_tr_b16 v[116:117], v20 offset:51232\n"
    "buffer_load_dwordx4 v7, s[40:43], s94 offen offset:1024 lds\n"
    "v_mfma_f32_16x16x32_bf16 v[100:103], a[28:31], v[124:127], v[100:103]\n"
    "s_waitcnt lgkmcnt(3)\n"
    "ds_read_b64_tr_b16 v[118:119], v20 offset:53536\n"
    "v_mfma_f32_16x16x32_bf16 v[32:35], a[32:35], v[104:107], v[32:35]\n"
    "ds_read_b64_tr_b16 v[120:121], v20 offset:41024\n"
    "buffer_load_dwordx4 v8, s[40:43], s94 offen offset:2048 lds\n"
    "v_mfma_f32_16x16x32_bf16 v[32:35], a[40:43], v[108:111], v[32:35]\n"
    "ds_read_b64_tr_b16 v[122:123], v20 offset:43328\n"
    "v_mfma_f32_16x16x32_bf16 v[36:39], a[36:39], v[104:107], v[36:39]\n"
    "ds_read_b64_tr_b16 v[124:125], v20 offset:51264\n"
    "buffer_load_dwordx4 v9, s[40:43], s94 offen offset:3072 lds\n"
    "v_mfma_f32_16x16x32_bf16 v[36:39], a[44:47], v[108:111], v[36:39]\n"
    "s_waitcnt lgkmcnt(3)\n"
    "ds_read_b64_tr_b16 v[126:127], v20 offset:53568\n"
    "v_mfma_f32_16x16x32_bf16 v[40:43], a[32:35], v[112:115], v[40:43]\n"
    "s_add_u32 s94, s94, 128\n"
    "s_cmp_eq_u32 s94, 1152\n"
    "s_cselect_b32 s94, 0, s94\n"
    "ds_read_b64_tr_b16 v[104:105], v20 offset:41056\n"
    "v_mfma_f32_16x16x32_bf16 v[40:43], a[40:43], v[116:119], v[40:43]\n"
    "ds_read_b64_tr_b16 v[106:107], v20 offset:43360\n"
    "v_mfma_f32_16x16x32_bf16 v[44:47], a[36:39], v[112:115], v[44:47]\n"
    "s_add_u32 s85, s68, 1152\n"
    "s_add_u32 m0, s89, 0\n"
    "ds_read_b64_tr_b16 v[108:109], v20 offset:51296\n"
    "buffer_load_dwordx4 v10, s[36:39], s85 offen lds\n"
    "v_mfma_f32_16x16x32_bf16 v[44:47], a[44:47], v[116:119], v[44:47]\n"
    "s_waitcnt lgkmcnt(3)\n"
    "ds_read_b64_tr_b16 v[110:111], v20 offset:53600\n"
    "v_mfma_f32_16x16x32_bf16 v[48:51], a[32:35], v[120:123], v[48:51]\n"
    "ds_read_b64_tr_b16 v[112:113], v20 offset:41088\n"
    "buffer_load_dwordx4 v11, s[36:39], s85 offen offset:1024 lds\n"
    "v_mfma_f32_16x16x32_bf16 v[48:51], a[40:43], v[124:127], v[48:51]\n"
    "ds_read_b64_tr_b16 v[114:115], v20 offset:43392\n"
    "v_mfma_f32_16x16x32_bf16 v[52:55], a[36:39], v[120:123], v[52:55]\n"
    "ds_read_b64_tr_b16 v[116:117], v20 offset:51328\n"
    "buffer_load_dwordx4 v12, s[36:39], s85 offen offset:2048 lds\n"
    "v_mfma_f32_16x16x32_bf16 v[52:55], a[44:47], v[124:127], v[52:55]\n"
    "s_waitcnt lgkmcnt(3)\n"
    "ds_read_b64_tr_b16 v[118:119], v20 offset:53632\n"
    "v_mfma_f32_16x16x32_bf16 v[56:59], a[32:35], v[104:107], v[56:59]\n"
    "ds_read_b64_tr_b16 v[120:121], v20 offset:41120\n"
    "buffer_load_dwordx4 v13, s[36:39], s85 offen offset:3072 lds\n"
    "v_mfma_f32_16x16x32_bf16 v[56:59], a[40:43], v[108:111], v[56:59]\n"
    "ds_read_b64_tr_b16 v[122:123], v20 offset:43424\n"
    "v_mfma_f32_16x16x32_bf16 v[60:63], a[36:39], v[104:107], v[60:63]\n"
    "s_add_u32 m0, s89, 4096\n"
    "ds_read_b64_tr_b16 v[124:125], v20 offset:51360\n"
    "buffer_load_dwordx4 v14, s[36:39], s85 offen lds\n"
    "v_mfma_f32_16x16x32_bf16 v[60:63], a[44:47], v[108:111], v[60:63]\n"
    "s_waitcnt lgkmcnt(3)\n"
    "ds_read_b64_tr_b16 v[126:127], v20 offset:53664\n"
    "v_mfma_f32_16x16x32_bf16 v[64:67], a[32:35], v[112:115], v[64:67]\n"
    "ds_read_b64_tr_b16 v[104:105], v20 offset:41152\n"
    "v_mfma_f32_16x16x32_bf16 v[64:67], a[40:43], v[116:119], v[64:67]\n"
    "ds_read_b64_tr_b16 v[106:107], v20 offset:43456\n"
    "v_mfma_f32_16x16x32_bf16 v[68:71], a[36:39], v[112:115], v[68:71]\n"
    "ds_read_b64_tr_b16 v[108:109], v20 offset:51392\n"
    "v_mfma_f32_16x16x32_bf16 v[68:71], a[44:47], v[116:119], v[68:71]\n"
    "s_waitcnt lgkmcnt(3)\n"
    "ds_read_b64_tr_b16 v[110:111], v20 offset:53696\n"
    "v_mfma_f32_16x16x32_bf16 v[72:75], a[32:35], v[120:123], v[72:75]\n"
    "ds_read_b64_tr_b16 v[112:113], v20 offset:41184\n"
    "v_mfma_f32_16x16x32_bf16 v[72:75], a[40:43], v[124:127], v[72:75]\n"
    "ds_read_b64_tr_b16 v[114:115], v20 offset:43488\n"
    "v_mfma_f32_16x16x32_bf16 v[76:79], a[36:39], v[120:123], v[76:79]\n"
    "ds_read_b64_tr_b16 v[116:117], v20 offset:51424\n"
    "v_mfma_f32_16x16x32_bf16 v[76:79], a[44:47], v[124:127], v[76:79]\n"
    "s_waitcnt lgkmcnt(3)\n"
    "ds_read_b64_tr_b16 v[118:119], v20 offset:53728\n"
    "v_mfma_f32_16x16x32_bf16 v[80:83], a[32:35], v[104:107], v[80:83]\n"
    "ds_read_b64_tr_b16 v[120:121], v20 offset:41216\n"
    "v_mfma_f32_16x16x32_bf16 v[80:83], a[40:43], v[108:111], v[80:83]\n"
    "ds_read_b64_tr_b16 v[122:123], v20 offset:43520\n"
    "v_mfma_f32_16x16x32_bf16 v[84:87], a[36:39], v[104:107], v[84:87]\n"
    "ds_read_b64_tr_b16 v[124:125], v20 offset:51456\n"
    "v_mfma_f32_16x16x32_bf16 v[84:87], a[44:47], v[108:111], v[84:87]\n"
    "s_waitcnt lgkmcnt(3)\n"
    "ds_read_b64_tr_b16 v[126:127], v20 offset:53760\n"
    "v_mfma_f32_16x16x32_bf16 v[88:91], a[32:35], v[112:115], v[88:91]\n"
    "s_waitcnt vmcnt(9)\n"
    "s_barrier\n"
    "ds_read_b64_tr_b16 v[104:105], v21 offset:0\n"
    "ds_read_b128 a[48:51], v22 offset:49152\n"
    "v_mfma_f32_16x16x32_bf16 v[88:91], a[40:43], v[116:119], v[88:91]\n"
    "ds_read_b64_tr_b16 v[106:107], v21 offset:2304\n"
    "ds_read_b128 a[52:55], v22 offset:51200\n"
    "v_mfma_f32_16x16x32_bf16 v[92:95], a[36:39], v[112:115], v[92:95]\n"
    "ds_read_b64_tr_b16 v[108:109], v21 offset:10240\n"
    "ds_read_b128 a[56:59], v23 offset:49152\n"
    "v_mfma_f32_16x16x32_bf16 v[92:95], a[44:47], v[116:119], v[92:95]\n"
    "s_waitcnt lgkmcnt(6)\n"
    "ds_read_b64_tr_b16 v[110:111], v21 offset:12544\n"
    "ds_read_b128 a[60:63], v23 offset:51200\n"
    "v_mfma_f32_16x16x32_bf16 v[96:99], a[32:35], v[120:123], v[96:99]\n"
    "s_add_u32 m0, s90, 0\n"
    "ds_read_b64_tr_b16 v[112:113], v21 offset:32\n"
    "buffer_load_dwordx4 v6, s[40:43], s94 offen lds\n"
    "v_mfma_f32_16x16x32_bf16 v[96:99], a[40:43], v[124:127], v[96:99]\n"
    "ds_read_b64_tr_b16 v[114:115], v21 offset:2336\n"
    "v_mfma_f32_16x16x32_bf16 v[100:103], a[36:39], v[120:123], v[100:103]\n"
    "ds_read_b64_tr_b16 v[116:117], v21 offset:10272\n"
    "buffer_load_dwordx4 v7, s[40:43], s94 offen offset:1024 lds\n"
    "v_mfma_f32_16x16x32_bf16 v[100:103], a[44:47], v[124:127], v[100:103]\n"
    "s_waitcnt lgkmcnt(3)\n"
    "ds_read_b64_tr_b16 v[118:119], v21 offset:12576\n"
    "v_mfma_f32_16x16x32_bf16 v[32:35], a[48:51], v[104:107], v[32:35]\n"
    "ds_read_b64_tr_b16 v[120:121], v21 offset:64\n"
    "buffer_load_dwordx4 v8, s[40:43], s94 offen offset:2048 lds\n"
    "v_mfma_f32_16x16x32_bf16 v[32:35], a[56:59], v[108:111], v[32:35]\n"
    "ds_read_b64_tr_b16 v[122:123], v21 offset:2368\n"
    "v_mfma_f32_16x16x32_bf16 v[36:39], a[52:55], v[104:107], v[36:39]\n"
    "ds_read_b64_tr_b16 v[124:125], v21 offset:10304\n"
    "buffer_load_dwordx4 v9, s[40:43], s94 offen offset:3072 lds\n"
    "v_mfma_f32_16x16x32_bf16 v[36:39], a[60:63], v[108:111], v[36:39]\n"
    "s_waitcnt lgkmcnt(3)\n"
    "ds_read_b64_tr_b16 v[126:127], v21 offset:12608\n"
    "v_mfma_f32_16x16x32_bf16 v[40:43], a[48:51], v[112:115], v[40:43]\n"
    "s_add_u32 s94, s94, 128\n"
    "s_cmp_eq_u32 s94, 1152\n"
    "s_cselect_b32 s94, 0, s94\n"
    "ds_read_b64_tr_b16 v[104:105], v21 offset:96\n"
    "v_mfma_f32_16x16x32_bf16 v[40:43], a[56:59], v[116:119], v[40:43]\n"
    "ds_read_b64_tr_b16 v[106:107], v21 offset:2400\n"
    "v_mfma_f32_16x16x32_bf16 v[44:47], a[52:55], v[112:115], v[44:47]\n"
    "s_add_u32 s85, s69, 1152\n"
    "s_add_u32 m0, s89, 20480\n"
    "ds_read_b64_tr_b16 v[108:109], v21 offset:10336\n"
    "buffer_load_dwordx4 v10, s[36:39], s85 offen lds\n"
    "v_mfma_f32_16x16x32_bf16 v[44:47], a[60:63], v[116:119], v[44:47]\n"
    "s_waitcnt lgkmcnt(3)\n"
    "ds_read_b64_tr_b16 v[110:111], v21 offset:12640\n"
    "v_mfma_f32_16x16x32_bf16 v[48:51], a[48:51], v[120:123], v[48:51]\n"
    "ds_read_b64_tr_b16 v[112:113], v21 offset:128\n"
    "buffer_load_dwordx4 v11, s[36:39], s85 offen offset:1024 lds\n"
    "v_mfma_f32_16x16x32_bf16 v[48:51], a[56:59], v[124:127], v[48:51]\n"
    "ds_read_b64_tr_b16 v[114:115], v21 offset:2432\n"
    "v_mfma_f32_16x16x32_bf16 v[52:55], a[52:55], v[120:123], v[52:55]\n"
    "ds_read_b64_tr_b16 v[116:117], v21 offset:10368\n"
    "buffer_load_dwordx4 v12, s[36:39], s85 offen offset:2048 lds\n"
    "v_mfma_f32_16x16x32_bf16 v[52:55], a[60:63], v[124:127], v[52:55]\n"
    "s_waitcnt lgkmcnt(3)\n"
    "ds_read_b64_tr_b16 v[118:119], v21 offset:12672\n"
    "v_mfma_f32_16x16x32_bf16 v[56:59], a[48:51], v[104:107], v[56:59]\n"
    "ds_read_b64_tr_b16 v[120:121], v21 offset:160\n"
    "buffer_load_dwordx4 v13, s[36:39], s85 offen offset:3072 lds\n"
    "v_mfma_f32_16x16x32_bf16 v[56:59], a[56:59], v[108:111], v[56:59]\n"
    "ds_read_b64_tr_b16 v[122:123], v21 offset:2464\n"
    "v_mfma_f32_16x16x32_bf16 v[60:63], a[52:55], v[104:107], v[60:63]\n"
    "s_add_u32 m0, s89, 24576\n"
    "ds_read_b64_tr_b16 v[124:125], v21 offset:10400\n"
    "buffer_load_dwordx4 v14, s[36:39], s85 offen lds\n"
    "v_mfma_f32_16x16x32_bf16 v[60:63], a[60:63], v[108:111], v[60:63]\n"
    "s_waitcnt lgkmcnt(3)\n"
    "ds_read_b64_tr_b16 v[126:127], v21 offset:12704\n"
    "v_mfma_f32_16x16x32_bf16 v[64:67], a[48:51], v[112:115], v[64:67]\n"
    "ds_read_b64_tr_b16 v[104:105], v21 offset:192\n"
    "v_mfma_f32_16x16x32_bf16 v[64:67], a[56:59], v[116:119], v[64:67]\n"
    "ds_read_b64_tr_b16 v[106:107], v21 offset:2496\n"
    "v_mfma_f32_16x16x32_bf16 v[68:71], a[52:55], v[112:115], v[68:71]\n"
    "ds_read_b64_tr_b16 v[108:109], v21 offset:10432\n"
    "v_mfma_f32_16x16x32_bf16 v[68:71], a[60:63], v[116:119], v[68:71]\n"
    "s_waitcnt lgkmcnt(3)\n"
    "ds_read_b64_tr_b16 v[110:111], v21 offset:12736\n"
    "v_mfma_f32_16x16x32_bf16 v[72:75], a[48:51], v[120:123], v[72:75]\n"
    "ds_read_b64_tr_b16 v[112:113], v21 offset:224\n"
    "v_mfma_f32_16x16x32_bf16 v[72:75], a[56:59], v[124:127], v[72:75]\n"
    "ds_read_b64_tr_b16 v[114:115], v21 offset:2528\n"
    "v_mfma_f32_16x16x32_bf16 v[76:79], a[52:55], v[120:123], v[76:79]\n"
    "ds_read_b64_tr_b16 v[116:117], v21 offset:10464\n"
    "v_mfma_f32_16x16x32_bf16 v[76:79], a[60:63], v[124:127], v[76:79]\n"
    "s_waitcnt lgkmcnt(3)\n"
    "ds_read_b64_tr_b16 v[118:119], v21 offset:12768\n"
    "v_mfma_f32_16x16x32_bf16 v[80:83], a[48:51], v[104:107], v[80:83]\n"
    "ds_read_b64_tr_b16 v[120:121], v21 offset:256\n"
    "v_mfma_f32_16x16x32_bf16 v[80:83], a[56:59], v[108:111], v[80:83]\n"
    "ds_read_b64_tr_b16 v[122:123], v21 offset:2560\n"
    "v_mfma_f32_16x16x32_bf16 v[84:87], a[52:55], v[104:107], v[84:87]\n"
    "ds_read_b64_tr_b16 v[124:125], v21 offset:10496\n"
    "v_mfma_f32_16x16x32_bf16 v[84:87], a[60:63], v[108:111], v[84:87]\n"
    "s_waitcnt lgkmcnt(3)\n"
    "ds_read_b64_tr_b16 v[126:127], v21 offset:12800\n"
    "v_mfma_f32_16x16x32_bf16 v[88:91], a[48:51], v[112:115], v[88:91]\n"
    "s_waitcnt vmcnt(9)\n"
    "s_barrier\n"
    "ds_read_b64_tr_b16 v[104:105], v20 offset:0\n"
    "ds_read_b128 a[64:67], v24 offset:0\n"
    "v_mfma_f32_16x16x32_bf16 v[88:91], a[56:59], v[116:119], v[88:91]\n"
    "ds_read_b64_tr_b16 v[106:107], v20 offset:2304\n"
    "ds_read_b128 a[68:71], v24 offset:2048\n"
    "v_mfma_f32_16x16x32_bf16 v[92:95], a[52:55], v[112:115], v[92:95]\n"
    "ds_read_b64_tr_b16 v[108:109], v20 offset:10240\n"
    "ds_read_b128 a[72:75], v25 offset:0\n"
    "v_mfma_f32_16x16x32_bf16 v[92:95], a[60:63], v[116:119], v[92:95]\n"
    "s_waitcnt lgkmcnt(6)\n"
    "ds_read_b64_tr_b16 v[110:111], v20 offset:12544\n"
    "ds_read_b128 a[76:79], v25 offset:2048\n"
    "v_mfma_f32_16x16x32_bf16 v[96:99], a[48:51], v[120:123], v[96:99]\n"
    "s_add_u32 m0, s90, 16384\n"
    "ds_read_b64_tr_b16 v[112:113], v20 offset:32\n"
    "buffer_load_dwordx4 v6, s[40:43], s94 offen lds\n"
    "v_mfma_f32_16x16x32_bf16 v[96:99], a[56:59], v[124:127], v[96:99]\n"
    "ds_read_b64_tr_b16 v[114:115], v20 offset:2336\n"
    "v_mfma_f32_16x16x32_bf16 v[100:103], a[52:55], v[120:123], v[100:103]\n"
    "ds_read_b64_tr_b16 v[116:117], v20 offset:10272\n"
    "buffer_load_dwordx4 v7, s[40:43], s94 offen offset:1024 lds\n"
    "v_mfma_f32_16x16x32_bf16 v[100:103], a[60:63], v[124:127], v[100:103]\n"
    "s_waitcnt lgkmcnt(3)\n"
    "ds_read_b64_tr_b16 v[118:119], v20 offset:12576\n"
    "v_mfma_f32_16x16x32_bf16 v[32:35], a[64:67], v[104:107], v[32:35]\n"
    "ds_read_b64_tr_b16 v[120:121], v20 offset:64\n"
    "buffer_load_dwordx4 v8, s[40:43], s94 offen offset:2048 lds\n"
    "v_mfma_f32_16x16x32_bf16 v[32:35], a[72:75], v[108:111], v[32:35]\n"
    "ds_read_b64_tr_b16 v[122:123], v20 offset:2368\n"
    "v_mfma_f32_16x16x32_bf16 v[36:39], a[68:71], v[104:107], v[36:39]\n"
    "ds_read_b64_tr_b16 v[124:125], v20 offset:10304\n"
    "buffer_load_dwordx4 v9, s[40:43], s94 offen offset:3072 lds\n"
    "v_mfma_f32_16x16x32_bf16 v[36:39], a[76:79], v[108:111], v[36:39]\n"
    "s_waitcnt lgkmcnt(3)\n"
    "ds_read_b64_tr_b16 v[126:127], v20 offset:12608\n"
    "v_mfma_f32_16x16x32_bf16 v[40:43], a[64:67], v[112:115], v[40:43]\n"
    "s_add_u32 s94, s94, 128\n"
    "s_cmp_eq_u32 s94, 1152\n"
    "s_cselect_b32 s94, 0, s94\n"
    "ds_read_b64_tr_b16 v[104:105], v20 offset:96\n"
    "v_mfma_f32_16x16x32_bf16 v[40:43], a[72:75], v[116:119], v[40:43]\n"
    "ds_read_b64_tr_b16 v[106:107], v20 offset:2400\n"
    "v_mfma_f32_16x16x32_bf16 v[44:47], a[68:71], v[112:115], v[44:47]\n"
    "s_add_u32 s85, s70, 1152\n"
    "s_add_u32 m0, s89, 40960\n"
    "ds_read_b64_tr_b16 v[108:109], v20 offset:10336\n"
    "buffer_load_dwordx4 v10, s[36:39], s85 offen lds\n"
    "v_mfma_f32_16x16x32_bf16 v[44:47], a[76:79], v[116:119], v[44:47]\n"
    "s_waitcnt lgkmcnt(3)\n"
    "ds_read_b64_tr_b16 v[110:111], v20 offset:12640\n"
    "v_mfma_f32_16x16x32_bf16 v[48:51], a[64:67], v[120:123], v[48:51]\n"
    "ds_read_b64_tr_b16 v[112:113], v20 offset:128\n"
    "buffer_load_dwordx4 v11, s[36:39], s85 offen offset:1024 lds\n"
    "v_mfma_f32_16x16x32_bf16 v[48:51], a[72:75], v[124:127], v[48:51]\n"
    "ds_read_b64_tr_b16 v[114:115], v20 offset:2432\n"
    "v_mfma_f32_16x16x32_bf16 v[52:55], a[68:71], v[120:123], v[52:55]\n"
    "ds_read_b64_tr_b16 v[116:117], v20 offset:10368\n"
    "buffer_load_dwordx4 v12, s[36:39], s85 offen offset:2048 lds\n"
    "v_mfma_f32_16x16x32_bf16 v[52:55], a[76:79], v[124:127], v[52:55]\n"
    "s_waitcnt lgkmcnt(3)\n"
    "ds_read_b64_tr_b16 v[118:119], v20 offset:12672\n"
    "v_mfma_f32_16x16x32_bf16 v[56:59], a[64:67], v[104:107], v[56:59]\n"
    "ds_read_b64_tr_b16 v[120:121], v20 offset:160\n"
    "buffer_load_dwordx4 v13, s[36:39], s85 offen offset:3072 lds\n"
    "v_mfma_f32_16x16x32_bf16 v[56:59], a[72:75], v[108:111], v[56:59]\n"
    "ds_read_b64_tr_b16 v[122:123], v20 offset:2464\n"
    "v_mfma_f32_16x16x32_bf16 v[60:63], a[68:71], v[104:107], v[60:63]\n"
    "s_add_u32 m0, s89, 45056\n"
    "ds_read_b64_tr_b16 v[124:125], v20 offset:10400\n"
    "buffer_load_dwordx4 v14, s[36:39], s85 offen lds\n"
    "v_mfma_f32_16x16x32_bf16 v[60:63], a[76:79], v[108:111], v[60:63]\n"
    "s_waitcnt lgkmcnt(3)\n"
    "ds_read_b64_tr_b16 v[126:127], v20 offset:12704\n"
    "v_mfma_f32_16x16x32_bf16 v[64:67], a[64:67], v[112:115], v[64:67]\n"
    "ds_read_b64_tr_b16 v[104:105], v20 offset:192\n"
    "v_mfma_f32_16x16x32_bf16 v[64:67], a[72:75], v[116:119], v[64:67]\n"
    "ds_read_b64_tr_b16 v[106:107], v20 offset:2496\n"
    "v_mfma_f32_16x16x32_bf16 v[68:71], a[68:71], v[112:115], v[68:71]\n"
    "ds_read_b64_tr_b16 v[108:109], v20 offset:10432\n"
    "v_mfma_f32_16x16x32_bf16 v[68:71], a[76:79], v[116:119], v[68:71]\n"
    "s_waitcnt lgkmcnt(3)\n"
    "ds_read_b64_tr_b16 v[110:111], v20 offset:12736\n"
    "v_mfma_f32_16x16x32_bf16 v[72:75], a[64:67], v[120:123], v[72:75]\n"
    "ds_read_b64_tr_b16 v[112:113], v20 offset:224\n"
    "v_mfma_f32_16x16x32_bf16 v[72:75], a[72:75], v[124:127], v[72:75]\n"
    "ds_read_b64_tr_b16 v[114:115], v20 offset:2528\n"
    "v_mfma_f32_16x16x32_bf16 v[76:79], a[68:71], v[120:123], v[76:79]\n"
    "ds_read_b64_tr_b16 v[116:117], v20 offset:10464\n"
    "v_mfma_f32_16x16x32_bf16 v[76:79], a[76:79], v[124:127], v[76:79]\n"
    "s_waitcnt lgkmcnt(3)\n"
    "ds_read_b64_tr_b16 v[118:119], v20 offset:12768\n"
    "v_mfma_f32_16x16x32_bf16 v[80:83], a[64:67], v[104:107], v[80:83]\n"
    "ds_read_b64_tr_b16 v[120:121], v20 offset:256\n"
    "v_mfma_f32_16x16x32_bf16 v[80:83], a[72:75], v[108:111], v[80:83]\n"
    "ds_read_b64_tr_b16 v[122:123], v20 offset:2560\n"
    "v_mfma_f32_16x16x32_bf16 v[84:87], a[68:71], v[104:107], v[84:87]\n"
    "ds_read_b64_tr_b16 v[124:125], v20 offset:10496\n"
    "v_mfma_f32_16x16x32_bf16 v[84:87], a[76:79], v[108:111], v[84:87]\n"
    "s_waitcnt lgkmcnt(3)\n"
    "ds_read_b64_tr_b16 v[126:127], v20 offset:12800\n"
    "v_mfma_f32_16x16x32_bf16 v[88:91], a[64:67], v[112:115], v[88:91]\n"
    "s_waitcnt vmcnt(9)\n"
    "s_barrier\n"
    "ds_read_b64_tr_b16 v[104:105], v20 offset:20480\n"
    "ds_read_b128 a[80:83], v22 offset:0\n"
    "v_mfma_f32_16x16x32_bf16 v[88:91], a[72:75], v[116:119], v[88:91]\n"
    "ds_read_b64_tr_b16 v[106:107], v20 offset:22784\n"
    "ds_read_b128 a[84:87], v22 offset:2048\n"
    "v_mfma_f32_16x16x32_bf16 v[92:95], a[68:71], v[112:115], v[92:95]\n"
    "ds_read_b64_tr_b16 v[108:109], v20 offset:30720\n"
    "ds_read_b128 a[88:91], v23 offset:0\n"
    "v_mfma_f32_16x16x32_bf16 v[92:95], a[76:79], v[116:119], v[92:95]\n"
    "s_waitcnt lgkmcnt(6)\n"
    "ds_read_b64_tr_b16 v[110:111], v20 offset:33024\n"
    "ds_read_b128 a[92:95], v23 offset:2048\n"
    "v_mfma_f32_16x16x32_bf16 v[96:99], a[64:67], v[120:123], v[96:99]\n"
    "s_add_u32 m0, s90, 32768\n"
    "ds_read_b64_tr_b16 v[112:113], v20 offset:20512\n"
    "buffer_load_dwordx4 v6, s[40:43], s94 offen lds\n"
    "v_mfma_f32_16x16x32_bf16 v[96:99], a[72:75], v[124:127], v[96:99]\n"
    "ds_read_b64_tr_b16 v[114:115], v20 offset:22816\n"
    "v_mfma_f32_16x16x32_bf16 v[100:103], a[68:71], v[120:123], v[100:103]\n"
    "ds_read_b64_tr_b16 v[116:117], v20 offset:30752\n"
    "buffer_load_dwordx4 v7, s[40:43], s94 offen offset:1024 lds\n"
    "v_mfma_f32_16x16x32_bf16 v[100:103], a[76:79], v[124:127], v[100:103]\n"
    "s_waitcnt lgkmcnt(3)\n"
    "ds_read_b64_tr_b16 v[118:119], v20 offset:33056\n"
    "v_mfma_f32_16x16x32_bf16 v[32:35], a[80:83], v[104:107], v[32:35]\n"
    "ds_read_b64_tr_b16 v[120:121], v20 offset:20544\n"
    "buffer_load_dwordx4 v8, s[40:43], s94 offen offset:2048 lds\n"
    "v_mfma_f32_16x16x32_bf16 v[32:35], a[88:91], v[108:111], v[32:35]\n"
    "ds_read_b64_tr_b16 v[122:123], v20 offset:22848\n"
    "v_mfma_f32_16x16x32_bf16 v[36:39], a[84:87], v[104:107], v[36:39]\n"
    "ds_read_b64_tr_b16 v[124:125], v20 offset:30784\n"
    "buffer_load_dwordx4 v9, s[40:43], s94 offen offset:3072 lds\n"
    "v_mfma_f32_16x16x32_bf16 v[36:39], a[92:95], v[108:111], v[36:39]\n"
    "s_waitcnt lgkmcnt(3)\n"
    "ds_read_b64_tr_b16 v[126:127], v20 offset:33088\n"
    "v_mfma_f32_16x16x32_bf16 v[40:43], a[80:83], v[112:115], v[40:43]\n"
    "s_add_u32 s94, s94, 128\n"
    "s_cmp_eq_u32 s94, 1152\n"
    "s_cselect_b32 s94, 0, s94\n"
    "ds_read_b64_tr_b16 v[104:105], v20 offset:20576\n"
    "v_mfma_f32_16x16x32_bf16 v[40:43], a[88:91], v[116:119], v[40:43]\n"
    "ds_read_b64_tr_b16 v[106:107], v20 offset:22880\n"
    "v_mfma_f32_16x16x32_bf16 v[44:47], a[84:87], v[112:115], v[44:47]\n"
    "s_add_u32 s85, s71, 1152\n"
    "s_add_u32 m0, s89, 61440\n"
    "ds_read_b64_tr_b16 v[108:109], v20 offset:30816\n"
    "buffer_load_dwordx4 v10, s[36:39], s85 offen lds\n"
    "v_mfma_f32_16x16x32_bf16 v[44:47], a[92:95], v[116:119], v[44:47]\n"
    "s_waitcnt lgkmcnt(3)\n"
    "ds_read_b64_tr_b16 v[110:111], v20 offset:33120\n"
    "v_mfma_f32_16x16x32_bf16 v[48:51], a[80:83], v[120:123], v[48:51]\n"
    "ds_read_b64_tr_b16 v[112:113], v20 offset:20608\n"
    "buffer_load_dwordx4 v11, s[36:39], s85 offen offset:1024 lds\n"
    "v_mfma_f32_16x16x32_bf16 v[48:51], a[88:91], v[124:127], v[48:51]\n"
    "ds_read_b64_tr_b16 v[114:115], v20 offset:22912\n"
    "v_mfma_f32_16x16x32_bf16 v[52:55], a[84:87], v[120:123], v[52:55]\n"
    "ds_read_b64_tr_b16 v[116:117], v20 offset:30848\n"
    "buffer_load_dwordx4 v12, s[36:39], s85 offen offset:2048 lds\n"
    "v_mfma_f32_16x16x32_bf16 v[52:55], a[92:95], v[124:127], v[52:55]\n"
    "s_waitcnt lgkmcnt(3)\n"
    "ds_read_b64_tr_b16 v[118:119], v20 offset:33152\n"
    "v_mfma_f32_16x16x32_bf16 v[56:59], a[80:83], v[104:107], v[56:59]\n"
    "ds_read_b64_tr_b16 v[120:121], v20 offset:20640\n"
    "buffer_load_dwordx4 v13, s[36:39], s85 offen offset:3072 lds\n"
    "v_mfma_f32_16x16x32_bf16 v[56:59], a[88:91], v[108:111], v[56:59]\n"
    "ds_read_b64_tr_b16 v[122:123], v20 offset:22944\n"
    "v_mfma_f32_16x16x32_bf16 v[60:63], a[84:87], v[104:107], v[60:63]\n"
    "s_add_u32 m0, s89, 65536\n"
    "ds_read_b64_tr_b16 v[124:125], v20 offset:30880\n"
    "buffer_load_dwordx4 v14, s[36:39], s85 offen lds\n"
    "v_mfma_f32_16x16x32_bf16 v[60:63], a[92:95], v[108:111], v[60:63]\n"
    "s_waitcnt lgkmcnt(3)\n"
    "ds_read_b64_tr_b16 v[126:127], v20 offset:33184\n"
    "v_mfma_f32_16x16x32_bf16 v[64:67], a[80:83], v[112:115], v[64:67]\n"
    "ds_read_b64_tr_b16 v[104:105], v20 offset:20672\n"
    "v_mfma_f32_16x16x32_bf16 v[64:67], a[88:91], v[116:119], v[64:67]\n"
    "ds_read_b64_tr_b16 v[106:107], v20 offset:22976\n"
    "v_mfma_f32_16x16x32_bf16 v[68:71], a[84:87], v[112:115], v[68:71]\n"
    "ds_read_b64_tr_b16 v[108:109], v20 offset:30912\n"
    "v_mfma_f32_16x16x32_bf16 v[68:71], a[92:95], v[116:119], v[68:71]\n"
    "s_waitcnt lgkmcnt(3)\n"
    "ds_read_b64_tr_b16 v[110:111], v20 offset:33216\n"
    "v_mfma_f32_16x16x32_bf16 v[72:75], a[80:83], v[120:123], v[72:75]\n"
    "ds_read_b64_tr_b16 v[112:113], v20 offset:20704\n"
    "v_mfma_f32_16x16x32_bf16 v[72:75], a[88:91], v[124:127], v[72:75]\n"
    "ds_read_b64_tr_b16 v[114:115], v20 offset:23008\n"
    "v_mfma_f32_16x16x32_bf16 v[76:79], a[84:87], v[120:123], v[76:79]\n"
    "ds_read_b64_tr_b16 v[116:117], v20 offset:30944\n"
    "v_mfma_f32_16x16x32_bf16 v[76:79], a[92:95], v[124:127], v[76:79]\n"
    "s_waitcnt lgkmcnt(3)\n"
    "ds_read_b64_tr_b16 v[118:119], v20 offset:33248\n"
    "v_mfma_f32_16x16x32_bf16 v[80:83], a[80:83], v[104:107], v[80:83]\n"
    "ds_read_b64_tr_b16 v[120:121], v20 offset:20736\n"
    "v_mfma_f32_16x16x32_bf16 v[80:83], a[88:91], v[108:111], v[80:83]\n"
    "ds_read_b64_tr_b16 v[122:123], v20 offset:23040\n"
    "v_mfma_f32_16x16x32_bf16 v[84:87], a[84:87], v[104:107], v[84:87]\n"
    "ds_read_b64_tr_b16 v[124:125], v20 offset:30976\n"
    "v_mfma_f32_16x16x32_bf16 v[84:87], a[92:95], v[108:111], v[84:87]\n"
    "s_waitcnt lgkmcnt(3)\n"
    "ds_read_b64_tr_b16 v[126:127], v20 offset:33280\n"
    "v_mfma_f32_16x16x32_bf16 v[88:91], a[80:83], v[112:115], v[88:91]\n"
    "s_waitcnt vmcnt(9)\n"
    "s_barrier\n"
    "ds_read_b64_tr_b16 v[104:105], v20 offset:40960\n"
    "ds_read_b128 a[96:99], v22 offset:16384\n"
    "v_mfma_f32_16x16x32_bf16 v[88:91], a[88:91], v[116:119], v[88:91]\n"
    "ds_read_b64_tr_b16 v[106:107], v20 offset:43264\n"
    "ds_read_b128 a[100:103], v22 offset:18432\n"
    "v_mfma_f32_16x16x32_bf16 v[92:95], a[84:87], v[112:115], v[92:95]\n"
    "ds_read_b64_tr_b16 v[108:109], v20 offset:51200\n"
    "ds_read_b128 a[104:107], v23 offset:16384\n"
    "v_mfma_f32_16x16x32_bf16 v[92:95], a[92:95], v[116:119], v[92:95]\n"
    "s_waitcnt lgkmcnt(6)\n"
    "ds_read_b64_tr_b16 v[110:111], v20 offset:53504\n"
    "ds_read_b128 a[108:111], v23 offset:18432\n"
    "v_mfma_f32_16x16x32_bf16 v[96:99], a[80:83], v[120:123], v[96:99]\n"
    "s_add_u32 m0, s90, 49152\n"
    "ds_read_b64_tr_b16 v[112:113], v20 offset:40992\n"
    "buffer_load_dwordx4 v6, s[40:43], s94 offen lds\n"
    "v_mfma_f32_16x16x32_bf16 v[96:99], a[88:91], v[124:127], v[96:99]\n"
    "ds_read_b64_tr_b16 v[114:115], v20 offset:43296\n"
    "v_mfma_f32_16x16x32_bf16 v[100:103], a[84:87], v[120:123], v[100:103]\n"
    "ds_read_b64_tr_b16 v[116:117], v20 offset:51232\n"
    "buffer_load_dwordx4 v7, s[40:43], s94 offen offset:1024 lds\n"
    "v_mfma_f32_16x16x32_bf16 v[100:103], a[92:95], v[124:127], v[100:103]\n"
    "s_waitcnt lgkmcnt(3)\n"
    "ds_read_b64_tr_b16 v[118:119], v20 offset:53536\n"
    "v_mfma_f32_16x16x32_bf16 v[32:35], a[96:99], v[104:107], v[32:35]\n"
    "ds_read_b64_tr_b16 v[120:121], v20 offset:41024\n"
    "buffer_load_dwordx4 v8, s[40:43], s94 offen offset:2048 lds\n"
    "v_mfma_f32_16x16x32_bf16 v[32:35], a[104:107], v[108:111], v[32:35]\n"
    "ds_read_b64_tr_b16 v[122:123], v20 offset:43328\n"
    "v_mfma_f32_16x16x32_bf16 v[36:39], a[100:103], v[104:107], v[36:39]\n"
    "ds_read_b64_tr_b16 v[124:125], v20 offset:51264\n"
    "buffer_load_dwordx4 v9, s[40:43], s94 offen offset:3072 lds\n"
    "v_mfma_f32_16x16x32_bf16 v[36:39], a[108:111], v[108:111], v[36:39]\n"
    "s_waitcnt lgkmcnt(3)\n"
    "ds_read_b64_tr_b16 v[126:127], v20 offset:53568\n"
    "v_mfma_f32_16x16x32_bf16 v[40:43], a[96:99], v[112:115], v[40:43]\n"
    "s_add_u32 s94, s94, 128\n"
    "s_cmp_eq_u32 s94, 1152\n"
    "s_cselect_b32 s94, 0, s94\n"
    "ds_read_b64_tr_b16 v[104:105], v20 offset:41056\n"
    "v_mfma_f32_16x16x32_bf16 v[40:43], a[104:107], v[116:119], v[40:43]\n"
    "ds_read_b64_tr_b16 v[106:107], v20 offset:43360\n"
    "v_mfma_f32_16x16x32_bf16 v[44:47], a[100:103], v[112:115], v[44:47]\n"
    "s_add_u32 s85, s72, 1152\n"
    "s_add_u32 m0, s89, 0\n"
    "ds_read_b64_tr_b16 v[108:109], v20 offset:51296\n"
    "buffer_load_dwordx4 v10, s[36:39], s85 offen lds\n"
    "v_mfma_f32_16x16x32_bf16 v[44:47], a[108:111], v[116:119], v[44:47]\n"
    "s_waitcnt lgkmcnt(3)\n"
    "ds_read_b64_tr_b16 v[110:111], v20 offset:53600\n"
    "v_mfma_f32_16x16x32_bf16 v[48:51], a[96:99], v[120:123], v[48:51]\n"
    "ds_read_b64_tr_b16 v[112:113], v20 offset:41088\n"
    "buffer_load_dwordx4 v11, s[36:39], s85 offen offset:1024 lds\n"
    "v_mfma_f32_16x16x32_bf16 v[48:51], a[104:107], v[124:127], v[48:51]\n"
    "ds_read_b64_tr_b16 v[114:115], v20 offset:43392\n"
    "v_mfma_f32_16x16x32_bf16 v[52:55], a[100:103], v[120:123], v[52:55]\n"
    "ds_read_b64_tr_b16 v[116:117], v20 offset:51328\n"
    "buffer_load_dwordx4 v12, s[36:39], s85 offen offset:2048 lds\n"
    "v_mfma_f32_16x16x32_bf16 v[52:55], a[108:111], v[124:127], v[52:55]\n"
    "s_waitcnt lgkmcnt(3)\n"
    "ds_read_b64_tr_b16 v[118:119], v20 offset:53632\n"
    "v_mfma_f32_16x16x32_bf16 v[56:59], a[96:99], v[104:107], v[56:59]\n"
    "ds_read_b64_tr_b16 v[120:121], v20 offset:41120\n"
    "buffer_load_dwordx4 v13, s[36:39], s85 offen offset:3072 lds\n"
    "v_mfma_f32_16x16x32_bf16 v[56:59], a[104:107], v[108:111], v[56:59]\n"
    "ds_read_b64_tr_b16 v[122:123], v20 offset:43424\n"
    "v_mfma_f32_16x16x32_bf16 v[60:63], a[100:103], v[104:107], v[60:63]\n"
    "s_add_u32 m0, s89, 4096\n"
    "ds_read_b64_tr_b16 v[124:125], v20 offset:51360\n"
    "buffer_load_dwordx4 v14, s[36:39], s85 offen lds\n"
    "v_mfma_f32_16x16x32_bf16 v[60:63], a[108:111], v[108:111], v[60:63]\n"
    "s_waitcnt lgkmcnt(3)\n"
    "ds_read_b64_tr_b16 v[126:127], v20 offset:53664\n"
    "v_mfma_f32_16x16x32_bf16 v[64:67], a[96:99], v[112:115], v[64:67]\n"
    "ds_read_b64_tr_b16 v[104:105], v20 offset:41152\n"
    "v_mfma_f32_16x16x32_bf16 v[64:67], a[104:107], v[116:119], v[64:67]\n"
    "ds_read_b64_tr_b16 v[106:107], v20 offset:43456\n"
    "v_mfma_f32_16x16x32_bf16 v[68:71], a[100:103], v[112:115], v[68:71]\n"
    "ds_read_b64_tr_b16 v[108:109], v20 offset:51392\n"
    "v_mfma_f32_16x16x32_bf16 v[68:71], a[108:111], v[116:119], v[68:71]\n"
    "s_waitcnt lgkmcnt(3)\n"
    "ds_read_b64_tr_b16 v[110:111], v20 offset:53696\n"
    "v_mfma_f32_16x16x32_bf16 v[72:75], a[96:99], v[120:123], v[72:75]\n"
    "ds_read_b64_tr_b16 v[112:113], v20 offset:41184\n"
    "v_mfma_f32_16x16x32_bf16 v[72:75], a[104:107], v[124:127], v[72:75]\n"
    "ds_read_b64_tr_b16 v[114:115], v20 offset:43488\n"
    "v_mfma_f32_16x16x32_bf16 v[76:79], a[100:103], v[120:123], v[76:79]\n"
    "ds_read_b64_tr_b16 v[116:117], v20 offset:51424\n"
    "v_mfma_f32_16x16x32_bf16 v[76:79], a[108:111], v[124:127], v[76:79]\n"
    "s_waitcnt lgkmcnt(3)\n"
    "ds_read_b64_tr_b16 v[118:119], v20 offset:53728\n"
    "v_mfma_f32_16x16x32_bf16 v[80:83], a[96:99], v[104:107], v[80:83]\n"
    "ds_read_b64_tr_b16 v[120:121], v20 offset:41216\n"
    "v_mfma_f32_16x16x32_bf16 v[80:83], a[104:107], v[108:111], v[80:83]\n"
    "ds_read_b64_tr_b16 v[122:123], v20 offset:43520\n"
    "v_mfma_f32_16x16x32_bf16 v[84:87], a[100:103], v[104:107], v[84:87]\n"
    "ds_read_b64_tr_b16 v[124:125], v20 offset:51456\n"
    "v_mfma_f32_16x16x32_bf16 v[84:87], a[108:111], v[108:111], v[84:87]\n"
    "s_waitcnt lgkmcnt(3)\n"
    "ds_read_b64_tr_b16 v[126:127], v20 offset:53760\n"
    "v_mfma_f32_16x16x32_bf16 v[88:91], a[96:99], v[112:115], v[88:91]\n"
    "s_waitcnt vmcnt(9)\n"
    "s_barrier\n"
    "ds_read_b64_tr_b16 v[104:105], v21 offset:0\n"
    "ds_read_b128 a[112:115], v22 offset:32768\n"
    "v_mfma_f32_16x16x32_bf16 v[88:91], a[104:107], v[116:119], v[88:91]\n"
    "ds_read_b64_tr_b16 v[106:107], v21 offset:2304\n"
    "ds_read_b128 a[116:119], v22 offset:34816\n"
    "v_mfma_f32_16x16x32_bf16 v[92:95], a[100:103], v[112:115], v[92:95]\n"
    "ds_read_b64_tr_b16 v[108:109], v21 offset:10240\n"
    "ds_read_b128 a[120:123], v23 offset:32768\n"
    "v_mfma_f32_16x16x32_bf16 v[92:95], a[108:111], v[116:119], v[92:95]\n"
    "s_waitcnt lgkmcnt(6)\n"
    "ds_read_b64_tr_b16 v[110:111], v21 offset:12544\n"
    "ds_read_b128 a[124:127], v23 offset:34816\n"
    "v_mfma_f32_16x16x32_bf16 v[96:99], a[96:99], v[120:123], v[96:99]\n"
    "s_add_u32 s85, s64, 2304\n"
    "s_add_u32 m0, s89, 20480\n"
    "ds_read_b64_tr_b16 v[112:113], v21 offset:32\n"
    "buffer_load_dwordx4 v10, s[36:39], s85 offen lds\n"
    "v_mfma_f32_16x16x32_bf16 v[96:99], a[104:107], v[124:127], v[96:99]\n"
    "ds_read_b64_tr_b16 v[114:115], v21 offset:2336\n"
    "v_mfma_f32_16x16x32_bf16 v[100:103], a[100:103], v[120:123], v[100:103]\n"
    "ds_read_b64_tr_b16 v[116:117], v21 offset:10272\n"
    "buffer_load_dwordx4 v11, s[36:39], s85 offen offset:1024 lds\n"
    "v_mfma_f32_16x16x32_bf16 v[100:103], a[108:111], v[124:127], v[100:103]\n"
    "s_waitcnt lgkmcnt(3)\n"
    "ds_read_b64_tr_b16 v[118:119], v21 offset:12576\n"
    "v_mfma_f32_16x16x32_bf16 v[32:35], a[112:115], v[104:107], v[32:35]\n"
    "ds_read_b64_tr_b16 v[120:121], v21 offset:64\n"
    "buffer_load_dwordx4 v12, s[36:39], s85 offen offset:2048 lds\n"
    "v_mfma_f32_16x16x32_bf16 v[32:35], a[120:123], v[108:111], v[32:35]\n"
    "ds_read_b64_tr_b16 v[122:123], v21 offset:2368\n"
    "v_mfma_f32_16x16x32_bf16 v[36:39], a[116:119], v[104:107], v[36:39]\n"
    "ds_read_b64_tr_b16 v[124:125], v21 offset:10304\n"
    "buffer_load_dwordx4 v13, s[36:39], s85 offen offset:3072 lds\n"
    "v_mfma_f32_16x16x32_bf16 v[36:39], a[124:127], v[108:111], v[36:39]\n"
    "s_waitcnt lgkmcnt(3)\n"
    "ds_read_b64_tr_b16 v[126:127], v21 offset:12608\n"
    "v_mfma_f32_16x16x32_bf16 v[40:43], a[112:115], v[112:115], v[40:43]\n"
    "s_add_u32 m0, s89, 24576\n"
    "ds_read_b64_tr_b16 v[104:105], v21 offset:96\n"
    "buffer_load_dwordx4 v14, s[36:39], s85 offen lds\n"
    "v_mfma_f32_16x16x32_bf16 v[40:43], a[120:123], v[116:119], v[40:43]\n"
    "ds_read_b64_tr_b16 v[106:107], v21 offset:2400\n"
    "v_mfma_f32_16x16x32_bf16 v[44:47], a[116:119], v[112:115], v[44:47]\n"
    "ds_read_b64_tr_b16 v[108:109], v21 offset:10336\n"
    "buffer_store_dwordx4 v[164:167], v168, s[44:47], s31 offen sc1\n"
    "v_mfma_f32_16x16x32_bf16 v[44:47], a[124:127], v[116:119], v[44:47]\n"
    "s_waitcnt lgkmcnt(3)\n"
    "ds_read_b64_tr_b16 v[110:111], v21 offset:12640\n"
    "v_mfma_f32_16x16x32_bf16 v[48:51], a[112:115], v[120:123], v[48:51]\n"
    "ds_read_b64_tr_b16 v[112:113], v21 offset:128\n"
    "buffer_store_dwordx4 v[188:191], v192, s[44:47], s97 offen sc1\n"
    "v_mfma_f32_16x16x32_bf16 v[48:51], a[120:123], v[124:127], v[48:51]\n"
    "ds_read_b64_tr_b16 v[114:115], v21 offset:2432\n"
    "v_mfma_f32_16x16x32_bf16 v[52:55], a[116:119], v[120:123], v[52:55]\n"
    "ds_read_b64_tr_b16 v[116:117], v21 offset:10368\n"
    "v_mfma_f32_16x16x32_bf16 v[52:55], a[124:127], v[124:127], v[52:55]\n"
    "s_waitcnt lgkmcnt(3)\n"
    "ds_read_b64_tr_b16 v[118:119], v21 offset:12672\n"
    "v_mfma_f32_16x16x32_bf16 v[56:59], a[112:115], v[104:107], v[56:59]\n"
    "ds_read_b64_tr_b16 v[120:121], v21 offset:160\n"
    "v_mfma_f32_16x16x32_bf16 v[56:59], a[120:123], v[108:111], v[56:59]\n"
    "ds_read_b64_tr_b16 v[122:123], v21 offset:2464\n"
    "v_mfma_f32_16x16x32_bf16 v[60:63], a[116:119], v[104:107], v[60:63]\n"
    "ds_read_b64_tr_b16 v[124:125], v21 offset:10400\n"
    "v_mfma_f32_16x16x32_bf16 v[60:63], a[124:127], v[108:111], v[60:63]\n"
    "s_waitcnt lgkmcnt(3)\n"
    "ds_read_b64_tr_b16 v[126:127], v21 offset:12704\n"
    "v_mfma_f32_16x16x32_bf16 v[64:67], a[112:115], v[112:115], v[64:67]\n"
    "ds_read_b64_tr_b16 v[104:105], v21 offset:192\n"
    "v_mfma_f32_16x16x32_bf16 v[64:67], a[120:123], v[116:119], v[64:67]\n"
    "ds_read_b64_tr_b16 v[106:107], v21 offset:2496\n"
    "v_mfma_f32_16x16x32_bf16 v[68:71], a[116:119], v[112:115], v[68:71]\n"
    "ds_read_b64_tr_b16 v[108:109], v21 offset:10432\n"
    "v_mfma_f32_16x16x32_bf16 v[68:71], a[124:127], v[116:119], v[68:71]\n"
    "s_waitcnt lgkmcnt(3)\n"
    "ds_read_b64_tr_b16 v[110:111], v21 offset:12736\n"
    "v_mfma_f32_16x16x32_bf16 v[72:75], a[112:115], v[120:123], v[72:75]\n"
    "ds_read_b64_tr_b16 v[112:113], v21 offset:224\n"
    "v_mfma_f32_16x16x32_bf16 v[72:75], a[120:123], v[124:127], v[72:75]\n"
    "ds_read_b64_tr_b16 v[114:115], v21 offset:2528\n"
    "v_mfma_f32_16x16x32_bf16 v[76:79], a[116:119], v[120:123], v[76:79]\n"
    "ds_read_b64_tr_b16 v[116:117], v21 offset:10464\n"
    "v_mfma_f32_16x16x32_bf16 v[76:79], a[124:127], v[124:127], v[76:79]\n"
    "s_waitcnt lgkmcnt(3)\n"
    "ds_read_b64_tr_b16 v[118:119], v21 offset:12768\n"
    "v_mfma_f32_16x16x32_bf16 v[80:83], a[112:115], v[104:107], v[80:83]\n"
    "ds_read_b64_tr_b16 v[120:121], v21 offset:256\n"
    "v_mfma_f32_16x16x32_bf16 v[80:83], a[120:123], v[108:111], v[80:83]\n"
    "ds_read_b64_tr_b16 v[122:123], v21 offset:2560\n"
    "v_mfma_f32_16x16x32_bf16 v[84:87], a[116:119], v[104:107], v[84:87]\n"
    "ds_read_b64_tr_b16 v[124:125], v21 offset:10496\n"
    "v_mfma_f32_16x16x32_bf16 v[84:87], a[124:127], v[108:111], v[84:87]\n"
    "s_waitcnt lgkmcnt(3)\n"
    "ds_read_b64_tr_b16 v[126:127], v21 offset:12800\n"
    "v_mfma_f32_16x16x32_bf16 v[88:91], a[112:115], v[112:115], v[88:91]\n"
    "s_waitcnt vmcnt(7)\n"
    "s_barrier\n"
    "ds_read_b64_tr_b16 v[104:105], v20 offset:0\n"
    "ds_read_b128 a[128:131], v22 offset:49152\n"
    "v_mfma_f32_16x16x32_bf16 v[88:91], a[120:123], v[116:119], v[88:91]\n"
    "ds_read_b64_tr_b16 v[106:107], v20 offset:2304\n"
    "ds_read_b128 a[132:135], v22 offset:51200\n"
    "v_mfma_f32_16x16x32_bf16 v[92:95], a[116:119], v[112:115], v[92:95]\n"
    "ds_read_b64_tr_b16 v[108:109], v20 offset:10240\n"
    "ds_read_b128 a[136:139], v23 offset:49152\n"
    "v_mfma_f32_16x16x32_bf16 v[92:95], a[124:127], v[116:119], v[92:95]\n"
    "s_waitcnt lgkmcnt(6)\n"
    "ds_read_b64_tr_b16 v[110:111], v20 offset:12544\n"
    "ds_read_b128 a[140:143], v23 offset:51200\n"
    "v_mfma_f32_16x16x32_bf16 v[96:99], a[112:115], v[120:123], v[96:99]\n"
    "s_add_u32 s85, s65, 2304\n"
    "s_add_u32 m0, s89, 40960\n"
    "ds_read_b64_tr_b16 v[112:113], v20 offset:32\n"
    "buffer_load_dwordx4 v10, s[36:39], s85 offen lds\n"
    "v_mfma_f32_16x16x32_bf16 v[96:99], a[120:123], v[124:127], v[96:99]\n"
    "ds_read_b64_tr_b16 v[114:115], v20 offset:2336\n"
    "v_mfma_f32_16x16x32_bf16 v[100:103], a[116:119], v[120:123], v[100:103]\n"
    "ds_read_b64_tr_b16 v[116:117], v20 offset:10272\n"
    "buffer_load_dwordx4 v11, s[36:39], s85 offen offset:1024 lds\n"
    "v_mfma_f32_16x16x32_bf16 v[100:103], a[124:127], v[124:127], v[100:103]\n"
    "s_waitcnt lgkmcnt(3)\n"
    "ds_read_b64_tr_b16 v[118:119], v20 offset:12576\n"
    "v_mfma_f32_16x16x32_bf16 v[32:35], a[128:131], v[104:107], v[32:35]\n"
    "ds_read_b64_tr_b16 v[120:121], v20 offset:64\n"
    "buffer_load_dwordx4 v12, s[36:39], s85 offen offset:2048 lds\n"
    "v_mfma_f32_16x16x32_bf16 v[32:35], a[136:139], v[108:111], v[32:35]\n"
    "ds_read_b64_tr_b16 v[122:123], v20 offset:2368\n"
    "v_mfma_f32_16x16x32_bf16 v[36:39], a[132:135], v[104:107], v[36:39]\n"
    "ds_read_b64_tr_b16 v[124:125], v20 offset:10304\n"
    "buffer_load_dwordx4 v13, s[36:39], s85 offen offset:3072 lds\n"
    "v_mfma_f32_16x16x32_bf16 v[36:39], a[140:143], v[108:111], v[36:39]\n"
    "s_waitcnt lgkmcnt(3)\n"
    "ds_read_b64_tr_b16 v[126:127], v20 offset:12608\n"
    "v_mfma_f32_16x16x32_bf16 v[40:43], a[128:131], v[112:115], v[40:43]\n"
    "s_add_u32 m0, s89, 45056\n"
    "ds_read_b64_tr_b16 v[104:105], v20 offset:96\n"
    "buffer_load_dwordx4 v14, s[36:39], s85 offen lds\n"
    "v_mfma_f32_16x16x32_bf16 v[40:43], a[136:139], v[116:119], v[40:43]\n"
    "ds_read_b64_tr_b16 v[106:107], v20 offset:2400\n"
    "v_mfma_f32_16x16x32_bf16 v[44:47], a[132:135], v[112:115], v[44:47]\n"
    "ds_read_b64_tr_b16 v[108:109], v20 offset:10336\n"
    "v_mfma_f32_16x16x32_bf16 v[44:47], a[140:143], v[116:119], v[44:47]\n"
    "s_waitcnt lgkmcnt(3)\n"
    "ds_read_b64_tr_b16 v[110:111], v20 offset:12640\n"
    "v_mfma_f32_16x16x32_bf16 v[48:51], a[128:131], v[120:123], v[48:51]\n"
    "ds_read_b64_tr_b16 v[112:113], v20 offset:128\n"
    "v_mfma_f32_16x16x32_bf16 v[48:51], a[136:139], v[124:127], v[48:51]\n"
    "ds_read_b64_tr_b16 v[114:115], v20 offset:2432\n"
    "v_mfma_f32_16x16x32_bf16 v[52:55], a[132:135], v[120:123], v[52:55]\n"
    "ds_read_b64_tr_b16 v[116:117], v20 offset:10368\n"
    "v_mfma_f32_16x16x32_bf16 v[52:55], a[140:143], v[124:127], v[52:55]\n"
    "s_waitcnt lgkmcnt(3)\n"
    "ds_read_b64_tr_b16 v[118:119], v20 offset:12672\n"
    "v_mfma_f32_16x16x32_bf16 v[56:59], a[128:131], v[104:107], v[56:59]\n"
    "ds_read_b64_tr_b16 v[120:121], v20 offset:160\n"
    "v_mfma_f32_16x16x32_bf16 v[56:59], a[136:139], v[108:111], v[56:59]\n"
    "ds_read_b64_tr_b16 v[122:123], v20 offset:2464\n"
    "v_mfma_f32_16x16x32_bf16 v[60:63], a[132:135], v[104:107], v[60:63]\n"
    "ds_read_b64_tr_b16 v[124:125], v20 offset:10400\n"
    "v_mfma_f32_16x16x32_bf16 v[60:63], a[140:143], v[108:111], v[60:63]\n"
    "s_waitcnt lgkmcnt(3)\n"
    "ds_read_b64_tr_b16 v[126:127], v20 offset:12704\n"
    "v_mfma_f32_16x16x32_bf16 v[64:67], a[128:131], v[112:115], v[64:67]\n"
    "ds_read_b64_tr_b16 v[104:105], v20 offset:192\n"
    "v_mfma_f32_16x16x32_bf16 v[64:67], a[136:139], v[116:119], v[64:67]\n"
    "ds_read_b64_tr_b16 v[106:107], v20 offset:2496\n"
    "v_mfma_f32_16x16x32_bf16 v[68:71], a[132:135], v[112:115], v[68:71]\n"
    "ds_read_b64_tr_b16 v[108:109], v20 offset:10432\n"
    "v_mfma_f32_16x16x32_bf16 v[68:71], a[140:143], v[116:119], v[68:71]\n"
    "s_waitcnt lgkmcnt(3)\n"
    "ds_read_b64_tr_b16 v[110:111], v20 offset:12736\n"
    "v_mfma_f32_16x16x32_bf16 v[72:75], a[128:131], v[120:123], v[72:75]\n"
    "ds_read_b64_tr_b16 v[112:113], v20 offset:224\n"
    "v_mfma_f32_16x16x32_bf16 v[72:75], a[136:139], v[124:127], v[72:75]\n"
    "ds_read_b64_tr_b16 v[114:115], v20 offset:2528\n"
    "v_mfma_f32_16x16x32_bf16 v[76:79], a[132:135], v[120:123], v[76:79]\n"
    "ds_read_b64_tr_b16 v[116:117], v20 offset:10464\n"
    "v_mfma_f32_16x16x32_bf16 v[76:79], a[140:143], v[124:127], v[76:79]\n"
    "s_waitcnt lgkmcnt(3)\n"
    "ds_read_b64_tr_b16 v[118:119], v20 offset:12768\n"
    "v_mfma_f32_16x16x32_bf16 v[80:83], a[128:131], v[104:107], v[80:83]\n"
    "ds_read_b64_tr_b16 v[120:121], v20 offset:256\n"
    "v_mfma_f32_16x16x32_bf16 v[80:83], a[136:139], v[108:111], v[80:83]\n"
    "ds_read_b64_tr_b16 v[122:123], v20 offset:2560\n"
    "v_mfma_f32_16x16x32_bf16 v[84:87], a[132:135], v[104:107], v[84:87]\n"
    "ds_read_b64_tr_b16 v[124:125], v20 offset:10496\n"
    "v_mfma_f32_16x16x32_bf16 v[84:87], a[140:143], v[108:111], v[84:87]\n"
    "s_waitcnt lgkmcnt(3)\n"
    "ds_read_b64_tr_b16 v[126:127], v20 offset:12800\n"
    "v_mfma_f32_16x16x32_bf16 v[88:91], a[128:131], v[112:115], v[88:91]\n"
    "s_waitcnt vmcnt(7)\n"
    "s_barrier\n"
    "ds_read_b64_tr_b16 v[104:105], v20 offset:20480\n"
    "v_mfma_f32_16x16x32_bf16 v[88:91], a[136:139], v[116:119], v[88:91]\n"
    "ds_read_b64_tr_b16 v[106:107], v20 offset:22784\n"
    "v_mfma_f32_16x16x32_bf16 v[92:95], a[132:135], v[112:115], v[92:95]\n"
    "s_add_u32 s85, s66, 2304\n"
    "s_add_u32 m0, s89, 61440\n"
    "ds_read_b64_tr_b16 v[108:109], v20 offset:30720\n"
    "buffer_load_dwordx4 v10, s[36:39], s85 offen lds\n"
    "v_mfma_f32_16x16x32_bf16 v[92:95], a[140:143], v[116:119], v[92:95]\n"
    "s_waitcnt lgkmcnt(3)\n"
    "ds_read_b64_tr_b16 v[110:111], v20 offset:33024\n"
    "v_mfma_f32_16x16x32_bf16 v[96:99], a[128:131], v[120:123], v[96:99]\n"
    "ds_read_b64_tr_b16 v[112:113], v20 offset:20512\n"
    "buffer_load_dwordx4 v11, s[36:39], s85 offen offset:1024 lds\n"
    "v_mfma_f32_16x16x32_bf16 v[96:99], a[136:139], v[124:127], v[96:99]\n"
    "ds_read_b64_tr_b16 v[114:115], v20 offset:22816\n"
    "v_mfma_f32_16x16x32_bf16 v[100:103], a[132:135], v[120:123], v[100:103]\n"
    "ds_read_b64_tr_b16 v[116:117], v20 offset:30752\n"
    "buffer_load_dwordx4 v12, s[36:39], s85 offen offset:2048 lds\n"
    "v_mfma_f32_16x16x32_bf16 v[100:103], a[140:143], v[124:127], v[100:103]\n"
    "s_waitcnt lgkmcnt(3)\n"
    "ds_read_b64_tr_b16 v[118:119], v20 offset:33056\n"
    "v_mfma_f32_16x16x32_bf16 v[176:179], a[0:3], v[104:107], 0\n"
    "ds_read_b64_tr_b16 v[120:121], v20 offset:20544\n"
    "buffer_load_dwordx4 v13, s[36:39], s85 offen offset:3072 lds\n"
    "v_mfma_f32_16x16x32_bf16 v[176:179], a[8:11], v[108:111], v[176:179]\n"
    "ds_read_b64_tr_b16 v[122:123], v20 offset:22848\n"
    "v_mfma_f32_16x16x32_bf16 v[180:183], a[4:7], v[104:107], 0\n"
    "s_add_u32 m0, s89, 65536\n"
    "ds_read_b64_tr_b16 v[124:125], v20 offset:30784\n"
    "buffer_load_dwordx4 v14, s[36:39], s85 offen lds\n"
    "v_mfma_f32_16x16x32_bf16 v[180:183], a[12:15], v[108:111], v[180:183]\n"
    "s_waitcnt lgkmcnt(3)\n"
    "ds_read_b64_tr_b16 v[126:127], v20 offset:33088\n"
    "v_mfma_f32_16x16x32_bf16 v[184:187], a[0:3], v[112:115], 0\n"
    "ds_read_b64_tr_b16 v[104:105], v20 offset:20576\n"
    "v_mfma_f32_16x16x32_bf16 v[184:187], a[8:11], v[116:119], v[184:187]\n"
    "ds_read_b64_tr_b16 v[106:107], v20 offset:22880\n"
    "v_mul_f32 v128, 0x3fb8aa3b, v32\n"
    "v_mfma_f32_16x16x32_bf16 v[188:191], a[4:7], v[112:115], 0\n"
    "ds_read_b64_tr_b16 v[108:109], v20 offset:30816\n"
    "v_max_f32 v32, s87, v32\n"
    "v_mfma_f32_16x16x32_bf16 v[188:191], a[12:15], v[116:119], v[188:191]\n"
    "s_waitcnt lgkmcnt(3)\n"
    "ds_read_b64_tr_b16 v[110:111], v20 offset:33120\n"
    "v_mul_f32 v129, 0x3fb8aa3b, v33\n"
    "v_mfma_f32_16x16x32_bf16 v[192:195], a[0:3], v[120:123], 0\n"
    "ds_read_b64_tr_b16 v[112:113], v20 offset:20608\n"
    "v_exp_f32 v128, v128 clamp\n"
    "v_max_f32 v33, s87, v33\n"
    "v_mfma_f32_16x16x32_bf16 v[192:195], a[8:11], v[124:127], v[192:195]\n"
    "ds_read_b64_tr_b16 v[114:115], v20 offset:22912\n"
    "v_mul_f32 v130, 0x3fb8aa3b, v34\n"
    "v_mfma_f32_16x16x32_bf16 v[196:199], a[4:7], v[120:123], 0\n"
    "ds_read_b64_tr_b16 v[116:117], v20 offset:30848\n"
    "v_exp_f32 v129, v129 clamp\n"
    "v_mfma_f32_16x16x32_bf16 v[196:199], a[12:15], v[124:127], v[196:199]\n"
    "s_waitcnt lgkmcnt(3)\n"
    "ds_read_b64_tr_b16 v[118:119], v20 offset:33152\n"
    "v_max_f32 v34, s87, v34\n"
    "v_add_f32 v32, v32, v128\n"
    "v_mfma_f32_16x16x32_bf16 v[200:203], a[0:3], v[104:107], 0\n"
    "ds_read_b64_tr_b16 v[120:121], v20 offset:20640\n"
    "v_mul_f32 v128, 0x3fb8aa3b, v35\n"
    "v_mfma_f32_16x16x32_bf16 v[200:203], a[8:11], v[108:111], v[200:203]\n"
    "ds_read_b64_tr_b16 v[122:123], v20 offset:22944\n"
    "v_exp_f32 v130, v130 clamp\n"
    "v_mfma_f32_16x16x32_bf16 v[204:207], a[4:7], v[104:107], 0\n"
    "ds_read_b64_tr_b16 v[124:125], v20 offset:30880\n"
    "v_max_f32 v35, s87, v35\n"
    "v_add_f32 v33, v33, v129\n"
    "v_mfma_f32_16x16x32_bf16 v[204:207], a[12:15], v[108:111], v[204:207]\n"
    "s_waitcnt lgkmcnt(3)\n"
    "ds_read_b64_tr_b16 v[126:127], v20 offset:33184\n"
    "v_mul_f32 v129, 0x3fb8aa3b, v36\n"
    "v_mfma_f32_16x16x32_bf16 v[208:211], a[0:3], v[112:115], 0\n"
    "ds_read_b64_tr_b16 v[104:105], v20 offset:20672\n"
    "v_exp_f32 v128, v128 clamp\n"
    "v_mfma_f32_16x16x32_bf16 v[208:211], a[8:11], v[116:119], v[208:211]\n"
    "ds_read_b64_tr_b16 v[106:107], v20 offset:22976\n"
    "v_max_f32 v36, s87, v36\n"
    "v_mfma_f32_16x16x32_bf16 v[212:215], a[4:7], v[112:115], 0\n"
    "ds_read_b64_tr_b16 v[108:109], v20 offset:30912\n"
    "v_add_f32 v34, v34, v130\n"
    "v_cvt_pk_bf16_f32 v144, v32, v33\n"
    "v_mfma_f32_16x16x32_bf16 v[212:215], a[12:15], v[116:119], v[212:215]\n"
    "s_waitcnt lgkmcnt(3)\n"
    "ds_read_b64_tr_b16 v[110:111], v20 offset:33216\n"
    "v_mul_f32 v130, 0x3fb8aa3b, v37\n"
    "v_mfma_f32_16x16x32_bf16 v[216:219], a[0:3], v[120:123], 0\n"
    "ds_read_b64_tr_b16 v[112:113], v20 offset:20704\n"
    "v_exp_f32 v129, v129 clamp\n"
    "v_mfma_f32_16x16x32_bf16 v[216:219], a[8:11], v[124:127], v[216:219]\n"
    "ds_read_b64_tr_b16 v[114:115], v20 offset:23008\n"
    "v_max_f32 v37, s87, v37\n"
    "v_add_f32 v35, v35, v128\n"
    "v_mfma_f32_16x16x32_bf16 v[220:223], a[4:7], v[120:123], 0\n"
    "ds_read_b64_tr_b16 v[116:117], v20 offset:30944\n"
    "v_mul_f32 v128, 0x3fb8aa3b, v38\n"
    "v_mfma_f32_16x16x32_bf16 v[220:223], a[12:15], v[124:127], v[220:223]\n"
    "s_waitcnt lgkmcnt(3)\n"
    "ds_read_b64_tr_b16 v[118:119], v20 offset:33248\n"
    "v_exp_f32 v130, v130 clamp\n"
    "v_mfma_f32_16x16x32_bf16 v[224:227], a[0:3], v[104:107], 0\n"
    "ds_read_b64_tr_b16 v[120:121], v20 offset:20736\n"
    "v_max_f32 v38, s87, v38\n"
    "v_add_f32 v36, v36, v129\n"
    "v_mfma_f32_16x16x32_bf16 v[224:227], a[8:11], v[108:111], v[224:227]\n"
    "ds_read_b64_tr_b16 v[122:123], v20 offset:23040\n"
    "v_cvt_pk_bf16_f32 v145, v34, v35\n"
    "v_mfma_f32_16x16x32_bf16 v[228:231], a[4:7], v[104:107], 0\n"
    "ds_read_b64_tr_b16 v[124:125], v20 offset:30976\n"
    "ds_write_b64 v26, v[144:145] offset:0\n"
    "v_mfma_f32_16x16x32_bf16 v[228:231], a[12:15], v[108:111], v[228:231]\n"
    "s_waitcnt lgkmcnt(4)\n"
    "ds_read_b64_tr_b16 v[126:127], v20 offset:33280\n"
    "v_mul_f32 v129, 0x3fb8aa3b, v39\n"
    "v_exp_f32 v128, v128 clamp\n"
    "v_mfma_f32_16x16x32_bf16 v[232:235], a[0:3], v[112:115], 0\n"
    "s_waitcnt vmcnt(5)\n"
    "s_barrier\n"
    "ds_read_b64_tr_b16 v[104:105], v20 offset:40960\n"
    "v_mfma_f32_16x16x32_bf16 v[232:235], a[8:11], v[116:119], v[232:235]\n"
    "ds_read_b64_tr_b16 v[106:107], v20 offset:43264\n"
    "v_max_f32 v39, s87, v39\n"
    "v_mfma_f32_16x16x32_bf16 v[236:239], a[4:7], v[112:115], 0\n"
    "s_add_u32 s85, s67, 2304\n"
    "s_add_u32 m0, s89, 0\n"
    "ds_read_b64_tr_b16 v[108:109], v20 offset:51200\n"
    "buffer_load_dwordx4 v10, s[36:39], s85 offen lds\n"
    "v_mfma_f32_16x16x32_bf16 v[236:239], a[12:15], v[116:119], v[236:239]\n"
    "s_waitcnt lgkmcnt(3)\n"
    "ds_read_b64_tr_b16 v[110:111], v20 offset:53504\n"
    "v_add_f32 v37, v37, v130\n"
    "v_mfma_f32_16x16x32_bf16 v[240:243], a[0:3], v[120:123], 0\n"
    "ds_read_b64_tr_b16 v[112:113], v20 offset:40992\n"
    "buffer_load_dwordx4 v11, s[36:39], s85 offen offset:1024 lds\n"
    "v_mfma_f32_16x16x32_bf16 v[240:243], a[8:11], v[124:127], v[240:243]\n"
    "ds_read_b64_tr_b16 v[114:115], v20 offset:43296\n"
    "v_mul_f32 v130, 0x3fb8aa3b, v40\n"
    "v_mfma_f32_16x16x32_bf16 v[244:247], a[4:7], v[120:123], 0\n"
    "ds_read_b64_tr_b16 v[116:117], v20 offset:51232\n"
    "buffer_load_dwordx4 v12, s[36:39], s85 offen offset:2048 lds\n"
    "v_mfma_f32_16x16x32_bf16 v[244:247], a[12:15], v[124:127], v[244:247]\n"
    "s_waitcnt lgkmcnt(3)\n"
    "ds_read_b64_tr_b16 v[118:119], v20 offset:53536\n"
    "v_exp_f32 v129, v129 clamp\n"
    "v_max_f32 v40, s87, v40\n"
    "v_mfma_f32_16x16x32_bf16 v[176:179], a[16:19], v[104:107], v[176:179]\n"
    "ds_read_b64_tr_b16 v[120:121], v20 offset:41024\n"
    "buffer_load_dwordx4 v13, s[36:39], s85 offen offset:3072 lds\n"
    "v_mfma_f32_16x16x32_bf16 v[176:179], a[24:27], v[108:111], v[176:179]\n"
    "ds_read_b64_tr_b16 v[122:123], v20 offset:43328\n"
    "v_add_f32 v38, v38, v128\n"
    "v_mfma_f32_16x16x32_bf16 v[180:183], a[20:23], v[104:107], v[180:183]\n"
    "s_add_u32 m0, s89, 4096\n"
    "ds_read_b64_tr_b16 v[124:125], v20 offset:51264\n"
    "buffer_load_dwordx4 v14, s[36:39], s85 offen lds\n"
    "v_mfma_f32_16x16x32_bf16 v[180:183], a[28:31], v[108:111], v[180:183]\n"
    "s_waitcnt lgkmcnt(3)\n"
    "ds_read_b64_tr_b16 v[126:127], v20 offset:53568\n"
    "v_cvt_pk_bf16_f32 v146, v36, v37\n"
    "v_mfma_f32_16x16x32_bf16 v[184:187], a[16:19], v[112:115], v[184:187]\n"
    "ds_read_b64_tr_b16 v[104:105], v20 offset:41056\n"
    "v_mul_f32 v128, 0x3fb8aa3b, v41\n"
    "v_exp_f32 v130, v130 clamp\n"
    "v_mfma_f32_16x16x32_bf16 v[184:187], a[24:27], v[116:119], v[184:187]\n"
    "ds_read_b64_tr_b16 v[106:107], v20 offset:43360\n"
    "v_max_f32 v41, s87, v41\n"
    "v_mfma_f32_16x16x32_bf16 v[188:191], a[20:23], v[112:115], v[188:191]\n"
    "ds_read_b64_tr_b16 v[108:109], v20 offset:51296\n"
    "v_add_f32 v39, v39, v129\n"
    "v_mfma_f32_16x16x32_bf16 v[188:191], a[28:31], v[116:119], v[188:191]\n"
    "s_waitcnt lgkmcnt(3)\n"
    "ds_read_b64_tr_b16 v[110:111], v20 offset:53600\n"
    "v_mul_f32 v129, 0x3fb8aa3b, v42\n"
    "v_exp_f32 v128, v128 clamp\n"
    "v_mfma_f32_16x16x32_bf16 v[192:195], a[16:19], v[120:123], v[192:195]\n"
    "ds_read_b64_tr_b16 v[112:113], v20 offset:41088\n"
    "v_max_f32 v42, s87, v42\n"
    "v_mfma_f32_16x16x32_bf16 v[192:195], a[24:27], v[124:127], v[192:195]\n"
    "ds_read_b64_tr_b16 v[114:115], v20 offset:43392\n"
    "v_add_f32 v40, v40, v130\n"
    "v_mfma_f32_16x16x32_bf16 v[196:199], a[20:23], v[120:123], v[196:199]\n"
    "ds_read_b64_tr_b16 v[116:117], v20 offset:51328\n"
    "v_cvt_pk_bf16_f32 v147, v38, v39\n"
    "v_mfma_f32_16x16x32_bf16 v[196:199], a[28:31], v[124:127], v[196:199]\n"
    "s_waitcnt lgkmcnt(3)\n"
    "ds_read_b64_tr_b16 v[118:119], v20 offset:53632\n"
    "ds_write_b64 v26, v[146:147] offset:8\n"
    "v_mul_f32 v130, 0x3fb8aa3b, v43\n"
    "v_mfma_f32_16x16x32_bf16 v[200:203], a[16:19], v[104:107], v[200:203]\n"
    "ds_read_b64_tr_b16 v[120:121], v20 offset:41120\n"
    "v_exp_f32 v129, v129 clamp\n"
    "v_mfma_f32_16x16x32_bf16 v[200:203], a[24:27], v[108:111], v[200:203]\n"
    "ds_read_b64_tr_b16 v[122:123], v20 offset:43424\n"
    "v_max_f32 v43, s87, v43\n"
    "v_mfma_f32_16x16x32_bf16 v[204:207], a[20:23], v[104:107], v[204:207]\n"
    "ds_read_b64_tr_b16 v[124:125], v20 offset:51360\n"
    "v_add_f32 v41, v41, v128\n"
    "v_mul_f32 v128, 0x3fb8aa3b, v44\n"
    "v_mfma_f32_16x16x32_bf16 v[204:207], a[28:31], v[108:111], v[204:207]\n"
    "s_waitcnt lgkmcnt(4)\n"
    "ds_read_b64_tr_b16 v[126:127], v20 offset:53664\n"
    "v_exp_f32 v130, v130 clamp\n"
    "v_mfma_f32_16x16x32_bf16 v[208:211], a[16:19], v[112:115], v[208:211]\n"
    "ds_read_b64_tr_b16 v[104:105], v20 offset:41152\n"
    "v_max_f32 v44, s87, v44\n"
    "v_mfma_f32_16x16x32_bf16 v[208:211], a[24:27], v[116:119], v[208:211]\n"
    "ds_read_b64_tr_b16 v[106:107], v20 offset:43456\n"
    "v_add_f32 v42, v42, v129\n"
    "v_cvt_pk_bf16_f32 v148, v40, v41\n"
    "v_mfma_f32_16x16x32_bf16 v[212:215], a[20:23], v[112:115], v[212:215]\n"
    "ds_read_b64_tr_b16 v[108:109], v20 offset:51392\n"
    "v_mul_f32 v129, 0x3fb8aa3b, v45\n"
    "v_mfma_f32_16x16x32_bf16 v[212:215], a[28:31], v[116:119], v[212:215]\n"
    "s_waitcnt lgkmcnt(3)\n"
    "ds_read_b64_tr_b16 v[110:111], v20 offset:53696\n"
    "v_exp_f32 v128, v128 clamp\n"
    "v_mfma_f32_16x16x32_bf16 v[216:219], a[16:19], v[120:123], v[216:219]\n"
    "ds_read_b64_tr_b16 v[112:113], v20 offset:41184\n"
    "v_max_f32 v45, s87, v45\n"
    "v_add_f32 v43, v43, v130\n"
    "v_mfma_f32_16x16x32_bf16 v[216:219], a[24:27], v[124:127], v[216:219]\n"
    "ds_read_b64_tr_b16 v[114:115], v20 offset:43488\n"
    "v_mul_f32 v130, 0x3fb8aa3b, v46\n"
    "v_mfma_f32_16x16x32_bf16 v[220:223], a[20:23], v[120:123], v[220:223]\n"
    "ds_read_b64_tr_b16 v[116:117], v20 offset:51424\n"
    "v_exp_f32 v129, v129 clamp\n"
    "v_mfma_f32_16x16x32_bf16 v[220:223], a[28:31], v[124:127], v[220:223]\n"
    "s_waitcnt lgkmcnt(3)\n"
    "ds_read_b64_tr_b16 v[118:119], v20 offset:53728\n"
    "v_max_f32 v46, s87, v46\n"
    "v_mfma_f32_16x16x32_bf16 v[224:227], a[16:19], v[104:107], v[224:227]\n"
    "ds_read_b64_tr_b16 v[120:121], v20 offset:41216\n"
    "v_add_f32 v44, v44, v128\n"
    "v_cvt_pk_bf16_f32 v149, v42, v43\n"
    "v_mfma_f32_16x16x32_bf16 v[224:227], a[24:27], v[108:111], v[224:227]\n"
    "ds_read_b64_tr_b16 v[122:123], v20 offset:43520\n"
    "ds_write_b64 v26, v[148:149] offset:1024\n"
    "v_mfma_f32_16x16x32_bf16 v[228:231], a[20:23], v[104:107], v[228:231]\n"
    "ds_read_b64_tr_b16 v[124:125], v20 offset:51456\n"
    "v_mul_f32 v128, 0x3fb8aa3b, v47\n"
    "v_mfma_f32_16x16x32_bf16 v[228:231], a[28:31], v[108:111], v[228:231]\n"
    "s_waitcnt lgkmcnt(4)\n"
    "ds_read_b64_tr_b16 v[126:127], v20 offset:53760\n"
    "v_exp_f32 v130, v130 clamp\n"
    "v_max_f32 v47, s87, v47\n"
    "v_mfma_f32_16x16x32_bf16 v[232:235], a[16:19], v[112:115], v[232:235]\n"
    "s_waitcnt vmcnt(5)\n"
    "s_barrier\n"
    "ds_read_b64_tr_b16 v[104:105], v21 offset:0\n"
    "v_mfma_f32_16x16x32_bf16 v[232:235], a[24:27], v[116:119], v[232:235]\n"
    "ds_read_b64_tr_b16 v[106:107], v21 offset:2304\n"
    "v_add_f32 v45, v45, v129\n"
    "v_mfma_f32_16x16x32_bf16 v[236:239], a[20:23], v[112:115], v[236:239]\n"
    "s_add_u32 s85, s68, 2304\n"
    "s_add_u32 m0, s89, 20480\n"
    "ds_read_b64_tr_b16 v[108:109], v21 offset:10240\n"
    "buffer_load_dwordx4 v10, s[36:39], s85 offen lds\n"
    "v_mfma_f32_16x16x32_bf16 v[236:239], a[28:31], v[116:119], v[236:239]\n"
    "s_waitcnt lgkmcnt(3)\n"
    "ds_read_b64_tr_b16 v[110:111], v21 offset:12544\n"
    "v_mul_f32 v129, 0x3fb8aa3b, v48\n"
    "v_mfma_f32_16x16x32_bf16 v[240:243], a[16:19], v[120:123], v[240:243]\n"
    "ds_read_b64_tr_b16 v[112:113], v21 offset:32\n"
    "buffer_load_dwordx4 v11, s[36:39], s85 offen offset:1024 lds\n"
    "v_mfma_f32_16x16x32_bf16 v[240:243], a[24:27], v[124:127], v[240:243]\n"
    "ds_read_b64_tr_b16 v[114:115], v21 offset:2336\n"
    "v_exp_f32 v128, v128 clamp\n"
    "v_max_f32 v48, s87, v48\n"
    "v_mfma_f32_16x16x32_bf16 v[244:247], a[20:23], v[120:123], v[244:247]\n"
    "ds_read_b64_tr_b16 v[116:117], v21 offset:10272\n"
    "buffer_load_dwordx4 v12, s[36:39], s85 offen offset:2048 lds\n"
    "v_mfma_f32_16x16x32_bf16 v[244:247], a[28:31], v[124:127], v[244:247]\n"
    "s_waitcnt lgkmcnt(3)\n"
    "ds_read_b64_tr_b16 v[118:119], v21 offset:12576\n"
    "v_add_f32 v46, v46, v130\n"
    "v_mfma_f32_16x16x32_bf16 v[176:179], a[32:35], v[104:107], v[176:179]\n"
    "ds_read_b64_tr_b16 v[120:121], v21 offset:64\n"
    "buffer_load_dwordx4 v13, s[36:39], s85 offen offset:3072 lds\n"
    "v_mfma_f32_16x16x32_bf16 v[176:179], a[40:43], v[108:111], v[176:179]\n"
    "ds_read_b64_tr_b16 v[122:123], v21 offset:2368\n"
    "v_cvt_pk_bf16_f32 v150, v44, v45\n"
    "v_mfma_f32_16x16x32_bf16 v[180:183], a[36:39], v[104:107], v[180:183]\n"
    "s_add_u32 m0, s89, 24576\n"
    "ds_read_b64_tr_b16 v[124:125], v21 offset:10304\n"
    "buffer_load_dwordx4 v14, s[36:39], s85 offen lds\n"
    "v_mfma_f32_16x16x32_bf16 v[180:183], a[44:47], v[108:111], v[180:183]\n"
    "s_waitcnt lgkmcnt(3)\n"
    "ds_read_b64_tr_b16 v[126:127], v21 offset:12608\n"
    "v_mul_f32 v130, 0x3fb8aa3b, v49\n"
    "v_exp_f32 v129, v129 clamp\n"
    "v_mfma_f32_16x16x32_bf16 v[184:187], a[32:35], v[112:115], v[184:187]\n"
    "ds_read_b64_tr_b16 v[104:105], v21 offset:96\n"
    "buffer_load_dword v31, v156, s[52:55], s99 offen\n"
    "v_mfma_f32_16x16x32_bf16 v[184:187], a[40:43], v[116:119], v[184:187]\n"
    "ds_read_b64_tr_b16 v[106:107], v21 offset:2400\n"
    "v_max_f32 v49, s87, v49\n"
    "v_mfma_f32_16x16x32_bf16 v[188:191], a[36:39], v[112:115], v[188:191]\n"
    "ds_read_b64_tr_b16 v[108:109], v21 offset:10336\n"
    "v_add_f32 v47, v47, v128\n"
    "v_mfma_f32_16x16x32_bf16 v[188:191], a[44:47], v[116:119], v[188:191]\n"
    "s_waitcnt lgkmcnt(3)\n"
    "ds_read_b64_tr_b16 v[110:111], v21 offset:12640\n"
    "v_mul_f32 v128, 0x3fb8aa3b, v50\n"
    "v_mfma_f32_16x16x32_bf16 v[192:195], a[32:35], v[120:123], v[192:195]\n"
    "ds_read_b64_tr_b16 v[112:113], v21 offset:128\n"
    "v_exp_f32 v130, v130 clamp\n"
    "v_max_f32 v50, s87, v50\n"
    "v_mfma_f32_16x16x32_bf16 v[192:195], a[40:43], v[124:127], v[192:195]\n"
    "ds_read_b64_tr_b16 v[114:115], v21 offset:2432\n"
    "v_add_f32 v48, v48, v129\n"
    "v_mfma_f32_16x16x32_bf16 v[196:199], a[36:39], v[120:123], v[196:199]\n"
    "ds_read_b64_tr_b16 v[116:117], v21 offset:10368\n"
    "v_cvt_pk_bf16_f32 v151, v46, v47\n"
    "v_mfma_f32_16x16x32_bf16 v[196:199], a[44:47], v[124:127], v[196:199]\n"
    "s_waitcnt lgkmcnt(3)\n"
    "ds_read_b64_tr_b16 v[118:119], v21 offset:12672\n"
    "ds_write_b64 v26, v[150:151] offset:1032\n"
    "v_mul_f32 v129, 0x3fb8aa3b, v51\n"
    "v_mfma_f32_16x16x32_bf16 v[200:203], a[32:35], v[104:107], v[200:203]\n"
    "ds_read_b64_tr_b16 v[120:121], v21 offset:160\n"
    "v_exp_f32 v128, v128 clamp\n"
    "v_mfma_f32_16x16x32_bf16 v[200:203], a[40:43], v[108:111], v[200:203]\n"
    "ds_read_b64_tr_b16 v[122:123], v21 offset:2464\n"
    "v_max_f32 v51, s87, v51\n"
    "v_mfma_f32_16x16x32_bf16 v[204:207], a[36:39], v[104:107], v[204:207]\n"
    "ds_read_b64_tr_b16 v[124:125], v21 offset:10400\n"
    "v_add_f32 v49, v49, v130\n"
    "v_mul_f32 v130, 0x3fb8aa3b, v52\n"
    "v_mfma_f32_16x16x32_bf16 v[204:207], a[44:47], v[108:111], v[204:207]\n"
    "s_waitcnt lgkmcnt(4)\n"
    "ds_read_b64_tr_b16 v[126:127], v21 offset:12704\n"
    "v_exp_f32 v129, v129 clamp\n"
    "v_mfma_f32_16x16x32_bf16 v[208:211], a[32:35], v[112:115], v[208:211]\n"
    "ds_read_b64_tr_b16 v[104:105], v21 offset:192\n"
    "v_max_f32 v52, s87, v52\n"
    "v_mfma_f32_16x16x32_bf16 v[208:211], a[40:43], v[116:119], v[208:211]\n"
    "ds_read_b64_tr_b16 v[106:107], v21 offset:2496\n"
    "v_add_f32 v50, v50, v128\n"
    "v_mfma_f32_16x16x32_bf16 v[212:215], a[36:39], v[112:115], v[212:215]\n"
    "ds_read_b64_tr_b16 v[108:109], v21 offset:10432\n"
    "v_cvt_pk_bf16_f32 v144, v48, v49\n"
    "v_mul_f32 v128, 0x3fb8aa3b, v53\n"
    "v_mfma_f32_16x16x32_bf16 v[212:215], a[44:47], v[116:119], v[212:215]\n"
    "s_waitcnt lgkmcnt(3)\n"
    "ds_read_b64_tr_b16 v[110:111], v21 offset:12736\n"
    "v_exp_f32 v130, v130 clamp\n"
    "v_mfma_f32_16x16x32_bf16 v[216:219], a[32:35], v[120:123], v[216:219]\n"
    "ds_read_b64_tr_b16 v[112:113], v21 offset:224\n"
    "v_max_f32 v53, s87, v53\n"
    "v_mfma_f32_16x16x32_bf16 v[216:219], a[40:43], v[124:127], v[216:219]\n"
    "ds_read_b64_tr_b16 v[114:115], v21 offset:2528\n"
    "v_add_f32 v51, v51, v129\n"
    "v_mul_f32 v129, 0x3fb8aa3b, v54\n"
    "v_mfma_f32_16x16x32_bf16 v[220:223], a[36:39], v[120:123], v[220:223]\n"
    "ds_read_b64_tr_b16 v[116:117], v21 offset:10464\n"
    "v_exp_f32 v128, v128 clamp\n"
    "v_mfma_f32_16x16x32_bf16 v[220:223], a[44:47], v[124:127], v[220:223]\n"
    "s_waitcnt lgkmcnt(3)\n"
    "ds_read_b64_tr_b16 v[118:119], v21 offset:12768\n"
    "v_max_f32 v54, s87, v54\n"
    "v_mfma_f32_16x16x32_bf16 v[224:227], a[32:35], v[104:107], v[224:227]\n"
    "ds_read_b64_tr_b16 v[120:121], v21 offset:256\n"
    "v_add_f32 v52, v52, v130\n"
    "v_cvt_pk_bf16_f32 v145, v50, v51\n"
    "v_mfma_f32_16x16x32_bf16 v[224:227], a[40:43], v[108:111], v[224:227]\n"
    "ds_read_b64_tr_b16 v[122:123], v21 offset:2560\n"
    "ds_write_b64 v26, v[144:145] offset:2048\n"
    "v_mfma_f32_16x16x32_bf16 v[228:231], a[36:39], v[104:107], v[228:231]\n"
    "ds_read_b64_tr_b16 v[124:125], v21 offset:10496\n"
    "v_mul_f32 v130, 0x3fb8aa3b, v55\n"
    "v_mfma_f32_16x16x32_bf16 v[228:231], a[44:47], v[108:111], v[228:231]\n"
    "s_waitcnt lgkmcnt(4)\n"
    "ds_read_b64_tr_b16 v[126:127], v21 offset:12800\n"
    "v_exp_f32 v129, v129 clamp\n"
    "v_max_f32 v55, s87, v55\n"
    "v_mfma_f32_16x16x32_bf16 v[232:235], a[32:35], v[112:115], v[232:235]\n"
    "s_waitcnt vmcnt(6)\n"
    "s_barrier\n"
    "ds_read_b64_tr_b16 v[104:105], v20 offset:0\n"
    "v_mfma_f32_16x16x32_bf16 v[232:235], a[40:43], v[116:119], v[232:235]\n"
    "ds_read_b64_tr_b16 v[106:107], v20 offset:2304\n"
    "v_add_f32 v53, v53, v128\n"
    "v_mfma_f32_16x16x32_bf16 v[236:239], a[36:39], v[112:115], v[236:239]\n"
    "s_add_u32 s85, s69, 2304\n"
    "s_add_u32 m0, s89, 40960\n"
    "ds_read_b64_tr_b16 v[108:109], v20 offset:10240\n"
    "buffer_load_dwordx4 v10, s[36:39], s85 offen lds\n"
    "v_mfma_f32_16x16x32_bf16 v[236:239], a[44:47], v[116:119], v[236:239]\n"
    "s_waitcnt lgkmcnt(3)\n"
    "ds_read_b64_tr_b16 v[110:111], v20 offset:12544\n"
    "v_mul_f32 v128, 0x3fb8aa3b, v56\n"
    "v_mfma_f32_16x16x32_bf16 v[240:243], a[32:35], v[120:123], v[240:243]\n"
    "ds_read_b64_tr_b16 v[112:113], v20 offset:32\n"
    "buffer_load_dwordx4 v11, s[36:39], s85 offen offset:1024 lds\n"
    "v_mfma_f32_16x16x32_bf16 v[240:243], a[40:43], v[124:127], v[240:243]\n"
    "ds_read_b64_tr_b16 v[114:115], v20 offset:2336\n"
    "v_exp_f32 v130, v130 clamp\n"
    "v_mfma_f32_16x16x32_bf16 v[244:247], a[36:39], v[120:123], v[244:247]\n"
    "ds_read_b64_tr_b16 v[116:117], v20 offset:10272\n"
    "buffer_load_dwordx4 v12, s[36:39], s85 offen offset:2048 lds\n"
    "v_mfma_f32_16x16x32_bf16 v[244:247], a[44:47], v[124:127], v[244:247]\n"
    "s_waitcnt lgkmcnt(3)\n"
    "ds_read_b64_tr_b16 v[118:119], v20 offset:12576\n"
    "v_max_f32 v56, s87, v56\n"
    "v_add_f32 v54, v54, v129\n"
    "v_mfma_f32_16x16x32_bf16 v[176:179], a[48:51], v[104:107], v[176:179]\n"
    "ds_read_b64_tr_b16 v[120:121], v20 offset:64\n"
    "buffer_load_dwordx4 v13, s[36:39], s85 offen offset:3072 lds\n"
    "v_mfma_f32_16x16x32_bf16 v[176:179], a[56:59], v[108:111], v[176:179]\n"
    "ds_read_b64_tr_b16 v[122:123], v20 offset:2368\n"
    "v_cvt_pk_bf16_f32 v146, v52, v53\n"
    "v_mfma_f32_16x16x32_bf16 v[180:183], a[52:55], v[104:107], v[180:183]\n"
    "s_add_u32 m0, s89, 45056\n"
    "ds_read_b64_tr_b16 v[124:125], v20 offset:10304\n"
    "buffer_load_dwordx4 v14, s[36:39], s85 offen lds\n"
    "v_mfma_f32_16x16x32_bf16 v[180:183], a[60:63], v[108:111], v[180:183]\n"
    "s_waitcnt lgkmcnt(3)\n"
    "ds_read_b64_tr_b16 v[126:127], v20 offset:12608\n"
    "v_mul_f32 v129, 0x3fb8aa3b, v57\n"
    "v_mfma_f32_16x16x32_bf16 v[184:187], a[48:51], v[112:115], v[184:187]\n"
    "ds_read_b64_tr_b16 v[104:105], v20 offset:96\n"
    "v_exp_f32 v128, v128 clamp\n"
    "v_max_f32 v57, s87, v57\n"
    "v_mfma_f32_16x16x32_bf16 v[184:187], a[56:59], v[116:119], v[184:187]\n"
    "ds_read_b64_tr_b16 v[106:107], v20 offset:2400\n"
    "v_add_f32 v55, v55, v130\n"
    "v_mfma_f32_16x16x32_bf16 v[188:191], a[52:55], v[112:115], v[188:191]\n"
    "ds_read_b64_tr_b16 v[108:109], v20 offset:10336\n"
    "v_mul_f32 v130, 0x3fb8aa3b, v58\n"
    "v_mfma_f32_16x16x32_bf16 v[188:191], a[60:63], v[116:119], v[188:191]\n"
    "s_waitcnt lgkmcnt(3)\n"
    "ds_read_b64_tr_b16 v[110:111], v20 offset:12640\n"
    "v_exp_f32 v129, v129 clamp\n"
    "v_max_f32 v58, s87, v58\n"
    "v_mfma_f32_16x16x32_bf16 v[192:195], a[48:51], v[120:123], v[192:195]\n"
    "ds_read_b64_tr_b16 v[112:113], v20 offset:128\n"
    "v_add_f32 v56, v56, v128\n"
    "v_mfma_f32_16x16x32_bf16 v[192:195], a[56:59], v[124:127], v[192:195]\n"
    "ds_read_b64_tr_b16 v[114:115], v20 offset:2432\n"
    "v_cvt_pk_bf16_f32 v147, v54, v55\n"
    "v_mfma_f32_16x16x32_bf16 v[196:199], a[52:55], v[120:123], v[196:199]\n"
    "ds_read_b64_tr_b16 v[116:117], v20 offset:10368\n"
    "ds_write_b64 v26, v[146:147] offset:2056\n"
    "v_mul_f32 v128, 0x3fb8aa3b, v59\n"
    "v_mfma_f32_16x16x32_bf16 v[196:199], a[60:63], v[124:127], v[196:199]\n"
    "s_waitcnt lgkmcnt(4)\n"
    "ds_read_b64_tr_b16 v[118:119], v20 offset:12672\n"
    "v_exp_f32 v130, v130 clamp\n"
    "v_mfma_f32_16x16x32_bf16 v[200:203], a[48:51], v[104:107], v[200:203]\n"
    "ds_read_b64_tr_b16 v[120:121], v20 offset:160\n"
    "v_max_f32 v59, s87, v59\n"
    "v_mfma_f32_16x16x32_bf16 v[200:203], a[56:59], v[108:111], v[200:203]\n"
    "ds_read_b64_tr_b16 v[122:123], v20 offset:2464\n"
    "v_add_f32 v57, v57, v129\n"
    "v_mfma_f32_16x16x32_bf16 v[204:207], a[52:55], v[104:107], v[204:207]\n"
    "ds_read_b64_tr_b16 v[124:125], v20 offset:10400\n"
    "v_mul_f32 v129, 0x3fb8aa3b, v60\n"
    "v_exp_f32 v128, v128 clamp\n"
    "v_mfma_f32_16x16x32_bf16 v[204:207], a[60:63], v[108:111], v[204:207]\n"
    "s_waitcnt lgkmcnt(3)\n"
    "ds_read_b64_tr_b16 v[126:127], v20 offset:12704\n"
    "v_max_f32 v60, s87, v60\n"
    "v_mfma_f32_16x16x32_bf16 v[208:211], a[48:51], v[112:115], v[208:211]\n"
    "ds_read_b64_tr_b16 v[104:105], v20 offset:192\n"
    "v_add_f32 v58, v58, v130\n"
    "v_mfma_f32_16x16x32_bf16 v[208:211], a[56:59], v[116:119], v[208:211]\n"
    "ds_read_b64_tr_b16 v[106:107], v20 offset:2496\n"
    "v_cvt_pk_bf16_f32 v148, v56, v57\n"
    "v_mul_f32 v130, 0x3fb8aa3b, v61\n"
    "v_mfma_f32_16x16x32_bf16 v[212:215], a[52:55], v[112:115], v[212:215]\n"
    "ds_read_b64_tr_b16 v[108:109], v20 offset:10432\n"
    "v_exp_f32 v129, v129 clamp\n"
    "v_mfma_f32_16x16x32_bf16 v[212:215], a[60:63], v[116:119], v[212:215]\n"
    "s_waitcnt lgkmcnt(3)\n"
    "ds_read_b64_tr_b16 v[110:111], v20 offset:12736\n"
    "v_max_f32 v61, s87, v61\n"
    "v_mfma_f32_16x16x32_bf16 v[216:219], a[48:51], v[120:123], v[216:219]\n"
    "ds_read_b64_tr_b16 v[112:113], v20 offset:224\n"
    "v_add_f32 v59, v59, v128\n"
    "v_mul_f32 v128, 0x3fb8aa3b, v62\n"
    "v_mfma_f32_16x16x32_bf16 v[216:219], a[56:59], v[124:127], v[216:219]\n"
    "ds_read_b64_tr_b16 v[114:115], v20 offset:2528\n"
    "v_exp_f32 v130, v130 clamp\n"
    "v_mfma_f32_16x16x32_bf16 v[220:223], a[52:55], v[120:123], v[220:223]\n"
    "ds_read_b64_tr_b16 v[116:117], v20 offset:10464\n"
    "v_max_f32 v62, s87, v62\n"
    "v_mfma_f32_16x16x32_bf16 v[220:223], a[60:63], v[124:127], v[220:223]\n"
    "s_waitcnt lgkmcnt(3)\n"
    "ds_read_b64_tr_b16 v[118:119], v20 offset:12768\n"
    "v_add_f32 v60, v60, v129\n"
    "v_mfma_f32_16x16x32_bf16 v[224:227], a[48:51], v[104:107], v[224:227]\n"
    "ds_read_b64_tr_b16 v[120:121], v20 offset:256\n"
    "v_cvt_pk_bf16_f32 v149, v58, v59\n"
    "ds_write_b64 v26, v[148:149] offset:3072\n"
    "v_mfma_f32_16x16x32_bf16 v[224:227], a[56:59], v[108:111], v[224:227]\n"
    "ds_read_b64_tr_b16 v[122:123], v20 offset:2560\n"
    "v_mul_f32 v129, 0x3fb8aa3b, v63\n"
    "v_mfma_f32_16x16x32_bf16 v[228:231], a[52:55], v[104:107], v[228:231]\n"
    "ds_read_b64_tr_b16 v[124:125], v20 offset:10496\n"
    "v_exp_f32 v128, v128 clamp\n"
    "v_mfma_f32_16x16x32_bf16 v[228:231], a[60:63], v[108:111], v[228:231]\n"
    "s_waitcnt lgkmcnt(4)\n"
    "ds_read_b64_tr_b16 v[126:127], v20 offset:12800\n"
    "v_max_f32 v63, s87, v63\n"
    "v_add_f32 v61, v61, v130\n"
    "v_mfma_f32_16x16x32_bf16 v[232:235], a[48:51], v[112:115], v[232:235]\n"
    "s_waitcnt vmcnt(6)\n"
    "s_barrier\n"
    "ds_read_b64_tr_b16 v[104:105], v20 offset:20480\n"
    "v_mfma_f32_16x16x32_bf16 v[232:235], a[56:59], v[116:119], v[232:235]\n"
    "ds_read_b64_tr_b16 v[106:107], v20 offset:22784\n"
    "v_mul_f32 v130, 0x3fb8aa3b, v64\n"
    "v_mfma_f32_16x16x32_bf16 v[236:239], a[52:55], v[112:115], v[236:239]\n"
    "s_add_u32 s85, s70, 2304\n"
    "s_add_u32 m0, s89, 61440\n"
    "ds_read_b64_tr_b16 v[108:109], v20 offset:30720\n"
    "buffer_load_dwordx4 v10, s[36:39], s85 offen lds\n"
    "v_mfma_f32_16x16x32_bf16 v[236:239], a[60:63], v[116:119], v[236:239]\n"
    "s_waitcnt lgkmcnt(3)\n"
    "ds_read_b64_tr_b16 v[110:111], v20 offset:33024\n"
    "v_exp_f32 v129, v129 clamp\n"
    "v_mfma_f32_16x16x32_bf16 v[240:243], a[48:51], v[120:123], v[240:243]\n"
    "ds_read_b64_tr_b16 v[112:113], v20 offset:20512\n"
    "buffer_load_dwordx4 v11, s[36:39], s85 offen offset:1024 lds\n"
    "v_mfma_f32_16x16x32_bf16 v[240:243], a[56:59], v[124:127], v[240:243]\n"
    "ds_read_b64_tr_b16 v[114:115], v20 offset:22816\n"
    "v_max_f32 v64, s87, v64\n"
    "v_add_f32 v62, v62, v128\n"
    "v_mfma_f32_16x16x32_bf16 v[244:247], a[52:55], v[120:123], v[244:247]\n"
    "ds_read_b64_tr_b16 v[116:117], v20 offset:30752\n"
    "buffer_load_dwordx4 v12, s[36:39], s85 offen offset:2048 lds\n"
    "v_mfma_f32_16x16x32_bf16 v[244:247], a[60:63], v[124:127], v[244:247]\n"
    "s_waitcnt lgkmcnt(3)\n"
    "ds_read_b64_tr_b16 v[118:119], v20 offset:33056\n"
    "v_cvt_pk_bf16_f32 v150, v60, v61\n"
    "v_mfma_f32_16x16x32_bf16 v[176:179], a[64:67], v[104:107], v[176:179]\n"
    "ds_read_b64_tr_b16 v[120:121], v20 offset:20544\n"
    "buffer_load_dwordx4 v13, s[36:39], s85 offen offset:3072 lds\n"
    "v_mfma_f32_16x16x32_bf16 v[176:179], a[72:75], v[108:111], v[176:179]\n"
    "ds_read_b64_tr_b16 v[122:123], v20 offset:22848\n"
    "v_mul_f32 v128, 0x3fb8aa3b, v65\n"
    "v_mfma_f32_16x16x32_bf16 v[180:183], a[68:71], v[104:107], v[180:183]\n"
    "s_add_u32 m0, s89, 65536\n"
    "ds_read_b64_tr_b16 v[124:125], v20 offset:30784\n"
    "buffer_load_dwordx4 v14, s[36:39], s85 offen lds\n"
    "v_mfma_f32_16x16x32_bf16 v[180:183], a[76:79], v[108:111], v[180:183]\n"
    "s_waitcnt lgkmcnt(3)\n"
    "ds_read_b64_tr_b16 v[126:127], v20 offset:33088\n"
    "v_exp_f32 v130, v130 clamp\n"
    "v_max_f32 v65, s87, v65\n"
    "v_mfma_f32_16x16x32_bf16 v[184:187], a[64:67], v[112:115], v[184:187]\n"
    "ds_read_b64_tr_b16 v[104:105], v20 offset:20576\n"
    "v_add_f32 v63, v63, v129\n"
    "v_mfma_f32_16x16x32_bf16 v[184:187], a[72:75], v[116:119], v[184:187]\n"
    "ds_read_b64_tr_b16 v[106:107], v20 offset:22880\n"
    "v_mul_f32 v129, 0x3fb8aa3b, v66\n"
    "v_mfma_f32_16x16x32_bf16 v[188:191], a[68:71], v[112:115], v[188:191]\n"
    "ds_read_b64_tr_b16 v[108:109], v20 offset:30816\n"
    "v_exp_f32 v128, v128 clamp\n"
    "v_mfma_f32_16x16x32_bf16 v[188:191], a[76:79], v[116:119], v[188:191]\n"
    "s_waitcnt lgkmcnt(3)\n"
    "ds_read_b64_tr_b16 v[110:111], v20 offset:33120\n"
    "v_max_f32 v66, s87, v66\n"
    "v_add_f32 v64, v64, v130\n"
    "v_mfma_f32_16x16x32_bf16 v[192:195], a[64:67], v[120:123], v[192:195]\n"
    "ds_read_b64_tr_b16 v[112:113], v20 offset:20608\n"
    "v_cvt_pk_bf16_f32 v151, v62, v63\n"
    "v_mfma_f32_16x16x32_bf16 v[192:195], a[72:75], v[124:127], v[192:195]\n"
    "ds_read_b64_tr_b16 v[114:115], v20 offset:22912\n"
    "ds_write_b64 v26, v[150:151] offset:3080\n"
    "v_mfma_f32_16x16x32_bf16 v[196:199], a[68:71], v[120:123], v[196:199]\n"
    "ds_read_b64_tr_b16 v[116:117], v20 offset:30848\n"
    "v_mul_f32 v130, 0x3fb8aa3b, v67\n"
    "v_exp_f32 v129, v129 clamp\n"
    "v_mfma_f32_16x16x32_bf16 v[196:199], a[76:79], v[124:127], v[196:199]\n"
    "s_waitcnt lgkmcnt(4)\n"
    "ds_read_b64_tr_b16 v[118:119], v20 offset:33152\n"
    "v_max_f32 v67, s87, v67\n"
    "v_mfma_f32_16x16x32_bf16 v[200:203], a[64:67], v[104:107], v[200:203]\n"
    "ds_read_b64_tr_b16 v[120:121], v20 offset:20640\n"
    "v_add_f32 v65, v65, v128\n"
    "v_mfma_f32_16x16x32_bf16 v[200:203], a[72:75], v[108:111], v[200:203]\n"
    "ds_read_b64_tr_b16 v[122:123], v20 offset:22944\n"
    "v_mul_f32 v128, 0x3fb8aa3b, v68\n"
    "v_exp_f32 v130, v130 clamp\n"
    "v_mfma_f32_16x16x32_bf16 v[204:207], a[68:71], v[104:107], v[204:207]\n"
    "ds_read_b64_tr_b16 v[124:125], v20 offset:30880\n"
    "v_max_f32 v68, s87, v68\n"
    "v_mfma_f32_16x16x32_bf16 v[204:207], a[76:79], v[108:111], v[204:207]\n"
    "s_waitcnt lgkmcnt(3)\n"
    "ds_read_b64_tr_b16 v[126:127], v20 offset:33184\n"
    "v_add_f32 v66, v66, v129\n"
    "v_mfma_f32_16x16x32_bf16 v[208:211], a[64:67], v[112:115], v[208:211]\n"
    "ds_read_b64_tr_b16 v[104:105], v20 offset:20672\n"
    "v_cvt_pk_bf16_f32 v144, v64, v65\n"
    "v_mul_f32 v129, 0x3fb8aa3b, v69\n"
    "v_mfma_f32_16x16x32_bf16 v[208:211], a[72:75], v[116:119], v[208:211]\n"
    "ds_read_b64_tr_b16 v[106:107], v20 offset:22976\n"
    "v_exp_f32 v128, v128 clamp\n"
    "v_mfma_f32_16x16x32_bf16 v[212:215], a[68:71], v[112:115], v[212:215]\n"
    "ds_read_b64_tr_b16 v[108:109], v20 offset:30912\n"
    "v_max_f32 v69, s87, v69\n"
    "v_mfma_f32_16x16x32_bf16 v[212:215], a[76:79], v[116:119], v[212:215]\n"
    "s_waitcnt lgkmcnt(3)\n"
    "ds_read_b64_tr_b16 v[110:111], v20 offset:33216\n"
    "v_add_f32 v67, v67, v130\n"
    "v_mfma_f32_16x16x32_bf16 v[216:219], a[64:67], v[120:123], v[216:219]\n"
    "ds_read_b64_tr_b16 v[112:113], v20 offset:20704\n"
    "v_mul_f32 v130, 0x3fb8aa3b, v70\n"
    "v_exp_f32 v129, v129 clamp\n"
    "v_mfma_f32_16x16x32_bf16 v[216:219], a[72:75], v[124:127], v[216:219]\n"
    "ds_read_b64_tr_b16 v[114:115], v20 offset:23008\n"
    "v_max_f32 v70, s87, v70\n"
    "v_mfma_f32_16x16x32_bf16 v[220:223], a[68:71], v[120:123], v[220:223]\n"
    "ds_read_b64_tr_b16 v[116:117], v20 offset:30944\n"
    "v_add_f32 v68, v68, v128\n"
    "v_mfma_f32_16x16x32_bf16 v[220:223], a[76:79], v[124:127], v[220:223]\n"
    "s_waitcnt lgkmcnt(3)\n"
    "ds_read_b64_tr_b16 v[118:119], v20 offset:33248\n"
    "v_cvt_pk_bf16_f32 v145, v66, v67\n"
    "ds_write_b64 v26, v[144:145] offset:4096\n"
    "v_mfma_f32_16x16x32_bf16 v[224:227], a[64:67], v[104:107], v[224:227]\n"
    "ds_read_b64_tr_b16 v[120:121], v20 offset:20736\n"
    "v_mul_f32 v128, 0x3fb8aa3b, v71\n"
    "v_mfma_f32_16x16x32_bf16 v[224:227], a[72:75], v[108:111], v[224:227]\n"
    "ds_read_b64_tr_b16 v[122:123], v20 offset:23040\n"
    "v_exp_f32 v130, v130 clamp\n"
    "v_mfma_f32_16x16x32_bf16 v[228:231], a[68:71], v[104:107], v[228:231]\n"
    "ds_read_b64_tr_b16 v[124:125], v20 offset:30976\n"
    "v_max_f32 v71, s87, v71\n"
    "v_add_f32 v69, v69, v129\n"
    "v_mfma_f32_16x16x32_bf16 v[228:231], a[76:79], v[108:111], v[228:231]\n"
    "s_waitcnt lgkmcnt(4)\n"
    "ds_read_b64_tr_b16 v[126:127], v20 offset:33280\n"
    "v_mul_f32 v129, 0x3fb8aa3b, v72\n"
    "v_mfma_f32_16x16x32_bf16 v[232:235], a[64:67], v[112:115], v[232:235]\n"
    "s_waitcnt vmcnt(5)\n"
    "s_barrier\n"
    "ds_read_b64_tr_b16 v[104:105], v20 offset:40960\n"
    "v_mfma_f32_16x16x32_bf16 v[232:235], a[72:75], v[116:119], v[232:235]\n"
    "ds_read_b64_tr_b16 v[106:107], v20 offset:43264\n"
    "v_exp_f32 v128, v128 clamp\n"
    "v_mfma_f32_16x16x32_bf16 v[236:239], a[68:71], v[112:115], v[236:239]\n"
    "s_add_u32 s85, s71, 2304\n"
    "s_add_u32 m0, s89, 0\n"
    "ds_read_b64_tr_b16 v[108:109], v20 offset:51200\n"
    "buffer_load_dwordx4 v10, s[36:39], s85 offen lds\n"
    "v_mfma_f32_16x16x32_bf16 v[236:239], a[76:79], v[116:119], v[236:239]\n"
    "s_waitcnt lgkmcnt(3)\n"
    "ds_read_b64_tr_b16 v[110:111], v20 offset:53504\n"
    "v_max_f32 v72, s87, v72\n"
    "v_mfma_f32_16x16x32_bf16 v[240:243], a[64:67], v[120:123], v[240:243]\n"
    "ds_read_b64_tr_b16 v[112:113], v20 offset:40992\n"
    "buffer_load_dwordx4 v11, s[36:39], s85 offen offset:1024 lds\n"
    "v_mfma_f32_16x16x32_bf16 v[240:243], a[72:75], v[124:127], v[240:243]\n"
    "ds_read_b64_tr_b16 v[114:115], v20 offset:43296\n"
    "v_add_f32 v70, v70, v130\n"
    "v_cvt_pk_bf16_f32 v146, v68, v69\n"
    "v_mfma_f32_16x16x32_bf16 v[244:247], a[68:71], v[120:123], v[244:247]\n"
    "ds_read_b64_tr_b16 v[116:117], v20 offset:51232\n"
    "buffer_load_dwordx4 v12, s[36:39], s85 offen offset:2048 lds\n"
    "v_mfma_f32_16x16x32_bf16 v[244:247], a[76:79], v[124:127], v[244:247]\n"
    "s_waitcnt lgkmcnt(3)\n"
    "ds_read_b64_tr_b16 v[118:119], v20 offset:53536\n"
    "v_mul_f32 v130, 0x3fb8aa3b, v73\n"
    "v_mfma_f32_16x16x32_bf16 v[176:179], a[80:83], v[104:107], v[176:179]\n"
    "ds_read_b64_tr_b16 v[120:121], v20 offset:41024\n"
    "buffer_load_dwordx4 v13, s[36:39], s85 offen offset:3072 lds\n"
    "v_mfma_f32_16x16x32_bf16 v[176:179], a[88:91], v[108:111], v[176:179]\n"
    "ds_read_b64_tr_b16 v[122:123], v20 offset:43328\n"
    "v_exp_f32 v129, v129 clamp\n"
    "v_mfma_f32_16x16x32_bf16 v[180:183], a[84:87], v[104:107], v[180:183]\n"
    "s_add_u32 m0, s89, 4096\n"
    "ds_read_b64_tr_b16 v[124:125], v20 offset:51264\n"
    "buffer_load_dwordx4 v14, s[36:39], s85 offen lds\n"
    "v_mfma_f32_16x16x32_bf16 v[180:183], a[92:95], v[108:111], v[180:183]\n"
    "s_waitcnt lgkmcnt(3)\n"
    "ds_read_b64_tr_b16 v[126:127], v20 offset:53568\n"
    "v_max_f32 v73, s87, v73\n"
    "v_add_f32 v71, v71, v128\n"
    "v_mfma_f32_16x16x32_bf16 v[184:187], a[80:83], v[112:115], v[184:187]\n"
    "ds_read_b64_tr_b16 v[104:105], v20 offset:41056\n"
    "v_mul_f32 v128, 0x3fb8aa3b, v74\n"
    "v_mfma_f32_16x16x32_bf16 v[184:187], a[88:91], v[116:119], v[184:187]\n"
    "ds_read_b64_tr_b16 v[106:107], v20 offset:43360\n"
    "v_exp_f32 v130, v130 clamp\n"
    "v_mfma_f32_16x16x32_bf16 v[188:191], a[84:87], v[112:115], v[188:191]\n"
    "ds_read_b64_tr_b16 v[108:109], v20 offset:51296\n"
    "v_max_f32 v74, s87, v74\n"
    "v_add_f32 v72, v72, v129\n"
    "v_mfma_f32_16x16x32_bf16 v[188:191], a[92:95], v[116:119], v[188:191]\n"
    "s_waitcnt lgkmcnt(3)\n"
    "ds_read_b64_tr_b16 v[110:111], v20 offset:53600\n"
    "v_cvt_pk_bf16_f32 v147, v70, v71\n"
    "v_mfma_f32_16x16x32_bf16 v[192:195], a[80:83], v[120:123], v[192:195]\n"
    "ds_read_b64_tr_b16 v[112:113], v20 offset:41088\n"
    "ds_write_b64 v26, v[146:147] offset:4104\n"
    "v_mfma_f32_16x16x32_bf16 v[192:195], a[88:91], v[124:127], v[192:195]\n"
    "ds_read_b64_tr_b16 v[114:115], v20 offset:43392\n"
    "v_mul_f32 v129, 0x3fb8aa3b, v75\n"
    "v_exp_f32 v128, v128 clamp\n"
    "v_mfma_f32_16x16x32_bf16 v[196:199], a[84:87], v[120:123], v[196:199]\n"
    "ds_read_b64_tr_b16 v[116:117], v20 offset:51328\n"
    "v_max_f32 v75, s87, v75\n"
    "v_mfma_f32_16x16x32_bf16 v[196:199], a[92:95], v[124:127], v[196:199]\n"
    "s_waitcnt lgkmcnt(4)\n"
    "ds_read_b64_tr_b16 v[118:119], v20 offset:53632\n"
    "v_add_f32 v73, v73, v130\n"
    "v_mfma_f32_16x16x32_bf16 v[200:203], a[80:83], v[104:107], v[200:203]\n"
    "ds_read_b64_tr_b16 v[120:121], v20 offset:41120\n"
    "v_mul_f32 v130, 0x3fb8aa3b, v76\n"
    "v_mfma_f32_16x16x32_bf16 v[200:203], a[88:91], v[108:111], v[200:203]\n"
    "ds_read_b64_tr_b16 v[122:123], v20 offset:43424\n"
    "v_exp_f32 v129, v129 clamp\n"
    "v_max_f32 v76, s87, v76\n"
    "v_mfma_f32_16x16x32_bf16 v[204:207], a[84:87], v[104:107], v[204:207]\n"
    "ds_read_b64_tr_b16 v[124:125], v20 offset:51360\n"
    "v_add_f32 v74, v74, v128\n"
    "v_mfma_f32_16x16x32_bf16 v[204:207], a[92:95], v[108:111], v[204:207]\n"
    "s_waitcnt lgkmcnt(3)\n"
    "ds_read_b64_tr_b16 v[126:127], v20 offset:53664\n"
    "v_cvt_pk_bf16_f32 v148, v72, v73\n"
    "v_mfma_f32_16x16x32_bf16 v[208:211], a[80:83], v[112:115], v[208:211]\n"
    "ds_read_b64_tr_b16 v[104:105], v20 offset:41152\n"
    "v_mul_f32 v128, 0x3fb8aa3b, v77\n"
    "v_exp_f32 v130, v130 clamp\n"
    "v_mfma_f32_16x16x32_bf16 v[208:211], a[88:91], v[116:119], v[208:211]\n"
    "ds_read_b64_tr_b16 v[106:107], v20 offset:43456\n"
    "v_max_f32 v77, s87, v77\n"
    "v_mfma_f32_16x16x32_bf16 v[212:215], a[84:87], v[112:115], v[212:215]\n"
    "ds_read_b64_tr_b16 v[108:109], v20 offset:51392\n"
    "v_add_f32 v75, v75, v129\n"
    "v_mfma_f32_16x16x32_bf16 v[212:215], a[92:95], v[116:119], v[212:215]\n"
    "s_waitcnt lgkmcnt(3)\n"
    "ds_read_b64_tr_b16 v[110:111], v20 offset:53696\n"
    "v_mul_f32 v129, 0x3fb8aa3b, v78\n"
    "v_exp_f32 v128, v128 clamp\n"
    "v_mfma_f32_16x16x32_bf16 v[216:219], a[80:83], v[120:123], v[216:219]\n"
    "ds_read_b64_tr_b16 v[112:113], v20 offset:41184\n"
    "v_max_f32 v78, s87, v78\n"
    "v_mfma_f32_16x16x32_bf16 v[216:219], a[88:91], v[124:127], v[216:219]\n"
    "ds_read_b64_tr_b16 v[114:115], v20 offset:43488\n"
    "v_add_f32 v76, v76, v130\n"
    "v_mfma_f32_16x16x32_bf16 v[220:223], a[84:87], v[120:123], v[220:223]\n"
    "ds_read_b64_tr_b16 v[116:117], v20 offset:51424\n"
    "v_cvt_pk_bf16_f32 v149, v74, v75\n"
    "v_mfma_f32_16x16x32_bf16 v[220:223], a[92:95], v[124:127], v[220:223]\n"
    "s_waitcnt lgkmcnt(3)\n"
    "ds_read_b64_tr_b16 v[118:119], v20 offset:53728\n"
    "ds_write_b64 v26, v[148:149] offset:5120\n"
    "v_mul_f32 v130, 0x3fb8aa3b, v79\n"
    "v_mfma_f32_16x16x32_bf16 v[224:227], a[80:83], v[104:107], v[224:227]\n"
    "ds_read_b64_tr_b16 v[120:121], v20 offset:41216\n"
    "v_exp_f32 v129, v129 clamp\n"
    "v_mfma_f32_16x16x32_bf16 v[224:227], a[88:91], v[108:111], v[224:227]\n"
    "ds_read_b64_tr_b16 v[122:123], v20 offset:43520\n"
    "v_max_f32 v79, s87, v79\n"
    "v_mfma_f32_16x16x32_bf16 v[228:231], a[84:87], v[104:107], v[228:231]\n"
    "ds_read_b64_tr_b16 v[124:125], v20 offset:51456\n"
    "v_add_f32 v77, v77, v128\n"
    "v_mul_f32 v128, 0x3fb8aa3b, v80\n"
    "v_mfma_f32_16x16x32_bf16 v[228:231], a[92:95], v[108:111], v[228:231]\n"
    "s_waitcnt lgkmcnt(4)\n"
    "ds_read_b64_tr_b16 v[126:127], v20 offset:53760\n"
    "v_exp_f32 v130, v130 clamp\n"
    "v_mfma_f32_16x16x32_bf16 v[232:235], a[80:83], v[112:115], v[232:235]\n"
    "s_waitcnt vmcnt(5)\n"
    "s_barrier\n"
    "ds_read_b64_tr_b16 v[104:105], v21 offset:0\n"
    "v_mfma_f32_16x16x32_bf16 v[232:235], a[88:91], v[116:119], v[232:235]\n"
    "ds_read_b64_tr_b16 v[106:107], v21 offset:2304\n"
    "v_max_f32 v80, s87, v80\n"
    "v_mfma_f32_16x16x32_bf16 v[236:239], a[84:87], v[112:115], v[236:239]\n"
    "s_add_u32 s85, s72, 2304\n"
    "s_add_u32 m0, s89, 20480\n"
    "ds_read_b64_tr_b16 v[108:109], v21 offset:10240\n"
    "buffer_load_dwordx4 v10, s[36:39], s85 offen lds\n"
    "v_mfma_f32_16x16x32_bf16 v[236:239], a[92:95], v[116:119], v[236:239]\n"
    "s_waitcnt lgkmcnt(3)\n"
    "ds_read_b64_tr_b16 v[110:111], v21 offset:12544\n"
    "v_add_f32 v78, v78, v129\n"
    "v_cvt_pk_bf16_f32 v150, v76, v77\n"
    "v_mfma_f32_16x16x32_bf16 v[240:243], a[80:83], v[120:123], v[240:243]\n"
    "ds_read_b64_tr_b16 v[112:113], v21 offset:32\n"
    "buffer_load_dwordx4 v11, s[36:39], s85 offen offset:1024 lds\n"
    "v_mfma_f32_16x16x32_bf16 v[240:243], a[88:91], v[124:127], v[240:243]\n"
    "ds_read_b64_tr_b16 v[114:115], v21 offset:2336\n"
    "v_mul_f32 v129, 0x3fb8aa3b, v81\n"
    "v_mfma_f32_16x16x32_bf16 v[244:247], a[84:87], v[120:123], v[244:247]\n"
    "ds_read_b64_tr_b16 v[116:117], v21 offset:10272\n"
    "buffer_load_dwordx4 v12, s[36:39], s85 offen offset:2048 lds\n"
    "v_mfma_f32_16x16x32_bf16 v[244:247], a[92:95], v[124:127], v[244:247]\n"
    "s_waitcnt lgkmcnt(3)\n"
    "ds_read_b64_tr_b16 v[118:119], v21 offset:12576\n"
    "v_exp_f32 v128, v128 clamp\n"
    "v_mfma_f32_16x16x32_bf16 v[176:179], a[96:99], v[104:107], v[176:179]\n"
    "ds_read_b64_tr_b16 v[120:121], v21 offset:64\n"
    "buffer_load_dwordx4 v13, s[36:39], s85 offen offset:3072 lds\n"
    "v_mfma_f32_16x16x32_bf16 v[176:179], a[104:107], v[108:111], v[176:179]\n"
    "ds_read_b64_tr_b16 v[122:123], v21 offset:2368\n"
    "v_max_f32 v81, s87, v81\n"
    "v_add_f32 v79, v79, v130\n"
    "v_mfma_f32_16x16x32_bf16 v[180:183], a[100:103], v[104:107], v[180:183]\n"
    "s_add_u32 m0, s89, 24576\n"
    "ds_read_b64_tr_b16 v[124:125], v21 offset:10304\n"
    "buffer_load_dwordx4 v14, s[36:39], s85 offen lds\n"
    "v_mfma_f32_16x16x32_bf16 v[180:183], a[108:111], v[108:111], v[180:183]\n"
    "s_waitcnt lgkmcnt(3)\n"
    "ds_read_b64_tr_b16 v[126:127], v21 offset:12608\n"
    "v_mul_f32 v130, 0x3fb8aa3b, v82\n"
    "v_mfma_f32_16x16x32_bf16 v[184:187], a[96:99], v[112:115], v[184:187]\n"
    "ds_read_b64_tr_b16 v[104:105], v21 offset:96\n"
    "v_exp_f32 v129, v129 clamp\n"
    "v_mfma_f32_16x16x32_bf16 v[184:187], a[104:107], v[116:119], v[184:187]\n"
    "ds_read_b64_tr_b16 v[106:107], v21 offset:2400\n"
    "v_max_f32 v82, s87, v82\n"
    "v_mfma_f32_16x16x32_bf16 v[188:191], a[100:103], v[112:115], v[188:191]\n"
    "ds_read_b64_tr_b16 v[108:109], v21 offset:10336\n"
    "v_add_f32 v80, v80, v128\n"
    "v_cvt_pk_bf16_f32 v151, v78, v79\n"
    "v_mfma_f32_16x16x32_bf16 v[188:191], a[108:111], v[116:119], v[188:191]\n"
    "s_waitcnt lgkmcnt(3)\n"
    "ds_read_b64_tr_b16 v[110:111], v21 offset:12640\n"
    "ds_write_b64 v26, v[150:151] offset:5128\n"
    "v_mfma_f32_16x16x32_bf16 v[192:195], a[96:99], v[120:123], v[192:195]\n"
    "ds_read_b64_tr_b16 v[112:113], v21 offset:128\n"
    "v_mul_f32 v128, 0x3fb8aa3b, v83\n"
    "v_mfma_f32_16x16x32_bf16 v[192:195], a[104:107], v[124:127], v[192:195]\n"
    "ds_read_b64_tr_b16 v[114:115], v21 offset:2432\n"
    "v_exp_f32 v130, v130 clamp\n"
    "v_max_f32 v83, s87, v83\n"
    "v_mfma_f32_16x16x32_bf16 v[196:199], a[100:103], v[120:123], v[196:199]\n"
    "ds_read_b64_tr_b16 v[116:117], v21 offset:10368\n"
    "v_add_f32 v81, v81, v129\n"
    "v_mfma_f32_16x16x32_bf16 v[196:199], a[108:111], v[124:127], v[196:199]\n"
    "s_waitcnt lgkmcnt(4)\n"
    "ds_read_b64_tr_b16 v[118:119], v21 offset:12672\n"
    "v_mul_f32 v129, 0x3fb8aa3b, v84\n"
    "v_mfma_f32_16x16x32_bf16 v[200:203], a[96:99], v[104:107], v[200:203]\n"
    "ds_read_b64_tr_b16 v[120:121], v21 offset:160\n"
    "v_exp_f32 v128, v128 clamp\n"
    "v_max_f32 v84, s87, v84\n"
    "v_mfma_f32_16x16x32_bf16 v[200:203], a[104:107], v[108:111], v[200:203]\n"
    "ds_read_b64_tr_b16 v[122:123], v21 offset:2464\n"
    "v_add_f32 v82, v82, v130\n"
    "v_mfma_f32_16x16x32_bf16 v[204:207], a[100:103], v[104:107], v[204:207]\n"
    "ds_read_b64_tr_b16 v[124:125], v21 offset:10400\n"
    "v_cvt_pk_bf16_f32 v144, v80, v81\n"
    "v_mfma_f32_16x16x32_bf16 v[204:207], a[108:111], v[108:111], v[204:207]\n"
    "s_waitcnt lgkmcnt(3)\n"
    "ds_read_b64_tr_b16 v[126:127], v21 offset:12704\n"
    "v_mul_f32 v130, 0x3fb8aa3b, v85\n"
    "v_exp_f32 v129, v129 clamp\n"
    "v_mfma_f32_16x16x32_bf16 v[208:211], a[96:99], v[112:115], v[208:211]\n"
    "ds_read_b64_tr_b16 v[104:105], v21 offset:192\n"
    "v_max_f32 v85, s87, v85\n"
    "v_mfma_f32_16x16x32_bf16 v[208:211], a[104:107], v[116:119], v[208:211]\n"
    "ds_read_b64_tr_b16 v[106:107], v21 offset:2496\n"
    "v_add_f32 v83, v83, v128\n"
    "v_mfma_f32_16x16x32_bf16 v[212:215], a[100:103], v[112:115], v[212:215]\n"
    "ds_read_b64_tr_b16 v[108:109], v21 offset:10432\n"
    "v_mul_f32 v128, 0x3fb8aa3b, v86\n"
    "v_mfma_f32_16x16x32_bf16 v[212:215], a[108:111], v[116:119], v[212:215]\n"
    "s_waitcnt lgkmcnt(3)\n"
    "ds_read_b64_tr_b16 v[110:111], v21 offset:12736\n"
    "v_exp_f32 v130, v130 clamp\n"
    "v_max_f32 v86, s87, v86\n"
    "v_mfma_f32_16x16x32_bf16 v[216:219], a[96:99], v[120:123], v[216:219]\n"
    "ds_read_b64_tr_b16 v[112:113], v21 offset:224\n"
    "v_add_f32 v84, v84, v129\n"
    "v_mfma_f32_16x16x32_bf16 v[216:219], a[104:107], v[124:127], v[216:219]\n"
    "ds_read_b64_tr_b16 v[114:115], v21 offset:2528\n"
    "v_cvt_pk_bf16_f32 v145, v82, v83\n"
    "v_mfma_f32_16x16x32_bf16 v[220:223], a[100:103], v[120:123], v[220:223]\n"
    "ds_read_b64_tr_b16 v[116:117], v21 offset:10464\n"
    "ds_write_b64 v26, v[144:145] offset:6144\n"
    "v_mul_f32 v129, 0x3fb8aa3b, v87\n"
    "v_mfma_f32_16x16x32_bf16 v[220:223], a[108:111], v[124:127], v[220:223]\n"
    "s_waitcnt lgkmcnt(4)\n"
    "ds_read_b64_tr_b16 v[118:119], v21 offset:12768\n"
    "v_exp_f32 v128, v128 clamp\n"
    "v_mfma_f32_16x16x32_bf16 v[224:227], a[96:99], v[104:107], v[224:227]\n"
    "ds_read_b64_tr_b16 v[120:121], v21 offset:256\n"
    "v_max_f32 v87, s87, v87\n"
    "v_mfma_f32_16x16x32_bf16 v[224:227], a[104:107], v[108:111], v[224:227]\n"
    "ds_read_b64_tr_b16 v[122:123], v21 offset:2560\n"
    "v_add_f32 v85, v85, v130\n"
    "v_mul_f32 v130, 0x3fb8aa3b, v88\n"
    "v_mfma_f32_16x16x32_bf16 v[228:231], a[100:103], v[104:107], v[228:231]\n"
    "ds_read_b64_tr_b16 v[124:125], v21 offset:10496\n"
    "v_exp_f32 v129, v129 clamp\n"
    "v_mfma_f32_16x16x32_bf16 v[228:231], a[108:111], v[108:111], v[228:231]\n"
    "s_waitcnt lgkmcnt(3)\n"
    "ds_read_b64_tr_b16 v[126:127], v21 offset:12800\n"
    "v_max_f32 v88, s87, v88\n"
    "v_mfma_f32_16x16x32_bf16 v[232:235], a[96:99], v[112:115], v[232:235]\n"
    "s_waitcnt vmcnt(5)\n"
    "s_barrier\n"
    "ds_read_b64_tr_b16 v[104:105], v20 offset:0\n"
    "v_mfma_f32_16x16x32_bf16 v[232:235], a[104:107], v[116:119], v[232:235]\n"
    "ds_read_b64_tr_b16 v[106:107], v20 offset:2304\n"
    "v_add_f32 v86, v86, v128\n"
    "v_mfma_f32_16x16x32_bf16 v[236:239], a[100:103], v[112:115], v[236:239]\n"
    "s_add_u32 s85, s64, 0\n"
    "s_add_u32 m0, s89, 40960\n"
    "ds_read_b64_tr_b16 v[108:109], v20 offset:10240\n"
    "buffer_load_dwordx4 v10, s[36:39], s85 offen lds\n"
    "v_mfma_f32_16x16x32_bf16 v[236:239], a[108:111], v[116:119], v[236:239]\n"
    "s_waitcnt lgkmcnt(3)\n"
    "ds_read_b64_tr_b16 v[110:111], v20 offset:12544\n"
    "v_cvt_pk_bf16_f32 v146, v84, v85\n"
    "v_mul_f32 v128, 0x3fb8aa3b, v89\n"
    "v_mfma_f32_16x16x32_bf16 v[240:243], a[96:99], v[120:123], v[240:243]\n"
    "ds_read_b64_tr_b16 v[112:113], v20 offset:32\n"
    "buffer_load_dwordx4 v11, s[36:39], s85 offen offset:1024 lds\n"
    "v_mfma_f32_16x16x32_bf16 v[240:243], a[104:107], v[124:127], v[240:243]\n"
    "ds_read_b64_tr_b16 v[114:115], v20 offset:2336\n"
    "v_exp_f32 v130, v130 clamp\n"
    "v_mfma_f32_16x16x32_bf16 v[244:247], a[100:103], v[120:123], v[244:247]\n"
    "ds_read_b64_tr_b16 v[116:117], v20 offset:10272\n"
    "buffer_load_dwordx4 v12, s[36:39], s85 offen offset:2048 lds\n"
    "v_mfma_f32_16x16x32_bf16 v[244:247], a[108:111], v[124:127], v[244:247]\n"
    "s_waitcnt lgkmcnt(3)\n"
    "ds_read_b64_tr_b16 v[118:119], v20 offset:12576\n"
    "v_max_f32 v89, s87, v89\n"
    "v_mfma_f32_16x16x32_bf16 v[176:179], a[112:115], v[104:107], v[176:179]\n"
    "ds_read_b64_tr_b16 v[120:121], v20 offset:64\n"
    "buffer_load_dwordx4 v13, s[36:39], s85 offen offset:3072 lds\n"
    "v_mfma_f32_16x16x32_bf16 v[176:179], a[120:123], v[108:111], v[176:179]\n"
    "ds_read_b64_tr_b16 v[122:123], v20 offset:2368\n"
    "v_add_f32 v87, v87, v129\n"
    "v_mul_f32 v129, 0x3fb8aa3b, v90\n"
    "v_mfma_f32_16x16x32_bf16 v[180:183], a[116:119], v[104:107], v[180:183]\n"
    "s_add_u32 m0, s89, 45056\n"
    "ds_read_b64_tr_b16 v[124:125], v20 offset:10304\n"
    "buffer_load_dwordx4 v14, s[36:39], s85 offen lds\n"
    "v_mfma_f32_16x16x32_bf16 v[180:183], a[124:127], v[108:111], v[180:183]\n"
    "s_waitcnt lgkmcnt(3)\n"
    "ds_read_b64_tr_b16 v[126:127], v20 offset:12608\n"
    "v_exp_f32 v128, v128 clamp\n"
    "v_mfma_f32_16x16x32_bf16 v[184:187], a[112:115], v[112:115], v[184:187]\n"
    "ds_read_b64_tr_b16 v[104:105], v20 offset:96\n"
    "v_max_f32 v90, s87, v90\n"
    "v_mfma_f32_16x16x32_bf16 v[184:187], a[120:123], v[116:119], v[184:187]\n"
    "ds_read_b64_tr_b16 v[106:107], v20 offset:2400\n"
    "v_add_f32 v88, v88, v130\n"
    "v_cvt_pk_bf16_f32 v147, v86, v87\n"
    "v_mfma_f32_16x16x32_bf16 v[188:191], a[116:119], v[112:115], v[188:191]\n"
    "ds_read_b64_tr_b16 v[108:109], v20 offset:10336\n"
    "ds_write_b64 v26, v[146:147] offset:6152\n"
    "v_mfma_f32_16x16x32_bf16 v[188:191], a[124:127], v[116:119], v[188:191]\n"
    "s_waitcnt lgkmcnt(4)\n"
    "ds_read_b64_tr_b16 v[110:111], v20 offset:12640\n"
    "v_mul_f32 v130, 0x3fb8aa3b, v91\n"
    "v_mfma_f32_16x16x32_bf16 v[192:195], a[112:115], v[120:123], v[192:195]\n"
    "ds_read_b64_tr_b16 v[112:113], v20 offset:128\n"
    "v_exp_f32 v129, v129 clamp\n"
    "v_max_f32 v91, s87, v91\n"
    "v_mfma_f32_16x16x32_bf16 v[192:195], a[120:123], v[124:127], v[192:195]\n"
    "ds_read_b64_tr_b16 v[114:115], v20 offset:2432\n"
    "v_add_f32 v89, v89, v128\n"
    "v_mfma_f32_16x16x32_bf16 v[196:199], a[116:119], v[120:123], v[196:199]\n"
    "ds_read_b64_tr_b16 v[116:117], v20 offset:10368\n"
    "v_mul_f32 v128, 0x3fb8aa3b, v92\n"
    "v_mfma_f32_16x16x32_bf16 v[196:199], a[124:127], v[124:127], v[196:199]\n"
    "s_waitcnt lgkmcnt(3)\n"
    "ds_read_b64_tr_b16 v[118:119], v20 offset:12672\n"
    "v_exp_f32 v130, v130 clamp\n"
    "v_mfma_f32_16x16x32_bf16 v[200:203], a[112:115], v[104:107], v[200:203]\n"
    "ds_read_b64_tr_b16 v[120:121], v20 offset:160\n"
    "v_max_f32 v92, s87, v92\n"
    "v_add_f32 v90, v90, v129\n"
    "v_mfma_f32_16x16x32_bf16 v[200:203], a[120:123], v[108:111], v[200:203]\n"
    "ds_read_b64_tr_b16 v[122:123], v20 offset:2464\n"
    "v_cvt_pk_bf16_f32 v148, v88, v89\n"
    "v_mfma_f32_16x16x32_bf16 v[204:207], a[116:119], v[104:107], v[204:207]\n"
    "ds_read_b64_tr_b16 v[124:125], v20 offset:10400\n"
    "v_mul_f32 v129, 0x3fb8aa3b, v93\n"
    "v_mfma_f32_16x16x32_bf16 v[204:207], a[124:127], v[108:111], v[204:207]\n"
    "s_waitcnt lgkmcnt(3)\n"
    "ds_read_b64_tr_b16 v[126:127], v20 offset:12704\n"
    "v_exp_f32 v128, v128 clamp\n"
    "v_max_f32 v93, s87, v93\n"
    "v_mfma_f32_16x16x32_bf16 v[208:211], a[112:115], v[112:115], v[208:211]\n"
    "ds_read_b64_tr_b16 v[104:105], v20 offset:192\n"
    "v_add_f32 v91, v91, v130\n"
    "v_mfma_f32_16x16x32_bf16 v[208:211], a[120:123], v[116:119], v[208:211]\n"
    "ds_read_b64_tr_b16 v[106:107], v20 offset:2496\n"
    "v_mul_f32 v130, 0x3fb8aa3b, v94\n"
    "v_mfma_f32_16x16x32_bf16 v[212:215], a[116:119], v[112:115], v[212:215]\n"
    "ds_read_b64_tr_b16 v[108:109], v20 offset:10432\n"
    "v_exp_f32 v129, v129 clamp\n"
    "v_max_f32 v94, s87, v94\n"
    "v_mfma_f32_16x16x32_bf16 v[212:215], a[124:127], v[116:119], v[212:215]\n"
    "s_waitcnt lgkmcnt(3)\n"
    "ds_read_b64_tr_b16 v[110:111], v20 offset:12736\n"
    "v_add_f32 v92, v92, v128\n"
    "v_mfma_f32_16x16x32_bf16 v[216:219], a[112:115], v[120:123], v[216:219]\n"
    "ds_read_b64_tr_b16 v[112:113], v20 offset:224\n"
    "v_cvt_pk_bf16_f32 v149, v90, v91\n"
    "v_mfma_f32_16x16x32_bf16 v[216:219], a[120:123], v[124:127], v[216:219]\n"
    "ds_read_b64_tr_b16 v[114:115], v20 offset:2528\n"
    "ds_write_b64 v26, v[148:149] offset:7168\n"
    "v_mul_f32 v128, 0x3fb8aa3b, v95\n"
    "v_mfma_f32_16x16x32_bf16 v[220:223], a[116:119], v[120:123], v[220:223]\n"
    "ds_read_b64_tr_b16 v[116:117], v20 offset:10464\n"
    "v_exp_f32 v130, v130 clamp\n"
    "v_mfma_f32_16x16x32_bf16 v[220:223], a[124:127], v[124:127], v[220:223]\n"
    "s_waitcnt lgkmcnt(4)\n"
    "ds_read_b64_tr_b16 v[118:119], v20 offset:12768\n"
    "v_max_f32 v95, s87, v95\n"
    "v_mfma_f32_16x16x32_bf16 v[224:227], a[112:115], v[104:107], v[224:227]\n"
    "ds_read_b64_tr_b16 v[120:121], v20 offset:256\n"
    "v_add_f32 v93, v93, v129\n"
    "v_mfma_f32_16x16x32_bf16 v[224:227], a[120:123], v[108:111], v[224:227]\n"
    "ds_read_b64_tr_b16 v[122:123], v20 offset:2560\n"
    "v_mul_f32 v129, 0x3fb8aa3b, v96\n"
    "v_exp_f32 v128, v128 clamp\n"
    "v_mfma_f32_16x16x32_bf16 v[228:231], a[116:119], v[104:107], v[228:231]\n"
    "ds_read_b64_tr_b16 v[124:125], v20 offset:10496\n"
    "v_max_f32 v96, s87, v96\n"
    "v_mfma_f32_16x16x32_bf16 v[228:231], a[124:127], v[108:111], v[228:231]\n"
    "s_waitcnt lgkmcnt(3)\n"
    "ds_read_b64_tr_b16 v[126:127], v20 offset:12800\n"
    "v_add_f32 v94, v94, v130\n"
    "v_mfma_f32_16x16x32_bf16 v[232:235], a[112:115], v[112:115], v[232:235]\n"
    "s_waitcnt vmcnt(5)\n"
    "s_barrier\n"
    "ds_read_b64_tr_b16 v[104:105], v20 offset:20480\n"
    "v_mfma_f32_16x16x32_bf16 v[232:235], a[120:123], v[116:119], v[232:235]\n"
    "ds_read_b64_tr_b16 v[106:107], v20 offset:22784\n"
    "v_cvt_pk_bf16_f32 v150, v92, v93\n"
    "v_mul_f32 v130, 0x3fb8aa3b, v97\n"
    "v_mfma_f32_16x16x32_bf16 v[236:239], a[116:119], v[112:115], v[236:239]\n"
    "s_add_u32 s85, s65, 0\n"
    "s_add_u32 m0, s89, 61440\n"
    "ds_read_b64_tr_b16 v[108:109], v20 offset:30720\n"
    "buffer_load_dwordx4 v10, s[36:39], s85 offen lds\n"
    "v_mfma_f32_16x16x32_bf16 v[236:239], a[124:127], v[116:119], v[236:239]\n"
    "s_waitcnt lgkmcnt(3)\n"
    "ds_read_b64_tr_b16 v[110:111], v20 offset:33024\n"
    "v_exp_f32 v129, v129 clamp\n"
    "v_mfma_f32_16x16x32_bf16 v[240:243], a[112:115], v[120:123], v[240:243]\n"
    "ds_read_b64_tr_b16 v[112:113], v20 offset:20512\n"
    "buffer_load_dwordx4 v11, s[36:39], s85 offen offset:1024 lds\n"
    "v_mfma_f32_16x16x32_bf16 v[240:243], a[120:123], v[124:127], v[240:243]\n"
    "ds_read_b64_tr_b16 v[114:115], v20 offset:22816\n"
    "v_max_f32 v97, s87, v97\n"
    "v_mfma_f32_16x16x32_bf16 v[244:247], a[116:119], v[120:123], v[244:247]\n"
    "ds_read_b64_tr_b16 v[116:117], v20 offset:30752\n"
    "buffer_load_dwordx4 v12, s[36:39], s85 offen offset:2048 lds\n"
    "v_mfma_f32_16x16x32_bf16 v[244:247], a[124:127], v[124:127], v[244:247]\n"
    "s_waitcnt lgkmcnt(3)\n"
    "ds_read_b64_tr_b16 v[118:119], v20 offset:33056\n"
    "v_add_f32 v95, v95, v128\n"
    "v_mul_f32 v128, 0x3fb8aa3b, v98\n"
    "v_mfma_f32_16x16x32_bf16 v[176:179], a[128:131], v[104:107], v[176:179]\n"
    "ds_read_b64_tr_b16 v[120:121], v20 offset:20544\n"
    "buffer_load_dwordx4 v13, s[36:39], s85 offen offset:3072 lds\n"
    "v_mfma_f32_16x16x32_bf16 v[176:179], a[136:139], v[108:111], v[176:179]\n"
    "ds_read_b64_tr_b16 v[122:123], v20 offset:22848\n"
    "v_exp_f32 v130, v130 clamp\n"
    "v_mfma_f32_16x16x32_bf16 v[180:183], a[132:135], v[104:107], v[180:183]\n"
    "s_add_u32 m0, s89, 65536\n"
    "ds_read_b64_tr_b16 v[124:125], v20 offset:30784\n"
    "buffer_load_dwordx4 v14, s[36:39], s85 offen lds\n"
    "v_mfma_f32_16x16x32_bf16 v[180:183], a[140:143], v[108:111], v[180:183]\n"
    "s_waitcnt lgkmcnt(3)\n"
    "ds_read_b64_tr_b16 v[126:127], v20 offset:33088\n"
    "v_max_f32 v98, s87, v98\n"
    "v_mfma_f32_16x16x32_bf16 v[184:187], a[128:131], v[112:115], v[184:187]\n"
    "ds_read_b64_tr_b16 v[104:105], v20 offset:20576\n"
    "v_add_f32 v96, v96, v129\n"
    "v_mfma_f32_16x16x32_bf16 v[184:187], a[136:139], v[116:119], v[184:187]\n"
    "ds_read_b64_tr_b16 v[106:107], v20 offset:22880\n"
    "v_cvt_pk_bf16_f32 v151, v94, v95\n"
    "ds_write_b64 v26, v[150:151] offset:7176\n"
    "v_mfma_f32_16x16x32_bf16 v[188:191], a[132:135], v[112:115], v[188:191]\n"
    "ds_read_b64_tr_b16 v[108:109], v20 offset:30816\n"
    "v_mul_f32 v129, 0x3fb8aa3b, v99\n"
    "v_mfma_f32_16x16x32_bf16 v[188:191], a[140:143], v[116:119], v[188:191]\n"
    "s_waitcnt lgkmcnt(4)\n"
    "ds_read_b64_tr_b16 v[110:111], v20 offset:33120\n"
    "v_exp_f32 v128, v128 clamp\n"
    "v_mfma_f32_16x16x32_bf16 v[192:195], a[128:131], v[120:123], v[192:195]\n"
    "ds_read_b64_tr_b16 v[112:113], v20 offset:20608\n"
    "v_max_f32 v99, s87, v99\n"
    "v_add_f32 v97, v97, v130\n"
    "v_mfma_f32_16x16x32_bf16 v[192:195], a[136:139], v[124:127], v[192:195]\n"
    "ds_read_b64_tr_b16 v[114:115], v20 offset:22912\n"
    "v_mul_f32 v130, 0x3fb8aa3b, v100\n"
    "v_mfma_f32_16x16x32_bf16 v[196:199], a[132:135], v[120:123], v[196:199]\n"
    "ds_read_b64_tr_b16 v[116:117], v20 offset:30848\n"
    "v_exp_f32 v129, v129 clamp\n"
    "v_mfma_f32_16x16x32_bf16 v[196:199], a[140:143], v[124:127], v[196:199]\n"
    "s_waitcnt lgkmcnt(3)\n"
    "ds_read_b64_tr_b16 v[118:119], v20 offset:33152\n"
    "v_max_f32 v100, s87, v100\n"
    "v_add_f32 v98, v98, v128\n"
    "v_mfma_f32_16x16x32_bf16 v[200:203], a[128:131], v[104:107], v[200:203]\n"
    "ds_read_b64_tr_b16 v[120:121], v20 offset:20640\n"
    "v_cvt_pk_bf16_f32 v144, v96, v97\n"
    "v_mfma_f32_16x16x32_bf16 v[200:203], a[136:139], v[108:111], v[200:203]\n"
    "ds_read_b64_tr_b16 v[122:123], v20 offset:22944\n"
    "v_mul_f32 v128, 0x3fb8aa3b, v101\n"
    "v_mfma_f32_16x16x32_bf16 v[204:207], a[132:135], v[104:107], v[204:207]\n"
    "ds_read_b64_tr_b16 v[124:125], v20 offset:30880\n"
    "v_exp_f32 v130, v130 clamp\n"
    "v_max_f32 v101, s87, v101\n"
    "v_mfma_f32_16x16x32_bf16 v[204:207], a[140:143], v[108:111], v[204:207]\n"
    "s_waitcnt lgkmcnt(3)\n"
    "ds_read_b64_tr_b16 v[126:127], v20 offset:33184\n"
    "v_add_f32 v99, v99, v129\n"
    "v_mfma_f32_16x16x32_bf16 v[208:211], a[128:131], v[112:115], v[208:211]\n"
    "ds_read_b64_tr_b16 v[104:105], v20 offset:20672\n"
    "v_mul_f32 v129, 0x3fb8aa3b, v102\n"
    "v_mfma_f32_16x16x32_bf16 v[208:211], a[136:139], v[116:119], v[208:211]\n"
    "ds_read_b64_tr_b16 v[106:107], v20 offset:22976\n"
    "v_exp_f32 v128, v128 clamp\n"
    "v_mfma_f32_16x16x32_bf16 v[212:215], a[132:135], v[112:115], v[212:215]\n"
    "ds_read_b64_tr_b16 v[108:109], v20 offset:30912\n"
    "v_max_f32 v102, s87, v102\n"
    "v_add_f32 v100, v100, v130\n"
    "v_mfma_f32_16x16x32_bf16 v[212:215], a[140:143], v[116:119], v[212:215]\n"
    "s_waitcnt lgkmcnt(3)\n"
    "ds_read_b64_tr_b16 v[110:111], v20 offset:33216\n"
    "v_cvt_pk_bf16_f32 v145, v98, v99\n"
    "v_mfma_f32_16x16x32_bf16 v[216:219], a[128:131], v[120:123], v[216:219]\n"
    "ds_read_b64_tr_b16 v[112:113], v20 offset:20704\n"
    "ds_write_b64 v26, v[144:145] offset:8192\n"
    "v_mfma_f32_16x16x32_bf16 v[216:219], a[136:139], v[124:127], v[216:219]\n"
    "ds_read_b64_tr_b16 v[114:115], v20 offset:23008\n"
    "v_mul_f32 v130, 0x3fb8aa3b, v103\n"
    "v_exp_f32 v129, v129 clamp\n"
    "v_mfma_f32_16x16x32_bf16 v[220:223], a[132:135], v[120:123], v[220:223]\n"
    "ds_read_b64_tr_b16 v[116:117], v20 offset:30944\n"
    "v_max_f32 v103, s87, v103\n"
    "v_mfma_f32_16x16x32_bf16 v[220:223], a[140:143], v[124:127], v[220:223]\n"
    "s_waitcnt lgkmcnt(4)\n"
    "ds_read_b64_tr_b16 v[118:119], v20 offset:33248\n"
    "v_add_f32 v101, v101, v128\n"
    "v_mfma_f32_16x16x32_bf16 v[224:227], a[128:131], v[104:107], v[224:227]\n"
    "ds_read_b64_tr_b16 v[120:121], v20 offset:20736\n"
    "v_exp_f32 v130, v130 clamp\n"
    "v_add_f32 v102, v102, v129\n"
    "v_mfma_f32_16x16x32_bf16 v[224:227], a[136:139], v[108:111], v[224:227]\n"
    "ds_read_b64_tr_b16 v[122:123], v20 offset:23040\n"
    "v_cvt_pk_bf16_f32 v146, v100, v101\n"
    "v_mfma_f32_16x16x32_bf16 v[228:231], a[132:135], v[104:107], v[228:231]\n"
    "ds_read_b64_tr_b16 v[124:125], v20 offset:30976\n"
    "v_add_f32 v103, v103, v130\n"
    "v_mfma_f32_16x16x32_bf16 v[228:231], a[140:143], v[108:111], v[228:231]\n"
    "s_waitcnt lgkmcnt(3)\n"
    "ds_read_b64_tr_b16 v[126:127], v20 offset:33280\n"
    "v_cvt_pk_bf16_f32 v147, v102, v103\n"
    "ds_write_b64 v26, v[146:147] offset:8200\n"
    "v_mfma_f32_16x16x32_bf16 v[232:235], a[128:131], v[112:115], v[232:235]\n"
    "s_waitcnt vmcnt(5)\n"
    "s_barrier\n"
    "ds_read_b64_tr_b16 v[104:105], v20 offset:40960\n"
    "v_mfma_f32_16x16x32_bf16 v[232:235], a[136:139], v[116:119], v[232:235]\n"
    "ds_read_b64_tr_b16 v[106:107], v20 offset:43264\n"
    "v_mfma_f32_16x16x32_bf16 v[236:239], a[132:135], v[112:115], v[236:239]\n"
    "s_add_u32 s85, s66, 0\n"
    "s_add_u32 m0, s89, 0\n"
    "ds_read_b64_tr_b16 v[108:109], v20 offset:51200\n"
    "buffer_load_dwordx4 v10, s[36:39], s85 offen lds\n"
    "v_mfma_f32_16x16x32_bf16 v[236:239], a[140:143], v[116:119], v[236:239]\n"
    "s_waitcnt lgkmcnt(4)\n"
    "ds_read_b64_tr_b16 v[110:111], v20 offset:53504\n"
    "v_mfma_f32_16x16x32_bf16 v[240:243], a[128:131], v[120:123], v[240:243]\n"
    "ds_read_b64_tr_b16 v[112:113], v20 offset:40992\n"
    "buffer_load_dwordx4 v11, s[36:39], s85 offen offset:1024 lds\n"
    "v_mfma_f32_16x16x32_bf16 v[240:243], a[136:139], v[124:127], v[240:243]\n"
    "ds_read_b64_tr_b16 v[114:115], v20 offset:43296\n"
    "v_mfma_f32_16x16x32_bf16 v[244:247], a[132:135], v[120:123], v[244:247]\n"
    "ds_read_b64_tr_b16 v[116:117], v20 offset:51232\n"
    "buffer_load_dwordx4 v12, s[36:39], s85 offen offset:2048 lds\n"
    "v_mfma_f32_16x16x32_bf16 v[244:247], a[140:143], v[124:127], v[244:247]\n"
    "s_waitcnt lgkmcnt(3)\n"
    "ds_read_b64_tr_b16 v[118:119], v20 offset:53536\n"
    "buffer_load_dwordx4 v13, s[36:39], s85 offen offset:3072 lds\n"
    "s_add_u32 m0, s89, 4096\n"
    "s_nop 0\n"
    "buffer_load_dwordx4 v14, s[36:39], s85 offen lds\n"
    "v_cvt_pk_bf16_f32 v144, v176, v177\n"
    "v_cvt_pk_bf16_f32 v145, v178, v179\n"
    "v_cvt_pk_bf16_f32 v146, v180, v181\n"
    "v_cvt_pk_bf16_f32 v147, v182, v183\n"
    "ds_write_b128 v26, v[144:147] offset:36864\n"
    "v_cvt_pk_bf16_f32 v148, v184, v185\n"
    "v_cvt_pk_bf16_f32 v149, v186, v187\n"
    "v_cvt_pk_bf16_f32 v150, v188, v189\n"
    "v_cvt_pk_bf16_f32 v151, v190, v191\n"
    "ds_write_b128 v26, v[148:151] offset:37888\n"
    "v_cvt_pk_bf16_f32 v144, v192, v193\n"
    "v_cvt_pk_bf16_f32 v145, v194, v195\n"
    "v_cvt_pk_bf16_f32 v146, v196, v197\n"
    "v_cvt_pk_bf16_f32 v147, v198, v199\n"
    "ds_write_b128 v26, v[144:147] offset:38912\n"
    "v_cvt_pk_bf16_f32 v148, v200, v201\n"
    "v_cvt_pk_bf16_f32 v149, v202, v203\n"
    "v_cvt_pk_bf16_f32 v150, v204, v205\n"
    "v_cvt_pk_bf16_f32 v151, v206, v207\n"
    "ds_write_b128 v26, v[148:151] offset:39936\n"
    "v_cvt_pk_bf16_f32 v144, v208, v209\n"
    "v_cvt_pk_bf16_f32 v145, v210, v211\n"
    "v_cvt_pk_bf16_f32 v146, v212, v213\n"
    "v_cvt_pk_bf16_f32 v147, v214, v215\n"
    "ds_write_b128 v26, v[144:147] offset:40960\n"
    "v_cvt_pk_bf16_f32 v148, v216, v217\n"
    "v_cvt_pk_bf16_f32 v149, v218, v219\n"
    "v_cvt_pk_bf16_f32 v150, v220, v221\n"
    "v_cvt_pk_bf16_f32 v151, v222, v223\n"
    "ds_write_b128 v26, v[148:151] offset:41984\n"
    "v_cvt_pk_bf16_f32 v144, v224, v225\n"
    "v_cvt_pk_bf16_f32 v145, v226, v227\n"
    "v_cvt_pk_bf16_f32 v146, v228, v229\n"
    "v_cvt_pk_bf16_f32 v147, v230, v231\n"
    "ds_write_b128 v26, v[144:147] offset:43008\n"
    "v_cvt_pk_bf16_f32 v148, v232, v233\n"
    "v_cvt_pk_bf16_f32 v149, v234, v235\n"
    "v_cvt_pk_bf16_f32 v150, v236, v237\n"
    "v_cvt_pk_bf16_f32 v151, v238, v239\n"
    "ds_write_b128 v26, v[148:151] offset:44032\n"
    "v_cvt_pk_bf16_f32 v144, v240, v241\n"
    "v_cvt_pk_bf16_f32 v145, v242, v243\n"
    "v_cvt_pk_bf16_f32 v146, v244, v245\n"
    "v_cvt_pk_bf16_f32 v147, v246, v247\n"
    "ds_write_b128 v26, v[144:147] offset:45056\n"
    "s_lshl_b32 s30, s80, 10\n"
    "s_add_u32 s30, s30, 125952\n"
    "v_add_u32 v128, s30, v154\n"
    "v_add_u32 v129, 90112, v154\n"
    "s_waitcnt lgkmcnt(0)\n"
    "s_barrier\n"
    "ds_read_b128 a[144:147], v27 offset:0\n"
    "ds_read_b128 a[148:151], v27 offset:1024\n"
    "ds_read_b128 a[152:155], v28 offset:0\n"
    "ds_read_b128 a[156:159], v28 offset:1024\n"
    "ds_read_b128 a[160:163], v28 offset:2048\n"
    "ds_read_b128 a[164:167], v28 offset:3072\n"
    "ds_read_b128 a[168:171], v129 offset:0\n"
    "ds_read_b128 a[172:175], v128 offset:0\n"
    "s_waitcnt lgkmcnt(0)\n"
    "v_mfma_f32_16x16x32_bf16 v[176:179], a[144:147], a[152:155], 0\n"
    "ds_read_b128 a[176:179], v27 offset:9216\n"
    "v_mfma_f32_16x16x32_bf16 v[180:183], a[144:147], a[156:159], 0\n"
    "ds_read_b128 a[180:183], v27 offset:10240\n"
    "v_mfma_f32_16x16x32_bf16 v[184:187], a[144:147], a[160:163], 0\n"
    "ds_read_b128 a[184:187], v28 offset:9216\n"
    "v_mfma_f32_16x16x32_bf16 v[188:191], a[144:147], v[140:143], 0\n"
    "ds_read_b128 a[188:191], v28 offset:10240\n"
    "v_mfma_f32_16x16x32_bf16 v[192:195], a[148:151], a[156:159], 0\n"
    "ds_read_b128 a[192:195], v28 offset:11264\n"
    "v_mfma_f32_16x16x32_bf16 v[196:199], a[148:151], a[160:163], 0\n"
    "ds_read_b128 a[196:199], v28 offset:12288\n"
    "v_mfma_f32_16x16x32_bf16 v[200:203], a[148:151], a[164:167], 0\n"
    "ds_read_b128 a[200:203], v129 offset:9216\n"
    "v_mfma_f32_16x16x32_bf16 v[204:207], a[148:151], v[140:143], 0\n"
    "ds_read_b128 a[204:207], v128 offset:9216\n"
    "v_mfma_f32_16x16x32_bf16 v[208:211], a[168:171], a[172:175], 0\n"
    "v_mfma_f32_16x16x32_bf16 v[212:215], a[168:171], v[140:143], 0\n"
    "s_waitcnt lgkmcnt(0)\n"
    "v_mfma_f32_16x16x32_bf16 v[176:179], a[176:179], a[184:187], v[176:179]\n"
    "ds_read_b128 a[144:147], v27 offset:18432\n"
    "v_mfma_f32_16x16x32_bf16 v[180:183], a[176:179], a[188:191], v[180:183]\n"
    "ds_read_b128 a[148:151], v27 offset:19456\n"
    "v_mfma_f32_16x16x32_bf16 v[184:187], a[176:179], a[192:195], v[184:187]\n"
    "ds_read_b128 a[152:155], v28 offset:18432\n"
    "v_mfma_f32_16x16x32_bf16 v[188:191], a[176:179], v[140:143], v[188:191]\n"
    "ds_read_b128 a[156:159], v28 offset:19456\n"
    "v_mfma_f32_16x16x32_bf16 v[192:195], a[180:183], a[188:191], v[192:195]\n"
    "ds_read_b128 a[160:163], v28 offset:20480\n"
    "v_mfma_f32_16x16x32_bf16 v[196:199], a[180:183], a[192:195], v[196:199]\n"
    "ds_read_b128 a[164:167], v28 offset:21504\n"
    "v_mfma_f32_16x16x32_bf16 v[200:203], a[180:183], a[196:199], v[200:203]\n"
    "ds_read_b128 a[168:171], v129 offset:18432\n"
    "v_mfma_f32_16x16x32_bf16 v[204:207], a[180:183], v[140:143], v[204:207]\n"
    "ds_read_b128 a[172:175], v128 offset:18432\n"
    "v_mfma_f32_16x16x32_bf16 v[208:211], a[200:203], a[204:207], v[208:211]\n"
    "v_mfma_f32_16x16x32_bf16 v[212:215], a[200:203], v[140:143], v[212:215]\n"
    "s_waitcnt lgkmcnt(0)\n"
    "v_mfma_f32_16x16x32_bf16 v[176:179], a[144:147], a[152:155], v[176:179]\n"
    "ds_read_b128 a[176:179], v27 offset:27648\n"
    "v_mfma_f32_16x16x32_bf16 v[180:183], a[144:147], a[156:159], v[180:183]\n"
    "ds_read_b128 a[180:183], v27 offset:28672\n"
    "v_mfma_f32_16x16x32_bf16 v[184:187], a[144:147], a[160:163], v[184:187]\n"
    "ds_read_b128 a[184:187], v28 offset:27648\n"
    "v_mfma_f32_16x16x32_bf16 v[188:191], a[144:147], v[140:143], v[188:191]\n"
    "ds_read_b128 a[188:191], v28 offset:28672\n"
    "v_mfma_f32_16x16x32_bf16 v[192:195], a[148:151], a[156:159], v[192:195]\n"
    "ds_read_b128 a[192:195], v28 offset:29696\n"
    "v_mfma_f32_16x16x32_bf16 v[196:199], a[148:151], a[160:163], v[196:199]\n"
    "ds_read_b128 a[196:199], v28 offset:30720\n"
    "v_mfma_f32_16x16x32_bf16 v[200:203], a[148:151], a[164:167], v[200:203]\n"
    "ds_read_b128 a[200:203], v129 offset:27648\n"
    "v_mfma_f32_16x16x32_bf16 v[204:207], a[148:151], v[140:143], v[204:207]\n"
    "ds_read_b128 a[204:207], v128 offset:27648\n"
    "v_mfma_f32_16x16x32_bf16 v[208:211], a[168:171], a[172:175], v[208:211]\n"
    "v_mfma_f32_16x16x32_bf16 v[212:215], a[168:171], v[140:143], v[212:215]\n"
    "s_waitcnt lgkmcnt(0)\n"
    "v_mfma_f32_16x16x32_bf16 v[176:179], a[176:179], a[184:187], v[176:179]\n"
    "v_mfma_f32_16x16x32_bf16 v[180:183], a[176:179], a[188:191], v[180:183]\n"
    "v_mfma_f32_16x16x32_bf16 v[184:187], a[176:179], a[192:195], v[184:187]\n"
    "v_mfma_f32_16x16x32_bf16 v[188:191], a[176:179], v[140:143], v[188:191]\n"
    "v_mfma_f32_16x16x32_bf16 v[192:195], a[180:183], a[188:191], v[192:195]\n"
    "v_mfma_f32_16x16x32_bf16 v[196:199], a[180:183], a[192:195], v[196:199]\n"
    "v_mfma_f32_16x16x32_bf16 v[200:203], a[180:183], a[196:199], v[200:203]\n"
    "v_mfma_f32_16x16x32_bf16 v[204:207], a[180:183], v[140:143], v[204:207]\n"
    "v_mfma_f32_16x16x32_bf16 v[208:211], a[200:203], a[204:207], v[208:211]\n"
    "v_mfma_f32_16x16x32_bf16 v[212:215], a[200:203], v[140:143], v[212:215]\n"
    "s_lshl_b32 s30, s80, 17\n"
    "s_add_u32 s29, s92, s30\n"
    "s_cmp_eq_u32 s80, 3\n"
    "s_cselect_b64 vcc, -1, 0\n"
    "buffer_store_dwordx4 v[176:179], v154, s[44:47], s29 offen sc1\n"
    "s_add_u32 s29, s29, 16384\n"
    "buffer_store_dwordx4 v[180:183], v154, s[44:47], s29 offen sc1\n"
    "s_add_u32 s29, s29, 16384\n"
    "buffer_store_dwordx4 v[184:187], v154, s[44:47], s29 offen sc1\n"
    "s_add_u32 s29, s29, 16384\n"
    "buffer_store_dwordx4 v[188:191], v154, s[44:47], s29 offen sc1\n"
    "s_add_u32 s29, s29, 16384\n"
    "buffer_store_dwordx4 v[192:195], v154, s[44:47], s29 offen sc1\n"
    "s_add_u32 s29, s29, 16384\n"
    "buffer_store_dwordx4 v[196:199], v154, s[44:47], s29 offen sc1\n"
    "s_add_u32 s29, s29, 16384\n"
    "buffer_store_dwordx4 v[200:203], v154, s[44:47], s29 offen sc1\n"
    "s_add_u32 s29, s29, 16384\n"
    "buffer_store_dwordx4 v[204:207], v154, s[44:47], s29 offen sc1\n"
    "v_cndmask_b32 v208, v208, v212, vcc\n"
    "v_cndmask_b32 v209, v209, v213, vcc\n"
    "v_cndmask_b32 v210, v210, v214, vcc\n"
    "v_cndmask_b32 v211, v211, v215, vcc\n"
    "s_lshl_b32 s30, s80, 14\n"
    "s_add_u32 s29, s92, s30\n"
    "s_add_u32 s29, s29, 524288\n"
    "buffer_store_dwordx4 v[208:211], v154, s[44:47], s29 offen sc1\n"
    "v_mfma_f32_16x16x32_bf16 v[32:35], v[104:107], a[0:3], v[136:139]\n"
    "ds_read_b64_tr_b16 v[120:121], v20 offset:41024\n"
    "v_mfma_f32_16x16x32_bf16 v[32:35], v[108:111], a[8:11], v[32:35]\n"
    "ds_read_b64_tr_b16 v[122:123], v20 offset:43328\n"
    "v_mfma_f32_16x16x32_bf16 v[36:39], v[104:107], a[4:7], v[136:139]\n"
    "ds_read_b64_tr_b16 v[124:125], v20 offset:51264\n"
    "v_mfma_f32_16x16x32_bf16 v[36:39], v[108:111], a[12:15], v[36:39]\n"
    "s_waitcnt lgkmcnt(15)\n"
    "ds_read_b64_tr_b16 v[126:127], v20 offset:53568\n"
    "v_mfma_f32_16x16x32_bf16 v[40:43], v[112:115], a[0:3], v[136:139]\n"
    "ds_read_b64_tr_b16 v[104:105], v20 offset:41056\n"
    "v_mfma_f32_16x16x32_bf16 v[40:43], v[116:119], a[8:11], v[40:43]\n"
    "ds_read_b64_tr_b16 v[106:107], v20 offset:43360\n"
    "v_mfma_f32_16x16x32_bf16 v[44:47], v[112:115], a[4:7], v[136:139]\n"
    "ds_read_b64_tr_b16 v[108:109], v20 offset:51296\n"
    "v_mfma_f32_16x16x32_bf16 v[44:47], v[116:119], a[12:15], v[44:47]\n"
    "s_waitcnt lgkmcnt(3)\n"
    "ds_read_b64_tr_b16 v[110:111], v20 offset:53600\n"
    "v_mfma_f32_16x16x32_bf16 v[48:51], v[120:123], a[0:3], v[136:139]\n"
    "ds_read_b64_tr_b16 v[112:113], v20 offset:41088\n"
    "v_mfma_f32_16x16x32_bf16 v[48:51], v[124:127], a[8:11], v[48:51]\n"
    "ds_read_b64_tr_b16 v[114:115], v20 offset:43392\n"
    "v_mfma_f32_16x16x32_bf16 v[52:55], v[120:123], a[4:7], v[136:139]\n"
    "ds_read_b64_tr_b16 v[116:117], v20 offset:51328\n"
    "v_mfma_f32_16x16x32_bf16 v[52:55], v[124:127], a[12:15], v[52:55]\n"
    "s_waitcnt lgkmcnt(3)\n"
    "ds_read_b64_tr_b16 v[118:119], v20 offset:53632\n"
    "v_mfma_f32_16x16x32_bf16 v[56:59], v[104:107], a[0:3], v[136:139]\n"
    "ds_read_b64_tr_b16 v[120:121], v20 offset:41120\n"
    "v_mfma_f32_16x16x32_bf16 v[56:59], v[108:111], a[8:11], v[56:59]\n"
    "ds_read_b64_tr_b16 v[122:123], v20 offset:43424\n"
    "v_mfma_f32_16x16x32_bf16 v[60:63], v[104:107], a[4:7], v[136:139]\n"
    "ds_read_b64_tr_b16 v[124:125], v20 offset:51360\n"
    "v_mfma_f32_16x16x32_bf16 v[60:63], v[108:111], a[12:15], v[60:63]\n"
    "s_waitcnt lgkmcnt(3)\n"
    "ds_read_b64_tr_b16 v[126:127], v20 offset:53664\n"
    "v_mfma_f32_16x16x32_bf16 v[64:67], v[112:115], a[0:3], v[136:139]\n"
    "ds_read_b64_tr_b16 v[104:105], v20 offset:41152\n"
    "v_mfma_f32_16x16x32_bf16 v[64:67], v[116:119], a[8:11], v[64:67]\n"
    "ds_read_b64_tr_b16 v[106:107], v20 offset:43456\n"
    "v_mfma_f32_16x16x32_bf16 v[68:71], v[112:115], a[4:7], v[136:139]\n"
    "ds_read_b64_tr_b16 v[108:109], v20 offset:51392\n"
    "v_mfma_f32_16x16x32_bf16 v[68:71], v[116:119], a[12:15], v[68:71]\n"
    "s_waitcnt lgkmcnt(3)\n"
    "ds_read_b64_tr_b16 v[110:111], v20 offset:53696\n"
    "v_mfma_f32_16x16x32_bf16 v[72:75], v[120:123], a[0:3], v[136:139]\n"
    "ds_read_b64_tr_b16 v[112:113], v20 offset:41184\n"
    "v_mfma_f32_16x16x32_bf16 v[72:75], v[124:127], a[8:11], v[72:75]\n"
    "ds_read_b64_tr_b16 v[114:115], v20 offset:43488\n"
    "v_mfma_f32_16x16x32_bf16 v[76:79], v[120:123], a[4:7], v[136:139]\n"
    "ds_read_b64_tr_b16 v[116:117], v20 offset:51424\n"
    "v_mfma_f32_16x16x32_bf16 v[76:79], v[124:127], a[12:15], v[76:79]\n"
    "s_waitcnt lgkmcnt(3)\n"
    "ds_read_b64_tr_b16 v[118:119], v20 offset:53728\n"
    "v_mfma_f32_16x16x32_bf16 v[80:83], v[104:107], a[0:3], v[136:139]\n"
    "ds_read_b64_tr_b16 v[120:121], v20 offset:41216\n"
    "v_mfma_f32_16x16x32_bf16 v[80:83], v[108:111], a[8:11], v[80:83]\n"
    "ds_read_b64_tr_b16 v[122:123], v20 offset:43520\n"
    "v_mfma_f32_16x16x32_bf16 v[84:87], v[104:107], a[4:7], v[136:139]\n"
    "ds_read_b64_tr_b16 v[124:125], v20 offset:51456\n"
    "v_mfma_f32_16x16x32_bf16 v[84:87], v[108:111], a[12:15], v[84:87]\n"
    "s_waitcnt lgkmcnt(3)\n"
    "ds_read_b64_tr_b16 v[126:127], v20 offset:53760\n"
    "v_mfma_f32_16x16x32_bf16 v[88:91], v[112:115], a[0:3], v[136:139]\n"
    "s_waitcnt vmcnt(14)\n"
    "s_barrier\n"
    "ds_read_b64_tr_b16 v[104:105], v21 offset:0\n"
    "v_mfma_f32_16x16x32_bf16 v[88:91], v[116:119], a[8:11], v[88:91]\n"
    "ds_read_b64_tr_b16 v[106:107], v21 offset:2304\n"
    "v_mfma_f32_16x16x32_bf16 v[92:95], v[112:115], a[4:7], v[136:139]\n"
    "s_add_u32 s85, s67, 0\n"
    "s_add_u32 m0, s89, 20480\n"
    "ds_read_b64_tr_b16 v[108:109], v21 offset:10240\n"
    "buffer_load_dwordx4 v10, s[36:39], s85 offen lds\n"
    "v_mfma_f32_16x16x32_bf16 v[92:95], v[116:119], a[12:15], v[92:95]\n"
    "s_waitcnt lgkmcnt(3)\n"
    "ds_read_b64_tr_b16 v[110:111], v21 offset:12544\n"
    "v_mfma_f32_16x16x32_bf16 v[96:99], v[120:123], a[0:3], v[136:139]\n"
    "ds_read_b64_tr_b16 v[112:113], v21 offset:32\n"
    "buffer_load_dwordx4 v11, s[36:39], s85 offen offset:1024 lds\n"
    "v_mfma_f32_16x16x32_bf16 v[96:99], v[124:127], a[8:11], v[96:99]\n"
    "ds_read_b64_tr_b16 v[114:115], v21 offset:2336\n"
    "v_mfma_f32_16x16x32_bf16 v[100:103], v[120:123], a[4:7], v[136:139]\n"
    "ds_read_b64_tr_b16 v[116:117], v21 offset:10272\n"
    "buffer_load_dwordx4 v12, s[36:39], s85 offen offset:2048 lds\n"
    "v_mfma_f32_16x16x32_bf16 v[100:103], v[124:127], a[12:15], v[100:103]\n"
    "s_waitcnt lgkmcnt(3)\n"
    "ds_read_b64_tr_b16 v[118:119], v21 offset:12576\n"
    "v_mfma_f32_16x16x32_bf16 v[32:35], v[104:107], a[16:19], v[32:35]\n"
    "ds_read_b64_tr_b16 v[120:121], v21 offset:64\n"
    "buffer_load_dwordx4 v13, s[36:39], s85 offen offset:3072 lds\n"
    "v_mfma_f32_16x16x32_bf16 v[32:35], v[108:111], a[24:27], v[32:35]\n"
    "ds_read_b64_tr_b16 v[122:123], v21 offset:2368\n"
    "v_mfma_f32_16x16x32_bf16 v[36:39], v[104:107], a[20:23], v[36:39]\n"
    "s_add_u32 m0, s89, 24576\n"
    "ds_read_b64_tr_b16 v[124:125], v21 offset:10304\n"
    "buffer_load_dwordx4 v14, s[36:39], s85 offen lds\n"
    "v_mfma_f32_16x16x32_bf16 v[36:39], v[108:111], a[28:31], v[36:39]\n"
    "s_waitcnt lgkmcnt(3)\n"
    "ds_read_b64_tr_b16 v[126:127], v21 offset:12608\n"
    "v_mfma_f32_16x16x32_bf16 v[40:43], v[112:115], a[16:19], v[40:43]\n"
    "ds_read_b64_tr_b16 v[104:105], v21 offset:96\n"
    "v_mfma_f32_16x16x32_bf16 v[40:43], v[116:119], a[24:27], v[40:43]\n"
    "ds_read_b64_tr_b16 v[106:107], v21 offset:2400\n"
    "v_mfma_f32_16x16x32_bf16 v[44:47], v[112:115], a[20:23], v[44:47]\n"
    "ds_read_b64_tr_b16 v[108:109], v21 offset:10336\n"
    "v_mfma_f32_16x16x32_bf16 v[44:47], v[116:119], a[28:31], v[44:47]\n"
    "s_waitcnt lgkmcnt(3)\n"
    "ds_read_b64_tr_b16 v[110:111], v21 offset:12640\n"
    "v_mfma_f32_16x16x32_bf16 v[48:51], v[120:123], a[16:19], v[48:51]\n"
    "ds_read_b64_tr_b16 v[112:113], v21 offset:128\n"
    "v_mfma_f32_16x16x32_bf16 v[48:51], v[124:127], a[24:27], v[48:51]\n"
    "ds_read_b64_tr_b16 v[114:115], v21 offset:2432\n"
    "v_mfma_f32_16x16x32_bf16 v[52:55], v[120:123], a[20:23], v[52:55]\n"
    "ds_read_b64_tr_b16 v[116:117], v21 offset:10368\n"
    "v_mfma_f32_16x16x32_bf16 v[52:55], v[124:127], a[28:31], v[52:55]\n"
    "s_waitcnt lgkmcnt(3)\n"
    "ds_read_b64_tr_b16 v[118:119], v21 offset:12672\n"
    "v_mfma_f32_16x16x32_bf16 v[56:59], v[104:107], a[16:19], v[56:59]\n"
    "ds_read_b64_tr_b16 v[120:121], v21 offset:160\n"
    "v_mfma_f32_16x16x32_bf16 v[56:59], v[108:111], a[24:27], v[56:59]\n"
    "ds_read_b64_tr_b16 v[122:123], v21 offset:2464\n"
    "v_mfma_f32_16x16x32_bf16 v[60:63], v[104:107], a[20:23], v[60:63]\n"
    "ds_read_b64_tr_b16 v[124:125], v21 offset:10400\n"
    "v_mfma_f32_16x16x32_bf16 v[60:63], v[108:111], a[28:31], v[60:63]\n"
    "s_waitcnt lgkmcnt(3)\n"
    "ds_read_b64_tr_b16 v[126:127], v21 offset:12704\n"
    "v_mfma_f32_16x16x32_bf16 v[64:67], v[112:115], a[16:19], v[64:67]\n"
    "ds_read_b64_tr_b16 v[104:105], v21 offset:192\n"
    "v_mfma_f32_16x16x32_bf16 v[64:67], v[116:119], a[24:27], v[64:67]\n"
    "ds_read_b64_tr_b16 v[106:107], v21 offset:2496\n"
    "v_mfma_f32_16x16x32_bf16 v[68:71], v[112:115], a[20:23], v[68:71]\n"
    "ds_read_b64_tr_b16 v[108:109], v21 offset:10432\n"
    "v_mfma_f32_16x16x32_bf16 v[68:71], v[116:119], a[28:31], v[68:71]\n"
    "s_waitcnt lgkmcnt(3)\n"
    "ds_read_b64_tr_b16 v[110:111], v21 offset:12736\n"
    "v_mfma_f32_16x16x32_bf16 v[72:75], v[120:123], a[16:19], v[72:75]\n"
    "ds_read_b64_tr_b16 v[112:113], v21 offset:224\n"
    "v_mfma_f32_16x16x32_bf16 v[72:75], v[124:127], a[24:27], v[72:75]\n"
    "ds_read_b64_tr_b16 v[114:115], v21 offset:2528\n"
    "v_mfma_f32_16x16x32_bf16 v[76:79], v[120:123], a[20:23], v[76:79]\n"
    "ds_read_b64_tr_b16 v[116:117], v21 offset:10464\n"
    "v_mfma_f32_16x16x32_bf16 v[76:79], v[124:127], a[28:31], v[76:79]\n"
    "s_waitcnt lgkmcnt(3)\n"
    "ds_read_b64_tr_b16 v[118:119], v21 offset:12768\n"
    "v_mfma_f32_16x16x32_bf16 v[80:83], v[104:107], a[16:19], v[80:83]\n"
    "ds_read_b64_tr_b16 v[120:121], v21 offset:256\n"
    "v_mfma_f32_16x16x32_bf16 v[80:83], v[108:111], a[24:27], v[80:83]\n"
    "ds_read_b64_tr_b16 v[122:123], v21 offset:2560\n"
    "v_mfma_f32_16x16x32_bf16 v[84:87], v[104:107], a[20:23], v[84:87]\n"
    "ds_read_b64_tr_b16 v[124:125], v21 offset:10496\n"
    "v_mfma_f32_16x16x32_bf16 v[84:87], v[108:111], a[28:31], v[84:87]\n"
    "s_waitcnt lgkmcnt(3)\n"
    "ds_read_b64_tr_b16 v[126:127], v21 offset:12800\n"
    "v_mfma_f32_16x16x32_bf16 v[88:91], v[112:115], a[16:19], v[88:91]\n"
    "s_waitcnt vmcnt(5)\n"
    "s_barrier\n"
    "ds_read_b64_tr_b16 v[104:105], v20 offset:0\n"
    "v_mfma_f32_16x16x32_bf16 v[88:91], v[116:119], a[24:27], v[88:91]\n"
    "ds_read_b64_tr_b16 v[106:107], v20 offset:2304\n"
    "s_add_u32 s29, s97, 32768\n"
    "buffer_store_dwordx4 v[164:167], v168, s[44:47], s29 offen sc1\n"
    "v_mfma_f32_16x16x32_bf16 v[92:95], v[112:115], a[20:23], v[92:95]\n"
    "s_add_u32 s85, s68, 0\n"
    "s_add_u32 m0, s89, 40960\n"
    "ds_read_b64_tr_b16 v[108:109], v20 offset:10240\n"
    "buffer_load_dwordx4 v10, s[36:39], s85 offen lds\n"
    "v_mfma_f32_16x16x32_bf16 v[92:95], v[116:119], a[28:31], v[92:95]\n"
    "s_waitcnt lgkmcnt(3)\n"
    "ds_read_b64_tr_b16 v[110:111], v20 offset:12544\n"
    "v_mfma_f32_16x16x32_bf16 v[96:99], v[120:123], a[16:19], v[96:99]\n"
    "ds_read_b64_tr_b16 v[112:113], v20 offset:32\n"
    "buffer_load_dwordx4 v11, s[36:39], s85 offen offset:1024 lds\n"
    "v_mfma_f32_16x16x32_bf16 v[96:99], v[124:127], a[24:27], v[96:99]\n"
    "ds_read_b64_tr_b16 v[114:115], v20 offset:2336\n"
    "v_mfma_f32_16x16x32_bf16 v[100:103], v[120:123], a[20:23], v[100:103]\n"
    "ds_read_b64_tr_b16 v[116:117], v20 offset:10272\n"
    "buffer_load_dwordx4 v12, s[36:39], s85 offen offset:2048 lds\n"
    "v_mfma_f32_16x16x32_bf16 v[100:103], v[124:127], a[28:31], v[100:103]\n"
    "s_waitcnt lgkmcnt(3)\n"
    "ds_read_b64_tr_b16 v[118:119], v20 offset:12576\n"
    "v_mfma_f32_16x16x32_bf16 v[32:35], v[104:107], a[32:35], v[32:35]\n"
    "ds_read_b64_tr_b16 v[120:121], v20 offset:64\n"
    "buffer_load_dwordx4 v13, s[36:39], s85 offen offset:3072 lds\n"
    "v_mfma_f32_16x16x32_bf16 v[32:35], v[108:111], a[40:43], v[32:35]\n"
    "ds_read_b64_tr_b16 v[122:123], v20 offset:2368\n"
    "v_mfma_f32_16x16x32_bf16 v[36:39], v[104:107], a[36:39], v[36:39]\n"
    "s_add_u32 m0, s89, 45056\n"
    "ds_read_b64_tr_b16 v[124:125], v20 offset:10304\n"
    "buffer_load_dwordx4 v14, s[36:39], s85 offen lds\n"
    "v_mfma_f32_16x16x32_bf16 v[36:39], v[108:111], a[44:47], v[36:39]\n"
    "s_waitcnt lgkmcnt(3)\n"
    "ds_read_b64_tr_b16 v[126:127], v20 offset:12608\n"
    "v_mfma_f32_16x16x32_bf16 v[40:43], v[112:115], a[32:35], v[40:43]\n"
    "ds_read_b64_tr_b16 v[104:105], v20 offset:96\n"
    "v_mfma_f32_16x16x32_bf16 v[40:43], v[116:119], a[40:43], v[40:43]\n"
    "ds_read_b64_tr_b16 v[106:107], v20 offset:2400\n"
    "v_mfma_f32_16x16x32_bf16 v[44:47], v[112:115], a[36:39], v[44:47]\n"
    "ds_read_b64_tr_b16 v[108:109], v20 offset:10336\n"
    "v_mfma_f32_16x16x32_bf16 v[44:47], v[116:119], a[44:47], v[44:47]\n"
    "s_waitcnt lgkmcnt(3)\n"
    "ds_read_b64_tr_b16 v[110:111], v20 offset:12640\n"
    "v_mfma_f32_16x16x32_bf16 v[48:51], v[120:123], a[32:35], v[48:51]\n"
    "ds_read_b64_tr_b16 v[112:113], v20 offset:128\n"
    "v_mfma_f32_16x16x32_bf16 v[48:51], v[124:127], a[40:43], v[48:51]\n"
    "ds_read_b64_tr_b16 v[114:115], v20 offset:2432\n"
    "v_mfma_f32_16x16x32_bf16 v[52:55], v[120:123], a[36:39], v[52:55]\n"
    "ds_read_b64_tr_b16 v[116:117], v20 offset:10368\n"
    "v_mfma_f32_16x16x32_bf16 v[52:55], v[124:127], a[44:47], v[52:55]\n"
    "s_waitcnt lgkmcnt(3)\n"
    "ds_read_b64_tr_b16 v[118:119], v20 offset:12672\n"
    "v_mfma_f32_16x16x32_bf16 v[56:59], v[104:107], a[32:35], v[56:59]\n"
    "ds_read_b64_tr_b16 v[120:121], v20 offset:160\n"
    "v_mfma_f32_16x16x32_bf16 v[56:59], v[108:111], a[40:43], v[56:59]\n"
    "ds_read_b64_tr_b16 v[122:123], v20 offset:2464\n"
    "v_mfma_f32_16x16x32_bf16 v[60:63], v[104:107], a[36:39], v[60:63]\n"
    "ds_read_b64_tr_b16 v[124:125], v20 offset:10400\n"
    "v_mfma_f32_16x16x32_bf16 v[60:63], v[108:111], a[44:47], v[60:63]\n"
    "s_waitcnt lgkmcnt(3)\n"
    "ds_read_b64_tr_b16 v[126:127], v20 offset:12704\n"
    "v_mfma_f32_16x16x32_bf16 v[64:67], v[112:115], a[32:35], v[64:67]\n"
    "ds_read_b64_tr_b16 v[104:105], v20 offset:192\n"
    "v_mfma_f32_16x16x32_bf16 v[64:67], v[116:119], a[40:43], v[64:67]\n"
    "ds_read_b64_tr_b16 v[106:107], v20 offset:2496\n"
    "v_mfma_f32_16x16x32_bf16 v[68:71], v[112:115], a[36:39], v[68:71]\n"
    "ds_read_b64_tr_b16 v[108:109], v20 offset:10432\n"
    "v_mfma_f32_16x16x32_bf16 v[68:71], v[116:119], a[44:47], v[68:71]\n"
    "s_waitcnt lgkmcnt(3)\n"
    "ds_read_b64_tr_b16 v[110:111], v20 offset:12736\n"
    "v_mfma_f32_16x16x32_bf16 v[72:75], v[120:123], a[32:35], v[72:75]\n"
    "ds_read_b64_tr_b16 v[112:113], v20 offset:224\n"
    "v_mfma_f32_16x16x32_bf16 v[72:75], v[124:127], a[40:43], v[72:75]\n"
    "ds_read_b64_tr_b16 v[114:115], v20 offset:2528\n"
    "v_mfma_f32_16x16x32_bf16 v[76:79], v[120:123], a[36:39], v[76:79]\n"
    "ds_read_b64_tr_b16 v[116:117], v20 offset:10464\n"
    "v_mfma_f32_16x16x32_bf16 v[76:79], v[124:127], a[44:47], v[76:79]\n"
    "s_waitcnt lgkmcnt(3)\n"
    "ds_read_b64_tr_b16 v[118:119], v20 offset:12768\n"
    "v_mfma_f32_16x16x32_bf16 v[80:83], v[104:107], a[32:35], v[80:83]\n"
    "ds_read_b64_tr_b16 v[120:121], v20 offset:256\n"
    "v_mfma_f32_16x16x32_bf16 v[80:83], v[108:111], a[40:43], v[80:83]\n"
    "ds_read_b64_tr_b16 v[122:123], v20 offset:2560\n"
    "v_mfma_f32_16x16x32_bf16 v[84:87], v[104:107], a[36:39], v[84:87]\n"
    "ds_read_b64_tr_b16 v[124:125], v20 offset:10496\n"
    "v_mfma_f32_16x16x32_bf16 v[84:87], v[108:111], a[44:47], v[84:87]\n"
    "s_waitcnt lgkmcnt(3)\n"
    "ds_read_b64_tr_b16 v[126:127], v20 offset:12800\n"
    "v_mfma_f32_16x16x32_bf16 v[88:91], v[112:115], a[32:35], v[88:91]\n"
    "s_waitcnt vmcnt(6)\n"
    "s_barrier\n"
    "ds_read_b64_tr_b16 v[104:105], v20 offset:20480\n"
    "v_mfma_f32_16x16x32_bf16 v[88:91], v[116:119], a[40:43], v[88:91]\n"
    "ds_read_b64_tr_b16 v[106:107], v20 offset:22784\n"
    "v_mfma_f32_16x16x32_bf16 v[92:95], v[112:115], a[36:39], v[92:95]\n"
    "s_add_u32 s85, s69, 0\n"
    "s_add_u32 m0, s89, 61440\n"
    "ds_read_b64_tr_b16 v[108:109], v20 offset:30720\n"
    "buffer_load_dwordx4 v10, s[36:39], s85 offen lds\n"
    "v_mfma_f32_16x16x32_bf16 v[92:95], v[116:119], a[44:47], v[92:95]\n"
    "s_waitcnt lgkmcnt(3)\n"
    "ds_read_b64_tr_b16 v[110:111], v20 offset:33024\n"
    "v_mfma_f32_16x16x32_bf16 v[96:99], v[120:123], a[32:35], v[96:99]\n"
    "ds_read_b64_tr_b16 v[112:113], v20 offset:20512\n"
    "buffer_load_dwordx4 v11, s[36:39], s85 offen offset:1024 lds\n"
    "v_mfma_f32_16x16x32_bf16 v[96:99], v[124:127], a[40:43], v[96:99]\n"
    "ds_read_b64_tr_b16 v[114:115], v20 offset:22816\n"
    "v_mfma_f32_16x16x32_bf16 v[100:103], v[120:123], a[36:39], v[100:103]\n"
    "ds_read_b64_tr_b16 v[116:117], v20 offset:30752\n"
    "buffer_load_dwordx4 v12, s[36:39], s85 offen offset:2048 lds\n"
    "v_mfma_f32_16x16x32_bf16 v[100:103], v[124:127], a[44:47], v[100:103]\n"
    "s_waitcnt lgkmcnt(3)\n"
    "ds_read_b64_tr_b16 v[118:119], v20 offset:33056\n"
    "v_mfma_f32_16x16x32_bf16 v[32:35], v[104:107], a[48:51], v[32:35]\n"
    "ds_read_b64_tr_b16 v[120:121], v20 offset:20544\n"
    "buffer_load_dwordx4 v13, s[36:39], s85 offen offset:3072 lds\n"
    "v_mfma_f32_16x16x32_bf16 v[32:35], v[108:111], a[56:59], v[32:35]\n"
    "ds_read_b64_tr_b16 v[122:123], v20 offset:22848\n"
    "buffer_load_dwordx2 v[158:159], v29, s[44:47], s98 offen sc1\n"
    "v_mfma_f32_16x16x32_bf16 v[36:39], v[104:107], a[52:55], v[36:39]\n"
    "s_add_u32 m0, s89, 65536\n"
    "ds_read_b64_tr_b16 v[124:125], v20 offset:30784\n"
    "buffer_load_dwordx4 v14, s[36:39], s85 offen lds\n"
    "v_mfma_f32_16x16x32_bf16 v[36:39], v[108:111], a[60:63], v[36:39]\n"
    "s_waitcnt lgkmcnt(3)\n"
    "ds_read_b64_tr_b16 v[126:127], v20 offset:33088\n"
    "v_mfma_f32_16x16x32_bf16 v[40:43], v[112:115], a[48:51], v[40:43]\n"
    "ds_read_b64_tr_b16 v[104:105], v20 offset:20576\n"
    "v_mfma_f32_16x16x32_bf16 v[40:43], v[116:119], a[56:59], v[40:43]\n"
    "ds_read_b64_tr_b16 v[106:107], v20 offset:22880\n"
    "v_mfma_f32_16x16x32_bf16 v[44:47], v[112:115], a[52:55], v[44:47]\n"
    "ds_read_b64_tr_b16 v[108:109], v20 offset:30816\n"
    "v_mfma_f32_16x16x32_bf16 v[44:47], v[116:119], a[60:63], v[44:47]\n"
    "s_waitcnt lgkmcnt(3)\n"
    "ds_read_b64_tr_b16 v[110:111], v20 offset:33120\n"
    "v_mfma_f32_16x16x32_bf16 v[48:51], v[120:123], a[48:51], v[48:51]\n"
    "ds_read_b64_tr_b16 v[112:113], v20 offset:20608\n"
    "v_mfma_f32_16x16x32_bf16 v[48:51], v[124:127], a[56:59], v[48:51]\n"
    "ds_read_b64_tr_b16 v[114:115], v20 offset:22912\n"
    "v_mfma_f32_16x16x32_bf16 v[52:55], v[120:123], a[52:55], v[52:55]\n"
    "ds_read_b64_tr_b16 v[116:117], v20 offset:30848\n"
    "v_mfma_f32_16x16x32_bf16 v[52:55], v[124:127], a[60:63], v[52:55]\n"
    "s_waitcnt lgkmcnt(3)\n"
    "ds_read_b64_tr_b16 v[118:119], v20 offset:33152\n"
    "v_mfma_f32_16x16x32_bf16 v[56:59], v[104:107], a[48:51], v[56:59]\n"
    "ds_read_b64_tr_b16 v[120:121], v20 offset:20640\n"
    "v_mfma_f32_16x16x32_bf16 v[56:59], v[108:111], a[56:59], v[56:59]\n"
    "ds_read_b64_tr_b16 v[122:123], v20 offset:22944\n"
    "v_mfma_f32_16x16x32_bf16 v[60:63], v[104:107], a[52:55], v[60:63]\n"
    "ds_read_b64_tr_b16 v[124:125], v20 offset:30880\n"
    "v_mfma_f32_16x16x32_bf16 v[60:63], v[108:111], a[60:63], v[60:63]\n"
    "s_waitcnt lgkmcnt(3)\n"
    "ds_read_b64_tr_b16 v[126:127], v20 offset:33184\n"
    "v_mfma_f32_16x16x32_bf16 v[64:67], v[112:115], a[48:51], v[64:67]\n"
    "ds_read_b64_tr_b16 v[104:105], v20 offset:20672\n"
    "v_mfma_f32_16x16x32_bf16 v[64:67], v[116:119], a[56:59], v[64:67]\n"
    "ds_read_b64_tr_b16 v[106:107], v20 offset:22976\n"
    "v_mfma_f32_16x16x32_bf16 v[68:71], v[112:115], a[52:55], v[68:71]\n"
    "ds_read_b64_tr_b16 v[108:109], v20 offset:30912\n"
    "v_mfma_f32_16x16x32_bf16 v[68:71], v[116:119], a[60:63], v[68:71]\n"
    "s_waitcnt lgkmcnt(3)\n"
    "ds_read_b64_tr_b16 v[110:111], v20 offset:33216\n"
    "v_mfma_f32_16x16x32_bf16 v[72:75], v[120:123], a[48:51], v[72:75]\n"
    "ds_read_b64_tr_b16 v[112:113], v20 offset:20704\n"
    "v_mfma_f32_16x16x32_bf16 v[72:75], v[124:127], a[56:59], v[72:75]\n"
    "ds_read_b64_tr_b16 v[114:115], v20 offset:23008\n"
    "v_mfma_f32_16x16x32_bf16 v[76:79], v[120:123], a[52:55], v[76:79]\n"
    "ds_read_b64_tr_b16 v[116:117], v20 offset:30944\n"
    "v_mfma_f32_16x16x32_bf16 v[76:79], v[124:127], a[60:63], v[76:79]\n"
    "s_waitcnt lgkmcnt(3)\n"
    "ds_read_b64_tr_b16 v[118:119], v20 offset:33248\n"
    "v_mfma_f32_16x16x32_bf16 v[80:83], v[104:107], a[48:51], v[80:83]\n"
    "ds_read_b64_tr_b16 v[120:121], v20 offset:20736\n"
    "v_mfma_f32_16x16x32_bf16 v[80:83], v[108:111], a[56:59], v[80:83]\n"
    "ds_read_b64_tr_b16 v[122:123], v20 offset:23040\n"
    "v_mfma_f32_16x16x32_bf16 v[84:87], v[104:107], a[52:55], v[84:87]\n"
    "ds_read_b64_tr_b16 v[124:125], v20 offset:30976\n"
    "v_mfma_f32_16x16x32_bf16 v[84:87], v[108:111], a[60:63], v[84:87]\n"
    "s_waitcnt lgkmcnt(3)\n"
    "ds_read_b64_tr_b16 v[126:127], v20 offset:33280\n"
    "v_mfma_f32_16x16x32_bf16 v[88:91], v[112:115], a[48:51], v[88:91]\n"
    "s_waitcnt vmcnt(6)\n"
    "s_barrier\n"
    "ds_read_b64_tr_b16 v[104:105], v20 offset:40960\n"
    "v_mfma_f32_16x16x32_bf16 v[88:91], v[116:119], a[56:59], v[88:91]\n"
    "ds_read_b64_tr_b16 v[106:107], v20 offset:43264\n"
    "v_mfma_f32_16x16x32_bf16 v[92:95], v[112:115], a[52:55], v[92:95]\n"
    "s_add_u32 s85, s70, 0\n"
    "s_add_u32 m0, s89, 0\n"
    "ds_read_b64_tr_b16 v[108:109], v20 offset:51200\n"
    "buffer_load_dwordx4 v10, s[36:39], s85 offen lds\n"
    "v_mfma_f32_16x16x32_bf16 v[92:95], v[116:119], a[60:63], v[92:95]\n"
    "s_waitcnt lgkmcnt(3)\n"
    "ds_read_b64_tr_b16 v[110:111], v20 offset:53504\n"
    "v_mfma_f32_16x16x32_bf16 v[96:99], v[120:123], a[48:51], v[96:99]\n"
    "ds_read_b64_tr_b16 v[112:113], v20 offset:40992\n"
    "buffer_load_dwordx4 v11, s[36:39], s85 offen offset:1024 lds\n"
    "v_mfma_f32_16x16x32_bf16 v[96:99], v[124:127], a[56:59], v[96:99]\n"
    "ds_read_b64_tr_b16 v[114:115], v20 offset:43296\n"
    "v_mfma_f32_16x16x32_bf16 v[100:103], v[120:123], a[52:55], v[100:103]\n"
    "ds_read_b64_tr_b16 v[116:117], v20 offset:51232\n"
    "buffer_load_dwordx4 v12, s[36:39], s85 offen offset:2048 lds\n"
    "v_mfma_f32_16x16x32_bf16 v[100:103], v[124:127], a[60:63], v[100:103]\n"
    "s_waitcnt lgkmcnt(3)\n"
    "ds_read_b64_tr_b16 v[118:119], v20 offset:53536\n"
    "v_mfma_f32_16x16x32_bf16 v[32:35], v[104:107], a[64:67], v[32:35]\n"
    "ds_read_b64_tr_b16 v[120:121], v20 offset:41024\n"
    "buffer_load_dwordx4 v13, s[36:39], s85 offen offset:3072 lds\n"
    "v_mfma_f32_16x16x32_bf16 v[32:35], v[108:111], a[72:75], v[32:35]\n"
    "ds_read_b64_tr_b16 v[122:123], v20 offset:43328\n"
    "v_mfma_f32_16x16x32_bf16 v[36:39], v[104:107], a[68:71], v[36:39]\n"
    "s_add_u32 m0, s89, 4096\n"
    "ds_read_b64_tr_b16 v[124:125], v20 offset:51264\n"
    "buffer_load_dwordx4 v14, s[36:39], s85 offen lds\n"
    "v_mfma_f32_16x16x32_bf16 v[36:39], v[108:111], a[76:79], v[36:39]\n"
    "s_waitcnt lgkmcnt(3)\n"
    "ds_read_b64_tr_b16 v[126:127], v20 offset:53568\n"
    "v_mfma_f32_16x16x32_bf16 v[40:43], v[112:115], a[64:67], v[40:43]\n"
    "ds_read_b64_tr_b16 v[104:105], v20 offset:41056\n"
    "v_mfma_f32_16x16x32_bf16 v[40:43], v[116:119], a[72:75], v[40:43]\n"
    "ds_read_b64_tr_b16 v[106:107], v20 offset:43360\n"
    "v_mfma_f32_16x16x32_bf16 v[44:47], v[112:115], a[68:71], v[44:47]\n"
    "ds_read_b64_tr_b16 v[108:109], v20 offset:51296\n"
    "v_mfma_f32_16x16x32_bf16 v[44:47], v[116:119], a[76:79], v[44:47]\n"
    "s_waitcnt lgkmcnt(3)\n"
    "ds_read_b64_tr_b16 v[110:111], v20 offset:53600\n"
    "v_mfma_f32_16x16x32_bf16 v[48:51], v[120:123], a[64:67], v[48:51]\n"
    "ds_read_b64_tr_b16 v[112:113], v20 offset:41088\n"
    "v_mfma_f32_16x16x32_bf16 v[48:51], v[124:127], a[72:75], v[48:51]\n"
    "ds_read_b64_tr_b16 v[114:115], v20 offset:43392\n"
    "s_waitcnt vmcnt(6)\n"
    "v_cmp_ne_u64 vcc, v[164:165], v[158:159]\n"
    "s_cbranch_vccnz .LL_H1RETRY_%=\n"
    ".LL_H1OK_%=:\n"
    "v_mfma_f32_16x16x32_bf16 v[52:55], v[120:123], a[68:71], v[52:55]\n"
    "s_add_u32 s29, s93, 0\n"
    "ds_read_b64_tr_b16 v[116:117], v20 offset:51328\n"
    "buffer_load_dwordx4 v[176:179], v154, s[44:47], s29 offen sc1\n"
    "v_mfma_f32_16x16x32_bf16 v[52:55], v[124:127], a[76:79], v[52:55]\n"
    "s_waitcnt lgkmcnt(3)\n"
    "ds_read_b64_tr_b16 v[118:119], v20 offset:53632\n"
    "v_mfma_f32_16x16x32_bf16 v[56:59], v[104:107], a[64:67], v[56:59]\n"
    "ds_read_b64_tr_b16 v[120:121], v20 offset:41120\n"
    "buffer_load_dwordx4 v[180:183], v154, s[44:47], s29 offen offset:1024 sc1\n"
    "v_mfma_f32_16x16x32_bf16 v[56:59], v[108:111], a[72:75], v[56:59]\n"
    "ds_read_b64_tr_b16 v[122:123], v20 offset:43424\n"
    "v_mfma_f32_16x16x32_bf16 v[60:63], v[104:107], a[68:71], v[60:63]\n"
    "ds_read_b64_tr_b16 v[124:125], v20 offset:51360\n"
    "buffer_load_dwordx4 v[184:187], v154, s[44:47], s29 offen offset:2048 sc1\n"
    "v_mfma_f32_16x16x32_bf16 v[60:63], v[108:111], a[76:79], v[60:63]\n"
    "s_waitcnt lgkmcnt(3)\n"
    "ds_read_b64_tr_b16 v[126:127], v20 offset:53664\n"
    "v_mfma_f32_16x16x32_bf16 v[64:67], v[112:115], a[64:67], v[64:67]\n"
    "ds_read_b64_tr_b16 v[104:105], v20 offset:41152\n"
    "buffer_load_dwordx4 v[188:191], v154, s[44:47], s29 offen offset:3072 sc1\n"
    "v_mfma_f32_16x16x32_bf16 v[64:67], v[116:119], a[72:75], v[64:67]\n"
    "ds_read_b64_tr_b16 v[106:107], v20 offset:43456\n"
    "v_mfma_f32_16x16x32_bf16 v[68:71], v[112:115], a[68:71], v[68:71]\n"
    "s_add_u32 s29, s93, 4096\n"
    "ds_read_b64_tr_b16 v[108:109], v20 offset:51392\n"
    "buffer_load_dwordx4 v[192:195], v154, s[44:47], s29 offen sc1\n"
    "v_mfma_f32_16x16x32_bf16 v[68:71], v[116:119], a[76:79], v[68:71]\n"
    "s_waitcnt lgkmcnt(3)\n"
    "ds_read_b64_tr_b16 v[110:111], v20 offset:53696\n"
    "v_mfma_f32_16x16x32_bf16 v[72:75], v[120:123], a[64:67], v[72:75]\n"
    "ds_read_b64_tr_b16 v[112:113], v20 offset:41184\n"
    "buffer_load_dwordx4 v[196:199], v154, s[44:47], s29 offen offset:1024 sc1\n"
    "v_mfma_f32_16x16x32_bf16 v[72:75], v[124:127], a[72:75], v[72:75]\n"
    "ds_read_b64_tr_b16 v[114:115], v20 offset:43488\n"
    "v_mfma_f32_16x16x32_bf16 v[76:79], v[120:123], a[68:71], v[76:79]\n"
    "ds_read_b64_tr_b16 v[116:117], v20 offset:51424\n"
    "buffer_load_dwordx4 v[200:203], v154, s[44:47], s29 offen offset:2048 sc1\n"
    "v_mfma_f32_16x16x32_bf16 v[76:79], v[124:127], a[76:79], v[76:79]\n"
    "s_waitcnt lgkmcnt(3)\n"
    "ds_read_b64_tr_b16 v[118:119], v20 offset:53728\n"
    "v_mfma_f32_16x16x32_bf16 v[80:83], v[104:107], a[64:67], v[80:83]\n"
    "ds_read_b64_tr_b16 v[120:121], v20 offset:41216\n"
    "buffer_load_dwordx4 v[204:207], v154, s[44:47], s29 offen offset:3072 sc1\n"
    "v_mfma_f32_16x16x32_bf16 v[80:83], v[108:111], a[72:75], v[80:83]\n"
    "ds_read_b64_tr_b16 v[122:123], v20 offset:43520\n"
    "v_mfma_f32_16x16x32_bf16 v[84:87], v[104:107], a[68:71], v[84:87]\n"
    "s_add_u32 s29, s93, 8192\n"
    "ds_read_b64_tr_b16 v[124:125], v20 offset:51456\n"
    "buffer_load_dwordx4 v[208:211], v154, s[44:47], s29 offen sc1\n"
    "v_mfma_f32_16x16x32_bf16 v[84:87], v[108:111], a[76:79], v[84:87]\n"
    "s_waitcnt lgkmcnt(3)\n"
    "ds_read_b64_tr_b16 v[126:127], v20 offset:53760\n"
    "v_mfma_f32_16x16x32_bf16 v[88:91], v[112:115], a[64:67], v[88:91]\n"
    "s_waitcnt vmcnt(14)\n"
    "s_barrier\n"
    "ds_read_b64_tr_b16 v[104:105], v21 offset:0\n"
    "v_mfma_f32_16x16x32_bf16 v[88:91], v[116:119], a[72:75], v[88:91]\n"
    "ds_read_b64_tr_b16 v[106:107], v21 offset:2304\n"
    "v_mfma_f32_16x16x32_bf16 v[92:95], v[112:115], a[68:71], v[92:95]\n"
    "ds_read_b64_tr_b16 v[108:109], v21 offset:10240\n"
    "buffer_load_dwordx4 v[212:215], v154, s[44:47], s29 offen offset:1024 sc1\n"
    "v_mfma_f32_16x16x32_bf16 v[92:95], v[116:119], a[76:79], v[92:95]\n"
    "s_waitcnt lgkmcnt(3)\n"
    "ds_read_b64_tr_b16 v[110:111], v21 offset:12544\n"
    "v_mfma_f32_16x16x32_bf16 v[96:99], v[120:123], a[64:67], v[96:99]\n"
    "ds_read_b64_tr_b16 v[112:113], v21 offset:32\n"
    "buffer_load_dwordx4 v[216:219], v154, s[44:47], s29 offen offset:2048 sc1\n"
    "v_mfma_f32_16x16x32_bf16 v[96:99], v[124:127], a[72:75], v[96:99]\n"
    "ds_read_b64_tr_b16 v[114:115], v21 offset:2336\n"
    "v_mfma_f32_16x16x32_bf16 v[100:103], v[120:123], a[68:71], v[100:103]\n"
    "ds_read_b64_tr_b16 v[116:117], v21 offset:10272\n"
    "buffer_load_dwordx4 v[220:223], v154, s[44:47], s29 offen offset:3072 sc1\n"
    "v_mfma_f32_16x16x32_bf16 v[100:103], v[124:127], a[76:79], v[100:103]\n"
    "s_waitcnt lgkmcnt(3)\n"
    "ds_read_b64_tr_b16 v[118:119], v21 offset:12576\n"
    "v_mfma_f32_16x16x32_bf16 v[32:35], v[104:107], a[80:83], v[32:35]\n"
    "s_add_u32 s29, s93, 12288\n"
    "ds_read_b64_tr_b16 v[120:121], v21 offset:64\n"
    "buffer_load_dwordx4 v[224:227], v154, s[44:47], s29 offen sc1\n"
    "v_mfma_f32_16x16x32_bf16 v[32:35], v[108:111], a[88:91], v[32:35]\n"
    "ds_read_b64_tr_b16 v[122:123], v21 offset:2368\n"
    "v_mfma_f32_16x16x32_bf16 v[36:39], v[104:107], a[84:87], v[36:39]\n"
    "ds_read_b64_tr_b16 v[124:125], v21 offset:10304\n"
    "buffer_load_dwordx4 v[228:231], v154, s[44:47], s29 offen offset:1024 sc1\n"
    "v_mfma_f32_16x16x32_bf16 v[36:39], v[108:111], a[92:95], v[36:39]\n"
    "s_waitcnt lgkmcnt(3)\n"
    "ds_read_b64_tr_b16 v[126:127], v21 offset:12608\n"
    "v_mfma_f32_16x16x32_bf16 v[40:43], v[112:115], a[80:83], v[40:43]\n"
    "ds_read_b64_tr_b16 v[104:105], v21 offset:96\n"
    "buffer_load_dwordx4 v[232:235], v154, s[44:47], s29 offen offset:2048 sc1\n"
    "v_mfma_f32_16x16x32_bf16 v[40:43], v[116:119], a[88:91], v[40:43]\n"
    "ds_read_b64_tr_b16 v[106:107], v21 offset:2400\n"
    "v_mfma_f32_16x16x32_bf16 v[44:47], v[112:115], a[84:87], v[44:47]\n"
    "ds_read_b64_tr_b16 v[108:109], v21 offset:10336\n"
    "buffer_load_dwordx4 v[236:239], v154, s[44:47], s29 offen offset:3072 sc1\n"
    "v_mfma_f32_16x16x32_bf16 v[44:47], v[116:119], a[92:95], v[44:47]\n"
    "s_waitcnt lgkmcnt(3)\n"
    "ds_read_b64_tr_b16 v[110:111], v21 offset:12640\n"
    "v_mfma_f32_16x16x32_bf16 v[48:51], v[120:123], a[80:83], v[48:51]\n"
    "s_add_u32 s85, s71, 0\n"
    "s_add_u32 m0, s89, 20480\n"
    "ds_read_b64_tr_b16 v[112:113], v21 offset:128\n"
    "buffer_load_dwordx4 v10, s[36:39], s85 offen lds\n"
    "v_mfma_f32_16x16x32_bf16 v[48:51], v[124:127], a[88:91], v[48:51]\n"
    "ds_read_b64_tr_b16 v[114:115], v21 offset:2432\n"
    "v_mfma_f32_16x16x32_bf16 v[52:55], v[120:123], a[84:87], v[52:55]\n"
    "ds_read_b64_tr_b16 v[116:117], v21 offset:10368\n"
    "buffer_load_dwordx4 v11, s[36:39], s85 offen offset:1024 lds\n"
    "v_mfma_f32_16x16x32_bf16 v[52:55], v[124:127], a[92:95], v[52:55]\n"
    "s_waitcnt lgkmcnt(3)\n"
    "ds_read_b64_tr_b16 v[118:119], v21 offset:12672\n"
    "v_mfma_f32_16x16x32_bf16 v[56:59], v[104:107], a[80:83], v[56:59]\n"
    "ds_read_b64_tr_b16 v[120:121], v21 offset:160\n"
    "buffer_load_dwordx4 v12, s[36:39], s85 offen offset:2048 lds\n"
    "v_mfma_f32_16x16x32_bf16 v[56:59], v[108:111], a[88:91], v[56:59]\n"
    "ds_read_b64_tr_b16 v[122:123], v21 offset:2464\n"
    "v_mfma_f32_16x16x32_bf16 v[60:63], v[104:107], a[84:87], v[60:63]\n"
    "ds_read_b64_tr_b16 v[124:125], v21 offset:10400\n"
    "buffer_load_dwordx4 v13, s[36:39], s85 offen offset:3072 lds\n"
    "v_mfma_f32_16x16x32_bf16 v[60:63], v[108:111], a[92:95], v[60:63]\n"
    "s_waitcnt lgkmcnt(3)\n"
    "ds_read_b64_tr_b16 v[126:127], v21 offset:12704\n"
    "v_mfma_f32_16x16x32_bf16 v[64:67], v[112:115], a[80:83], v[64:67]\n"
    "s_add_u32 m0, s89, 24576\n"
    "ds_read_b64_tr_b16 v[104:105], v21 offset:192\n"
    "buffer_load_dwordx4 v14, s[36:39], s85 offen lds\n"
    "v_mfma_f32_16x16x32_bf16 v[64:67], v[116:119], a[88:91], v[64:67]\n"
    "ds_read_b64_tr_b16 v[106:107], v21 offset:2496\n"
    "v_mfma_f32_16x16x32_bf16 v[68:71], v[112:115], a[84:87], v[68:71]\n"
    "ds_read_b64_tr_b16 v[108:109], v21 offset:10432\n"
    "v_mfma_f32_16x16x32_bf16 v[68:71], v[116:119], a[92:95], v[68:71]\n"
    "s_waitcnt lgkmcnt(3)\n"
    "ds_read_b64_tr_b16 v[110:111], v21 offset:12736\n"
    "v_mfma_f32_16x16x32_bf16 v[72:75], v[120:123], a[80:83], v[72:75]\n"
    "ds_read_b64_tr_b16 v[112:113], v21 offset:224\n"
    "v_mfma_f32_16x16x32_bf16 v[72:75], v[124:127], a[88:91], v[72:75]\n"
    "ds_read_b64_tr_b16 v[114:115], v21 offset:2528\n"
    "v_mfma_f32_16x16x32_bf16 v[76:79], v[120:123], a[84:87], v[76:79]\n"
    "ds_read_b64_tr_b16 v[116:117], v21 offset:10464\n"
    "v_mfma_f32_16x16x32_bf16 v[76:79], v[124:127], a[92:95], v[76:79]\n"
    "s_waitcnt lgkmcnt(3)\n"
    "ds_read_b64_tr_b16 v[118:119], v21 offset:12768\n"
    "v_mfma_f32_16x16x32_bf16 v[80:83], v[104:107], a[80:83], v[80:83]\n"
    "ds_read_b64_tr_b16 v[120:121], v21 offset:256\n"
    "v_mfma_f32_16x16x32_bf16 v[80:83], v[108:111], a[88:91], v[80:83]\n"
    "ds_read_b64_tr_b16 v[122:123], v21 offset:2560\n"
    "v_mfma_f32_16x16x32_bf16 v[84:87], v[104:107], a[84:87], v[84:87]\n"
    "ds_read_b64_tr_b16 v[124:125], v21 offset:10496\n"
    "v_mfma_f32_16x16x32_bf16 v[84:87], v[108:111], a[92:95], v[84:87]\n"
    "s_waitcnt lgkmcnt(3)\n"
    "ds_read_b64_tr_b16 v[126:127], v21 offset:12800\n"
    "v_mfma_f32_16x16x32_bf16 v[88:91], v[112:115], a[80:83], v[88:91]\n"
    "s_waitcnt vmcnt(21)\n"
    "s_barrier\n"
    "ds_read_b64_tr_b16 v[104:105], v20 offset:0\n"
    "v_mfma_f32_16x16x32_bf16 v[88:91], v[116:119], a[88:91], v[88:91]\n"
    "ds_read_b64_tr_b16 v[106:107], v20 offset:2304\n"
    "v_mfma_f32_16x16x32_bf16 v[92:95], v[112:115], a[84:87], v[92:95]\n"
    "s_add_u32 s85, s72, 0\n"
    "s_add_u32 m0, s89, 40960\n"
    "ds_read_b64_tr_b16 v[108:109], v20 offset:10240\n"
    "buffer_load_dwordx4 v10, s[36:39], s85 offen lds\n"
    "v_mfma_f32_16x16x32_bf16 v[92:95], v[116:119], a[92:95], v[92:95]\n"
    "s_waitcnt lgkmcnt(3)\n"
    "ds_read_b64_tr_b16 v[110:111], v20 offset:12544\n"
    "v_mfma_f32_16x16x32_bf16 v[96:99], v[120:123], a[80:83], v[96:99]\n"
    "ds_read_b64_tr_b16 v[112:113], v20 offset:32\n"
    "buffer_load_dwordx4 v11, s[36:39], s85 offen offset:1024 lds\n"
    "v_mfma_f32_16x16x32_bf16 v[96:99], v[124:127], a[88:91], v[96:99]\n"
    "ds_read_b64_tr_b16 v[114:115], v20 offset:2336\n"
    "v_mfma_f32_16x16x32_bf16 v[100:103], v[120:123], a[84:87], v[100:103]\n"
    "ds_read_b64_tr_b16 v[116:117], v20 offset:10272\n"
    "buffer_load_dwordx4 v12, s[36:39], s85 offen offset:2048 lds\n"
    "v_mfma_f32_16x16x32_bf16 v[100:103], v[124:127], a[92:95], v[100:103]\n"
    "s_waitcnt lgkmcnt(3)\n"
    "ds_read_b64_tr_b16 v[118:119], v20 offset:12576\n"
    "v_mfma_f32_16x16x32_bf16 v[32:35], v[104:107], a[96:99], v[32:35]\n"
    "ds_read_b64_tr_b16 v[120:121], v20 offset:64\n"
    "buffer_load_dwordx4 v13, s[36:39], s85 offen offset:3072 lds\n"
    "v_mfma_f32_16x16x32_bf16 v[32:35], v[108:111], a[104:107], v[32:35]\n"
    "ds_read_b64_tr_b16 v[122:123], v20 offset:2368\n"
    "v_mfma_f32_16x16x32_bf16 v[36:39], v[104:107], a[100:103], v[36:39]\n"
    "s_add_u32 m0, s89, 45056\n"
    "ds_read_b64_tr_b16 v[124:125], v20 offset:10304\n"
    "buffer_load_dwordx4 v14, s[36:39], s85 offen lds\n"
    "v_mfma_f32_16x16x32_bf16 v[36:39], v[108:111], a[108:111], v[36:39]\n"
    "s_waitcnt lgkmcnt(3)\n"
    "ds_read_b64_tr_b16 v[126:127], v20 offset:12608\n"
    "v_mfma_f32_16x16x32_bf16 v[40:43], v[112:115], a[96:99], v[40:43]\n"
    "ds_read_b64_tr_b16 v[104:105], v20 offset:96\n"
    "v_mfma_f32_16x16x32_bf16 v[40:43], v[116:119], a[104:107], v[40:43]\n"
    "ds_read_b64_tr_b16 v[106:107], v20 offset:2400\n"
    "v_mfma_f32_16x16x32_bf16 v[44:47], v[112:115], a[100:103], v[44:47]\n"
    "ds_read_b64_tr_b16 v[108:109], v20 offset:10336\n"
    "v_mfma_f32_16x16x32_bf16 v[44:47], v[116:119], a[108:111], v[44:47]\n"
    "s_waitcnt lgkmcnt(3)\n"
    "ds_read_b64_tr_b16 v[110:111], v20 offset:12640\n"
    "v_mfma_f32_16x16x32_bf16 v[48:51], v[120:123], a[96:99], v[48:51]\n"
    "ds_read_b64_tr_b16 v[112:113], v20 offset:128\n"
    "v_mfma_f32_16x16x32_bf16 v[48:51], v[124:127], a[104:107], v[48:51]\n"
    "ds_read_b64_tr_b16 v[114:115], v20 offset:2432\n"
    "v_mfma_f32_16x16x32_bf16 v[52:55], v[120:123], a[100:103], v[52:55]\n"
    "ds_read_b64_tr_b16 v[116:117], v20 offset:10368\n"
    "v_mfma_f32_16x16x32_bf16 v[52:55], v[124:127], a[108:111], v[52:55]\n"
    "s_waitcnt lgkmcnt(3)\n"
    "ds_read_b64_tr_b16 v[118:119], v20 offset:12672\n"
    "v_mfma_f32_16x16x32_bf16 v[56:59], v[104:107], a[96:99], v[56:59]\n"
    "ds_read_b64_tr_b16 v[120:121], v20 offset:160\n"
    "v_mfma_f32_16x16x32_bf16 v[56:59], v[108:111], a[104:107], v[56:59]\n"
    "ds_read_b64_tr_b16 v[122:123], v20 offset:2464\n"
    "v_mfma_f32_16x16x32_bf16 v[60:63], v[104:107], a[100:103], v[60:63]\n"
    "ds_read_b64_tr_b16 v[124:125], v20 offset:10400\n"
    "v_mfma_f32_16x16x32_bf16 v[60:63], v[108:111], a[108:111], v[60:63]\n"
    "s_waitcnt lgkmcnt(3)\n"
    "ds_read_b64_tr_b16 v[126:127], v20 offset:12704\n"
    "v_mfma_f32_16x16x32_bf16 v[64:67], v[112:115], a[96:99], v[64:67]\n"
    "ds_read_b64_tr_b16 v[104:105], v20 offset:192\n"
    "v_mfma_f32_16x16x32_bf16 v[64:67], v[116:119], a[104:107], v[64:67]\n"
    "ds_read_b64_tr_b16 v[106:107], v20 offset:2496\n"
    "s_waitcnt vmcnt(10)\n"
    "v_add_f32 v176, v176, v180\n"
    "v_add_f32 v177, v177, v181\n"
    "v_mfma_f32_16x16x32_bf16 v[68:71], v[112:115], a[100:103], v[68:71]\n"
    "ds_read_b64_tr_b16 v[108:109], v20 offset:10432\n"
    "v_add_f32 v178, v178, v182\n"
    "v_add_f32 v179, v179, v183\n"
    "v_mfma_f32_16x16x32_bf16 v[68:71], v[116:119], a[108:111], v[68:71]\n"
    "s_waitcnt lgkmcnt(3)\n"
    "ds_read_b64_tr_b16 v[110:111], v20 offset:12736\n"
    "v_add_f32 v184, v184, v188\n"
    "v_add_f32 v185, v185, v189\n"
    "v_mfma_f32_16x16x32_bf16 v[72:75], v[120:123], a[96:99], v[72:75]\n"
    "ds_read_b64_tr_b16 v[112:113], v20 offset:224\n"
    "v_add_f32 v186, v186, v190\n"
    "v_add_f32 v187, v187, v191\n"
    "v_mfma_f32_16x16x32_bf16 v[72:75], v[124:127], a[104:107], v[72:75]\n"
    "ds_read_b64_tr_b16 v[114:115], v20 offset:2528\n"
    "v_add_f32 v192, v192, v196\n"
    "v_add_f32 v193, v193, v197\n"
    "v_mfma_f32_16x16x32_bf16 v[76:79], v[120:123], a[100:103], v[76:79]\n"
    "ds_read_b64_tr_b16 v[116:117], v20 offset:10464\n"
    "v_add_f32 v194, v194, v198\n"
    "v_add_f32 v195, v195, v199\n"
    "v_mfma_f32_16x16x32_bf16 v[76:79], v[124:127], a[108:111], v[76:79]\n"
    "s_waitcnt lgkmcnt(3)\n"
    "ds_read_b64_tr_b16 v[118:119], v20 offset:12768\n"
    "v_add_f32 v200, v200, v204\n"
    "v_add_f32 v201, v201, v205\n"
    "v_mfma_f32_16x16x32_bf16 v[80:83], v[104:107], a[96:99], v[80:83]\n"
    "ds_read_b64_tr_b16 v[120:121], v20 offset:256\n"
    "v_add_f32 v202, v202, v206\n"
    "v_add_f32 v203, v203, v207\n"
    "v_mfma_f32_16x16x32_bf16 v[80:83], v[108:111], a[104:107], v[80:83]\n"
    "ds_read_b64_tr_b16 v[122:123], v20 offset:2560\n"
    "v_add_f32 v208, v208, v212\n"
    "v_add_f32 v209, v209, v213\n"
    "v_mfma_f32_16x16x32_bf16 v[84:87], v[104:107], a[100:103], v[84:87]\n"
    "ds_read_b64_tr_b16 v[124:125], v20 offset:10496\n"
    "v_add_f32 v210, v210, v214\n"
    "v_add_f32 v211, v211, v215\n"
    "v_mfma_f32_16x16x32_bf16 v[84:87], v[108:111], a[108:111], v[84:87]\n"
    "s_waitcnt lgkmcnt(3)\n"
    "ds_read_b64_tr_b16 v[126:127], v20 offset:12800\n"
    "v_add_f32 v216, v216, v220\n"
    "v_add_f32 v217, v217, v221\n"
    "v_mfma_f32_16x16x32_bf16 v[88:91], v[112:115], a[96:99], v[88:91]\n"
    "s_waitcnt vmcnt(5)\n"
    "s_barrier\n"
    "ds_read_b64_tr_b16 v[104:105], v20 offset:20480\n"
    "v_mfma_f32_16x16x32_bf16 v[88:91], v[116:119], a[104:107], v[88:91]\n"
    "ds_read_b64_tr_b16 v[106:107], v20 offset:22784\n"
    "v_add_f32 v218, v218, v222\n"
    "v_add_f32 v219, v219, v223\n"
    "v_mfma_f32_16x16x32_bf16 v[92:95], v[112:115], a[100:103], v[92:95]\n"
    "s_mov_b32 s82, 0\n"
    "s_add_u32 m0, s89, 81920\n"
    "ds_read_b64_tr_b16 v[108:109], v20 offset:30720\n"
    "buffer_load_dwordx4 v15, s[52:55], s82 offen lds\n"
    "v_mfma_f32_16x16x32_bf16 v[92:95], v[116:119], a[108:111], v[92:95]\n"
    "s_waitcnt lgkmcnt(3)\n"
    "ds_read_b64_tr_b16 v[110:111], v20 offset:33024\n"
    "v_add_f32 v224, v224, v228\n"
    "v_add_f32 v225, v225, v229\n"
    "v_mfma_f32_16x16x32_bf16 v[96:99], v[120:123], a[96:99], v[96:99]\n"
    "ds_read_b64_tr_b16 v[112:113], v20 offset:20512\n"
    "buffer_load_dwordx4 v16, s[52:55], s82 offen offset:1024 lds\n"
    "v_mfma_f32_16x16x32_bf16 v[96:99], v[124:127], a[104:107], v[96:99]\n"
    "ds_read_b64_tr_b16 v[114:115], v20 offset:22816\n"
    "v_add_f32 v226, v226, v230\n"
    "v_add_f32 v227, v227, v231\n"
    "v_mfma_f32_16x16x32_bf16 v[100:103], v[120:123], a[100:103], v[100:103]\n"
    "ds_read_b64_tr_b16 v[116:117], v20 offset:30752\n"
    "buffer_load_dwordx4 v17, s[52:55], s82 offen offset:2048 lds\n"
    "v_mfma_f32_16x16x32_bf16 v[100:103], v[124:127], a[108:111], v[100:103]\n"
    "s_waitcnt lgkmcnt(3)\n"
    "ds_read_b64_tr_b16 v[118:119], v20 offset:33056\n"
    "v_add_f32 v232, v232, v236\n"
    "v_add_f32 v233, v233, v237\n"
    "v_mfma_f32_16x16x32_bf16 v[32:35], v[104:107], a[112:115], v[32:35]\n"
    "ds_read_b64_tr_b16 v[120:121], v20 offset:20544\n"
    "buffer_load_dwordx4 v18, s[52:55], s82 offen offset:3072 lds\n"
    "v_mfma_f32_16x16x32_bf16 v[32:35], v[108:111], a[120:123], v[32:35]\n"
    "ds_read_b64_tr_b16 v[122:123], v20 offset:22848\n"
    "v_add_f32 v234, v234, v238\n"
    "v_add_f32 v235, v235, v239\n"
    "v_mfma_f32_16x16x32_bf16 v[36:39], v[104:107], a[116:119], v[36:39]\n"
    "s_add_u32 m0, s89, 86016\n"
    "ds_read_b64_tr_b16 v[124:125], v20 offset:30784\n"
    "buffer_load_dwordx4 v19, s[52:55], s82 offen lds\n"
    "v_mfma_f32_16x16x32_bf16 v[36:39], v[108:111], a[124:127], v[36:39]\n"
    "s_waitcnt lgkmcnt(3)\n"
    "ds_read_b64_tr_b16 v[126:127], v20 offset:33088\n"
    "v_add_f32 v176, v176, v184\n"
    "v_add_f32 v177, v177, v185\n"
    "v_mfma_f32_16x16x32_bf16 v[40:43], v[112:115], a[112:115], v[40:43]\n"
    "s_mov_b32 s82, 128\n"
    "s_add_u32 m0, s89, 102400\n"
    "ds_read_b64_tr_b16 v[104:105], v20 offset:20576\n"
    "buffer_load_dwordx4 v15, s[52:55], s82 offen lds\n"
    "v_mfma_f32_16x16x32_bf16 v[40:43], v[116:119], a[120:123], v[40:43]\n"
    "ds_read_b64_tr_b16 v[106:107], v20 offset:22880\n"
    "v_add_f32 v178, v178, v186\n"
    "v_add_f32 v179, v179, v187\n"
    "v_mfma_f32_16x16x32_bf16 v[44:47], v[112:115], a[116:119], v[44:47]\n"
    "ds_read_b64_tr_b16 v[108:109], v20 offset:30816\n"
    "buffer_load_dwordx4 v16, s[52:55], s82 offen offset:1024 lds\n"
    "v_mfma_f32_16x16x32_bf16 v[44:47], v[116:119], a[124:127], v[44:47]\n"
    "s_waitcnt lgkmcnt(3)\n"
    "ds_read_b64_tr_b16 v[110:111], v20 offset:33120\n"
    "v_add_f32 v192, v192, v200\n"
    "v_add_f32 v193, v193, v201\n"
    "v_mfma_f32_16x16x32_bf16 v[48:51], v[120:123], a[112:115], v[48:51]\n"
    "ds_read_b64_tr_b16 v[112:113], v20 offset:20608\n"
    "buffer_load_dwordx4 v17, s[52:55], s82 offen offset:2048 lds\n"
    "v_mfma_f32_16x16x32_bf16 v[48:51], v[124:127], a[120:123], v[48:51]\n"
    "ds_read_b64_tr_b16 v[114:115], v20 offset:22912\n"
    "v_add_f32 v194, v194, v202\n"
    "v_add_f32 v195, v195, v203\n"
    "v_mfma_f32_16x16x32_bf16 v[52:55], v[120:123], a[116:119], v[52:55]\n"
    "ds_read_b64_tr_b16 v[116:117], v20 offset:30848\n"
    "buffer_load_dwordx4 v18, s[52:55], s82 offen offset:3072 lds\n"
    "v_mfma_f32_16x16x32_bf16 v[52:55], v[124:127], a[124:127], v[52:55]\n"
    "s_waitcnt lgkmcnt(3)\n"
    "ds_read_b64_tr_b16 v[118:119], v20 offset:33152\n"
    "v_add_f32 v208, v208, v216\n"
    "v_add_f32 v209, v209, v217\n"
    "v_mfma_f32_16x16x32_bf16 v[56:59], v[104:107], a[112:115], v[56:59]\n"
    "s_add_u32 m0, s89, 106496\n"
    "ds_read_b64_tr_b16 v[120:121], v20 offset:20640\n"
    "buffer_load_dwordx4 v19, s[52:55], s82 offen lds\n"
    "v_mfma_f32_16x16x32_bf16 v[56:59], v[108:111], a[120:123], v[56:59]\n"
    "ds_read_b64_tr_b16 v[122:123], v20 offset:22944\n"
    "v_add_f32 v210, v210, v218\n"
    "v_add_f32 v211, v211, v219\n"
    "v_mfma_f32_16x16x32_bf16 v[60:63], v[104:107], a[116:119], v[60:63]\n"
    "ds_read_b64_tr_b16 v[124:125], v20 offset:30880\n"
    "v_add_f32 v224, v224, v232\n"
    "v_add_f32 v225, v225, v233\n"
    "v_mfma_f32_16x16x32_bf16 v[60:63], v[108:111], a[124:127], v[60:63]\n"
    "s_waitcnt lgkmcnt(3)\n"
    "ds_read_b64_tr_b16 v[126:127], v20 offset:33184\n"
    "v_add_f32 v226, v226, v234\n"
    "v_add_f32 v227, v227, v235\n"
    "v_mfma_f32_16x16x32_bf16 v[64:67], v[112:115], a[112:115], v[64:67]\n"
    "ds_read_b64_tr_b16 v[104:105], v20 offset:20672\n"
    "v_add_f32 v176, v176, v192\n"
    "v_add_f32 v177, v177, v193\n"
    "v_mfma_f32_16x16x32_bf16 v[64:67], v[116:119], a[120:123], v[64:67]\n"
    "ds_read_b64_tr_b16 v[106:107], v20 offset:22976\n"
    "v_add_f32 v178, v178, v194\n"
    "v_add_f32 v179, v179, v195\n"
    "v_mfma_f32_16x16x32_bf16 v[68:71], v[112:115], a[116:119], v[68:71]\n"
    "ds_read_b64_tr_b16 v[108:109], v20 offset:30912\n"
    "v_add_f32 v208, v208, v224\n"
    "v_add_f32 v209, v209, v225\n"
    "v_mfma_f32_16x16x32_bf16 v[68:71], v[116:119], a[124:127], v[68:71]\n"
    "s_waitcnt lgkmcnt(3)\n"
    "ds_read_b64_tr_b16 v[110:111], v20 offset:33216\n"
    "v_add_f32 v210, v210, v226\n"
    "v_add_f32 v211, v211, v227\n"
    "v_mfma_f32_16x16x32_bf16 v[72:75], v[120:123], a[112:115], v[72:75]\n"
    "ds_read_b64_tr_b16 v[112:113], v20 offset:20704\n"
    "v_add_f32 v176, v176, v208\n"
    "v_add_f32 v177, v177, v209\n"
    "v_mfma_f32_16x16x32_bf16 v[72:75], v[124:127], a[120:123], v[72:75]\n"
    "ds_read_b64_tr_b16 v[114:115], v20 offset:23008\n"
    "v_add_f32 v178, v178, v210\n"
    "v_add_f32 v179, v179, v211\n"
    "v_mfma_f32_16x16x32_bf16 v[76:79], v[120:123], a[116:119], v[76:79]\n"
    "ds_read_b64_tr_b16 v[116:117], v20 offset:30944\n"
    "v_cvt_pk_bf16_f32 v176, v176, v177\n"
    "v_cvt_pk_bf16_f32 v177, v178, v179\n"
    "v_mfma_f32_16x16x32_bf16 v[76:79], v[124:127], a[124:127], v[76:79]\n"
    "s_waitcnt lgkmcnt(3)\n"
    "ds_read_b64_tr_b16 v[118:119], v20 offset:33248\n"
    "v_and_b32 v176, v176, v152\n"
    "v_and_b32 v177, v177, v153\n"
    "v_mfma_f32_16x16x32_bf16 v[80:83], v[104:107], a[112:115], v[80:83]\n"
    "ds_read_b64_tr_b16 v[120:121], v20 offset:20736\n"
    "buffer_store_dwordx2 v[176:177], v155, s[44:47], s81 offen sc1\n"
    "v_mfma_f32_16x16x32_bf16 v[80:83], v[108:111], a[120:123], v[80:83]\n"
    "ds_read_b64_tr_b16 v[122:123], v20 offset:23040\n"
    "v_mfma_f32_16x16x32_bf16 v[84:87], v[104:107], a[116:119], v[84:87]\n"
    "ds_read_b64_tr_b16 v[124:125], v20 offset:30976\n"
    "v_mfma_f32_16x16x32_bf16 v[84:87], v[108:111], a[124:127], v[84:87]\n"
    "s_waitcnt lgkmcnt(3)\n"
    "ds_read_b64_tr_b16 v[126:127], v20 offset:33280\n"
    "v_mfma_f32_16x16x32_bf16 v[88:91], v[112:115], a[112:115], v[88:91]\n"
    "s_waitcnt vmcnt(11)\n"
    "s_barrier\n"
    "ds_read_b64_tr_b16 v[104:105], v20 offset:40960\n"
    "v_mfma_f32_16x16x32_bf16 v[88:91], v[116:119], a[120:123], v[88:91]\n"
    "ds_read_b64_tr_b16 v[106:107], v20 offset:43264\n"
    "v_mfma_f32_16x16x32_bf16 v[92:95], v[112:115], a[116:119], v[92:95]\n"
    "s_mov_b32 s82, 256\n"
    "s_add_u32 m0, s89, 122880\n"
    "ds_read_b64_tr_b16 v[108:109], v20 offset:51200\n"
    "buffer_load_dwordx4 v15, s[52:55], s82 offen lds\n"
    "v_mfma_f32_16x16x32_bf16 v[92:95], v[116:119], a[124:127], v[92:95]\n"
    "s_waitcnt lgkmcnt(3)\n"
    "ds_read_b64_tr_b16 v[110:111], v20 offset:53504\n"
    "v_mfma_f32_16x16x32_bf16 v[96:99], v[120:123], a[112:115], v[96:99]\n"
    "ds_read_b64_tr_b16 v[112:113], v20 offset:40992\n"
    "buffer_load_dwordx4 v16, s[52:55], s82 offen offset:1024 lds\n"
    "v_mfma_f32_16x16x32_bf16 v[96:99], v[124:127], a[120:123], v[96:99]\n"
    "ds_read_b64_tr_b16 v[114:115], v20 offset:43296\n"
    "v_mfma_f32_16x16x32_bf16 v[100:103], v[120:123], a[116:119], v[100:103]\n"
    "ds_read_b64_tr_b16 v[116:117], v20 offset:51232\n"
    "buffer_load_dwordx4 v17, s[52:55], s82 offen offset:2048 lds\n"
    "v_mfma_f32_16x16x32_bf16 v[100:103], v[124:127], a[124:127], v[100:103]\n"
    "s_waitcnt lgkmcnt(3)\n"
    "ds_read_b64_tr_b16 v[118:119], v20 offset:53536\n"
    "v_mfma_f32_16x16x32_bf16 v[32:35], v[104:107], a[128:131], v[32:35]\n"
    "ds_read_b64_tr_b16 v[120:121], v20 offset:41024\n"
    "buffer_load_dwordx4 v18, s[52:55], s82 offen offset:3072 lds\n"
    "v_mfma_f32_16x16x32_bf16 v[32:35], v[108:111], a[136:139], v[32:35]\n"
    "ds_read_b64_tr_b16 v[122:123], v20 offset:43328\n"
    "v_mfma_f32_16x16x32_bf16 v[36:39], v[104:107], a[132:135], v[36:39]\n"
    "s_add_u32 m0, s89, 126976\n"
    "ds_read_b64_tr_b16 v[124:125], v20 offset:51264\n"
    "buffer_load_dwordx4 v19, s[52:55], s82 offen lds\n"
    "v_mfma_f32_16x16x32_bf16 v[36:39], v[108:111], a[140:143], v[36:39]\n"
    "s_waitcnt lgkmcnt(3)\n"
    "ds_read_b64_tr_b16 v[126:127], v20 offset:53568\n"
    "v_mfma_f32_16x16x32_bf16 v[40:43], v[112:115], a[128:131], v[40:43]\n"
    "s_mov_b32 s82, 384\n"
    "s_add_u32 m0, s89, 143360\n"
    "ds_read_b64_tr_b16 v[104:105], v20 offset:41056\n"
    "buffer_load_dwordx4 v15, s[52:55], s82 offen lds\n"
    "v_mfma_f32_16x16x32_bf16 v[40:43], v[116:119], a[136:139], v[40:43]\n"
    "ds_read_b64_tr_b16 v[106:107], v20 offset:43360\n"
    "v_mfma_f32_16x16x32_bf16 v[44:47], v[112:115], a[132:135], v[44:47]\n"
    "ds_read_b64_tr_b16 v[108:109], v20 offset:51296\n"
    "buffer_load_dwordx4 v16, s[52:55], s82 offen offset:1024 lds\n"
    "v_mfma_f32_16x16x32_bf16 v[44:47], v[116:119], a[140:143], v[44:47]\n"
    "s_waitcnt lgkmcnt(3)\n"
    "ds_read_b64_tr_b16 v[110:111], v20 offset:53600\n"
    "v_mfma_f32_16x16x32_bf16 v[48:51], v[120:123], a[128:131], v[48:51]\n"
    "ds_read_b64_tr_b16 v[112:113], v20 offset:41088\n"
    "buffer_load_dwordx4 v17, s[52:55], s82 offen offset:2048 lds\n"
    "v_mfma_f32_16x16x32_bf16 v[48:51], v[124:127], a[136:139], v[48:51]\n"
    "ds_read_b64_tr_b16 v[114:115], v20 offset:43392\n"
    "v_mfma_f32_16x16x32_bf16 v[52:55], v[120:123], a[132:135], v[52:55]\n"
    "ds_read_b64_tr_b16 v[116:117], v20 offset:51328\n"
    "buffer_load_dwordx4 v18, s[52:55], s82 offen offset:3072 lds\n"
    "v_mfma_f32_16x16x32_bf16 v[52:55], v[124:127], a[140:143], v[52:55]\n"
    "s_waitcnt lgkmcnt(3)\n"
    "ds_read_b64_tr_b16 v[118:119], v20 offset:53632\n"
    "v_mfma_f32_16x16x32_bf16 v[56:59], v[104:107], a[128:131], v[56:59]\n"
    "s_add_u32 m0, s89, 147456\n"
    "ds_read_b64_tr_b16 v[120:121], v20 offset:41120\n"
    "buffer_load_dwordx4 v19, s[52:55], s82 offen lds\n"
    "v_mfma_f32_16x16x32_bf16 v[56:59], v[108:111], a[136:139], v[56:59]\n"
    "ds_read_b64_tr_b16 v[122:123], v20 offset:43424\n"
    "v_mfma_f32_16x16x32_bf16 v[60:63], v[104:107], a[132:135], v[60:63]\n"
    "s_mov_b32 s82, 512\n"
    "s_add_u32 m0, s89, 0\n"
    "ds_read_b64_tr_b16 v[124:125], v20 offset:51360\n"
    "buffer_load_dwordx4 v15, s[52:55], s82 offen lds\n"
    "v_mfma_f32_16x16x32_bf16 v[60:63], v[108:111], a[140:143], v[60:63]\n"
    "s_waitcnt lgkmcnt(3)\n"
    "ds_read_b64_tr_b16 v[126:127], v20 offset:53664\n"
    "v_mfma_f32_16x16x32_bf16 v[64:67], v[112:115], a[128:131], v[64:67]\n"
    "ds_read_b64_tr_b16 v[104:105], v20 offset:41152\n"
    "buffer_load_dwordx4 v16, s[52:55], s82 offen offset:1024 lds\n"
    "v_mfma_f32_16x16x32_bf16 v[64:67], v[116:119], a[136:139], v[64:67]\n"
    "ds_read_b64_tr_b16 v[106:107], v20 offset:43456\n"
    "v_mfma_f32_16x16x32_bf16 v[68:71], v[112:115], a[132:135], v[68:71]\n"
    "ds_read_b64_tr_b16 v[108:109], v20 offset:51392\n"
    "buffer_load_dwordx4 v17, s[52:55], s82 offen offset:2048 lds\n"
    "v_mfma_f32_16x16x32_bf16 v[68:71], v[116:119], a[140:143], v[68:71]\n"
    "s_waitcnt lgkmcnt(3)\n"
    "ds_read_b64_tr_b16 v[110:111], v20 offset:53696\n"
    "v_mfma_f32_16x16x32_bf16 v[72:75], v[120:123], a[128:131], v[72:75]\n"
    "ds_read_b64_tr_b16 v[112:113], v20 offset:41184\n"
    "buffer_load_dwordx4 v18, s[52:55], s82 offen offset:3072 lds\n"
    "v_mfma_f32_16x16x32_bf16 v[72:75], v[124:127], a[136:139], v[72:75]\n"
    "ds_read_b64_tr_b16 v[114:115], v20 offset:43488\n"
    "v_mfma_f32_16x16x32_bf16 v[76:79], v[120:123], a[132:135], v[76:79]\n"
    "s_add_u32 m0, s89, 4096\n"
    "ds_read_b64_tr_b16 v[116:117], v20 offset:51424\n"
    "buffer_load_dwordx4 v19, s[52:55], s82 offen lds\n"
    "v_mfma_f32_16x16x32_bf16 v[76:79], v[124:127], a[140:143], v[76:79]\n"
    "s_waitcnt lgkmcnt(3)\n"
    "ds_read_b64_tr_b16 v[118:119], v20 offset:53728\n"
    "v_mfma_f32_16x16x32_bf16 v[80:83], v[104:107], a[128:131], v[80:83]\n"
    "ds_read_b64_tr_b16 v[120:121], v20 offset:41216\n"
    "v_mfma_f32_16x16x32_bf16 v[80:83], v[108:111], a[136:139], v[80:83]\n"
    "ds_read_b64_tr_b16 v[122:123], v20 offset:43520\n"
    "v_mfma_f32_16x16x32_bf16 v[84:87], v[104:107], a[132:135], v[84:87]\n"
    "ds_read_b64_tr_b16 v[124:125], v20 offset:51456\n"
    "v_mfma_f32_16x16x32_bf16 v[84:87], v[108:111], a[140:143], v[84:87]\n"
    "s_waitcnt lgkmcnt(3)\n"
    "ds_read_b64_tr_b16 v[126:127], v20 offset:53760\n"
    "v_mfma_f32_16x16x32_bf16 v[88:91], v[112:115], a[128:131], v[88:91]\n"
    "v_mfma_f32_16x16x32_bf16 v[88:91], v[116:119], a[136:139], v[88:91]\n"
    "v_mfma_f32_16x16x32_bf16 v[92:95], v[112:115], a[132:135], v[92:95]\n"
    "v_mfma_f32_16x16x32_bf16 v[92:95], v[116:119], a[140:143], v[92:95]\n"
    "s_waitcnt lgkmcnt(0)\n"
    "v_mfma_f32_16x16x32_bf16 v[96:99], v[120:123], a[128:131], v[96:99]\n"
    "v_mfma_f32_16x16x32_bf16 v[96:99], v[124:127], a[136:139], v[96:99]\n"
    "v_mfma_f32_16x16x32_bf16 v[100:103], v[120:123], a[132:135], v[100:103]\n"
    "v_mfma_f32_16x16x32_bf16 v[100:103], v[124:127], a[140:143], v[100:103]\n"
    "v_mov_b32 v134, 0x3fb8aa3b\n"
    "v_accvgpr_write_b32 a154, 0\n"
    "v_accvgpr_write_b32 a155, 0\n"
    "v_accvgpr_write_b32 a156, 0\n"
    "v_accvgpr_write_b32 a157, 0\n"
    "v_accvgpr_write_b32 a170, 0\n"
    "v_accvgpr_write_b32 a171, 0\n"
    "v_accvgpr_write_b32 a172, 0\n"
    "v_accvgpr_write_b32 a173, 0\n"
    "v_accvgpr_write_b32 a186, 0\n"
    "v_accvgpr_write_b32 a187, 0\n"
    "v_accvgpr_write_b32 a188, 0\n"
    "v_accvgpr_write_b32 a189, 0\n"
    "v_accvgpr_write_b32 a202, 0\n"
    "v_accvgpr_write_b32 a203, 0\n"
    "v_accvgpr_write_b32 a204, 0\n"
    "v_accvgpr_write_b32 a205, 0\n"
    "v_accvgpr_write_b32 a210, 0\n"
    "v_accvgpr_write_b32 a211, 0\n"
    "v_accvgpr_write_b32 a222, 0\n"
    "v_accvgpr_write_b32 a223, 0\n"
    "v_accvgpr_write_b32 a234, 0\n"
    "v_accvgpr_write_b32 a235, 0\n"
    "v_pk_mul_f32 v[144:145], v[32:33], v[134:135] op_sel_hi:[1,0]\n"
    "v_pk_mul_f32 v[146:147], v[34:35], v[134:135] op_sel_hi:[1,0]\n"
    "v_pk_mul_f32 v[148:149], v[36:37], v[134:135] op_sel_hi:[1,0]\n"
    "v_pk_mul_f32 v[150:151], v[38:39], v[134:135] op_sel_hi:[1,0]\n"
    "v_exp_f32 v144, v144 clamp\n"
    "v_max_f32 v32, s87, v32\n"
    "v_exp_f32 v145, v145 clamp\n"
    "v_max_f32 v33, s87, v33\n"
    "v_exp_f32 v146, v146 clamp\n"
    "v_max_f32 v34, s87, v34\n"
    "v_exp_f32 v147, v147 clamp\n"
    "v_max_f32 v35, s87, v35\n"
    "v_exp_f32 v148, v148 clamp\n"
    "v_max_f32 v36, s87, v36\n"
    "v_exp_f32 v149, v149 clamp\n"
    "v_max_f32 v37, s87, v37\n"
    "v_exp_f32 v150, v150 clamp\n"
    "v_max_f32 v38, s87, v38\n"
    "v_exp_f32 v151, v151 clamp\n"
    "v_max_f32 v39, s87, v39\n"
    "v_pk_add_f32 v[32:33], v[32:33], v[144:145]\n"
    "v_pk_add_f32 v[34:35], v[34:35], v[146:147]\n"
    "v_pk_add_f32 v[36:37], v[36:37], v[148:149]\n"
    "v_pk_add_f32 v[38:39], v[38:39], v[150:151]\n"
    "v_cvt_pk_bf16_f32 v176, v32, v33\n"
    "v_cvt_pk_bf16_f32 v177, v34, v35\n"
    "v_cvt_pk_bf16_f32 v180, v36, v37\n"
    "v_cvt_pk_bf16_f32 v181, v38, v39\n"
    "v_pk_mul_f32 v[144:145], v[40:41], v[134:135] op_sel_hi:[1,0]\n"
    "v_pk_mul_f32 v[146:147], v[42:43], v[134:135] op_sel_hi:[1,0]\n"
    "v_pk_mul_f32 v[148:149], v[44:45], v[134:135] op_sel_hi:[1,0]\n"
    "v_pk_mul_f32 v[150:151], v[46:47], v[134:135] op_sel_hi:[1,0]\n"
    "v_exp_f32 v144, v144 clamp\n"
    "v_max_f32 v40, s87, v40\n"
    "v_exp_f32 v145, v145 clamp\n"
    "v_max_f32 v41, s87, v41\n"
    "v_exp_f32 v146, v146 clamp\n"
    "v_max_f32 v42, s87, v42\n"
    "v_exp_f32 v147, v147 clamp\n"
    "v_max_f32 v43, s87, v43\n"
    "v_exp_f32 v148, v148 clamp\n"
    "v_max_f32 v44, s87, v44\n"
    "v_exp_f32 v149, v149 clamp\n"
    "v_max_f32 v45, s87, v45\n"
    "v_exp_f32 v150, v150 clamp\n"
    "v_max_f32 v46, s87, v46\n"
    "v_exp_f32 v151, v151 clamp\n"
    "v_max_f32 v47, s87, v47\n"
    "v_pk_add_f32 v[40:41], v[40:41], v[144:145]\n"
    "v_pk_add_f32 v[42:43], v[42:43], v[146:147]\n"
    "v_pk_add_f32 v[44:45], v[44:45], v[148:149]\n"
    "v_pk_add_f32 v[46:47], v[46:47], v[150:151]\n"
    "v_cvt_pk_bf16_f32 v178, v40, v41\n"
    "v_cvt_pk_bf16_f32 v179, v42, v43\n"
    "v_cvt_pk_bf16_f32 v182, v44, v45\n"
    "v_cvt_pk_bf16_f32 v183, v46, v47\n"
    "v_pk_mul_f32 v[144:145], v[48:49], v[134:135] op_sel_hi:[1,0]\n"
    "v_pk_mul_f32 v[146:147], v[50:51], v[134:135] op_sel_hi:[1,0]\n"
    "v_pk_mul_f32 v[148:149], v[52:53], v[134:135] op_sel_hi:[1,0]\n"
    "v_pk_mul_f32 v[150:151], v[54:55], v[134:135] op_sel_hi:[1,0]\n"
    "v_exp_f32 v144, v144 clamp\n"
    "v_max_f32 v48, s87, v48\n"
    "v_exp_f32 v145, v145 clamp\n"
    "v_max_f32 v49, s87, v49\n"
    "v_exp_f32 v146, v146 clamp\n"
    "v_max_f32 v50, s87, v50\n"
    "v_exp_f32 v147, v147 clamp\n"
    "v_max_f32 v51, s87, v51\n"
    "v_exp_f32 v148, v148 clamp\n"
    "v_max_f32 v52, s87, v52\n"
    "v_exp_f32 v149, v149 clamp\n"
    "v_max_f32 v53, s87, v53\n"
    "v_exp_f32 v150, v150 clamp\n"
    "v_max_f32 v54, s87, v54\n"
    "v_exp_f32 v151, v151 clamp\n"
    "v_max_f32 v55, s87, v55\n"
    "v_pk_add_f32 v[48:49], v[48:49], v[144:145]\n"
    "v_pk_add_f32 v[50:51], v[50:51], v[146:147]\n"
    "v_pk_add_f32 v[52:53], v[52:53], v[148:149]\n"
    "v_pk_add_f32 v[54:55], v[54:55], v[150:151]\n"
    "v_cvt_pk_bf16_f32 v184, v48, v49\n"
    "v_cvt_pk_bf16_f32 v185, v50, v51\n"
    "v_cvt_pk_bf16_f32 v188, v52, v53\n"
    "v_cvt_pk_bf16_f32 v189, v54, v55\n"
    "s_waitcnt vmcnt(15) lgkmcnt(0)\n"
    "s_barrier\n"
    "s_add_u32 s29, s97, 65536\n"
    "buffer_store_dwordx4 v[164:167], v168, s[44:47], s29 offen sc1\n"
    "s_mov_b32 s82, 640\n"
    "s_add_u32 m0, s89, 20480\n"
    "s_nop 0\n"
    "buffer_load_dwordx4 v15, s[52:55], s82 offen lds\n"
    "buffer_load_dwordx4 v16, s[52:55], s82 offen offset:1024 lds\n"
    "buffer_load_dwordx4 v17, s[52:55], s82 offen offset:2048 lds\n"
    "buffer_load_dwordx4 v18, s[52:55], s82 offen offset:3072 lds\n"
    "s_add_u32 m0, s89, 24576\n"
    "s_nop 0\n"
    "buffer_load_dwordx4 v19, s[52:55], s82 offen lds\n"
    "s_mov_b32 s82, 768\n"
    "s_add_u32 m0, s89, 40960\n"
    "s_nop 0\n"
    "buffer_load_dwordx4 v15, s[52:55], s82 offen lds\n"
    "buffer_load_dwordx4 v16, s[52:55], s82 offen offset:1024 lds\n"
    "buffer_load_dwordx4 v17, s[52:55], s82 offen offset:2048 lds\n"
    "buffer_load_dwordx4 v18, s[52:55], s82 offen offset:3072 lds\n"
    "s_add_u32 m0, s89, 45056\n"
    "s_nop 0\n"
    "buffer_load_dwordx4 v19, s[52:55], s82 offen lds\n"
    "s_mov_b32 s82, 896\n"
    "s_add_u32 m0, s89, 61440\n"
    "s_nop 0\n"
    "buffer_load_dwordx4 v15, s[52:55], s82 offen lds\n"
    "buffer_load_dwordx4 v16, s[52:55], s82 offen offset:1024 lds\n"
    "buffer_load_dwordx4 v17, s[52:55], s82 offen offset:2048 lds\n"
    "buffer_load_dwordx4 v18, s[52:55], s82 offen offset:3072 lds\n"
    "s_add_u32 m0, s89, 65536\n"
    "s_nop 0\n"
    "buffer_load_dwordx4 v19, s[52:55], s82 offen lds\n"
    "v_pk_mul_f32 v[144:145], v[56:57], v[134:135] op_sel_hi:[1,0]\n"
    "v_pk_mul_f32 v[146:147], v[58:59], v[134:135] op_sel_hi:[1,0]\n"
    "v_pk_mul_f32 v[148:149], v[60:61], v[134:135] op_sel_hi:[1,0]\n"
    "v_pk_mul_f32 v[150:151], v[62:63], v[134:135] op_sel_hi:[1,0]\n"
    "v_exp_f32 v144, v144 clamp\n"
    "v_max_f32 v56, s87, v56\n"
    "v_exp_f32 v145, v145 clamp\n"
    "v_max_f32 v57, s87, v57\n"
    "v_exp_f32 v146, v146 clamp\n"
    "v_max_f32 v58, s87, v58\n"
    "v_exp_f32 v147, v147 clamp\n"
    "v_max_f32 v59, s87, v59\n"
    "v_exp_f32 v148, v148 clamp\n"
    "v_max_f32 v60, s87, v60\n"
    "v_exp_f32 v149, v149 clamp\n"
    "v_max_f32 v61, s87, v61\n"
    "v_exp_f32 v150, v150 clamp\n"
    "v_max_f32 v62, s87, v62\n"
    "v_exp_f32 v151, v151 clamp\n"
    "v_max_f32 v63, s87, v63\n"
    "v_pk_add_f32 v[56:57], v[56:57], v[144:145]\n"
    "v_pk_add_f32 v[58:59], v[58:59], v[146:147]\n"
    "v_pk_add_f32 v[60:61], v[60:61], v[148:149]\n"
    "v_pk_add_f32 v[62:63], v[62:63], v[150:151]\n"
    "v_cvt_pk_bf16_f32 v186, v56, v57\n"
    "v_cvt_pk_bf16_f32 v187, v58, v59\n"
    "v_cvt_pk_bf16_f32 v190, v60, v61\n"
    "v_cvt_pk_bf16_f32 v191, v62, v63\n"
    "v_pk_mul_f32 v[144:145], v[64:65], v[134:135] op_sel_hi:[1,0]\n"
    "v_pk_mul_f32 v[146:147], v[66:67], v[134:135] op_sel_hi:[1,0]\n"
    "v_pk_mul_f32 v[148:149], v[68:69], v[134:135] op_sel_hi:[1,0]\n"
    "v_pk_mul_f32 v[150:151], v[70:71], v[134:135] op_sel_hi:[1,0]\n"
    "v_exp_f32 v144, v144 clamp\n"
    "v_max_f32 v64, s87, v64\n"
    "v_exp_f32 v145, v145 clamp\n"
    "v_max_f32 v65, s87, v65\n"
    "v_exp_f32 v146, v146 clamp\n"
    "v_max_f32 v66, s87, v66\n"
    "v_exp_f32 v147, v147 clamp\n"
    "v_max_f32 v67, s87, v67\n"
    "v_exp_f32 v148, v148 clamp\n"
    "v_max_f32 v68, s87, v68\n"
    "v_exp_f32 v149, v149 clamp\n"
    "v_max_f32 v69, s87, v69\n"
    "v_exp_f32 v150, v150 clamp\n"
    "v_max_f32 v70, s87, v70\n"
    "v_exp_f32 v151, v151 clamp\n"
    "v_max_f32 v71, s87, v71\n"
    "v_pk_add_f32 v[64:65], v[64:65], v[144:145]\n"
    "v_pk_add_f32 v[66:67], v[66:67], v[146:147]\n"
    "v_pk_add_f32 v[68:69], v[68:69], v[148:149]\n"
    "v_pk_add_f32 v[70:71], v[70:71], v[150:151]\n"
    "v_cvt_pk_bf16_f32 v192, v64, v65\n"
    "v_cvt_pk_bf16_f32 v193, v66, v67\n"
    "v_cvt_pk_bf16_f32 v196, v68, v69\n"
    "v_cvt_pk_bf16_f32 v197, v70, v71\n"
    "v_pk_mul_f32 v[144:145], v[72:73], v[134:135] op_sel_hi:[1,0]\n"
    "v_pk_mul_f32 v[146:147], v[74:75], v[134:135] op_sel_hi:[1,0]\n"
    "v_pk_mul_f32 v[148:149], v[76:77], v[134:135] op_sel_hi:[1,0]\n"
    "v_pk_mul_f32 v[150:151], v[78:79], v[134:135] op_sel_hi:[1,0]\n"
    "v_exp_f32 v144, v144 clamp\n"
    "v_max_f32 v72, s87, v72\n"
    "v_exp_f32 v145, v145 clamp\n"
    "v_max_f32 v73, s87, v73\n"
    "v_exp_f32 v146, v146 clamp\n"
    "v_max_f32 v74, s87, v74\n"
    "v_exp_f32 v147, v147 clamp\n"
    "v_max_f32 v75, s87, v75\n"
    "v_exp_f32 v148, v148 clamp\n"
    "v_max_f32 v76, s87, v76\n"
    "v_exp_f32 v149, v149 clamp\n"
    "v_max_f32 v77, s87, v77\n"
    "v_exp_f32 v150, v150 clamp\n"
    "v_max_f32 v78, s87, v78\n"
    "v_exp_f32 v151, v151 clamp\n"
    "v_max_f32 v79, s87, v79\n"
    "v_pk_add_f32 v[72:73], v[72:73], v[144:145]\n"
    "v_pk_add_f32 v[74:75], v[74:75], v[146:147]\n"
    "v_pk_add_f32 v[76:77], v[76:77], v[148:149]\n"
    "v_pk_add_f32 v[78:79], v[78:79], v[150:151]\n"
    "v_cvt_pk_bf16_f32 v194, v72, v73\n"
    "v_cvt_pk_bf16_f32 v195, v74, v75\n"
    "v_cvt_pk_bf16_f32 v198, v76, v77\n"
    "v_cvt_pk_bf16_f32 v199, v78, v79\n"
    "v_pk_mul_f32 v[144:145], v[80:81], v[134:135] op_sel_hi:[1,0]\n"
    "v_pk_mul_f32 v[146:147], v[82:83], v[134:135] op_sel_hi:[1,0]\n"
    "v_pk_mul_f32 v[148:149], v[84:85], v[134:135] op_sel_hi:[1,0]\n"
    "v_pk_mul_f32 v[150:151], v[86:87], v[134:135] op_sel_hi:[1,0]\n"
    "v_exp_f32 v144, v144 clamp\n"
    "v_max_f32 v80, s87, v80\n"
    "v_exp_f32 v145, v145 clamp\n"
    "v_max_f32 v81, s87, v81\n"
    "v_exp_f32 v146, v146 clamp\n"
    "v_max_f32 v82, s87, v82\n"
    "v_exp_f32 v147, v147 clamp\n"
    "v_max_f32 v83, s87, v83\n"
    "v_exp_f32 v148, v148 clamp\n"
    "v_max_f32 v84, s87, v84\n"
    "v_exp_f32 v149, v149 clamp\n"
    "v_max_f32 v85, s87, v85\n"
    "v_exp_f32 v150, v150 clamp\n"
    "v_max_f32 v86, s87, v86\n"
    "v_exp_f32 v151, v151 clamp\n"
    "v_max_f32 v87, s87, v87\n"
    "v_pk_add_f32 v[80:81], v[80:81], v[144:145]\n"
    "v_pk_add_f32 v[82:83], v[82:83], v[146:147]\n"
    "v_pk_add_f32 v[84:85], v[84:85], v[148:149]\n"
    "v_pk_add_f32 v[86:87], v[86:87], v[150:151]\n"
    "v_cvt_pk_bf16_f32 v200, v80, v81\n"
    "v_cvt_pk_bf16_f32 v201, v82, v83\n"
    "v_cvt_pk_bf16_f32 v204, v84, v85\n"
    "v_cvt_pk_bf16_f32 v205, v86, v87\n"
    "s_add_u32 s31, s98, 32768\n"
    "buffer_load_dwordx2 v[158:159], v29, s[44:47], s31 offen sc1\n"
    "v_pk_mul_f32 v[144:145], v[88:89], v[134:135] op_sel_hi:[1,0]\n"
    "v_pk_mul_f32 v[146:147], v[90:91], v[134:135] op_sel_hi:[1,0]\n"
    "v_pk_mul_f32 v[148:149], v[92:93], v[134:135] op_sel_hi:[1,0]\n"
    "v_pk_mul_f32 v[150:151], v[94:95], v[134:135] op_sel_hi:[1,0]\n"
    "v_exp_f32 v144, v144 clamp\n"
    "v_max_f32 v88, s87, v88\n"
    "v_exp_f32 v145, v145 clamp\n"
    "v_max_f32 v89, s87, v89\n"
    "v_exp_f32 v146, v146 clamp\n"
    "v_max_f32 v90, s87, v90\n"
    "v_exp_f32 v147, v147 clamp\n"
    "v_max_f32 v91, s87, v91\n"
    "v_exp_f32 v148, v148 clamp\n"
    "v_max_f32 v92, s87, v92\n"
    "v_exp_f32 v149, v149 clamp\n"
    "v_max_f32 v93, s87, v93\n"
    "v_exp_f32 v150, v150 clamp\n"
    "v_max_f32 v94, s87, v94\n"
    "v_exp_f32 v151, v151 clamp\n"
    "v_max_f32 v95, s87, v95\n"
    "v_pk_add_f32 v[88:89], v[88:89], v[144:145]\n"
    "v_pk_add_f32 v[90:91], v[90:91], v[146:147]\n"
    "v_pk_add_f32 v[92:93], v[92:93], v[148:149]\n"
    "v_pk_add_f32 v[94:95], v[94:95], v[150:151]\n"
    "v_cvt_pk_bf16_f32 v202, v88, v89\n"
    "v_cvt_pk_bf16_f32 v203, v90, v91\n"
    "v_cvt_pk_bf16_f32 v206, v92, v93\n"
    "v_cvt_pk_bf16_f32 v207, v94, v95\n"
    "v_pk_mul_f32 v[144:145], v[96:97], v[134:135] op_sel_hi:[1,0]\n"
    "v_pk_mul_f32 v[146:147], v[98:99], v[134:135] op_sel_hi:[1,0]\n"
    "v_pk_mul_f32 v[148:149], v[100:101], v[134:135] op_sel_hi:[1,0]\n"
    "v_pk_mul_f32 v[150:151], v[102:103], v[134:135] op_sel_hi:[1,0]\n"
    "v_exp_f32 v144, v144 clamp\n"
    "v_max_f32 v96, s87, v96\n"
    "v_exp_f32 v145, v145 clamp\n"
    "v_max_f32 v97, s87, v97\n"
    "v_exp_f32 v146, v146 clamp\n"
    "v_max_f32 v98, s87, v98\n"
    "v_exp_f32 v147, v147 clamp\n"
    "v_max_f32 v99, s87, v99\n"
    "v_exp_f32 v148, v148 clamp\n"
    "v_max_f32 v100, s87, v100\n"
    "v_exp_f32 v149, v149 clamp\n"
    "v_max_f32 v101, s87, v101\n"
    "v_exp_f32 v150, v150 clamp\n"
    "v_max_f32 v102, s87, v102\n"
    "v_exp_f32 v151, v151 clamp\n"
    "v_max_f32 v103, s87, v103\n"
    "v_pk_add_f32 v[96:97], v[96:97], v[144:145]\n"
    "v_pk_add_f32 v[98:99], v[98:99], v[146:147]\n"
    "v_pk_add_f32 v[100:101], v[100:101], v[148:149]\n"
    "v_pk_add_f32 v[102:103], v[102:103], v[150:151]\n"
    "v_cvt_pk_bf16_f32 v208, v96, v97\n"
    "v_cvt_pk_bf16_f32 v209, v98, v99\n"
    "v_cvt_pk_bf16_f32 v212, v100, v101\n"
    "v_cvt_pk_bf16_f32 v213, v102, v103\n"
    "v_mov_b32 v210, 0\n"
    "v_mov_b32 v211, 0\n"
    "v_mov_b32 v250, 0\n"
    "v_mov_b32 v251, 0\n"
    "v_mov_b32 v214, 0\n"
    "v_mov_b32 v215, 0\n"
    "v_mov_b32 v254, 0\n"
    "v_mov_b32 v255, 0\n"
    "s_waitcnt vmcnt(0)\n"
    "v_cmp_ne_u64 vcc, v[164:165], v[158:159]\n"
    "s_cbranch_vccnz .LL_H2RETRY_%=\n"
    ".LL_H2OK_%=:\n"
    "s_mul_i32 s30, s84, 18432\n"
    "s_add_u32 s30, s30, 655360\n"
    "buffer_load_dwordx2 a[224:225], v155, s[44:47], s30 offen offset:0 sc1\n"
    "buffer_load_dwordx2 a[144:145], v155, s[44:47], s30 offen offset:512 sc1\n"
    "buffer_load_dwordx2 a[148:149], v155, s[44:47], s30 offen offset:1024 sc1\n"
    "buffer_load_dwordx2 a[212:213], v155, s[44:47], s30 offen offset:1536 sc1\n"
    "buffer_load_dwordx2 a[146:147], v155, s[44:47], s30 offen offset:2048 sc1\n"
    "buffer_load_dwordx2 a[150:151], v155, s[44:47], s30 offen offset:2560 sc1\n"
    "buffer_load_dwordx2 a[158:159], v155, s[44:47], s30 offen offset:3072 sc1\n"
    "buffer_load_dwordx2 a[214:215], v155, s[44:47], s30 offen offset:3584 sc1\n"
    "s_add_u32 s30, s30, 4096\n"
    "buffer_load_dwordx2 a[152:153], v155, s[44:47], s30 offen offset:0 sc1\n"
    "buffer_load_dwordx2 a[160:161], v155, s[44:47], s30 offen offset:512 sc1\n"
    "buffer_load_dwordx2 a[164:165], v155, s[44:47], s30 offen offset:1024 sc1\n"
    "buffer_load_dwordx2 a[216:217], v155, s[44:47], s30 offen offset:1536 sc1\n"
    "buffer_load_dwordx2 a[162:163], v155, s[44:47], s30 offen offset:2048 sc1\n"
    "buffer_load_dwordx2 a[166:167], v155, s[44:47], s30 offen offset:2560 sc1\n"
    "buffer_load_dwordx2 a[174:175], v155, s[44:47], s30 offen offset:3072 sc1\n"
    "buffer_load_dwordx2 a[218:219], v155, s[44:47], s30 offen offset:3584 sc1\n"
    "s_add_u32 s30, s30, 4096\n"
    "buffer_load_dwordx2 a[168:169], v155, s[44:47], s30 offen offset:0 sc1\n"
    "buffer_load_dwordx2 a[176:177], v155, s[44:47], s30 offen offset:512 sc1\n"
    "buffer_load_dwordx2 a[180:181], v155, s[44:47], s30 offen offset:1024 sc1\n"
    "buffer_load_dwordx2 a[220:221], v155, s[44:47], s30 offen offset:1536 sc1\n"
    "buffer_load_dwordx2 a[178:179], v155, s[44:47], s30 offen offset:2048 sc1\n"
    "buffer_load_dwordx2 a[182:183], v155, s[44:47], s30 offen offset:2560 sc1\n"
    "buffer_load_dwordx2 a[190:191], v155, s[44:47], s30 offen offset:3072 sc1\n"
    "buffer_load_dwordx2 a[226:227], v155, s[44:47], s30 offen offset:3584 sc1\n"
    "s_add_u32 s30, s30, 4096\n"
    "buffer_load_dwordx2 a[184:185], v155, s[44:47], s30 offen offset:0 sc1\n"
    "buffer_load_dwordx2 a[192:193], v155, s[44:47], s30 offen offset:512 sc1\n"
    "buffer_load_dwordx2 a[196:197], v155, s[44:47], s30 offen offset:1024 sc1\n"
    "buffer_load_dwordx2 a[228:229], v155, s[44:47], s30 offen offset:1536 sc1\n"
    "buffer_load_dwordx2 a[194:195], v155, s[44:47], s30 offen offset:2048 sc1\n"
    "buffer_load_dwordx2 a[198:199], v155, s[44:47], s30 offen offset:2560 sc1\n"
    "buffer_load_dwordx2 a[206:207], v155, s[44:47], s30 offen offset:3072 sc1\n"
    "buffer_load_dwordx2 a[230:231], v155, s[44:47], s30 offen offset:3584 sc1\n"
    "s_add_u32 s30, s30, 4096\n"
    "buffer_load_dwordx2 a[200:201], v155, s[44:47], s30 offen offset:0 sc1\n"
    "buffer_load_dwordx2 a[208:209], v155, s[44:47], s30 offen offset:512 sc1\n"
    "buffer_load_dwordx2 a[232:233], v155, s[44:47], s30 offen offset:1536 sc1\n"
    "v_bfe_u32 v2, v1, 2, 3\n"
    "v_and_b32 v3, 3, v1\n"
    "v_lshlrev_b32 v3, 3, v3\n"
    "v_lshrrev_b32 v4, 1, v2\n"
    "v_lshl_or_b32 v3, v4, 5, v3\n"
    "v_lshl_add_u32 v3, v2, 7, v3\n"
    "v_lshrrev_b32 v4, 5, v1\n"
    "v_lshl_add_u32 v3, v4, 10, v3\n"
    "v_add_u32 v20, 81920, v3\n"
    "v_xor_b32 v21, 32, v20\n"
    "v_xor_b32 v22, 64, v20\n"
    "v_xor_b32 v23, 0x60, v20\n"
    "v_and_b32 v2, 15, v1\n"
    "v_lshrrev_b32 v3, 4, v1\n"
    "v_lshrrev_b32 v6, 2, v2\n"
    "v_lshlrev_b32 v6, 8, v6\n"
    "v_and_b32 v2, 3, v2\n"
    "v_lshl_add_u32 v6, v2, 4, v6\n"
    "v_lshl_add_u32 v6, v3, 6, v6\n"
    "v_add_u32 v7, 147456, v6\n"
    "v_add_u32 v8, 294912, v6\n"
    "v_add_u32 v9, 442368, v6\n"
    "s_lshl_b32 s60, s79, 4\n"
    "s_lshl_b32 s61, s80, 2\n"
    "s_add_u32 s60, s60, s61\n"
    "s_mul_i32 s95, s60, 147456\n"
    "s_lshl_b32 s61, s83, 10\n"
    "s_add_u32 s95, s95, s61\n"
    "s_add_u32 s95, s95, 4194304\n"
    "s_mov_b32 s86, 1879048192\n"
    "s_mov_b32 s88, 0\n"
    "s_mov_b32 s74, 0xffff\n"
    "s_mov_b32 s75, 0\n"
    "s_mov_b32 s76, -1\n"
    "s_mov_b32 s77, 0\n"
    "s_mov_b32 s62, -1\n"
    "s_mov_b32 s63, 0xffff\n"
    "s_waitcnt vmcnt(0)\n"
    "s_barrier\n"
    "v_mfma_f32_16x16x32_bf16 v[144:147], a[212:215], v[176:179], v[136:139]\n"
    "v_mfma_f32_16x16x32_bf16 v[144:147], a[216:219], v[184:187], v[144:147]\n"
    "v_mfma_f32_16x16x32_bf16 v[144:147], a[220:223], v[192:195], v[144:147]\n"
    "v_mfma_f32_16x16x32_bf16 v[152:155], a[212:215], v[180:183], v[136:139]\n"
    "v_mfma_f32_16x16x32_bf16 v[152:155], a[216:219], v[188:191], v[152:155]\n"
    "v_mfma_f32_16x16x32_bf16 v[152:155], a[220:223], v[196:199], v[152:155]\n"
    "v_mfma_f32_16x16x32_bf16 v[148:151], a[224:227], v[192:195], v[136:139]\n"
    "v_mfma_f32_16x16x32_bf16 v[148:151], a[228:231], v[200:203], v[148:151]\n"
    "v_mfma_f32_16x16x32_bf16 v[148:151], a[232:235], v[208:211], v[148:151]\n"
    "v_mfma_f32_16x16x32_bf16 v[156:159], a[224:227], v[196:199], v[136:139]\n"
    "v_mfma_f32_16x16x32_bf16 v[156:159], a[228:231], v[204:207], v[156:159]\n"
    "v_mfma_f32_16x16x32_bf16 v[156:159], a[232:235], v[212:215], v[156:159]\n"
    "v_mfma_f32_16x16x32_bf16 v[32:35], a[144:147], v[176:179], 0\n"
    "ds_read_b64_tr_b16 v[104:105], v20 offset:0\n"
    "v_mfma_f32_16x16x32_bf16 v[36:39], a[144:147], v[180:183], 0\n"
    "ds_read_b64_tr_b16 v[106:107], v20 offset:2048\n"
    "v_mfma_f32_16x16x32_bf16 v[40:43], a[148:151], v[176:179], 0\n"
    "ds_read_b64_tr_b16 v[108:109], v20 offset:4096\n"
    "v_mfma_f32_16x16x32_bf16 v[40:43], a[152:155], v[184:187], v[40:43]\n"
    "ds_read_b64_tr_b16 v[110:111], v20 offset:6144\n"
    "v_mfma_f32_16x16x32_bf16 v[44:47], a[148:151], v[180:183], 0\n"
    "ds_read_b64_tr_b16 v[112:113], v20 offset:8192\n"
    "v_mfma_f32_16x16x32_bf16 v[44:47], a[152:155], v[188:191], v[44:47]\n"
    "ds_read_b64_tr_b16 v[114:115], v20 offset:10240\n"
    "v_mfma_f32_16x16x32_bf16 v[48:51], a[156:159], v[176:179], 0\n"
    "ds_read_b64_tr_b16 v[116:117], v20 offset:12288\n"
    "v_mfma_f32_16x16x32_bf16 v[48:51], a[160:163], v[184:187], v[48:51]\n"
    "ds_read_b64_tr_b16 v[118:119], v20 offset:14336\n"
    "v_mfma_f32_16x16x32_bf16 v[52:55], a[156:159], v[180:183], 0\n"
    "ds_read_b64_tr_b16 v[120:121], v20 offset:16384\n"
    "v_mfma_f32_16x16x32_bf16 v[52:55], a[160:163], v[188:191], v[52:55]\n"
    "v_mov_b32 v122, 0\n"
    "v_mfma_f32_16x16x32_bf16 v[56:59], a[164:167], v[184:187], 0\n"
    "v_mov_b32 v123, 0\n"
    "v_mfma_f32_16x16x32_bf16 v[56:59], a[168:171], v[192:195], v[56:59]\n"
    "v_rcp_f32 v144, v144\n"
    "v_mfma_f32_16x16x32_bf16 v[60:63], a[164:167], v[188:191], 0\n"
    "v_rcp_f32 v145, v145\n"
    "v_mfma_f32_16x16x32_bf16 v[60:63], a[168:171], v[196:199], v[60:63]\n"
    "v_rcp_f32 v146, v146\n"
    "v_mfma_f32_16x16x32_bf16 v[64:67], a[172:175], v[184:187], 0\n"
    "v_rcp_f32 v147, v147\n"
    "v_mfma_f32_16x16x32_bf16 v[64:67], a[176:179], v[192:195], v[64:67]\n"
    "v_rcp_f32 v148, v148\n"
    "v_mfma_f32_16x16x32_bf16 v[68:71], a[172:175], v[188:191], 0\n"
    "v_rcp_f32 v149, v149\n"
    "v_mfma_f32_16x16x32_bf16 v[68:71], a[176:179], v[196:199], v[68:71]\n"
    "v_rcp_f32 v150, v150\n"
    "v_mfma_f32_16x16x32_bf16 v[72:75], a[180:183], v[192:195], 0\n"
    "v_rcp_f32 v151, v151\n"
    "v_mfma_f32_16x16x32_bf16 v[72:75], a[184:187], v[200:203], v[72:75]\n"
    "v_rcp_f32 v152, v152\n"
    "v_mfma_f32_16x16x32_bf16 v[76:79], a[180:183], v[196:199], 0\n"
    "v_rcp_f32 v153, v153\n"
    "v_mfma_f32_16x16x32_bf16 v[76:79], a[184:187], v[204:207], v[76:79]\n"
    "v_rcp_f32 v154, v154\n"
    "v_mfma_f32_16x16x32_bf16 v[80:83], a[188:191], v[192:195], 0\n"
    "v_rcp_f32 v155, v155\n"
    "v_mfma_f32_16x16x32_bf16 v[80:83], a[192:195], v[200:203], v[80:83]\n"
    "v_rcp_f32 v156, v156\n"
    "v_mfma_f32_16x16x32_bf16 v[84:87], a[188:191], v[196:199], 0\n"
    "v_rcp_f32 v157, v157\n"
    "v_mfma_f32_16x16x32_bf16 v[84:87], a[192:195], v[204:207], v[84:87]\n"
    "v_rcp_f32 v158, v158\n"
    "v_mfma_f32_16x16x32_bf16 v[88:91], a[196:199], v[200:203], 0\n"
    "v_rcp_f32 v159, v159\n"
    "v_mfma_f32_16x16x32_bf16 v[88:91], a[200:203], v[208:211], v[88:91]\n"
    "v_mfma_f32_16x16x32_bf16 v[92:95], a[196:199], v[204:207], 0\n"
    "v_mfma_f32_16x16x32_bf16 v[92:95], a[200:203], v[212:215], v[92:95]\n"
    "v_mfma_f32_16x16x32_bf16 v[96:99], a[204:207], v[200:203], 0\n"
    "v_mfma_f32_16x16x32_bf16 v[96:99], a[208:211], v[208:211], v[96:99]\n"
    "v_mfma_f32_16x16x32_bf16 v[100:103], a[204:207], v[204:207], 0\n"
    "v_mfma_f32_16x16x32_bf16 v[100:103], a[208:211], v[212:215], v[100:103]\n"
    "v_mov_b32 v142, 0\n"
    "v_mov_b32 v143, 0\n"
    "v_mul_f32 v32, v32, v144\n"
    "v_mul_f32 v33, v33, v144\n"
    "v_mul_f32 v34, v34, v144\n"
    "v_mul_f32 v35, v35, v144\n"
    "v_cvt_pk_bf16_f32 v216, v32, v33\n"
    "v_cvt_pk_bf16_f32 v217, v34, v35\n"
    "v_mul_f32 v36, v36, v152\n"
    "v_mul_f32 v37, v37, v152\n"
    "v_mul_f32 v38, v38, v152\n"
    "v_mul_f32 v39, v39, v152\n"
    "v_cvt_pk_bf16_f32 v220, v36, v37\n"
    "v_cvt_pk_bf16_f32 v221, v38, v39\n"
    "v_cndmask_b32 v2, v145, v144, s[74:75]\n"
    "v_mul_f32 v40, v40, v2\n"
    "v_mul_f32 v41, v41, v2\n"
    "v_mul_f32 v42, v42, v145\n"
    "v_mul_f32 v43, v43, v145\n"
    "v_cvt_pk_bf16_f32 v218, v40, v41\n"
    "v_cvt_pk_bf16_f32 v219, v42, v43\n"
    "v_cndmask_b32 v4, v153, v152, s[74:75]\n"
    "v_mul_f32 v44, v44, v4\n"
    "v_mul_f32 v45, v45, v4\n"
    "v_mul_f32 v46, v46, v153\n"
    "v_mul_f32 v47, v47, v153\n"
    "v_cvt_pk_bf16_f32 v222, v44, v45\n"
    "v_cvt_pk_bf16_f32 v223, v46, v47\n"
    "v_cndmask_b32 v2, v146, v145, s[74:75]\n"
    "v_cndmask_b32 v3, v146, v145, s[74:75]\n"
    "v_mul_f32 v48, v48, v2\n"
    "v_mul_f32 v49, v49, v2\n"
    "v_mul_f32 v50, v50, v3\n"
    "v_mul_f32 v51, v51, v3\n"
    "v_cvt_pk_bf16_f32 v224, v48, v49\n"
    "v_cvt_pk_bf16_f32 v225, v50, v51\n"
    "v_cndmask_b32 v4, v154, v153, s[74:75]\n"
    "v_cndmask_b32 v5, v154, v153, s[74:75]\n"
    "v_mul_f32 v52, v52, v4\n"
    "v_mul_f32 v53, v53, v4\n"
    "v_mul_f32 v54, v54, v5\n"
    "v_mul_f32 v55, v55, v5\n"
    "v_cvt_pk_bf16_f32 v228, v52, v53\n"
    "v_cvt_pk_bf16_f32 v229, v54, v55\n"
    "v_cndmask_b32 v2, v147, v146, s[76:77]\n"
    "v_cndmask_b32 v3, v147, v146, s[74:75]\n"
    "v_mul_f32 v56, v56, v2\n"
    "v_mul_f32 v57, v57, v2\n"
    "v_mul_f32 v58, v58, v3\n"
    "v_mul_f32 v59, v59, v3\n"
    "v_cvt_pk_bf16_f32 v226, v56, v57\n"
    "v_cvt_pk_bf16_f32 v227, v58, v59\n"
    "v_cndmask_b32 v4, v155, v154, s[76:77]\n"
    "v_cndmask_b32 v5, v155, v154, s[74:75]\n"
    "v_mul_f32 v60, v60, v4\n"
    "v_mul_f32 v61, v61, v4\n"
    "v_mul_f32 v62, v62, v5\n"
    "v_mul_f32 v63, v63, v5\n"
    "v_cvt_pk_bf16_f32 v230, v60, v61\n"
    "v_cvt_pk_bf16_f32 v231, v62, v63\n"
    "v_cndmask_b32 v2, v148, v147, s[76:77]\n"
    "v_cndmask_b32 v3, v148, v147, s[76:77]\n"
    "v_mul_f32 v64, v64, v2\n"
    "v_mul_f32 v65, v65, v2\n"
    "v_mul_f32 v66, v66, v3\n"
    "v_mul_f32 v67, v67, v3\n"
    "v_cvt_pk_bf16_f32 v232, v64, v65\n"
    "v_cvt_pk_bf16_f32 v233, v66, v67\n"
    "v_cndmask_b32 v4, v156, v155, s[76:77]\n"
    "v_cndmask_b32 v5, v156, v155, s[76:77]\n"
    "v_mul_f32 v68, v68, v4\n"
    "v_mul_f32 v69, v69, v4\n"
    "v_mul_f32 v70, v70, v5\n"
    "v_mul_f32 v71, v71, v5\n"
    "v_cvt_pk_bf16_f32 v236, v68, v69\n"
    "v_cvt_pk_bf16_f32 v237, v70, v71\n"
    "v_cndmask_b32 v2, v149, v148, s[62:63]\n"
    "v_cndmask_b32 v3, v149, v148, s[76:77]\n"
    "v_mul_f32 v72, v72, v2\n"
    "v_mul_f32 v73, v73, v2\n"
    "v_mul_f32 v74, v74, v3\n"
    "v_mul_f32 v75, v75, v3\n"
    "v_cvt_pk_bf16_f32 v234, v72, v73\n"
    "v_cvt_pk_bf16_f32 v235, v74, v75\n"
    "v_cndmask_b32 v4, v157, v156, s[62:63]\n"
    "v_cndmask_b32 v5, v157, v156, s[76:77]\n"
    "v_mul_f32 v76, v76, v4\n"
    "v_mul_f32 v77, v77, v4\n"
    "v_mul_f32 v78, v78, v5\n"
    "v_mul_f32 v79, v79, v5\n"
    "v_cvt_pk_bf16_f32 v238, v76, v77\n"
    "v_cvt_pk_bf16_f32 v239, v78, v79\n"
    "v_cndmask_b32 v2, v150, v149, s[62:63]\n"
    "v_cndmask_b32 v3, v150, v149, s[62:63]\n"
    "v_mul_f32 v80, v80, v2\n"
    "v_mul_f32 v81, v81, v2\n"
    "v_mul_f32 v82, v82, v3\n"
    "v_mul_f32 v83, v83, v3\n"
    "v_cvt_pk_bf16_f32 v240, v80, v81\n"
    "v_cvt_pk_bf16_f32 v241, v82, v83\n"
    "v_cndmask_b32 v4, v158, v157, s[62:63]\n"
    "v_cndmask_b32 v5, v158, v157, s[62:63]\n"
    "v_mul_f32 v84, v84, v4\n"
    "v_mul_f32 v85, v85, v4\n"
    "v_mul_f32 v86, v86, v5\n"
    "v_mul_f32 v87, v87, v5\n"
    "v_cvt_pk_bf16_f32 v244, v84, v85\n"
    "v_cvt_pk_bf16_f32 v245, v86, v87\n"
    "v_cndmask_b32 v3, v151, v150, s[62:63]\n"
    "v_mul_f32 v88, v88, v150\n"
    "v_mul_f32 v89, v89, v150\n"
    "v_mul_f32 v90, v90, v3\n"
    "v_mul_f32 v91, v91, v3\n"
    "v_cvt_pk_bf16_f32 v242, v88, v89\n"
    "v_cvt_pk_bf16_f32 v243, v90, v91\n"
    "v_cndmask_b32 v5, v159, v158, s[62:63]\n"
    "v_mul_f32 v92, v92, v158\n"
    "v_mul_f32 v93, v93, v158\n"
    "v_mul_f32 v94, v94, v5\n"
    "v_mul_f32 v95, v95, v5\n"
    "v_cvt_pk_bf16_f32 v246, v92, v93\n"
    "v_cvt_pk_bf16_f32 v247, v94, v95\n"
    "v_mul_f32 v96, v96, v151\n"
    "v_mul_f32 v97, v97, v151\n"
    "v_mul_f32 v98, v98, v151\n"
    "v_mul_f32 v99, v99, v151\n"
    "v_cvt_pk_bf16_f32 v248, v96, v97\n"
    "v_cvt_pk_bf16_f32 v249, v98, v99\n"
    "v_mul_f32 v100, v100, v159\n"
    "v_mul_f32 v101, v101, v159\n"
    "v_mul_f32 v102, v102, v159\n"
    "v_mul_f32 v103, v103, v159\n"
    "v_cvt_pk_bf16_f32 v252, v100, v101\n"
    "v_cvt_pk_bf16_f32 v253, v102, v103\n"
    "s_waitcnt lgkmcnt(7)\n"
    "s_mov_b32 s94, 20480\n"
    ".LL_AP2_%=:\n"
    "v_mfma_f32_16x16x32_bf16 v[32:35], v[104:107], v[216:219], 0\n"
    "s_waitcnt lgkmcnt(5)\n"
    "ds_read_b64_tr_b16 v[124:125], v21 offset:0\n"
    "v_mfma_f32_16x16x32_bf16 v[32:35], v[108:111], v[224:227], v[32:35]\n"
    "s_waitcnt lgkmcnt(4)\n"
    "ds_read_b64_tr_b16 v[126:127], v21 offset:2048\n"
    "v_cvt_pk_bf16_f32 v86, v56, v57\n"
    "v_mfma_f32_16x16x32_bf16 v[32:35], v[112:115], v[232:235], v[32:35]\n"
    "s_waitcnt lgkmcnt(3)\n"
    "ds_read_b64_tr_b16 v[128:129], v21 offset:4096\n"
    "v_cvt_pk_bf16_f32 v87, v58, v59\n"
    "v_mfma_f32_16x16x32_bf16 v[32:35], v[116:119], v[240:243], v[32:35]\n"
    "s_waitcnt lgkmcnt(3)\n"
    "ds_read_b64_tr_b16 v[130:131], v21 offset:6144\n"
    "v_mfma_f32_16x16x32_bf16 v[32:35], v[120:123], v[248:251], v[32:35]\n"
    "ds_read_b64_tr_b16 v[132:133], v21 offset:8192\n"
    "v_mfma_f32_16x16x32_bf16 v[36:39], v[104:107], v[220:223], 0\n"
    "s_cmp_eq_u32 s88, 3\n"
    "s_cselect_b32 s30, 4294823936, s94\n"
    "ds_read_b64_tr_b16 v[134:135], v21 offset:10240\n"
    "v_cvt_pk_bf16_f32 v94, v60, v61\n"
    "v_mfma_f32_16x16x32_bf16 v[36:39], v[108:111], v[228:231], v[36:39]\n"
    "ds_read_b64_tr_b16 v[136:137], v21 offset:12288\n"
    "v_cvt_pk_bf16_f32 v95, v62, v63\n"
    "v_mfma_f32_16x16x32_bf16 v[36:39], v[112:115], v[236:239], v[36:39]\n"
    "ds_read_b64_tr_b16 v[138:139], v21 offset:14336\n"
    "v_add_u32 v20, s30, v20\n"
    "v_mfma_f32_16x16x32_bf16 v[36:39], v[116:119], v[244:247], v[36:39]\n"
    "ds_read_b64_tr_b16 v[140:141], v21 offset:16384\n"
    "v_mfma_f32_16x16x32_bf16 v[36:39], v[120:123], v[252:255], v[36:39]\n"
    "s_waitcnt lgkmcnt(7)\n"
    "buffer_store_dwordx4 v[84:87], v7, s[44:47], s86 offen sc1\n"
    "v_mfma_f32_16x16x32_bf16 v[40:43], v[124:127], v[216:219], 0\n"
    "s_waitcnt lgkmcnt(5)\n"
    "ds_read_b64_tr_b16 v[104:105], v22 offset:0\n"
    "v_mfma_f32_16x16x32_bf16 v[40:43], v[128:131], v[224:227], v[40:43]\n"
    "s_waitcnt lgkmcnt(4)\n"
    "ds_read_b64_tr_b16 v[106:107], v22 offset:2048\n"
    "v_cvt_pk_bf16_f32 v80, v32, v33\n"
    "v_mfma_f32_16x16x32_bf16 v[40:43], v[132:135], v[232:235], v[40:43]\n"
    "s_waitcnt lgkmcnt(3)\n"
    "ds_read_b64_tr_b16 v[108:109], v22 offset:4096\n"
    "v_cvt_pk_bf16_f32 v81, v34, v35\n"
    "v_mfma_f32_16x16x32_bf16 v[40:43], v[136:139], v[240:243], v[40:43]\n"
    "s_waitcnt lgkmcnt(3)\n"
    "ds_read_b64_tr_b16 v[110:111], v22 offset:6144\n"
    "v_mfma_f32_16x16x32_bf16 v[40:43], v[140:143], v[248:251], v[40:43]\n"
    "ds_read_b64_tr_b16 v[112:113], v22 offset:8192\n"
    "v_mfma_f32_16x16x32_bf16 v[44:47], v[124:127], v[220:223], 0\n"
    "s_cmp_eq_u32 s88, 1\n"
    "s_cbranch_scc1 .LL_AC8_%=\n"
    ".LL_AC8R_%=:\n"
    "ds_read_b64_tr_b16 v[114:115], v22 offset:10240\n"
    "v_cvt_pk_bf16_f32 v88, v36, v37\n"
    "v_mfma_f32_16x16x32_bf16 v[44:47], v[128:131], v[228:231], v[44:47]\n"
    "ds_read_b64_tr_b16 v[116:117], v22 offset:12288\n"
    "v_cvt_pk_bf16_f32 v89, v38, v39\n"
    "v_mfma_f32_16x16x32_bf16 v[44:47], v[132:135], v[236:239], v[44:47]\n"
    "ds_read_b64_tr_b16 v[118:119], v22 offset:14336\n"
    "v_add_u32 v21, s30, v21\n"
    "v_mfma_f32_16x16x32_bf16 v[44:47], v[136:139], v[244:247], v[44:47]\n"
    "ds_read_b64_tr_b16 v[120:121], v22 offset:16384\n"
    "v_mfma_f32_16x16x32_bf16 v[44:47], v[140:143], v[252:255], v[44:47]\n"
    "s_waitcnt lgkmcnt(7)\n"
    "buffer_store_dwordx4 v[92:95], v9, s[44:47], s86 offen sc1\n"
    "v_mfma_f32_16x16x32_bf16 v[48:51], v[104:107], v[216:219], 0\n"
    "s_waitcnt lgkmcnt(5)\n"
    "ds_read_b64_tr_b16 v[124:125], v23 offset:0\n"
    "v_mfma_f32_16x16x32_bf16 v[48:51], v[108:111], v[224:227], v[48:51]\n"
    "s_waitcnt lgkmcnt(4)\n"
    "ds_read_b64_tr_b16 v[126:127], v23 offset:2048\n"
    "v_cvt_pk_bf16_f32 v82, v40, v41\n"
    "v_mfma_f32_16x16x32_bf16 v[48:51], v[112:115], v[232:235], v[48:51]\n"
    "s_waitcnt lgkmcnt(3)\n"
    "ds_read_b64_tr_b16 v[128:129], v23 offset:4096\n"
    "v_cvt_pk_bf16_f32 v83, v42, v43\n"
    "v_mfma_f32_16x16x32_bf16 v[48:51], v[116:119], v[240:243], v[48:51]\n"
    "s_waitcnt lgkmcnt(3)\n"
    "ds_read_b64_tr_b16 v[130:131], v23 offset:6144\n"
    "v_mfma_f32_16x16x32_bf16 v[48:51], v[120:123], v[248:251], v[48:51]\n"
    "ds_read_b64_tr_b16 v[132:133], v23 offset:8192\n"
    "v_mfma_f32_16x16x32_bf16 v[52:55], v[104:107], v[220:223], 0\n"
    "s_cmp_eq_u32 s88, 7\n"
    "s_cbranch_scc1 .LL_AW8_%=\n"
    ".LL_AW8R_%=:\n"
    "ds_read_b64_tr_b16 v[134:135], v23 offset:10240\n"
    "v_cvt_pk_bf16_f32 v90, v44, v45\n"
    "v_mfma_f32_16x16x32_bf16 v[52:55], v[108:111], v[228:231], v[52:55]\n"
    "s_mov_b32 s86, s95\n"
    "ds_read_b64_tr_b16 v[136:137], v23 offset:12288\n"
    "v_cvt_pk_bf16_f32 v91, v46, v47\n"
    "v_mfma_f32_16x16x32_bf16 v[52:55], v[112:115], v[236:239], v[52:55]\n"
    "ds_read_b64_tr_b16 v[138:139], v23 offset:14336\n"
    "v_add_u32 v22, s30, v22\n"
    "v_mfma_f32_16x16x32_bf16 v[52:55], v[116:119], v[244:247], v[52:55]\n"
    "ds_read_b64_tr_b16 v[140:141], v23 offset:16384\n"
    "v_mfma_f32_16x16x32_bf16 v[52:55], v[120:123], v[252:255], v[52:55]\n"
    "s_waitcnt lgkmcnt(7)\n"
    "buffer_store_dwordx4 v[80:83], v6, s[44:47], s95 offen sc1\n"
    "v_mfma_f32_16x16x32_bf16 v[56:59], v[124:127], v[216:219], 0\n"
    "s_waitcnt lgkmcnt(5)\n"
    "ds_read_b64_tr_b16 v[104:105], v20 offset:0\n"
    "v_mfma_f32_16x16x32_bf16 v[56:59], v[128:131], v[224:227], v[56:59]\n"
    "s_waitcnt lgkmcnt(4)\n"
    "ds_read_b64_tr_b16 v[106:107], v20 offset:2048\n"
    "v_cvt_pk_bf16_f32 v84, v48, v49\n"
    "v_mfma_f32_16x16x32_bf16 v[56:59], v[132:135], v[232:235], v[56:59]\n"
    "s_waitcnt lgkmcnt(3)\n"
    "ds_read_b64_tr_b16 v[108:109], v20 offset:4096\n"
    "v_cvt_pk_bf16_f32 v85, v50, v51\n"
    "v_mfma_f32_16x16x32_bf16 v[56:59], v[136:139], v[240:243], v[56:59]\n"
    "s_waitcnt lgkmcnt(3)\n"
    "ds_read_b64_tr_b16 v[110:111], v20 offset:6144\n"
    "v_mfma_f32_16x16x32_bf16 v[56:59], v[140:143], v[248:251], v[56:59]\n"
    "ds_read_b64_tr_b16 v[112:113], v20 offset:8192\n"
    "v_mfma_f32_16x16x32_bf16 v[60:63], v[124:127], v[220:223], 0\n"
    "s_add_u32 s88, s88, 1\n"
    "ds_read_b64_tr_b16 v[114:115], v20 offset:10240\n"
    "v_cvt_pk_bf16_f32 v92, v52, v53\n"
    "v_mfma_f32_16x16x32_bf16 v[60:63], v[128:131], v[228:231], v[60:63]\n"
    "ds_read_b64_tr_b16 v[116:117], v20 offset:12288\n"
    "v_cvt_pk_bf16_f32 v93, v54, v55\n"
    "v_mfma_f32_16x16x32_bf16 v[60:63], v[132:135], v[236:239], v[60:63]\n"
    "ds_read_b64_tr_b16 v[118:119], v20 offset:14336\n"
    "v_add_u32 v23, s30, v23\n"
    "v_mfma_f32_16x16x32_bf16 v[60:63], v[136:139], v[244:247], v[60:63]\n"
    "ds_read_b64_tr_b16 v[120:121], v20 offset:16384\n"
    "v_mfma_f32_16x16x32_bf16 v[60:63], v[140:143], v[252:255], v[60:63]\n"
    "s_waitcnt lgkmcnt(7)\n"
    "buffer_store_dwordx4 v[88:91], v8, s[44:47], s95 offen sc1\n"
    "s_add_u32 s95, s95, 16384\n"
    "s_cmp_lg_u32 s88, 9\n"
    "s_cbranch_scc1 .LL_AP2_%=\n"
    "s_nop 3\n"
    "v_cvt_pk_bf16_f32 v86, v56, v57\n"
    "v_cvt_pk_bf16_f32 v87, v58, v59\n"
    "v_cvt_pk_bf16_f32 v94, v60, v61\n"
    "v_cvt_pk_bf16_f32 v95, v62, v63\n"
    "buffer_store_dwordx4 v[84:87], v7, s[44:47], s86 offen sc1\n"
    "buffer_store_dwordx4 v[92:95], v9, s[44:47], s86 offen sc1\n"
    "s_mov_b32 s31, s97\n"
    "s_waitcnt lgkmcnt(0)\n"
    "v_min_u32 v210, 11, v1\n"
    "v_lshlrev_b32 v210, 7, v210\n"
    "v_and_b32 v2, 3, v1\n"
    "v_lshlrev_b32 v211, 7, v2\n"
    "v_cmp_gt_u32 vcc, 8, %0\n"
    "v_lshlrev_b32 v4, 4, v1\n"
    "v_mov_b32 v168, 1879048192\n"
    "s_lshl_b32 s60, s80, 8\n"
    "v_cndmask_b32 v168, v168, v4, vcc\n"
    "v_bfe_u32 v3, v1, 2, 2\n"
    "v_lshlrev_b32 v3, 4, v3\n"
    "v_lshl_add_u32 v3, v2, 6, v3\n"
    "v_lshrrev_b32 v4, 4, v1\n"
    "v_lshl_add_u32 v3, v4, 10, v3\n"
    "v_add_u32 v212, s60, v3\n"
    "v_add_u32 v213, 0x1000, v212\n"
    "v_add_u32 v214, 0x2000, v212\n"
    "v_add_u32 v215, 0x3000, v212\n"
    "v_lshlrev_b32 v3, 4, v1\n"
    "s_lshl_b32 s60, s80, 13\n"
    "s_add_u32 s60, s60, 131072\n"
    "v_add_u32 v170, s60, v3\n"
    "s_lshl_b32 s60, s79, 4\n"
    "s_add_u32 s60, s60, s83\n"
    "s_lshl_b32 s98, s79, 11\n"
    "s_add_u32 s98, s98, 196608\n"
    "s_lshl_b32 s97, s83, 7\n"
    "s_add_u32 s97, s97, s98\n"
    "s_mul_i32 s99, s60, 147456\n"
    "s_add_u32 s99, s99, 4194304\n"
    "v_and_b32 v3, 15, v1\n"
    "v_and_b32 v4, 1, v3\n"
    "v_lshlrev_b32 v4, 4, v4\n"
    "v_mov_b32 v5, 0x3f80\n"
    "v_lshlrev_b32 v4, v4, v5\n"
    "v_lshrrev_b32 v5, 1, v3\n"
    "v_cmp_eq_u32 vcc, 0, v5\n"
    "v_cmp_eq_u32 s[62:63], 1, v5\n"
    "s_nop 0\n"
    "v_cndmask_b32 v172, 0, v4, vcc\n"
    "v_cndmask_b32 v173, 0, v4, s[62:63]\n"
    "v_cmp_eq_u32 vcc, 2, v5\n"
    "v_cmp_eq_u32 s[62:63], 3, v5\n"
    "s_nop 0\n"
    "v_cndmask_b32 v174, 0, v4, vcc\n"
    "v_cndmask_b32 v175, 0, v4, s[62:63]\n"
    "v_cmp_eq_u32 vcc, 4, v5\n"
    "v_cmp_eq_u32 s[62:63], 5, v5\n"
    "s_nop 0\n"
    "v_cndmask_b32 v176, 0, v4, vcc\n"
    "v_cndmask_b32 v177, 0, v4, s[62:63]\n"
    "v_cmp_eq_u32 vcc, 6, v5\n"
    "v_cmp_eq_u32 s[62:63], 7, v5\n"
    "s_nop 0\n"
    "v_cndmask_b32 v178, 0, v4, vcc\n"
    "v_cndmask_b32 v179, 0, v4, s[62:63]\n"
    "s_lshr_b32 s60, s83, 1\n"
    "s_lshl_b32 s60, s60, 2\n"
    "s_add_u32 s60, s60, s80\n"
    "s_lshl_b32 s60, s60, 2\n"
    "v_lshrrev_b32 v3, 2, v3\n"
    "v_add_u32 v3, s60, v3\n"
    "v_mul_u32_u24 v171, 1152, v3\n"
    "s_mov_b64 s[48:49], %9\n"
    "s_mov_b32 s50, 1152\n"
    "s_mov_b32 s51, 0x20000\n"
    "v_lshrrev_b32 v3, 4, v1\n"
    "v_and_b32 v2, 3, v1\n"
    "v_lshl_add_u32 v4, v3, 2, v2\n"
    "v_lshlrev_b32 v4, 4, v4\n"
    "v_add_u32 v5, v4, v171\n"
    "buffer_load_dwordx4 v[132:135], v4, s[48:51], 0 offen offset:0\n"
    "buffer_load_dwordx4 v[136:139], v4, s[48:51], 0 offen offset:256\n"
    "buffer_load_dwordx4 v[140:143], v4, s[48:51], 0 offen offset:512\n"
    "buffer_load_dwordx4 v[144:147], v4, s[48:51], 0 offen offset:768\n"
    "buffer_load_dwordx4 v[148:151], v4, s[48:51], 0 offen offset:1024\n"
    "buffer_load_dwordx4 v[112:115], v5, s[40:43], 0 offen offset:0\n"
    "buffer_load_dwordx4 v[116:119], v5, s[40:43], 0 offen offset:256\n"
    "buffer_load_dwordx4 v[120:123], v5, s[40:43], 0 offen offset:512\n"
    "buffer_load_dwordx4 v[124:127], v5, s[40:43], 0 offen offset:768\n"
    "v_cmp_gt_u32 vcc, 32, v1\n"
    "v_mov_b32 v4, 1879048192\n"
    "s_and_b32 s30, s83, 1\n"
    "v_cndmask_b32 v5, v4, v5, vcc\n"
    "s_mov_b64 s[48:49], %8\n"
    "buffer_load_dwordx4 v[128:131], v5, s[40:43], 0 offen offset:1024\n"
    "s_lshl_b32 s30, s30, 6\n"
    "v_lshlrev_b32 v2, 3, v2\n"
    "v_and_b32 v4, 1, v3\n"
    "v_lshl_add_u32 v2, v4, 5, v2\n"
    "v_lshrrev_b32 v4, 1, v3\n"
    "v_lshl_add_u32 v2, v4, 7, v2\n"
    "v_add_u32 v2, s30, v2\n"
    "v_add_u32 v171, v2, v171\n"
    "buffer_load_dwordx2 v[200:201], v2, s[48:51], 0 offen offset:0\n"
    "buffer_load_dwordx2 v[202:203], v2, s[48:51], 0 offen offset:256\n"
    "buffer_load_dwordx2 v[204:205], v2, s[48:51], 0 offen offset:512\n"
    "buffer_load_dwordx2 v[206:207], v2, s[48:51], 0 offen offset:768\n"
    "buffer_load_dwordx2 v[208:209], v2, s[48:51], 0 offen offset:1024\n"
    "s_waitcnt vmcnt(15) lgkmcnt(0)\n"
    "s_barrier\n"
    "buffer_store_dwordx4 v[164:167], v168, s[44:47], s31 offen sc1\n"
    "s_cmp_eq_u32 %1, 192\n"
    "s_cbranch_scc0 .LL_SKEP_%=\n"
    "s_cmp_eq_u32 s80, 0\n"
    "s_cbranch_scc0 .LL_SKEP_%=\n"
    "v_lshlrev_b32 v2, 9, v1\n"
    "s_mov_b32 s28, 0\n"
    "s_mov_b32 s29, 65536\n"
    ".LL_EPA_%=:\n"
    "buffer_load_dwordx2 v[216:217], v2, s[44:47], s29 offen offset:0 sc1\n"
    "buffer_load_dwordx2 v[218:219], v2, s[44:47], s29 offen offset:128 sc1\n"
    "buffer_load_dwordx2 v[220:221], v2, s[44:47], s29 offen offset:256 sc1\n"
    "buffer_load_dwordx2 v[222:223], v2, s[44:47], s29 offen offset:384 sc1\n"
    "s_waitcnt vmcnt(0)\n"
    "v_cmp_ne_u64 vcc, v[164:165], v[216:217]\n"
    "v_cmp_ne_u64 s[62:63], v[164:165], v[218:219]\n"
    "s_or_b64 vcc, vcc, s[62:63]\n"
    "v_cmp_ne_u64 s[62:63], v[164:165], v[220:221]\n"
    "s_or_b64 vcc, vcc, s[62:63]\n"
    "v_cmp_ne_u64 s[62:63], v[164:165], v[222:223]\n"
    "s_or_b64 vcc, vcc, s[62:63]\n"
    "s_add_u32 s28, s28, 1\n"
    "s_cmp_gt_u32 s28, 32768\n"
    "s_cbranch_scc1 .LL_EPB_%=\n"
    "s_cbranch_vccnz .LL_EPA_%=\n"
    ".LL_EPB_%=:\n"
    "buffer_store_dwordx4 v[164:167], v168, s[44:47], 0 offen sc1\n"
    ".LL_SKEP_%=:\n"
    "s_waitcnt vmcnt(0)\n"
    "v_dot2_f32_bf16 v152, v112, v132, 0\n"
    "v_dot2_f32_bf16 v153, v113, v133, 0\n"
    "v_dot2_f32_bf16 v152, v114, v134, v152\n"
    "v_dot2_f32_bf16 v153, v115, v135, v153\n"
    "v_dot2_f32_bf16 v152, v116, v136, v152\n"
    "v_dot2_f32_bf16 v153, v117, v137, v153\n"
    "v_dot2_f32_bf16 v152, v118, v138, v152\n"
    "v_dot2_f32_bf16 v153, v119, v139, v153\n"
    "v_dot2_f32_bf16 v152, v120, v140, v152\n"
    "v_dot2_f32_bf16 v153, v121, v141, v153\n"
    "v_dot2_f32_bf16 v152, v122, v142, v152\n"
    "v_dot2_f32_bf16 v153, v123, v143, v153\n"
    "v_dot2_f32_bf16 v152, v124, v144, v152\n"
    "v_dot2_f32_bf16 v153, v125, v145, v153\n"
    "v_dot2_f32_bf16 v152, v126, v146, v152\n"
    "v_dot2_f32_bf16 v153, v127, v147, v153\n"
    "v_dot2_f32_bf16 v152, v128, v148, v152\n"
    "v_dot2_f32_bf16 v153, v129, v149, v153\n"
    "v_dot2_f32_bf16 v152, v130, v150, v152\n"
    "v_dot2_f32_bf16 v153, v131, v151, v153\n"
    "s_mov_b32 s60, 0xffff0000\n"
    "s_nop 2\n"
    "v_add_f32 v152, v152, v153\n"
    "v_lshlrev_b32 v32, 16, v200\n"
    "v_and_b32 v33, s60, v200\n"
    "v_add_f32_dpp v152, v152, v152 quad_perm:[1,0,3,2] row_mask:0xf bank_mask:0xf\n"
    "v_lshlrev_b32 v34, 16, v201\n"
    "v_and_b32 v35, s60, v201\n"
    "v_add_f32_dpp v152, v152, v152 quad_perm:[2,3,0,1] row_mask:0xf bank_mask:0xf\n"
    "v_lshlrev_b32 v36, 16, v202\n"
    "v_mov_b32 v153, v152\n"
    "v_and_b32 v37, s60, v202\n"
    "v_lshlrev_b32 v38, 16, v203\n"
    "v_permlane16_swap_b32 v152, v153\n"
    "v_and_b32 v39, s60, v203\n"
    "v_add_f32 v152, v152, v153\n"
    "v_lshlrev_b32 v40, 16, v204\n"
    "v_mov_b32 v153, v152\n"
    "v_and_b32 v41, s60, v204\n"
    "v_lshlrev_b32 v42, 16, v205\n"
    "v_permlane32_swap_b32 v152, v153\n"
    "v_and_b32 v43, s60, v205\n"
    "v_add_f32 v152, v152, v153\n"
    "v_lshlrev_b32 v44, 16, v206\n"
    "v_mul_f32 v152, 0x3fb8aa3b, v152\n"
    "v_and_b32 v45, s60, v206\n"
    "v_exp_f32 v153, v152\n"
    "v_exp_f32 v152, -v152\n"
    "v_lshlrev_b32 v46, 16, v207\n"
    "v_add_f32 v153, 1.0, v153\n"
    "v_add_f32 v152, 1.0, v152\n"
    "v_and_b32 v47, s60, v207\n"
    "v_rcp_f32 v154, v153\n"
    "v_rcp_f32 v152, v152\n"
    "v_lshlrev_b32 v48, 16, v208\n"
    "v_and_b32 v49, s60, v208\n"
    "v_lshlrev_b32 v50, 16, v209\n"
    "v_and_b32 v51, s60, v209\n"
    "v_mul_f32 v153, 0x40088e99, v154\n"
    "v_mul_f32 v32, v32, v154\n"
    "v_mul_f32 v33, v33, v154\n"
    "v_mul_f32 v34, v34, v154\n"
    "v_mul_f32 v35, v35, v154\n"
    "v_mul_f32 v36, v36, v154\n"
    "v_mul_f32 v37, v37, v154\n"
    "v_mul_f32 v38, v38, v154\n"
    "v_mul_f32 v39, v39, v154\n"
    "v_mul_f32 v40, v40, v154\n"
    "v_mul_f32 v41, v41, v154\n"
    "v_mul_f32 v42, v42, v154\n"
    "v_mul_f32 v43, v43, v154\n"
    "v_mul_f32 v44, v44, v154\n"
    "v_mul_f32 v45, v45, v154\n"
    "v_mul_f32 v46, v46, v154\n"
    "v_mul_f32 v47, v47, v154\n"
    "v_mul_f32 v48, v48, v154\n"
    "v_mul_f32 v49, v49, v154\n"
    "v_mul_f32 v50, v50, v154\n"
    "v_mul_f32 v51, v51, v154\n"
    "s_mov_b32 s28, 0\n"
    ".LL_SPA0_%=:\n"
    "s_sleep 8\n"
    "s_add_u32 s30, s98, 65536\n"
    "buffer_load_dwordx2 v[180:181], v210, s[44:47], s30 offen sc1\n"
    "s_waitcnt vmcnt(0)\n"
    ".LL_SPB0_%=:\n"
    "v_cmp_ne_u64 vcc, v[164:165], v[180:181]\n"
    "s_add_u32 s28, s28, 1\n"
    "s_cmp_gt_u32 s28, 32768\n"
    "s_cbranch_scc1 .LL_SPC0_%=\n"
    "s_cbranch_vccnz .LL_SPA0_%=\n"
    ".LL_SPC0_%=:\n"
    "s_add_u32 s29, s99, 0\n"
    "buffer_load_dwordx4 v[216:219], v212, s[44:47], s29 offen sc1\n"
    "buffer_load_dwordx4 v[220:223], v213, s[44:47], s29 offen sc1\n"
    "buffer_load_dwordx4 v[224:227], v214, s[44:47], s29 offen sc1\n"
    "s_add_u32 s29, s99, 16384\n"
    "buffer_load_dwordx4 v[228:231], v212, s[44:47], s29 offen sc1\n"
    "buffer_load_dwordx4 v[232:235], v213, s[44:47], s29 offen sc1\n"
    "buffer_load_dwordx4 v[236:239], v214, s[44:47], s29 offen sc1\n"
    "s_add_u32 s29, s99, 32768\n"
    "buffer_load_dwordx4 v[240:243], v212, s[44:47], s29 offen sc1\n"
    "buffer_load_dwordx4 v[244:247], v213, s[44:47], s29 offen sc1\n"
    "buffer_load_dwordx4 v[248:251], v214, s[44:47], s29 offen sc1\n"
    "s_waitcnt vmcnt(6)\n"
    "v_mfma_f32_16x16x32_bf16 v[116:119], v[172:175], v[216:219], 0\n"
    "v_mfma_f32_16x16x32_bf16 v[116:119], v[172:175], v[220:223], v[116:119]\n"
    "v_mfma_f32_16x16x32_bf16 v[116:119], v[172:175], v[224:227], v[116:119]\n"
    "s_waitcnt vmcnt(3)\n"
    "v_mfma_f32_16x16x32_bf16 v[120:123], v[172:175], v[228:231], 0\n"
    "v_mfma_f32_16x16x32_bf16 v[120:123], v[172:175], v[232:235], v[120:123]\n"
    "v_mfma_f32_16x16x32_bf16 v[120:123], v[172:175], v[236:239], v[120:123]\n"
    "s_waitcnt vmcnt(0)\n"
    "v_mfma_f32_16x16x32_bf16 v[124:127], v[172:175], v[240:243], 0\n"
    "v_mfma_f32_16x16x32_bf16 v[124:127], v[172:175], v[244:247], v[124:127]\n"
    "v_mfma_f32_16x16x32_bf16 v[124:127], v[172:175], v[248:251], v[124:127]\n"
    "s_nop 7\n"
    "ds_write_b128 v170, v[116:119] offset:1536\n"
    "ds_write_b128 v170, v[120:123] offset:2048\n"
    "ds_write_b128 v170, v[124:127] offset:2560\n"
    "s_mov_b32 s28, 0\n"
    ".LL_SPA1_%=:\n"
    "s_sleep 8\n"
    "s_add_u32 s30, s98, 163840\n"
    "buffer_load_dwordx2 v[180:181], v210, s[44:47], s30 offen sc1\n"
    "s_waitcnt vmcnt(0)\n"
    ".LL_SPB1_%=:\n"
    "v_cmp_ne_u64 vcc, v[164:165], v[180:181]\n"
    "s_add_u32 s28, s28, 1\n"
    "s_cmp_gt_u32 s28, 32768\n"
    "s_cbranch_scc1 .LL_SPC1_%=\n"
    "s_cbranch_vccnz .LL_SPA1_%=\n"
    ".LL_SPC1_%=:\n"
    "s_add_u32 s29, s99, 49152\n"
    "buffer_load_dwordx4 v[216:219], v212, s[44:47], s29 offen sc1\n"
    "buffer_load_dwordx4 v[220:223], v213, s[44:47], s29 offen sc1\n"
    "buffer_load_dwordx4 v[224:227], v214, s[44:47], s29 offen sc1\n"
    "s_add_u32 s29, s99, 65536\n"
    "buffer_load_dwordx4 v[228:231], v212, s[44:47], s29 offen sc1\n"
    "buffer_load_dwordx4 v[232:235], v213, s[44:47], s29 offen sc1\n"
    "buffer_load_dwordx4 v[236:239], v214, s[44:47], s29 offen sc1\n"
    "s_add_u32 s29, s99, 81920\n"
    "buffer_load_dwordx4 v[240:243], v212, s[44:47], s29 offen sc1\n"
    "buffer_load_dwordx4 v[244:247], v213, s[44:47], s29 offen sc1\n"
    "buffer_load_dwordx4 v[248:251], v214, s[44:47], s29 offen sc1\n"
    "s_waitcnt vmcnt(6)\n"
    "v_mfma_f32_16x16x32_bf16 v[116:119], v[172:175], v[216:219], 0\n"
    "v_mfma_f32_16x16x32_bf16 v[116:119], v[172:175], v[220:223], v[116:119]\n"
    "v_mfma_f32_16x16x32_bf16 v[116:119], v[172:175], v[224:227], v[116:119]\n"
    "s_waitcnt vmcnt(3)\n"
    "v_mfma_f32_16x16x32_bf16 v[120:123], v[172:175], v[228:231], 0\n"
    "v_mfma_f32_16x16x32_bf16 v[120:123], v[172:175], v[232:235], v[120:123]\n"
    "v_mfma_f32_16x16x32_bf16 v[120:123], v[172:175], v[236:239], v[120:123]\n"
    "s_waitcnt vmcnt(0)\n"
    "v_mfma_f32_16x16x32_bf16 v[124:127], v[172:175], v[240:243], 0\n"
    "v_mfma_f32_16x16x32_bf16 v[124:127], v[172:175], v[244:247], v[124:127]\n"
    "v_mfma_f32_16x16x32_bf16 v[124:127], v[172:175], v[248:251], v[124:127]\n"
    "s_nop 7\n"
    "ds_write_b128 v170, v[116:119] offset:3072\n"
    "ds_write_b128 v170, v[120:123] offset:3584\n"
    "ds_write_b128 v170, v[124:127] offset:4096\n"
    "v_add_u32 v170, 4096, v170\n"
    "s_branch .LL_SLOW2_%=\n"
    ".LL_AC8_%=:\n"
    "s_barrier\n"
    "s_mov_b32 s82, 1024\n"
    "s_add_u32 m0, s89, 81920\n"
    "s_nop 0\n"
    "buffer_load_dwordx4 v15, s[52:55], s82 offen lds\n"
    "buffer_load_dwordx4 v16, s[52:55], s82 offen offset:1024 lds\n"
    "buffer_load_dwordx4 v17, s[52:55], s82 offen offset:2048 lds\n"
    "buffer_load_dwordx4 v18, s[52:55], s82 offen offset:3072 lds\n"
    "s_add_u32 m0, s89, 86016\n"
    "s_nop 0\n"
    "buffer_load_dwordx4 v19, s[52:55], s82 offen lds\n"
    "s_branch .LL_AC8R_%=\n"
    ".LL_AW8_%=:\n"
    "s_waitcnt vmcnt(16)\n"
    "s_barrier\n"
    "s_branch .LL_AW8R_%=\n"
    ".LL_H1RETRY_%=:\n"
    "s_mov_b32 s28, 0\n"
    ".LL_H1R_%=:\n"
    "s_sleep 1\n"
    "buffer_load_dwordx2 v[158:159], v29, s[44:47], s98 offen sc1\n"
    "s_waitcnt vmcnt(0)\n"
    "v_cmp_ne_u64 vcc, v[164:165], v[158:159]\n"
    "s_add_u32 s28, s28, 1\n"
    "s_cmp_gt_u32 s28, 32768\n"
    "s_cbranch_scc1 .LL_H1OK_%=\n"
    "s_cbranch_vccnz .LL_H1R_%=\n"
    "s_branch .LL_H1OK_%=\n"
    ".LL_H2RETRY_%=:\n"
    "s_mov_b32 s28, 0\n"
    ".LL_H2R_%=:\n"
    "s_sleep 1\n"
    "buffer_load_dwordx2 v[158:159], v29, s[44:47], s31 offen sc1\n"
    "s_waitcnt vmcnt(0)\n"
    "v_cmp_ne_u64 vcc, v[164:165], v[158:159]\n"
    "s_add_u32 s28, s28, 1\n"
    "s_cmp_gt_u32 s28, 32768\n"
    "s_cbranch_scc1 .LL_H2OK_%=\n"
    "s_cbranch_vccnz .LL_H2R_%=\n"
    "s_branch .LL_H2OK_%=\n"
    ".LL_CODEEND_%=:\n"
  :: "v"(threadIdx.x),"s"(blockIdx.x),"s"(x),"s"(wqkv),"s"(wo),"s"(w1),"s"(b1),"s"(w2),"s"(b2),"s"(gw),"s"(out),"s"(ws)
  : "v1","v2","v3","v4","v5","v6","v7","v8","v9","v10","v11","v12","v13","v14","v15","v16","v17","v18","v19","v20","v21","v22","v23","v24","v25","v26","v27","v28","v29","v30","v31","v32","v33","v34","v35","v36","v37","v38","v39","v40","v41","v42","v43","v44","v45","v46","v47","v48","v49","v50","v51","v52","v53","v54","v55","v56","v57","v58","v59","v60","v61","v62","v63","v64","v65","v66","v67","v68","v69","v70","v71","v72","v73","v74","v75","v76","v77","v78","v79","v80","v81","v82","v83","v84","v85","v86","v87","v88","v89","v90","v91","v92","v93","v94","v95","v96","v97","v98","v99","v100","v101","v102","v103","v104","v105","v106","v107","v108","v109","v110","v111","v112","v113","v114","v115","v116","v117","v118","v119","v120","v121","v122","v123","v124","v125","v126","v127","v128","v129","v130","v131","v132","v133","v134","v135","v136","v137","v138","v139","v140","v141","v142","v143","v144","v145","v146","v147","v148","v149","v150","v151","v152","v153","v154","v155","v156","v157","v158","v159","v160","v161","v162","v163","v164","v165","v166","v167","v168","v169","v170","v171","v172","v173","v174","v175","v176","v177","v178","v179","v180","v181","v182","v183","v184","v185","v186","v187","v188","v189","v190","v191","v192","v193","v194","v195","v196","v197","v198","v199","v200","v201","v202","v203","v204","v205","v206","v207","v208","v209","v210","v211","v212","v213","v214","v215","v216","v217","v218","v219","v220","v221","v222","v223","v224","v225","v226","v227","v228","v229","v230","v231","v232","v233","v234","v235","v236","v237","v238","v239","v240","v241","v242","v243","v244","v245","v246","v247","v248","v249","v250","v251","v252","v253","v254","v255","a0","a1","a2","a3","a4","a5","a6","a7","a8","a9","a10","a11","a12","a13","a14","a15","a16","a17","a18","a19","a20","a21","a22","a23","a24","a25","a26","a27","a28","a29","a30","a31","a32","a33","a34","a35","a36","a37","a38","a39","a40","a41","a42","a43","a44","a45","a46","a47","a48","a49","a50","a51","a52","a53","a54","a55","a56","a57","a58","a59","a60","a61","a62","a63","a64","a65","a66","a67","a68","a69","a70","a71","a72","a73","a74","a75","a76","a77","a78","a79","a80","a81","a82","a83","a84","a85","a86","a87","a88","a89","a90","a91","a92","a93","a94","a95","a96","a97","a98","a99","a100","a101","a102","a103","a104","a105","a106","a107","a108","a109","a110","a111","a112","a113","a114","a115","a116","a117","a118","a119","a120","a121","a122","a123","a124","a125","a126","a127","a128","a129","a130","a131","a132","a133","a134","a135","a136","a137","a138","a139","a140","a141","a142","a143","a144","a145","a146","a147","a148","a149","a150","a151","a152","a153","a154","a155","a156","a157","a158","a159","a160","a161","a162","a163","a164","a165","a166","a167","a168","a169","a170","a171","a172","a173","a174","a175","a176","a177","a178","a179","a180","a181","a182","a183","a184","a185","a186","a187","a188","a189","a190","a191","a192","a193","a194","a195","a196","a197","a198","a199","a200","a201","a202","a203","a204","a205","a206","a207","a208","a209","a210","a211","a212","a213","a214","a215","a216","a217","a218","a219","a220","a221","a222","a223","a224","a225","a226","a227","a228","a229","a230","a231","a232","a233","a234","a235","a236","a237","a238","a239","a240","a241","a242","a243","a244","a245","a246","a247","a248","a249","a250","a251","a252","a253","a254","a255","s24","s25","s26","s27","s28","s29","s30","s31","s36","s37","s38","s39","s40","s41","s42","s43","s44","s45","s46","s47","s48","s49","s50","s51","s52","s53","s54","s55","s56","s57","s58","s59","s60","s61","s62","s63","s64","s65","s66","s67","s68","s69","s70","s71","s72","s73","s74","s75","s76","s77","s78","s79","s80","s81","s82","s83","s84","s85","s86","s87","s88","s89","s90","s91","s92","s93","s94","s95","s96","s97","s98","s99","vcc","scc","memory");
}
extern "C" void kernel_launch(void* const* d_in, const int* in_sizes, int n_in, void* d_out, int out_size, void* d_ws, size_t ws_size, hipStream_t stream) {
  (void)hipFuncSetAttribute((const void*)fused,hipFuncAttributeMaxDynamicSharedMemorySize,163840);
  fused<<<256,256,163840,stream>>>((const u16*)d_in[0],(const u16*)d_in[1],(const u16*)d_in[2],(const u16*)d_in[4],(const u16*)d_in[5],(const u16*)d_in[6],(const u16*)d_in[7],(const u16*)d_in[8],(u16*)d_out,(char*)d_ws);
}
```
